```python
import torch

def _softmax(x, axis=-1):
    return torch.softmax(x, dim=axis)
def _gelu(x):
    return torch.nn.functional.gelu(x, approximate="tanh")
def _silu(x):
    return torch.nn.functional.silu(x)
def _sigmoid(x):
    return torch.sigmoid(x)
def _layernorm(x, axis=-1, eps=1e-5):
    m = x.mean(dim=axis, keepdim=True)
    v = x.var(dim=axis, keepdim=True, unbiased=False)
    return (x - m) / torch.sqrt(v + eps)
def _scatter_add(tgt, idx, src, axis=0):
    return tgt.clone().index_add_(axis, idx, src)
def _segment_sum(src, seg, n):
    out = torch.zeros((n,) + src.shape[1:], dtype=src.dtype)
    return out.index_add_(0, seg, src)
def _pad(x, pw):
    flat = tuple(v for pair in reversed(pw) for v in pair)
    return torch.nn.functional.pad(x, flat)

kernel_name = 'sequential_preLN__spectral_gfnet__glu_gated_geglu__prompt'

def setup_inputs():

    _w = (lambda a: (lambda t: t.to(torch.bfloat16) if t.is_floating_point() else t)(torch.as_tensor(a)))
    def _A_setup():
        B, S, D = 1, 2048, 512
        Df = D // 2 + 1
        return {
            "x": _w((lambda *s: torch.randn(*s, dtype=torch.bfloat16) * 0.05)(B, S, D)),
            "w_r": _w((lambda *s: torch.randn(*s, dtype=torch.bfloat16) * 0.4)(S, Df) * 0.02),
            "w_i": _w((lambda *s: torch.randn(*s, dtype=torch.bfloat16) * 0.4)(S, Df) * 0.02),
        }
    _A_in = _A_setup()

    def _B_setup():
        B, S, Dm, Df = 1, 2048, 512, 1376
        return {
            "x": _w((lambda *s: torch.randn(*s, dtype=torch.bfloat16) * 0.05)(B, S, Dm)),
            "w_gate": _w((lambda *s: torch.randn(*s, dtype=torch.bfloat16) * 0.4)(Dm, Df) * (Dm ** -0.5)),
            "w_up": _w((lambda *s: torch.randn(*s, dtype=torch.bfloat16) * 0.4)(Dm, Df) * (Dm ** -0.5)),
            "w_down": _w((lambda *s: torch.randn(*s, dtype=torch.bfloat16) * 0.4)(Df, Dm) * (Df ** -0.5)),
        }
    _B_in = _B_setup()

    return {
        "A_x": _A_in["x"],
        "A_w_r": _A_in["w_r"],
        "A_w_i": _A_in["w_i"],
        "B_x": _B_in["x"],
        "B_w_gate": _B_in["w_gate"],
        "B_w_up": _B_in["w_up"],
        "B_w_down": _B_in["w_down"],
    }

def reference(A_x, A_w_r, A_w_i, B_x, B_w_gate, B_w_up, B_w_down):
    def _A_ref(x, w_r, w_i):
        B, S, D = x.shape
        X = (lambda x, axes=(-2,-1): torch.fft.rfft2(x.float(), dim=axes))(x, axes=(-2, -1))
        W = w_r + 1j * w_i
        X = X * W
        y = (lambda x, s, axes=(-2,-1): torch.fft.irfft2(x, s=s, dim=axes).to(torch.bfloat16))(X, (S, D), axes=(-2, -1))
        return y

    def _B_ref(x, w_gate, w_up, w_down):
        g = torch.einsum("bsd,df->bsf", x, w_gate)
        u = torch.einsum("bsd,df->bsf", x, w_up)
        h = _gelu(g) * u
        return torch.einsum("bsf,fd->bsd", h, w_down)

    x = A_x
    h = x + _A_ref(_layernorm(x, axis=-1), A_w_r, A_w_i)
    h2 = h + _B_ref(_layernorm(h, axis=-1), B_w_gate, B_w_up, B_w_down)
    return h2
```

```cpp
#include <hip/hip_runtime.h>
#include <hip/hip_bf16.h>

typedef unsigned u32;
typedef unsigned short u16;
typedef u32 v4u __attribute__((ext_vector_type(4)));
typedef u32 v2u __attribute__((ext_vector_type(2)));
typedef float f32x2 __attribute__((ext_vector_type(2)));
typedef float f32x4 __attribute__((ext_vector_type(4)));
typedef float f32x16 __attribute__((ext_vector_type(16)));
typedef __bf16 bf16x2 __attribute__((ext_vector_type(2)));
typedef __bf16 bf16x8 __attribute__((ext_vector_type(8)));
typedef short s16x4 __attribute__((ext_vector_type(4)));
#define LAS __attribute__((address_space(3)))
#define DI __device__ __forceinline__
typedef __amdgpu_buffer_rsrc_t rsrc_t;

constexpr int S_ = 2048, D_ = 512, F_ = 1376, KF = 257;
constexpr int NFT = 86;
constexpr int WS_E = 0;
constexpr int WS_A = 4096;
constexpr int WS_W = WS_A + (4 << 20);
constexpr int WS_B = WS_W + (2 << 20);
constexpr int WS_L = WS_B + (2 << 20);
constexpr int WS_R = WS_L + (2 << 20);
constexpr int WS_H = WS_R + (2 << 20);
constexpr int WS_END = WS_H + 64 * NFT * 1024;
constexpr int WS_ST = WS_END;
constexpr u32 SENT = 0xFFFFFFFFu;
constexpr u32 SPIN_CAP = 1u << 14;
constexpr int AUX_SC1 = 16;
constexpr int SLOT = 32768, SCR = 4 * SLOT;
constexpr int L_X = SCR, L_T1 = SCR + 8192, L_I2 = SCR + 16384, L_EX = SCR + 24576;
constexpr int L_I3 = SCR + 8192, L_O3 = SCR + 16384, L_HO = SCR + 24576;

DI u32 pk(float a, float b) { f32x2 v = {a, b}; return __builtin_bit_cast(u32, __builtin_convertvector(v, bf16x2)); }
DI float blo(u32 v) { return __builtin_bit_cast(float, v << 16); }
DI float bhi(u32 v) { return __builtin_bit_cast(float, v & 0xffff0000u); }
DI u32 clean(u32 v) { return v < SENT - 1 ? v : SENT - 1; }
DI v4u clean4(v4u v) { v4u r = {clean(v.x), clean(v.y), clean(v.z), clean(v.w)}; return r; }
DI u32 max3u(u32 a, u32 b, u32 c) { u32 m = a > b ? a : b; return m > c ? m : c; }
DI u32 max4(u32 m, v4u v) { return max3u(max3u(m, v.x, v.y), v.z, v.w); }
DI f32x4 mm16(v4u a, v4u b, f32x4 c) { return __builtin_amdgcn_mfma_f32_16x16x32_bf16(__builtin_bit_cast(bf16x8, a), __builtin_bit_cast(bf16x8, b), c, 0, 0, 0); }
DI f32x16 mm32(v4u a, v4u b, f32x16 c) { return __builtin_amdgcn_mfma_f32_32x32x16_bf16(__builtin_bit_cast(bf16x8, a), __builtin_bit_cast(bf16x8, b), c, 0, 0, 0); }
DI v2u lds_tr(u32 a) { return __builtin_bit_cast(v2u, __builtin_amdgcn_ds_read_tr16_b64_v4i16((LAS s16x4*)a)); }
DI v4u lds_tr2(u32 a, u32 b) { v2u x = lds_tr(a), y = lds_tr(b); v4u r = {x.x, x.y, y.x, y.y}; return r; }
DI v4u lds_r128(u32 a) { return *(LAS v4u*)a; }
DI v2u lds_r64(u32 a) { return *(LAS v2u*)a; }
DI void lds_w128(u32 a, v4u v) { *(LAS v4u*)a = v; }
DI void lds_w64(u32 a, v2u v) { *(LAS v2u*)a = v; }
DI rsrc_t mkrs(const void* p, u32 bytes) { return __builtin_amdgcn_make_buffer_rsrc((void*)p, 0, bytes, 0x00020000); }
DI v4u ld_dev(rsrc_t r, int v, int s) { return __builtin_amdgcn_raw_buffer_load_b128(r, v, s, AUX_SC1); }
DI void st_dev(rsrc_t r, int v, int s, v4u x) { __builtin_amdgcn_raw_buffer_store_b128(x, r, v, s, AUX_SC1); }
DI v4u ld_in(rsrc_t r, int v, int s) { return __builtin_amdgcn_raw_buffer_load_b128(r, v, s, 0); }
DI float csn(int n, float inv) { return __builtin_amdgcn_cosf((float)n * inv); }
DI float snn(int n, float inv) { return __builtin_amdgcn_sinf((float)n * inv); }
#define DRAIN() asm volatile("s_waitcnt vmcnt(0)" ::: "memory")
#define MEMFENCE() asm volatile("" ::: "memory")
#define BAR() asm volatile("s_waitcnt lgkmcnt(0)\n\ts_barrier" ::: "memory")
template <int CTRL, int RM> DI float dpp_add(float v) {
  int t = __builtin_amdgcn_update_dpp(0, __builtin_bit_cast(int, v), CTRL, RM, 0xf, true);
  return v + __builtin_bit_cast(float, t);
}
DI float wave_sum(float v) {
  v = dpp_add<0x111, 0xf>(v); v = dpp_add<0x112, 0xf>(v); v = dpp_add<0x114, 0xf>(v); v = dpp_add<0x118, 0xf>(v);
  v = dpp_add<0x142, 0xa>(v); v = dpp_add<0x143, 0xc>(v);
  return __builtin_bit_cast(float, __builtin_amdgcn_readlane(__builtin_bit_cast(int, v), 63));
}
DI int slot_of(int k1, int k2) { return k2 < 8 ? k1 + 32 * k2 : (k1 ? 32 - k1 : 16) + 32 * (15 - k2); }
DI int tile0_of(int c) { return 5 * c + (c < 6 ? c : 6); }

#define STAMP(i) do { } while (0)

struct Params {
  const u16* x; const u16* wr; const u16* wi; const u16* wg; const u16* wu; const u16* wd; u16* out; void* ws;
};

DI float geglu(float g, float u) {
  const float e = __builtin_amdgcn_exp2f(g * __builtin_fmaf(g * g, -0.10294324f, -2.3022082f));
  return g * u * __builtin_amdgcn_rcpf(1.0f + e);
}
DI v4u geglu_tile(const f32x16& a) {
  v4u r = {pk(geglu(a[0], a[8]), geglu(a[1], a[9])), pk(geglu(a[2], a[10]), geglu(a[3], a[11])),
           pk(geglu(a[4], a[12]), geglu(a[5], a[13])), pk(geglu(a[6], a[14]), geglu(a[7], a[15]))};
  return clean4(r);
}

__global__ void __launch_bounds__(512, 2) gfnet_geglu_kernel(Params p) {
  extern __shared__ __attribute__((aligned(16))) char smem[];
  const u32 lds = (u32)(size_t)(LAS char*)smem;
  const int wg = blockIdx.x, tid = threadIdx.x, lane = tid & 63;
  const int wv = __builtin_amdgcn_readfirstlane(tid >> 6);
  const int xcd = wg & 7, li = wg >> 3;
  const int un = xcd * 32 + li;
  const int rg = 4 * (xcd >> 1) + (li >> 3), ch = 8 * (xcd & 1) + (li & 7);
  const int rt2 = 4 * xcd + (li >> 3), ct2 = li & 7;
  const int ft0 = tile0_of(ch);
  const bool six = ch < 6;
  const rsrc_t ws = mkrs(p.ws, WS_END);
  STAMP(0);

  if (wv >= 4) {
    const int T = tid - 256, hw = wv - 4;
    const v4u sent = {SENT, SENT, SENT, SENT};
    {
      const int j0 = 16 * (un >> 6) + (un & 15) + 64 * (16 * (T & 1) + 4 * ((un >> 4) & 3));
      u32 wv_[2][4];
#pragma unroll
      for (int i = 0; i < 2; ++i) {
        const int k = (T >> 1) + 128 * i, col = k ? k : 256;
#pragma unroll
        for (int r = 0; r < 4; ++r) {
          const int idx = (j0 + 64 * r) * KF + col;
          wv_[i][r] = (u32)p.wr[idx] | ((u32)p.wi[idx] << 16);
        }
      }
#pragma unroll
      for (int i = 0; i < 2; ++i) st_dev(ws, T * 16 + i * 4096, WS_B + un * 8192, sent);
#pragma unroll
      for (int i = 0; i < 2; ++i) { v4u v = {wv_[i][0], wv_[i][1], wv_[i][2], wv_[i][3]}; st_dev(ws, T * 16 + i * 4096, WS_W + un * 8192, v); }
    }
    DRAIN();
    STAMP(1);
    const rsrc_t rwg = mkrs(p.wg, D_ * F_ * 2), rwu = mkrs(p.wu, D_ * F_ * 2), rwd = mkrs(p.wd, D_ * F_ * 2);
    v4u xa[32];
    {
      const int vo = (128 * hw + (lane >> 3)) * (F_ * 2) + ft0 * 32 + (lane & 7) * 16;
#pragma unroll
      for (int i = 0; i < 16; ++i) { xa[2 * i] = ld_in(rwg, vo, i * 8 * F_ * 2); xa[2 * i + 1] = ld_in(rwu, vo, i * 8 * F_ * 2); }
    }
    BAR();
    {
#pragma unroll
      for (int i = 0; i < 2; ++i) { st_dev(ws, T * 16 + i * 4096, WS_L + un * 8192, sent); st_dev(ws, T * 16 + i * 4096, WS_R + un * 8192, sent); }
#pragma unroll
      for (int t = 0; t < 6; ++t) if (t < 5 || six) st_dev(ws, lane * 16, WS_H + ((4 * rg + hw) * NFT + ft0 + t) * 1024, sent);
      const int tl = (lane >> 1) & 3;
      const u32 a0 = lds + tl * SLOT + ((((128 * hw + (lane >> 3)) * 64) + (lane & 1) * 16) ^ (tl << 5));
#pragma unroll
      for (int i = 0; i < 16; ++i) { lds_w128(a0 + i * 512, xa[2 * i]); lds_w128((a0 ^ 32) + i * 512, xa[2 * i + 1]); }
    }
    v4u ya[16];
    {
      const int vo = (128 * hw + (lane >> 2)) * (F_ * 2) + (ft0 + 4) * 32 + (lane & (six ? 3 : 1)) * 16;
#pragma unroll
      for (int i = 0; i < 8; ++i) { ya[2 * i] = ld_in(rwg, vo, i * 16 * F_ * 2); ya[2 * i + 1] = ld_in(rwu, vo, i * 16 * F_ * 2); }
    }
    BAR();
    DRAIN();
    STAMP(2);
    BAR();
    BAR();
    BAR();
    {
#pragma unroll
      for (int i = 0; i < 2; ++i) st_dev(ws, T * 16 + i * 4096, WS_R + un * 8192, clean4(lds_r128(lds + L_HO + T * 16 + i * 4096)));
    }
    BAR();
    const u32 ay = lds + ((128 * hw + (lane >> 2)) * 64) + (lane & 1) * 16;
    if (!(lane & 2)) {
#pragma unroll
      for (int i = 0; i < 8; ++i) { lds_w128(ay + 4 * SLOT + i * 1024, ya[2 * i]); lds_w128(ay + 4 * SLOT + 32 + i * 1024, ya[2 * i + 1]); }
    }
    const int vd = (lane >> 3) * (D_ * 2) + ct2 * 128 + (lane & 7) * 16;
    const int dct = (lane >> 2) & 1;
    const u32 ad = lds + hw * 8192 + dct * 1024 + (((lane >> 3) ^ dct) * 64) + (lane & 3) * 16;
#define LOAD_WD(dst, t) _Pragma("unroll") for (int i = 0; i < 8; ++i) dst[i] = ld_in(rwd, vd, ((tile0_of(4 * hw + (i >> 1)) + (t)) * 16 + (i & 1) * 8) * (D_ * 2))
#define PUT_WD(src, slot) _Pragma("unroll") for (int i = 0; i < 8; ++i) lds_w128(ad + (slot) * SLOT + (i >> 1) * 2048 + (i & 1) * 512, src[i])
    v4u d0[8], d1[8], d2[8];
    LOAD_WD(d0, 0); LOAD_WD(d1, 1);
    BAR();
    if (lane & 2) {
#pragma unroll
      for (int i = 0; i < 8; ++i) { lds_w128(ay + i * 1024, ya[2 * i]); lds_w128(ay + 32 + i * 1024, ya[2 * i + 1]); }
    }
    LOAD_WD(d2, 2);
    PUT_WD(d0, 1);
    BAR();
    LOAD_WD(d0, 3);
    PUT_WD(d1, 2);
    BAR();
    LOAD_WD(d1, 4);
    PUT_WD(d2, 3);
    BAR();
    if (hw < 2) { LOAD_WD(d2, 5); }
    PUT_WD(d0, 4);
    BAR();
    PUT_WD(d1, 0);
    BAR();
    if (hw < 2) { PUT_WD(d2, 1); }
    BAR();
    STAMP(3);
    return;
  }

  const int w = wv, c = lane & 15, g = lane >> 4;
  const u32 tag = __builtin_amdgcn_readfirstlane(__builtin_amdgcn_raw_buffer_load_b32(ws, 0, WS_E, AUX_SC1)) + 1u;
  float twc[4], tws[4];
  {
    const rsrc_t rx = mkrs(p.x, S_ * D_ * 2);
    v4u xr[2];
#pragma unroll
    for (int rr = 0; rr < 2; ++rr) xr[rr] = ld_in(rx, lane * 16, (32 * (8 * (un >> 5) + 2 * w + rr) + (un & 31)) * (D_ * 2));
    v4u fare, faim, fbre, fbim, f0re, f0im;
#pragma unroll
    for (int jj = 0; jj < 4; ++jj) {
      const int a = 16 * (jj >> 1) + 4 * g + 2 * (jj & 1);
      const int n0 = (a * c) & 31, n1 = ((a + 1) * c) & 31;
      fare[jj] = pk(csn(n0, 1.f / 32), csn(n1, 1.f / 32));
      faim[jj] = c ? pk(-snn(n0, 1.f / 32), -snn(n1, 1.f / 32)) : pk(1.f, -1.f);
    }
#pragma unroll
    for (int r = 0; r < 4; ++r) {
      const int b = 4 * g + r;
      twc[r] = csn(b * c, 1.f / 512); tws[r] = snn(b * c, 1.f / 512);
      const float cs = csn((b * c) & 15, 1.f / 16), sn = snn((b * c) & 15, 1.f / 16);
      fbre[r] = pk(cs, sn);
      fbim[r] = c < 8 ? pk(-sn, cs) : pk(sn, -cs);
      const int n = (b * (31 - 2 * c)) & 31;
      f0re[r] = c == 0 ? pk((r & 1) ? -1.f : 1.f, 0.f) : c < 8 ? pk(cs, 0.f) : pk(0.f, csn(n, 1.f / 32));
      f0im[r] = c < 8 ? pk(-sn, 0.f) : pk(0.f, -snn(n, 1.f / 32));
    }
    u32 o[2][4];
    STAMP(1);
#pragma unroll
    for (int rr = 0; rr < 2; ++rr) {
      const u32 xrow = lds + L_X + (2 * w + rr) * 1024;
      lds_w128(xrow + lane * 16, xr[rr]);
      float s1 = 0.f, s2 = 0.f;
#pragma unroll
      for (int i = 0; i < 4; ++i) { const u32 v = xr[rr][i]; const float lo = blo(v), hi = bhi(v); s1 += lo + hi; s2 = __builtin_fmaf(lo, lo, __builtin_fmaf(hi, hi, s2)); }
      s1 = wave_sum(s1); s2 = wave_sum(s2);
      const float mean = s1 * (1.f / 512), var = fmaxf(s2 * (1.f / 512) - mean * mean, 0.f);
      const float rstd = __builtin_amdgcn_rsqf(var + 1e-5f);
      const v4u a = lds_tr2(xrow + lane * 8, xrow + 512 + lane * 8);
      const f32x4 z = {0.f, 0.f, 0.f, 0.f};
      f32x4 ur = mm16(a, fare, z), ui = mm16(a, faim, z);
      const float mc = c == 0 ? 32.f * mean : 0.f;
      v4u a2;
#pragma unroll
      for (int r = 0; r < 4; ++r) { const float x = ur[r] - mc, y = ui[r]; a2[r] = pk(x * twc[r] + y * tws[r], y * twc[r] - x * tws[r]); }
      f32x4 orr = mm16(a2, fbre, z), oi = mm16(a2, fbim, z), sr = mm16(a2, f0re, z), si = mm16(a2, f0im, z);
      if (g == 0) { orr[0] = sr[0]; oi[0] = si[0]; }
#pragma unroll
      for (int r = 0; r < 4; ++r) o[rr][r] = pk(orr[r] * rstd, oi[r] * rstd);
    }
#pragma unroll
    for (int r = 0; r < 4; ++r) {
      const int sl = slot_of(4 * g + r, c);
      const int q = (sl * 4 + w) ^ ((sl >> 5) | ((sl >> 1) & 8));
      v2u v = {o[0][r], o[1][r]};
      lds_w64(lds + L_T1 + q * 8, v);
    }
    STAMP(2);
    asm volatile("" :: "s"(tag));
    BAR();
#pragma unroll
    for (int i = 0; i < 4; ++i) {
      const int q = i * 256 + tid, sl = q >> 2;
      const v2u v = lds_r64(lds + L_T1 + (q ^ ((sl >> 5) | ((sl >> 1) & 8))) * 8);
      v4u t = {v.x, tag, v.y, tag};
      st_dev(ws, tid * 16 + i * 4096, WS_A + un * 16384, t);
    }
  }
  STAMP(3);
  {
    v4u f1a[4], f1b[4], f2a[2][2], f2b[2][2];
    float tfc[2][4], tfs[2][4], tic[2][4], tis[2][4];
#pragma unroll
    for (int ks = 0; ks < 4; ++ks)
#pragma unroll
      for (int r = 0; r < 4; ++r) {
        const int n = ((16 * w + c) * (16 * ks + 4 * g + r)) & 63;
        const float cs = csn(n, 1.f / 64), sn = snn(n, 1.f / 64);
        f1a[ks][r] = pk(cs, sn); f1b[ks][r] = pk(-sn, cs);
      }
#pragma unroll
    for (int m = 0; m < 2; ++m)
#pragma unroll
      for (int r = 0; r < 4; ++r) {
#pragma unroll
        for (int k = 0; k < 2; ++k) {
          const int n = ((16 * m + c) * (16 * k + 4 * g + r)) & 31;
          const float cs = csn(n, 1.f / 32), sn = snn(n, 1.f / 32);
          f2a[m][k][r] = pk(cs, sn); f2b[m][k][r] = pk(-sn, cs);
        }
        const int nf = (16 * w + c) * (16 * m + 4 * g + r), ni = (16 * w + 4 * g + r) * (16 * m + c);
        tfc[m][r] = csn(nf, 1.f / 2048); tfs[m][r] = snn(nf, 1.f / 2048);
        tic[m][r] = csn(ni, 1.f / 2048); tis[m][r] = snn(ni, 1.f / 2048);
      }
    DRAIN();
    STAMP(4);
    v4u xg[4];
    for (u32 spin = 0;; ++spin) {
      MEMFENCE();
#pragma unroll
      for (int i = 0; i < 4; ++i) xg[i] = ld_dev(ws, (lane >> 2) * 16384 + (lane & 3) * 16, WS_A + (64 * w + 16 * i) * 16384 + un * 64);
      bool ok = true;
#pragma unroll
      for (int i = 0; i < 4; ++i) ok &= (xg[i].y == tag) & (xg[i].w == tag);
      if (__all(ok) || spin > SPIN_CAP) break;
    }
    MEMFENCE();
    v4u wq[2];
#pragma unroll
    for (int i = 0; i < 2; ++i) wq[i] = ld_dev(ws, tid * 8192 + i * 16, WS_W + un * 32);
#pragma unroll
    for (int i = 0; i < 4; ++i) { v2u v = {xg[i].x, xg[i].z}; lds_w64(lds + L_I2 + (w * 256 + i * 64 + lane) * 8, v); }
    STAMP(5);
    BAR();
    if (wg == 0 && tid == 0) __builtin_amdgcn_raw_buffer_store_b32(tag, ws, 0, WS_E, AUX_SC1);
    const f32x4 z = {0.f, 0.f, 0.f, 0.f};
    v4u b2[2];
#pragma unroll
    for (int m = 0; m < 2; ++m) {
      f32x4 dr = z, di = z;
#pragma unroll
      for (int ks = 0; ks < 4; ++ks) {
        const v4u a = lds_r128(lds + L_I2 + ((2 * ks + (g >> 1)) * 32 + 16 * m + c) * 32 + (g & 1) * 16);
        dr = mm16(a, f1a[ks], dr); di = mm16(a, f1b[ks], di);
      }
#pragma unroll
      for (int r = 0; r < 4; ++r) b2[m][r] = pk(dr[r] * tfc[m][r] + di[r] * tfs[m][r], di[r] * tfc[m][r] - dr[r] * tfs[m][r]);
    }
    v4u a3[2];
#pragma unroll
    for (int m = 0; m < 2; ++m) {
      const f32x4 dr = mm16(f2a[m][1], b2[1], mm16(f2a[m][0], b2[0], z)), di = mm16(f2b[m][1], b2[1], mm16(f2b[m][0], b2[0], z));
#pragma unroll
      for (int r = 0; r < 4; ++r) {
        const u32 wv_ = wq[m][r];
        const float wr = blo(wv_) * (2.f / (2048.f * 512.f)), wi = bhi(wv_) * (2.f / (2048.f * 512.f));
        a3[m][r] = pk(dr[r] * wi + di[r] * wr, dr[r] * wr - di[r] * wi);
      }
    }
#pragma unroll
    for (int n = 0; n < 2; ++n) {
      const f32x4 di = mm16(a3[1], f2a[n][1], mm16(a3[0], f2a[n][0], z)), dr = mm16(a3[1], f2b[n][1], mm16(a3[0], f2b[n][0], z));
      v4u e;
#pragma unroll
      for (int r = 0; r < 4; ++r) e[r] = pk(di[r] * tic[n][r] + dr[r] * tis[n][r], dr[r] * tic[n][r] - di[r] * tis[n][r]);
      lds_w128(lds + L_EX + ((w * 2 + n) * 64 + lane) * 16, e);
    }
    STAMP(6);
    BAR();
#pragma unroll
    for (int n = 0; n < 2; ++n) {
      f32x4 dr = z, di = z;
#pragma unroll
      for (int ks = 0; ks < 4; ++ks) {
        const v4u b = lds_r128(lds + L_EX + ((ks * 2 + n) * 64 + lane) * 16);
        di = mm16(f1a[ks], b, di); dr = mm16(f1b[ks], b, dr);
      }
      v4u e;
#pragma unroll
      for (int r = 0; r < 4; ++r) e[r] = clean(pk(dr[r], di[r]));
      st_dev(ws, ((g >> 1) * 32 + c) * 32 + (g & 1) * 16 + n * 512, WS_B + un * 8192 + w * 2048, e);
    }
  }
  STAMP(7);
  {
    v4u gbre, gbim, g0re, g0im, ga[2];
#pragma unroll
    for (int r = 0; r < 4; ++r) {
      const int k = 4 * g + r;
      const float cs = csn((k * c) & 15, 1.f / 16), sn = snn((k * c) & 15, 1.f / 16);
      gbre[r] = g < 2 ? pk(cs, -sn) : pk(cs, sn);
      gbim[r] = g < 2 ? pk(sn, cs) : pk(sn, -cs);
      const int n = (c * (31 - 2 * k)) & 31;
      g0re[r] = k == 0 ? pk((c & 1) ? -0.5f : 0.5f, 0.f) : g < 2 ? pk(cs, -sn) : 0u;
      g0im[r] = g < 2 ? 0u : pk(csn(n, 1.f / 32), -snn(n, 1.f / 32));
#pragma unroll
      for (int t = 0; t < 2; ++t) {
        const int m = ((16 * t + c) * k) & 31;
        ga[t][r] = k == 0 ? pk(1.f, (c & 1) ? -1.f : 1.f) : pk(csn(m, 1.f / 32), -snn(m, 1.f / 32));
      }
    }
    f32x4 xin[2][2];
#pragma unroll
    for (int rr = 0; rr < 2; ++rr)
#pragma unroll
      for (int t = 0; t < 2; ++t) {
        const v2u v = lds_r64(lds + L_X + (2 * w + rr) * 1024 + t * 512 + (4 * c + g) * 8);
        const u32 v0 = v.x, v1 = v.y;
        f32x4 q = {blo(v0), bhi(v0), blo(v1), bhi(v1)};
        xin[rr][t] = q;
      }
    DRAIN();
    STAMP(8);
    v4u zg[2];
    for (u32 spin = 0;; ++spin) {
      MEMFENCE();
#pragma unroll
      for (int i = 0; i < 2; ++i) zg[i] = ld_dev(ws, (lane >> 1) * 8192 + (lane & 1) * 16, WS_B + (64 * w + 32 * i) * 8192 + un * 32);
      const u32 m = max4(max4(0u, zg[0]), zg[1]);
      if (__all(m != SENT) || spin > SPIN_CAP) break;
    }
#pragma unroll
    for (int i = 0; i < 2; ++i) lds_w128(lds + L_I3 + (w * 128 + i * 64 + lane) * 16, zg[i]);
    STAMP(9);
    BAR();
    v4u a[2];
#pragma unroll
    for (int r = 0; r < 4; ++r) { const v2u v = lds_r64(lds + L_I3 + slot_of(c, 4 * g + r) * 32 + w * 8); a[0][r] = v.x; a[1][r] = v.y; }
    const f32x4 z = {0.f, 0.f, 0.f, 0.f};
#pragma unroll
    for (int rr = 0; rr < 2; ++rr) {
      f32x4 tr = mm16(a[rr], gbre, z), ti = mm16(a[rr], gbim, z), sr = mm16(a[rr], g0re, z), si = mm16(a[rr], g0im, z);
      if (g == 0) { tr[0] = sr[0]; ti[0] = si[0]; }
      v4u a2;
#pragma unroll
      for (int r = 0; r < 4; ++r) a2[r] = pk(tr[r] * twc[r] - ti[r] * tws[r], ti[r] * twc[r] + tr[r] * tws[r]);
      const f32x4 h0 = mm16(a2, ga[0], xin[rr][0]), h1 = mm16(a2, ga[1], xin[rr][1]);
      float s1 = 0.f, s2 = 0.f;
#pragma unroll
      for (int r = 0; r < 4; ++r) { s1 += h0[r] + h1[r]; s2 = __builtin_fmaf(h0[r], h0[r], __builtin_fmaf(h1[r], h1[r], s2)); }
      s1 = wave_sum(s1); s2 = wave_sum(s2);
      const float mean = s1 * (1.f / 512), var = fmaxf(s2 * (1.f / 512) - mean * mean, 0.f);
      const float rstd = __builtin_amdgcn_rsqf(var + 1e-5f), nb = -mean * rstd;
      v4u l4 = {pk(h0[0] * rstd + nb, h0[1] * rstd + nb), pk(h0[2] * rstd + nb, h0[3] * rstd + nb),
                pk(h1[0] * rstd + nb, h1[1] * rstd + nb), pk(h1[2] * rstd + nb, h1[3] * rstd + nb)};
      lds_w128(lds + L_O3 + ((4 * c + g) * 8 + ((2 * w + rr) ^ (c & 7))) * 16, clean4(l4));
      v2u q0 = {pk(h0[0], h0[1]), pk(h0[2], h0[3])}, q1 = {pk(h1[0], h1[1]), pk(h1[2], h1[3])};
      lds_w64(lds + L_HO + (2 * w + rr) * 1024 + (4 * c + g) * 8, q0);
      lds_w64(lds + L_HO + (2 * w + rr) * 1024 + 512 + (4 * c + g) * 8, q1);
    }
    STAMP(10);
    BAR();
#pragma unroll
    for (int i = 0; i < 2; ++i) {
      const int q = i * 256 + tid;
      st_dev(ws, tid * 16 + i * 4096, WS_L + un * 8192, lds_r128(lds + L_O3 + (q ^ ((q >> 5) & 7)) * 16));
    }
    BAR();
  }
  STAMP(11);
  const int hh = lane >> 5;
  {
    const int rt = 4 * rg + w;
    v4u act[32];
    const u32 abase = lds + (4 * hh + ((lane & 15) >> 2)) * 64 + ((lane >> 4) & 1) * 32 + (lane & 3) * 8;
    f32x16 acc0, acc1;
    DRAIN();
    STAMP(12);
    for (u32 spin = 0;; ++spin) {
      MEMFENCE();
#pragma unroll
      for (int ks = 0; ks < 32; ++ks) act[ks] = ld_dev(ws, ((lane >> 3) & 3) * 8192 + hh * 128 + (lane & 7) * 16, WS_L + rt * 32768 + ks * 256);
#pragma unroll
      for (int i = 0; i < 16; ++i) { acc0[i] = 0.f; acc1[i] = 0.f; }
      u32 m = 0;
      const u32 a1 = (abase ^ 32) + SLOT;
#pragma unroll
      for (int ks = 0; ks < 32; ++ks) {
        acc0 = mm32(lds_tr2(abase + ks * 512, abase + ks * 512 + 16384), act[ks], acc0);
        acc1 = mm32(lds_tr2(a1 + ks * 512, a1 + ks * 512 + 16384), act[ks], acc1);
        m = max4(m, act[ks]);
      }
      if (__all(m != SENT) || spin > SPIN_CAP) break;
    }
    STAMP(13);
    BAR();
    const int hoff = WS_H + (rt * NFT + ft0) * 1024;
#define G1_TILE(acc, base) do { const u32 ab_ = (base); _Pragma("unroll") for (int i = 0; i < 16; ++i) acc[i] = 0.f; \
      _Pragma("unroll") for (int ks = 0; ks < 32; ++ks) acc = mm32(lds_tr2(ab_ + ks * 512, ab_ + ks * 512 + 16384), act[ks], acc); } while (0)
    f32x16 acc2;
    G1_TILE(acc2, (abase ^ 64) + 2 * SLOT);
    st_dev(ws, lane * 16, hoff, geglu_tile(acc0));
    st_dev(ws, lane * 16, hoff + 1024, geglu_tile(acc1));
    BAR();
    G1_TILE(acc0, (abase ^ 96) + 3 * SLOT);
    st_dev(ws, lane * 16, hoff + 2048, geglu_tile(acc2));
    BAR();
    G1_TILE(acc1, abase + 4 * SLOT);
    st_dev(ws, lane * 16, hoff + 3072, geglu_tile(acc0));
    BAR();
    if (six) {
      G1_TILE(acc2, abase);
      st_dev(ws, lane * 16, hoff + 4096, geglu_tile(acc1));
      st_dev(ws, lane * 16, hoff + 5120, geglu_tile(acc2));
    } else {
      st_dev(ws, lane * 16, hoff + 4096, geglu_tile(acc1));
    }
    BAR();
  }
  STAMP(14);
  {
    f32x16 acc[2][2];
#pragma unroll
    for (int i = 0; i < 16; ++i) { acc[0][0][i] = 0.f; acc[0][1][i] = 0.f; acc[1][0][i] = 0.f; acc[1][1][i] = 0.f; }
    const int pp = lane & 3;
    const u32 dlane = (4 * hh + ((lane & 15) >> 2)) * 64 + (pp & 1) * 32 + ((lane >> 4) & 1) * 16 + (pp >> 1) * 8;
    const u32 db0 = lds + dlane, db1 = lds + (dlane ^ 64) + 1024;
    const int hb = WS_H + 2 * rt2 * NFT * 1024;
#define G2_STEP(h0, h1, off) do { const v4u w0_ = lds_tr2(db0 + (off), db0 + (off) + 512), w1_ = lds_tr2(db1 + (off), db1 + (off) + 512); \
      acc[0][0] = mm32(w0_, h0, acc[0][0]); acc[0][1] = mm32(w0_, h1, acc[0][1]); acc[1][0] = mm32(w1_, h0, acc[1][0]); acc[1][1] = mm32(w1_, h1, acc[1][1]); } while (0)
#define G2_ROUND(t, slot) do { v4u hf[8]; \
      for (u32 spin = 0;; ++spin) { MEMFENCE(); u32 m = 0; \
        _Pragma("unroll") for (int i = 0; i < 8; ++i) hf[i] = ld_dev(ws, lane * 16, hb + ((i & 1) * NFT + tile0_of(4 * w + (i >> 1)) + (t)) * 1024); \
        _Pragma("unroll") for (int i = 0; i < 8; ++i) m = max4(m, hf[i]); \
        if (__all(m != SENT) || spin > SPIN_CAP) break; } \
      _Pragma("unroll") for (int i = 0; i < 4; ++i) G2_STEP(hf[2 * i], hf[2 * i + 1], (slot) * SLOT + w * 8192 + i * 2048); } while (0)
    G2_ROUND(0, 1);
    BAR();
    G2_ROUND(1, 2);
    G2_ROUND(2, 3);
    G2_ROUND(3, 4);
    BAR();
    G2_ROUND(4, 0);
    {
      const int c0 = w < 2 ? 2 * w : w + 2, n5 = w < 2 ? 2 : 1;
      for (int i = 0; i < n5; ++i) {
        v4u h0, h1;
        for (u32 spin = 0;; ++spin) {
          MEMFENCE();
          h0 = ld_dev(ws, lane * 16, hb + (6 * (c0 + i) + 5) * 1024);
          h1 = ld_dev(ws, lane * 16, hb + (NFT + 6 * (c0 + i) + 5) * 1024);
          if (__all(max4(max4(0u, h0), h1) != SENT) || spin > SPIN_CAP) break;
        }
        G2_STEP(h0, h1, SLOT + (c0 + i) * 2048);
      }
    }
    STAMP(15);
    const int n = lane & 31, u = 4 * (2 * rt2 + (w >> 1)) + (n >> 3), col = 64 * ct2 + 32 * (w & 1) + 16 * hh;
    v4u hr[2];
    for (u32 spin = 0;; ++spin) {
      MEMFENCE();
#pragma unroll
      for (int i = 0; i < 2; ++i) hr[i] = ld_dev(ws, (u * 8 + (n & 7)) * 1024 + col * 2 + i * 16, WS_R);
      if (__all(max4(max4(0u, hr[0]), hr[1]) != SENT) || spin > SPIN_CAP) break;
    }
    const u32 red = lds + 2 * SLOT + lane * 16;
#pragma unroll
    for (int q = 0; q < 4; ++q) {
      if (q != w) {
#pragma unroll
        for (int i = 0; i < 4; ++i) {
          const f32x16& a = acc[q & 1][q >> 1];
          f32x4 v = {a[4 * i], a[4 * i + 1], a[4 * i + 2], a[4 * i + 3]};
          lds_w128(red + (q * 4 + w) * 4096 + i * 1024, __builtin_bit_cast(v4u, v));
        }
      }
    }
    f32x16 f = w == 0 ? acc[0][0] : w == 1 ? acc[1][0] : w == 2 ? acc[0][1] : acc[1][1];
    BAR();
#pragma unroll
    for (int sI = 1; sI < 4; ++sI) {
      const int sw = (w + sI) & 3;
#pragma unroll
      for (int i = 0; i < 4; ++i) {
        const f32x4 v = __builtin_bit_cast(f32x4, lds_r128(red + (w * 4 + sw) * 4096 + i * 1024));
        f[4 * i] += v[0]; f[4 * i + 1] += v[1]; f[4 * i + 2] += v[2]; f[4 * i + 3] += v[3];
      }
    }
    const int s = 32 * (8 * (u >> 5) + (n & 7)) + (u & 31);
    v4u o[2];
#pragma unroll
    for (int i = 0; i < 8; ++i) { const u32 hv = hr[i >> 2][i & 3]; o[i >> 2][i & 3] = pk(f[2 * i] + blo(hv), f[2 * i + 1] + bhi(hv)); }
    v4u* op = (v4u*)(p.out + s * D_ + col);
    op[0] = o[0]; op[1] = o[1];
  }
  STAMP(16);
}

extern "C" void kernel_launch(void* const* d_in, const int* in_sizes, int n_in,
                              void* d_out, int out_size, void* d_ws, size_t ws_size,
                              hipStream_t stream) {
  Params p;
  p.x = (const u16*)d_in[0]; p.wr = (const u16*)d_in[1]; p.wi = (const u16*)d_in[2];
  p.wg = (const u16*)d_in[4]; p.wu = (const u16*)d_in[5]; p.wd = (const u16*)d_in[6];
  p.out = (u16*)d_out; p.ws = d_ws;
  (void)hipFuncSetAttribute((const void*)gfnet_geglu_kernel, hipFuncAttributeMaxDynamicSharedMemorySize, 5 * SLOT);
  gfnet_geglu_kernel<<<256, 512, 5 * SLOT, stream>>>(p);
}
```

```cpp
#include <hip/hip_runtime.h>
#include <hip/hip_bf16.h>
#include <type_traits>

typedef unsigned u32;
typedef unsigned short u16;
typedef u32 v4u __attribute__((ext_vector_type(4)));
typedef u32 v2u __attribute__((ext_vector_type(2)));
typedef float f32x2 __attribute__((ext_vector_type(2)));
typedef float f32x4 __attribute__((ext_vector_type(4)));
typedef float f32x16 __attribute__((ext_vector_type(16)));
typedef __bf16 bf16x2 __attribute__((ext_vector_type(2)));
typedef __bf16 bf16x8 __attribute__((ext_vector_type(8)));
typedef short s16x4 __attribute__((ext_vector_type(4)));
#define LAS __attribute__((address_space(3)))
#define DI __device__ __forceinline__
typedef __amdgpu_buffer_rsrc_t rsrc_t;
typedef LAS char* lp_t;

constexpr int S_ = 2048, D_ = 512, F_ = 1376, KF = 257;
constexpr int NFT = 86;
constexpr int WS_E = 0;
constexpr int WS_A = 4096;
constexpr int WS_W = WS_A + (2 << 20);
constexpr int WS_B = WS_W + (2 << 20);
constexpr int WS_L = WS_B + (2 << 20);
constexpr int WS_CA = WS_L + (2 << 20);
constexpr int WS_CB = WS_CA + (1 << 18);
constexpr int WS_S = WS_CB + (1 << 18);
constexpr int WS_H = WS_S + (1 << 17);
constexpr int HREC = 1152;
constexpr int WS_END = WS_H + 64 * NFT * HREC;
constexpr int WS_ST = WS_END;
constexpr u32 NEG2 = 0x80008000u, NEGH = 0x80000000u, NEGL = 0x8000u;
constexpr u32 SPIN_CAP = 1u << 14;
constexpr int AUX_SC1 = 16;
constexpr int OOB = 0x40000000;
constexpr int CODE_LINES = 200;
constexpr int SLOT = 32768, SCR = 4 * SLOT;
constexpr int L_X = 0, L_T1 = 8192, L_I2 = 16384, L_EX = 24576, L_CW = 24576;
constexpr int L_I3 = 8192, L_O3 = 16384, L_SO = 24576;

#define DRAIN() asm volatile("s_waitcnt vmcnt(0)" ::: "memory")
#define MEMFENCE() asm volatile("" ::: "memory")
#define PIN(x) asm volatile("" : "+v"(x))
#define PINS(x) asm volatile("" : "+s"(x))
#define SB() __builtin_amdgcn_sched_barrier(0)
#define BAR() asm volatile("s_waitcnt lgkmcnt(0)\n\ts_barrier" ::: "memory")
#define BAR_N(n) asm volatile("s_waitcnt lgkmcnt(" #n ")\n\ts_barrier" ::: "memory")

DI u32 pk(float a, float b) { f32x2 v = {a, b}; return __builtin_bit_cast(u32, __builtin_convertvector(v, bf16x2)); }
DI v4u pk4(f32x4 a, f32x4 b) { v4u r = {pk(a[0], b[0]), pk(a[1], b[1]), pk(a[2], b[2]), pk(a[3], b[3])}; return r; }
DI float asf(u32 v) { return __builtin_bit_cast(float, v); }
DI u32 asu(float v) { return __builtin_bit_cast(u32, v); }
DI float blo(u32 v) { return asf(v << 16); }
DI float bhi(u32 v) { return asf(v & 0xffff0000u); }
DI float dot2(u32 a, u32 b, float c) { return __builtin_amdgcn_fdot2_f32_bf16(__builtin_bit_cast(bf16x2, a), __builtin_bit_cast(bf16x2, b), c, false); }
DI u32 sum4(u32 m, v4u v) { return (m + v.x + v.y) + v.z + v.w; }
template <int CTRL> DI u32 dppu(u32 v) { return (u32)__builtin_amdgcn_update_dpp(0, (int)v, CTRL, 0xf, 0xf, true); }
template <int CTRL> DI float dppf(float v) { return asf(dppu<CTRL>(asu(v))); }
DI u32 pair_total(u32 s0, u32 s1, bool odd) { return (odd ? s1 : s0) + dppu<0xb1>(odd ? s0 : s1); }
DI u32 add_x16(u32 v) { const auto s = __builtin_amdgcn_permlane16_swap(v, v, false, false); return s[0] + s[1]; }
DI u32 add_x32(u32 v) { const auto s = __builtin_amdgcn_permlane32_swap(v, v, false, false); return s[0] + s[1]; }
template <int N> DI void wave_sums(float (&v)[N]) {
#define WS_STEP(C) _Pragma("unroll") for (int i = 0; i < N; ++i) v[i] += dppf<C>(v[i]);
  WS_STEP(0x111) WS_STEP(0x112) WS_STEP(0x114) WS_STEP(0x118) WS_STEP(0x142) WS_STEP(0x143)
#pragma unroll
  for (int i = 0; i < N; ++i) v[i] = asf(__builtin_amdgcn_readlane(asu(v[i]), 63));
}
DI int chk_pos(int k) { return (k & 0xc7) | ((k & 8) << 2) | ((k >> 1) & 0x18); }
DI f32x4 mm16(v4u a, v4u b, f32x4 c) { return __builtin_amdgcn_mfma_f32_16x16x32_bf16(__builtin_bit_cast(bf16x8, a), __builtin_bit_cast(bf16x8, b), c, 0, 0, 0); }
DI f32x4 mm16p(v4u a, v4u b, f32x4 c) { f32x4 d = mm16(a, b, c); PIN(d); return d; }
DI f32x16 mm32p(v4u a, v4u b, f32x16 c) { f32x16 d = __builtin_amdgcn_mfma_f32_32x32x16_bf16(__builtin_bit_cast(bf16x8, a), __builtin_bit_cast(bf16x8, b), c, 0, 0, 0); PIN(d); return d; }
DI v2u lds_tr(lp_t a) { return __builtin_bit_cast(v2u, __builtin_amdgcn_ds_read_tr16_b64_v4i16((LAS s16x4*)a)); }
DI v4u lds_tr2(lp_t a, lp_t b) { v2u x = lds_tr(a), y = lds_tr(b); v4u r = {x.x, x.y, y.x, y.y}; return r; }
DI v4u lds_r128(lp_t a) { return *(LAS v4u*)a; }
DI f32x4 lds_rf(lp_t a) { return *(LAS f32x4*)a; }
DI v2u lds_r64(lp_t a) { return *(LAS v2u*)a; }
DI void lds_w128(lp_t a, v4u v) { *(LAS v4u*)a = v; }
DI void lds_wf(lp_t a, f32x4 v) { *(LAS f32x4*)a = v; }
DI void lds_w64(lp_t a, v2u v) { *(LAS v2u*)a = v; }
DI lp_t lptr(u32 off) { lp_t q = (lp_t)(size_t)off; PIN(q); return q; }
DI rsrc_t mkrs(const void* p, u32 bytes) { return __builtin_amdgcn_make_buffer_rsrc((void*)p, 0, bytes, 0x00020000); }
DI v4u ld_dev(rsrc_t r, int v, int s) { return __builtin_amdgcn_raw_buffer_load_b128(r, v, s, AUX_SC1); }
DI u32 ld_dev1(rsrc_t r, int v, int s) { return __builtin_amdgcn_raw_buffer_load_b32(r, v, s, AUX_SC1); }
DI void st_dev(rsrc_t r, int v, v4u x) { __builtin_amdgcn_raw_buffer_store_b128(x, r, v, 0, AUX_SC1); }
DI void st_dev1(rsrc_t r, int v, u32 x) { __builtin_amdgcn_raw_buffer_store_b32(x, r, v, 0, AUX_SC1); }
DI v4u ld_in(rsrc_t r, int v, int s) { return __builtin_amdgcn_raw_buffer_load_b128(r, v, s, 0); }
DI float cosr(float rev) { return __builtin_amdgcn_cosf(rev); }
DI float sinr(float rev) { return __builtin_amdgcn_sinf(rev); }
__host__ __device__ constexpr int slot_of(int k1, int k2) { return ((k2 < 8 ? k1 + 32 * k2 : (k1 ? 32 - k1 : 16) + 32 * (15 - k2)) - 1) & 255; }
DI int tile0_of(int c) { return 4 * c + 2 * (c < 11 ? c : 11); }
template <int I, int N, class F> DI void static_for(F&& f) {
  if constexpr (I < N) { f(std::integral_constant<int, I>{}); static_for<I + 1, N>(f); }
}
DI void transpose8(v4u (&m)[8]) {
  v4u o[8];
#pragma unroll
  for (int i = 0; i < 8; ++i)
#pragma unroll
    for (int j = 0; j < 4; ++j) { const u32 a = m[2 * j][i >> 1], b = m[2 * j + 1][i >> 1]; o[i][j] = __builtin_amdgcn_perm(b, a, (i & 1) ? 0x07060302u : 0x05040100u); }
#pragma unroll
  for (int i = 0; i < 8; ++i) { m[i] = o[i]; PIN(m[i]); }
}
struct Epi { float t, lo; v4u r; u32 chk; };
template <int J> DI void epi_slice(Epi& e, const f32x16& a) {
  constexpr int v = J >> 2, ph = J & 3;
  if constexpr (J >= 32) {
    if constexpr (J < 35) { e.chk += dppu<J == 32 ? 0xb1 : J == 33 ? 0x4e : 0x128>(e.chk); PIN(e.chk); }
  } else if constexpr (ph == 0) { e.t = __builtin_fmaf(a[v] * a[v], -0.10294324f, -2.3022082f); PIN(e.t); }
  else if constexpr (ph == 1) { e.t = __builtin_amdgcn_exp2f(e.t * a[v]); PIN(e.t); }
  else if constexpr (ph == 2) { e.t = __builtin_amdgcn_rcpf(1.0f + e.t); PIN(e.t); }
  else {
    const float q = a[v] * a[v + 8] * e.t;
    if constexpr (v & 1) { u32 r = pk(e.lo, q); e.chk = v == 1 ? r : e.chk + r; PIN(r); PIN(e.chk); e.r[v >> 1] = r; } else { e.lo = q; PIN(e.lo); }
  }
}

constexpr double ct_sin(long n, long d) {
  n %= d; if (n < 0) n += d;
  if (2 * n > d) return -ct_sin(d - n, d);
  if (4 * n > d) return ct_sin(d - 2 * n, 2 * d);
  const double x = 6.283185307179586476925 * (double)n / (double)d;
  double t = x, r = x;
  for (int i = 1; i < 14; ++i) { t *= -x * x / ((2 * i) * (2 * i + 1)); r += t; }
  return r;
}
constexpr double ct_cos(long n, long d) { return ct_sin(4 * n + d, 4 * d); }
constexpr u32 ct_f32(double v) { return __builtin_bit_cast(u32, (float)v); }
constexpr u32 ct_bf(double v) { const u32 u = ct_f32(v); return (u + 0x7fffu + ((u >> 16) & 1u)) >> 16; }
constexpr u32 ct_pk(double a, double b) { return ct_bf(a) | (ct_bf(b) << 16); }
struct Tab { u32 v[6][64][4]; };
constexpr Tab make_tab() {
  Tab t = {};
  for (int l = 0; l < 64; ++l) {
    const int c = l & 15, g = l >> 4;
    for (int j = 0; j < 2; ++j) {
      const int a = 4 * g + 2 * j;
      t.v[0][l][j] = ct_pk(ct_cos(a * c, 32), ct_cos((a + 1) * c, 32));
      t.v[0][l][2 + j] = c ? ct_pk(-ct_sin(a * c, 32), -ct_sin((a + 1) * c, 32)) : ct_pk(1, -1);
    }
    for (int r = 0; r < 4; ++r) {
      const int b = 4 * g + r, sl = slot_of(b, c);
      t.v[1][l][r] = ct_f32(ct_cos(b * c, 512));
      t.v[2][l][r] = ct_f32(ct_sin(b * c, 512));
      t.v[3][l][r] = ct_pk(ct_cos(b * c, 16), ct_sin(b * c, 16));
      t.v[4][l][r] = c == 0 ? ct_pk((r & 1) ? -1 : 1, 0) : c < 8 ? ct_pk(ct_cos(b * c, 16), -ct_sin(b * c, 16)) : ct_pk(ct_cos(b * (31 - 2 * c), 32), -ct_sin(b * (31 - 2 * c), 32));
      t.v[5][l][r] = SCR + L_T1 + ((sl * 4) ^ ((sl >> 4) & 14)) * 8;
    }
  }
  return t;
}
static __device__ const __attribute__((aligned(128))) Tab g_tab = make_tab();

#define STAMP(i) do { } while (0)
#define STAMP_DECL do { } while (0)
#define STAMP_FLUSH() do { } while (0)
#define COUNT(i, v) do { } while (0)

struct Params {
  const u16* x; const u16* wr; const u16* wi; const u16* wg; const u16* wu; const u16* wd; u16* out; void* ws;
};

__global__ void __launch_bounds__(512, 2) gfnet_geglu_kernel(Params p) {
  const int wg = blockIdx.x, tid = threadIdx.x, lane = tid & 63;
  int wv = __builtin_amdgcn_readfirstlane(tid >> 6);
  STAMP_DECL;
  const int rg = 4 * ((wg & 7) >> 1) + (wg >> 6), ch = 8 * (wg & 1) + ((wg >> 3) & 7);
  const int un = 16 * rg + ch;
  const int ft0 = tile0_of(ch);
  const bool six = ch < 11;
#define X_PIECE(b) ((((b) ^ (rg & 1)) * 16) + ((wv >> 1) & 1) * 8)
#define Y_PIECE(b) ((2 * (b) + ((wv >> 1) & 1)) * 16)
#define LOAD_W1(dst, v, piece) _Pragma("unroll") for (int j = 0; j < 8; ++j) dst[j] = ld_in(rw1, v, (4 * (piece) + (j & 3) + 256 * (j >> 2)) * (F_ * 2))
#define W1_SETUP() const rsrc_t rw1 = mkrs((wv & 1) ? p.wu : p.wg, D_ * F_ * 2); const int vx = 4 * (lane >> 3) * (F_ * 2) + ft0 * 32 + (lane & 7) * 16
  v4u ra[8];
  u32 cpf;
#define UNDEF_RA() do { asm volatile("" : "=v"(cpf)); _Pragma("unroll") for (int i = 0; i < 8; ++i) asm volatile("" : "=v"(ra[i])); } while (0)
  if (wv >= 6) {
    unsigned long long pc0;
    asm volatile("s_getpc_b64 %0" : "=s"(pc0));
    cpf = __builtin_amdgcn_raw_buffer_load_b32(mkrs((const void*)(pc0 & ~127ull), CODE_LINES * 128), ((((wg >> 2) & 2) + (wv & 1)) * 64 + lane) * 128, 0, 0);
    SB();
    W1_SETUP();
    LOAD_W1(ra, vx, X_PIECE(0));
  } else if (wv >= 4) {
    const rsrc_t rwr = mkrs(p.wr, S_ * KF * 2), rwi = mkrs(p.wi, S_ * KF * 2);
    const int vf = (lane & 1) * (KF * 2 + 2) + ((tid & 127) >> 1) * 8;
    const int sf = (16 * (un >> 6) + 2 * (un & 7) + 256 * ((un >> 3) & 7)) * (KF * 2);
#pragma unroll
    for (int r = 0; r < 4; ++r) { ra[r] = ld_in(rwr, vf, sf + r * (64 * KF * 2)); ra[4 + r] = ld_in(rwi, vf, sf + r * (64 * KF * 2)); }
    asm volatile("" : "=v"(cpf));
  } else {
    UNDEF_RA();
  }
  SB();
  PINS(wv);

  if (wv < 4) {
  const int w = wv, c = lane & 15, g = lane >> 4;
  const float fc = (float)c, fg4 = (float)(4 * g);
  const f32x4 z = {0.f, 0.f, 0.f, 0.f};
  f32x4 twc, tws;
  v4u fbre, fbsw;
  u32 tag;
  float mean[2];
  const rsrc_t ws = mkrs(p.ws, WS_END);
  {
    v4u tb[6], xr[2];
    const rsrc_t rt = mkrs(&g_tab, sizeof(Tab));
    int vt = lane * 16;
    const int sx = (32 * (8 * (un >> 5) + 2 * w) + (un & 31)) * (D_ * 2);
#pragma unroll
    for (int i = 0; i < 6; ++i) tb[i] = ld_in(rt, vt + (i & 3) * 1024, (i >> 2) * 4096);
    SB();
    const rsrc_t rx = mkrs(p.x, S_ * D_ * 2);
    xr[0] = ld_in(rx, vt, sx); xr[1] = ld_in(rx, vt, sx + 32 * D_ * 2);
    tag = ld_dev1(ws, 0, WS_E);
    SB();
    STAMP(0);
    const u32 sc = (c & 1) ? NEG2 : 0u, sm = c < 8 ? NEGL : NEGH, p0 = c < 8 ? 0x0c0c0100u : 0x01000c0cu, p1 = c < 8 ? 0x0c0c0302u : 0x03020c0cu;
    v4u fare = {tb[0].x, tb[0].y, tb[0].x ^ sc, tb[0].y ^ sc}, faim = {tb[0].z, tb[0].w, tb[0].z ^ sc, tb[0].w ^ sc}, fbim, f0re, f0im;
    twc = __builtin_bit_cast(f32x4, tb[1]); tws = __builtin_bit_cast(f32x4, tb[2]); fbre = tb[3];
    lp_t t1w[4], t1r[2];
#pragma unroll
    for (int r = 0; r < 4; ++r) {
      const u32 a = tb[3][r], q = tb[4][r];
      fbsw[r] = __builtin_amdgcn_alignbit(a, a, 16);
      fbim[r] = fbsw[r] ^ sm;
      f0re[r] = __builtin_amdgcn_perm(q, q, p0); f0im[r] = __builtin_amdgcn_perm(q, q, p1);
      t1w[r] = lptr(tb[5][r] ^ (w * 8));
    }
#pragma unroll
    for (int i = 0; i < 2; ++i) t1r[i] = lptr(SCR + L_T1 + ((w * 128 + i * 64 + lane) ^ (2 * w + i)) * 16);
    const int sl = 64 * w + 32 * (lane & 1) + (lane >> 1);
    lp_t cwr = lptr(SCR + L_CW + sl * 4);
    int vc = chk_pos(sl) * 4 + WS_CA + un * 1024, vs = lane * 16 + WS_A + un * 8192 + w * 2048;
    float c0m = c == 0 ? 1.f / 16 : 0.f;
    lp_t xw = lptr(SCR + L_X + w * 2048 + lane * 16), xt = lptr(SCR + L_X + w * 2048 + lane * 8);
    PIN(vc); PIN(vs); PIN(c0m);
    PIN(fare); PIN(faim); PIN(fbre); PIN(fbim); PIN(f0re); PIN(f0im); PIN(twc); PIN(tws);
    STAMP(1);
    SB();
    v4u a[2];
#pragma unroll
    for (int rr = 0; rr < 2; ++rr) { lds_w128(xw + rr * 1024, xr[rr]); a[rr] = lds_tr2(xt + rr * 1024, xt + rr * 1024 + 512); }
    STAMP(2);
    float st[4] = {0.f, 0.f, 0.f, 0.f};
#pragma unroll
    for (int i = 0; i < 4; ++i)
#pragma unroll
      for (int rr = 0; rr < 2; ++rr) { const u32 v = xr[rr][i]; st[rr] = dot2(v, 0x3f803f80u, st[rr]); st[2 + rr] = dot2(v, v, st[2 + rr]); }
    SB();
    f32x4 ur[2], ui[2];
#pragma unroll
    for (int rr = 0; rr < 2; ++rr) { ur[rr] = mm16p(a[rr], fare, z); ui[rr] = mm16p(a[rr], faim, z); }
    wave_sums<4>(st);
    SB();
    v4u a2[2];
    f32x4 orr[2], oi[2], sr[2], si[2];
#pragma unroll
    for (int rr = 0; rr < 2; ++rr) {
      const f32x4 x = ur[rr] - st[rr] * c0m, y = ui[rr];
      a2[rr] = pk4(x * twc + y * tws, y * twc - x * tws);
      orr[rr] = mm16p(a2[rr], fbre, z); oi[rr] = mm16p(a2[rr], fbim, z); sr[rr] = mm16p(a2[rr], f0re, z); si[rr] = mm16p(a2[rr], f0im, z);
    }
    v4u o[2];
#pragma unroll
    for (int rr = 0; rr < 2; ++rr) {
      mean[rr] = st[rr] * (1.f / 512);
      const float rstd = __builtin_amdgcn_rsqf(fmaxf(st[2 + rr] * (1.f / 512) - mean[rr] * mean[rr], 0.f) + 1e-5f);
      orr[rr][0] = g == 0 ? sr[rr][0] : orr[rr][0]; oi[rr][0] = g == 0 ? si[rr][0] : oi[rr][0];
      o[rr] = pk4(orr[rr] * rstd, oi[rr] * rstd);
    }
#pragma unroll
    for (int r = 0; r < 4; ++r) { v2u v = {o[0][r], o[1][r]}; lds_w64(t1w[r], v); }
    tag += 1u;
    PIN(tag);
    STAMP(3);
    BAR();
    const v4u v0 = lds_r128(t1r[0]), v1 = lds_r128(t1r[1]);
    const u32 cw = *(LAS u32*)cwr;
    st_dev(ws, vs, v0);
    st_dev(ws, vs + 1024, v1);
    st_dev1(ws, vc, tag - cw - pair_total(sum4(0u, v0), sum4(0u, v1), lane & 1));
  }
  int vg = (lane >> 1) * 8192 + (lane & 1) * 16, vk = (32 * (lane & 1) + (lane >> 1)) * 1024;
  const int sg = w * (64 * 8192) + un * 32, sk = w * (64 * 1024) + chk_pos(un) * 4;
  const int sw = WS_W + w * (64 * 8192) + ((un & 3) * 64 + ((((un >> 2) & 0x39) | ((un >> 1) & 4) | ((un >> 3) & 2)))) * 32;
  v4u wq[2];
  wq[0] = ld_dev(ws, vg, sw); wq[1] = ld_dev(ws, vg, sw + 32 * 8192);
  STAMP(4);
  SB();
  v4u f2a[2][2];
  {
    v4u f1a[4], f1b[4], f2b[2][2];
    f32x4 tfc[2], tfs[2], tic[2], tis[2];
    const u32 sc = (c & 1) ? NEG2 : 0u;
    const float fj = (float)(16 * w) + fc;
#pragma unroll
    for (int r = 0; r < 4; ++r) {
#pragma unroll
      for (int ks = 0; ks < 2; ++ks) {
        const float n = (fg4 + (16 * ks + r)) * (fj * (1.f / 64));
        const float cs = cosr(n), sn = sinr(n);
        f1a[ks][r] = pk(cs, sn); f1b[ks][r] = pk(-sn, cs);
        f1a[ks + 2][r] = f1a[ks][r] ^ sc; f1b[ks + 2][r] = f1b[ks][r] ^ sc;
      }
      const float n = (fg4 + r) * (fc * (1.f / 32));
      const float cs = cosr(n), sn = sinr(n);
      const u32 a = pk(cs, sn), b = pk(-sn, cs), sr = (r & 1) ? NEG2 : 0u;
      f2a[0][0][r] = a; f2a[1][0][r] = a ^ sr; f2a[0][1][r] = a ^ sc; f2a[1][1][r] = a ^ sc ^ sr;
      f2b[0][0][r] = b; f2b[1][0][r] = b ^ sr; f2b[0][1][r] = b ^ sc; f2b[1][1][r] = b ^ sc ^ sr;
#pragma unroll
      for (int m = 0; m < 2; ++m) {
        const float nf = (fg4 + (16 * m + r)) * (fj * (1.f / 2048)), ni = (fg4 + (float)(16 * w) + r) * ((fc + 16 * m) * (1.f / 2048));
        tfc[m][r] = cosr(nf); tfs[m][r] = sinr(nf); tic[m][r] = cosr(ni); tis[m][r] = sinr(ni);
      }
    }
#pragma unroll
    for (int i = 0; i < 4; ++i) { PIN(f1a[i]); PIN(f1b[i]); PIN(f2a[i >> 1][i & 1]); PIN(f2b[i >> 1][i & 1]); }
#pragma unroll
    for (int i = 0; i < 2; ++i) { PIN(tfc[i]); PIN(tfs[i]); PIN(tic[i]); PIN(tis[i]); }
    lp_t i2w = lptr(SCR + L_I2 + w * 2048 + lane * 16), i2r = lptr(SCR + L_I2 + ((g >> 1) * 32 + c) * 32 + (g & 1) * 16);
    lp_t exw = lptr(SCR + L_EX + w * 2048 + lane * 16), exr = lptr(SCR + L_EX + lane * 16);
    int vb = ((g >> 1) * 32 + c) * 32 + (g & 1) * 16 + WS_B + un * 8192 + w * 2048, vc = chk_pos(lane) * 4 + WS_CB + un * 1024 + w * 256;
    PIN(vg); PIN(vk); PIN(vb); PIN(vc);
    STAMP(5);
    DRAIN();
    STAMP(6);
    v4u xg[2];
    f32x4 wr[2], wi[2];
    u32 spin = 0;
    for (;; ++spin) {
      MEMFENCE();
      xg[0] = ld_dev(ws, vg, WS_A + sg); xg[1] = ld_dev(ws, vg, WS_A + sg + 32 * 8192);
      const u32 k_ = ld_dev1(ws, vk, WS_CA + sk);
      SB();
      u32 s0 = sum4(0u, wq[0]), s1 = sum4(0u, wq[1]);
#pragma unroll
      for (int m = 0; m < 2; ++m) {
#pragma unroll
        for (int r = 0; r < 4; ++r) { const u32 v = wq[m][r]; wr[m][r] = blo(v); wi[m][r] = bhi(v); }
        PIN(wr[m]); PIN(wi[m]);
      }
      PIN(s0); PIN(s1);
      SB();
      lds_w128(i2w, xg[0]); lds_w128(i2w + 1024, xg[1]);
      if (__all(pair_total(sum4(s0, xg[0]), sum4(s1, xg[1]), lane & 1) + k_ == tag) || spin > SPIN_CAP) break;
      MEMFENCE();
      wq[0] = ld_dev(ws, vg, sw); wq[1] = ld_dev(ws, vg, sw + 32 * 8192);
    }
    COUNT(20, spin);
    STAMP(7);
    BAR();
    STAMP(8);
    v4u af[2][4], b2[2], a3[2];
#pragma unroll
    for (int m = 0; m < 2; ++m)
#pragma unroll
      for (int ks = 0; ks < 4; ++ks) af[m][ks] = lds_r128(i2r + ks * 2048 + m * 512);
    SB();
    f32x4 dr[2], di[2];
#pragma unroll
    for (int m = 0; m < 2; ++m)
#pragma unroll
      for (int ks = 0; ks < 4; ++ks) { dr[m] = mm16(af[m][ks], f1a[ks], ks ? dr[m] : z); di[m] = mm16(af[m][ks], f1b[ks], ks ? di[m] : z); }
#pragma unroll
    for (int m = 0; m < 2; ++m) b2[m] = pk4(dr[m] * tfc[m] + di[m] * tfs[m], di[m] * tfc[m] - dr[m] * tfs[m]);
#pragma unroll
    for (int k = 0; k < 2; ++k)
#pragma unroll
      for (int m = 0; m < 2; ++m) { dr[m] = mm16(f2a[m][k], b2[k], k ? dr[m] : z); di[m] = mm16(f2b[m][k], b2[k], k ? di[m] : z); }
#pragma unroll
    for (int m = 0; m < 2; ++m) a3[m] = pk4(dr[m] * wi[m] + di[m] * wr[m], dr[m] * wr[m] - di[m] * wi[m]);
#pragma unroll
    for (int k = 0; k < 2; ++k)
#pragma unroll
      for (int n = 0; n < 2; ++n) { di[n] = mm16(a3[k], f2a[n][k], k ? di[n] : z); dr[n] = mm16(a3[k], f2b[n][k], k ? dr[n] : z); }
#pragma unroll
    for (int n = 0; n < 2; ++n) lds_w128(exw + n * 1024, pk4(di[n] * tic[n] + dr[n] * tis[n], dr[n] * tic[n] - di[n] * tis[n]));
    STAMP(9);
    BAR();
#pragma unroll
    for (int n = 0; n < 2; ++n)
#pragma unroll
      for (int ks = 0; ks < 4; ++ks) af[n][ks] = lds_r128(exr + ks * 2048 + n * 1024);
    SB();
    u32 cs[2];
#pragma unroll
    for (int n = 0; n < 2; ++n)
#pragma unroll
      for (int ks = 0; ks < 4; ++ks) { di[n] = mm16(f1a[ks], af[n][ks], ks ? di[n] : z); dr[n] = mm16(f1b[ks], af[n][ks], ks ? dr[n] : z); }
#pragma unroll
    for (int n = 0; n < 2; ++n) {
      const v4u e = pk4(dr[n], di[n]);
      st_dev(ws, vb + n * 512, e);
      cs[n] = sum4(0u, e);
    }
    const auto sx = __builtin_amdgcn_permlane16_swap(cs[0], cs[1], false, false);
    st_dev1(ws, vc, tag - sx[0] - sx[1]);
    BAR();
    if (wg == 0 && w == 0) st_dev1(ws, WS_E, tag);
  }
  STAMP(10);
  SB();
  {
    v4u gbre, gbim, g0re, g0im, ga[2];
#pragma unroll
    for (int r = 0; r < 4; ++r) {
      gbre[r] = fbre[r] ^ (g < 2 ? NEGH : 0u);
      gbim[r] = fbsw[r] ^ (g < 2 ? 0u : NEGH);
      const float n = fc * ((31.f - 2.f * r - 2.f * fg4) * (1.f / 32));
      u32 q = pk(cosr(n), -sinr(n));
      PIN(q);
      g0re[r] = g < 2 ? gbre[r] : 0u;
      g0im[r] = g < 2 ? 0u : q;
      ga[0][r] = f2a[0][0][r] ^ NEGH; ga[1][r] = f2a[1][0][r] ^ NEGH;
    }
    twc *= 2.f / (2048.f * 512.f); tws *= 2.f / (2048.f * 512.f);
    g0re[0] = g == 0 ? ((c & 1) ? 0xbf00u : 0x3f00u) : g0re[0];
    ga[0][0] = g == 0 ? ((c & 1) ? 0xbf803f80u : 0x3f803f80u) : ga[0][0]; ga[1][0] = ga[0][0];
    PIN(gbre); PIN(gbim); PIN(g0re); PIN(g0im); PIN(ga[0]); PIN(ga[1]); PIN(twc); PIN(tws);
    f32x4 xin[2][2];
    lp_t xq = lptr(SCR + L_X + w * 2048 + (4 * c + g) * 8);
#pragma unroll
    for (int rr = 0; rr < 2; ++rr)
#pragma unroll
      for (int t = 0; t < 2; ++t) {
        const v2u v = lds_r64(xq + rr * 1024 + t * 512);
        const u32 v0 = v.x, v1 = v.y;
        f32x4 q = {blo(v0), bhi(v0), blo(v1), bhi(v1)};
        xin[rr][t] = q - mean[rr]; PIN(xin[rr][t]);
      }
    lp_t i3w = lptr(SCR + L_I3 + w * 2048 + lane * 16), i3r[4], o3w[2], o3r[2];
#pragma unroll
    for (int r = 0; r < 4; ++r) i3r[r] = lptr(SCR + L_I3 + slot_of(c, 4 * g + r) * 32 + w * 8);
#pragma unroll
    for (int i = 0; i < 2; ++i) {
      o3w[i] = lptr(SCR + L_O3 + ((4 * c + g) * 8 + ((2 * w + i) ^ (c & 7))) * 16);
      const int q = w * 128 + i * 64 + lane;
      o3r[i] = lptr(SCR + L_O3 + (q ^ ((q >> 5) & 7)) * 16);
    }
    int vs = lane * 16 + WS_L + un * 8192 + w * 2048, vt = lane < 8 ? lane * 16 + WS_S + un * 512 + w * 128 : OOB;
    PIN(vs); PIN(vt);
    lp_t sow = lptr(SCR + L_SO + tid * 16), sor = lptr(SCR + L_SO + (lane & 6) * 512 + (lane & 1) * 8);
    DRAIN();
    STAMP(11);
    u32 spin = 0;
    for (;; ++spin) {
      MEMFENCE();
      const v4u x0 = ld_dev(ws, vg, WS_B + sg), x1 = ld_dev(ws, vg, WS_B + sg + 32 * 8192);
      const u32 k_ = ld_dev1(ws, vk, WS_CB + sk);
      SB();
      lds_w128(i3w, x0); lds_w128(i3w + 1024, x1);
      if (__all(pair_total(sum4(0u, x0), sum4(0u, x1), lane & 1) + k_ == tag) || spin > SPIN_CAP) break;
    }
    COUNT(21, spin);
    STAMP(12);
    BAR();
    STAMP(13);
    v4u a[2], a2[2];
#pragma unroll
    for (int r = 0; r < 4; ++r) { const v2u v = lds_r64(i3r[r]); a[0][r] = v.x; a[1][r] = v.y; }
    f32x4 tr[2], ti[2], sr[2], si[2], h[2][2];
#pragma unroll
    for (int rr = 0; rr < 2; ++rr) { tr[rr] = mm16p(a[rr], gbre, z); ti[rr] = mm16p(a[rr], gbim, z); sr[rr] = mm16p(a[rr], g0re, z); si[rr] = mm16p(a[rr], g0im, z); }
#pragma unroll
    for (int rr = 0; rr < 2; ++rr) {
      tr[rr][0] = g == 0 ? sr[rr][0] : tr[rr][0]; ti[rr][0] = g == 0 ? si[rr][0] : ti[rr][0];
      a2[rr] = pk4(tr[rr] * twc - ti[rr] * tws, ti[rr] * twc + tr[rr] * tws);
      h[rr][0] = mm16p(a2[rr], ga[0], xin[rr][0]); h[rr][1] = mm16p(a2[rr], ga[1], xin[rr][1]);
    }
    float st[2];
#pragma unroll
    for (int rr = 0; rr < 2; ++rr) { const f32x4 q = h[rr][0] * h[rr][0] + h[rr][1] * h[rr][1]; st[rr] = (q[0] + q[2]) + (q[1] + q[3]); }
    wave_sums<2>(st);
    v4u so;
#pragma unroll
    for (int rr = 0; rr < 2; ++rr) {
      const float ve = st[rr] * (1.f / 512) + 1e-5f, rstd = __builtin_amdgcn_rsqf(ve);
      const f32x4 l0 = h[rr][0] * rstd, l1 = h[rr][1] * rstd;
      v4u l4 = {pk(l0[0], l0[1]), pk(l0[2], l0[3]), pk(l1[0], l1[1]), pk(l1[2], l1[3])};
      lds_w128(o3w[rr], l4);
      so[2 * rr] = asu(mean[rr]); so[2 * rr + 1] = asu(ve * rstd);
    }
    lds_w128(sow, so);
    STAMP(14);
    BAR();
    const v4u v0 = lds_r128(o3r[0]), v1 = lds_r128(o3r[1]);
    const v2u ms = lds_r64(sor);
    st_dev(ws, vs, v0); st_dev(ws, vs + 1024, v1);
    u32 s = sum4(sum4(0u, v0), v1);
    s += dppu<0x128>(s); s = add_x32(add_x16(s));
    v4u rec = {tag - s - ms.x - ms.y, ms.x, ms.y, tag};
    st_dev(ws, vt, rec);
    BAR();
  }
  SB();
  {
    constexpr int PD = 4;
    constexpr int LOOK = 14;
    const int kh = w & 1, rl = w >> 1, hh = lane >> 5;
    v4u ring[8], act[32], hfA[8], hfB[8], hfC[8];
    u32 hkA, hkB, hkC;
    f32x16 zz;
#pragma unroll
    for (int i = 0; i < 16; ++i) zz[i] = 0.f;
    lp_t wa[4], wb[4], wd[2][2], we[2];
#pragma unroll
    for (int t = 0; t < 4; ++t) wa[t] = lptr(t * SLOT + ((lane ^ (2 * t + ((lane >> 3) & 1))) * 16));
    {
      const int q = (lane & 15) >> 2, pp = lane & 3;
      const u32 dl = (4 * hh + q) * 128 + (q >> 1) * 64 + (pp & 1) * 32 + ((lane >> 4) & 1) * 16 + (pp >> 1) * 8;
#pragma unroll
      for (int i = 0; i < 2; ++i) {
        wd[0][i] = lptr((dl ^ (i * 64)) + SLOT + kh * 16384); wd[1][i] = lptr((dl ^ (i * 64)) + 3 * SLOT + kh * 16384);
        we[i] = lptr((dl ^ (i * 64)) + kh * 12288);
      }
    }
    auto frag = [&](auto NN) -> v4u {
      constexpr int N = NN.value;
      if constexpr (N < 64) return lds_r128(wa[N & 1] + (N >> 1) * 1024);
      else if constexpr (N < 128) return lds_r128(wa[N >> 5] + (N & 31) * 1024);
      else if constexpr (N < 192) return lds_r128(wb[(N >> 4) & 3] + (N & 31) * 1024);
      else {
        constexpr int t = N < 256 ? (N - 192) >> 4 : N < 268 ? 4 : 5, s = N < 256 ? (N & 15) : N < 268 ? N - 256 : N - 268;
        const lp_t b = (t < 4 ? wd[(t >> 1) & 1][s & 1] : we[s & 1]) + (t & 1) * SLOT + (s >> 1) * 2048;
        return lds_tr2(b, b + 1024);
      }
    };
#define FRAG(N) frag(std::integral_constant<int, (N)>{})
    const int hb = WS_H + (4 * rg + 2 * (ch >> 3) + rl) * NFT * HREC;
    int hs[8], hs4 = hb + (36 * kh + 4) * HREC;
#pragma unroll
    for (int i = 0; i < 8; ++i) { hs[i] = hb + tile0_of(8 * kh + i) * HREC; PINS(hs[i]); }
    PINS(hs4);
    int vl = lane * 16, vlz = kh ? OOB : vl, vq, vq4;
    {
      const int st = (lane & 3) + ((lane >> 1) & 4), wo = hb + 1024 + ((lane >> 4) * 8 + (lane & 4)) * 4;
      vq = wo + tile0_of(8 * kh + st) * HREC;
      vq4 = st < 6 - kh ? wo + (36 * kh + 6 * st + 4) * HREC : OOB;
    }
    int vo = WS_H + ((4 * rg + w) * NFT + ft0) * HREC + lane * 16, vk = (lane & 8) ? OOB : vo - lane * 16 + 1024 + ((lane >> 4) * 8 + (lane & 7)) * 4;
    int vo4 = six ? vo + 4 * HREC : OOB, vk4 = six ? vk + 4 * HREC : OOB;
    PIN(vl); PIN(vlz); PIN(vq); PIN(vq4); PIN(vo); PIN(vk); PIN(vo4); PIN(vk4);
#define H_LD(X, t, j) do { \
      if constexpr ((j) == 8) hk##X = ld_dev1(ws, ((t) < 4 ? vq : vq4) + ((t) & 3) * HREC, 0); \
      else if constexpr ((t) < 4) hf##X[(j) & 7] = ld_dev(ws, vl + (t) * HREC, hs[(j) & 7]); \
      else if constexpr ((j) < 6) hf##X[(j) & 7] = ld_dev(ws, ((j) == 5 ? vlz : vl) + ((t) & 3) * HREC, hs4 + 6 * (j) * HREC); } while (0)
    Epi e;
#define EPI(t, acc, J0, J1) static_for<((J0) < 0 ? 0 : (J0) > 36 ? 36 : (J0)), ((J1) < 0 ? 0 : (J1) > 36 ? 36 : (J1))>([&](auto JJ) { epi_slice<JJ.value>(e, acc); \
      if constexpr (JJ.value == 31) st_dev(ws, ((t) < 4 ? vo : vo4) + ((t) & 3) * HREC, e.r); \
      if constexpr (JJ.value == 35) st_dev1(ws, ((t) < 4 ? vk : vk4) + ((t) & 3) * HREC, tag - e.chk); })
    int va = ((lane >> 3) & 3) * 8192 + hh * 128 + (lane & 7) * 16;
    int vr = ((lane >> 3) & 3) * 512 + hh * 128 + (lane & 7) * 16;
    PIN(vr);
    const int sa = WS_L + (4 * rg + w) * 32768;
#define ACT_LD(ks) act[ks] = ld_dev(ws, va + ((ks) & 15) * 256, sa + ((ks) >> 4) * 4096)
    f32x16 tA, tB, tC;
    DRAIN();
    STAMP(15);
    u32 spin = 0;
    for (;; ++spin) {
      MEMFENCE();
      PIN(va);
      const v4u s0 = ld_dev(ws, vr, WS_S + (4 * rg + w) * 2048), s1 = ld_dev(ws, vr + 256, WS_S + (4 * rg + w) * 2048);
      SB();
      static_for<0, LOOK>([&](auto K) { ACT_LD(K.value); SB(); });
      static_for<0, PD>([&](auto K) { ring[K.value] = FRAG(K.value); });
      SB();
      if (__any(s0.w != tag || s1.w != tag) && spin <= SPIN_CAP) continue;
      u32 m = (s0.x + s0.y + s0.z) + (s1.x + s1.y + s1.z);
      static_for<0, 64>([&](auto K) {
        constexpr int n = K.value, ks = n >> 1;
        if constexpr (n & 1) tB = mm32p(ring[n & 7], act[ks], ks ? tB : zz); else tA = mm32p(ring[n & 7], act[ks], ks ? tA : zz);
        ring[(n + PD) & 7] = FRAG(n + PD);
        if constexpr (!(n & 1) && ks + LOOK < 32) ACT_LD(ks + LOOK);
        { const v4u v = act[ks]; u32 t = (n & 1) ? m + v.z + v.w : m + v.x + v.y; PIN(t); m = t; }
        SB();
      });
      if (__all(add_x32(m) == 4u * tag) || spin > SPIN_CAP) break;
    }
    COUNT(22, spin);
#pragma unroll
    for (int i = 0; i < 4; ++i) wb[i] = lptr((i < 2 ? 4 * SLOT : 0) + ((lane ^ (4 * (i & 1) + 2 * hh + ((lane >> 3) & 1))) * 16));
    BAR_N(15);
    STAMP(16);
#define G1_TILE(T, acc, ...) static_for<0, 32>([&](auto K) { constexpr int s = K.value, n = 32 * (T) + s; \
      acc = mm32p(ring[n & 7], act[s], s ? acc : zz); \
      ring[(n + PD) & 7] = FRAG(n + PD); \
      __VA_ARGS__; SB(); })
    G1_TILE(2, tC, { constexpr int j0 = (27 * s) / 16, j1 = (27 * s + 27) / 16; EPI(0, tA, j0, j1); EPI(1, tB, j0 - 36, j1 - 36); });
    BAR_N(15);
    G1_TILE(3, tA, { constexpr int j0 = (27 * s) / 16 + 18, j1 = (27 * s + 27) / 16 + 18; EPI(1, tB, j0, j1); EPI(2, tC, j0 - 36, j1 - 36); });
    BAR_N(15);
    G1_TILE(4, tB, { EPI(3, tA, (6 * (s - 2)) / 5, (6 * (s - 1)) / 5);
      if constexpr (s >= 12 && s < 30 && !(s & 1)) H_LD(A, 0, (s - 12) >> 1); });
    BAR_N(15);
    G1_TILE(5, tC, { EPI(4, tB, (6 * (s - 2)) / 5, (6 * (s - 1)) / 5);
      if constexpr (s >= 12 && s < 30 && !(s & 1)) H_LD(B, 1, (s - 12) >> 1); });
    BAR_N(15);
    STAMP(17);
    f32x16 o0, o1;
    u32 nfix = 0;
#define H_TOT(X, nf) ({ u32 m_ = hk##X; _Pragma("unroll") for (int i = 0; i < (nf); ++i) m_ = sum4(m_, hf##X[i]); \
      m_ += dppu<0xb1>(m_); m_ += dppu<0x4e>(m_); m_ += dppu<0x128>(m_); m_; })
#define H_FIX(X, t, nf, want) do { if (__builtin_expect(__any(H_TOT(X, nf) != (want)), 0)) { u32 spin_ = 0; _Pragma("nounroll") do { MEMFENCE(); nfix++; \
        static_for<0, 9>([&](auto J) { H_LD(X, t, J.value); }); } while (__any(H_TOT(X, nf) != (want)) && ++spin_ < SPIN_CAP); } SB(); } while (0)
#define G2_ROUND(X, n0, ns, ...) static_for<0, ns>([&](auto K) { constexpr int s = K.value, n = (n0) + s; \
      if constexpr (s & 1) o1 = mm32p(ring[n & 7], hf##X[s >> 1], n < 194 ? zz : o1); else o0 = mm32p(ring[n & 7], hf##X[s >> 1], n < 194 ? zz : o0); \
      if constexpr (n + PD < 280) ring[(n + PD) & 7] = FRAG(n + PD < 280 ? n + PD : 0); \
      __VA_ARGS__; SB(); })
    const u32 t8 = 8u * tag, t6 = (6u - kh) * tag;
    H_FIX(A, 0, 8, t8);
    G2_ROUND(A, 192, 16, { EPI(5, tC, (9 * s) / 4, (9 * s + 9) / 4); if constexpr (!(s & 1)) H_LD(C, 2, s >> 1); if constexpr (s == 15) H_LD(C, 2, 8); });
    BAR_N(15);
    H_FIX(B, 1, 8, t8);
    G2_ROUND(B, 208, 16, { if constexpr (!(s & 1)) H_LD(A, 3, s >> 1); if constexpr (s == 15) H_LD(A, 3, 8); });
    BAR_N(15);
    H_FIX(C, 2, 8, t8);
    G2_ROUND(C, 224, 16, { if constexpr (!(s & 1)) H_LD(B, 4, s < 12 ? s >> 1 : s == 12 ? 8 : 7); });
    const int u = 16 * rg + 8 * (ch >> 3) + 4 * rl + ((lane >> 3) & 3), col = 64 * (ch & 7) + 32 * kh + 16 * hh;
    v2u hr[4];
    v4u so;
    int vh = u * 8192 + (col & 255) * 32 + (lane & 7) * 16 + (col >> 8) * 8, vs = u * 512 + (lane & 7) * 16;
    H_FIX(A, 3, 8, t8);
    G2_ROUND(A, 240, 16, { if constexpr (s < 7) H_LD(C, 5, s < 6 ? s : 8);
      if constexpr (s >= 8 && s < 12) hr[s & 3] = __builtin_amdgcn_raw_buffer_load_b64(ws, vh + (s & 3) * 128, WS_L, AUX_SC1);
      if constexpr (s == 12) so = ld_dev(ws, vs, WS_S);
      if constexpr (s == 10) BAR_N(15); });
    H_FIX(B, 4, 6, t6);
    G2_ROUND(B, 256, 12, { });
    H_FIX(C, 5, 6, t6);
    G2_ROUND(C, 268, 12, { });
    COUNT(23, nfix);
    STAMP(18);
    lp_t rw = lptr(2 * SLOT + w * 4096 + lane * 16), rr_ = lptr(2 * SLOT + (w ^ 1) * 4096 + lane * 16);
    f32x4 f[4];
#define Q4(a, i) f32x4{a[4 * (i)], a[4 * (i) + 1], a[4 * (i) + 2], a[4 * (i) + 3]}
    if (kh) {
#pragma unroll
      for (int i = 0; i < 4; ++i) { lds_wf(rw + i * 1024, Q4(o0, i)); f[i] = Q4(o1, i); }
    } else {
#pragma unroll
      for (int i = 0; i < 4; ++i) { lds_wf(rw + i * 1024, Q4(o1, i)); f[i] = Q4(o0, i); }
    }
    {
      const u32 mb = so.y, sb = so.z;
#pragma unroll
      for (int i = 0; i < 4; ++i) { const u32 v0 = hr[i].x, v1 = hr[i].y; const f32x4 q = {blo(v0), bhi(v0), blo(v1), bhi(v1)}; f[i] += q * asf(sb) + asf(mb); }
    }
    v4u* op = (v4u*)(p.out + (32 * (8 * (u >> 5) + (lane & 7)) + (u & 31)) * D_ + col);
    BAR();
#pragma unroll
    for (int i = 0; i < 2; ++i) {
      const f32x4 v0 = f[2 * i] + lds_rf(rr_ + i * 2048), v1 = f[2 * i + 1] + lds_rf(rr_ + i * 2048 + 1024);
      v4u q = {pk(v0[0], v0[1]), pk(v0[2], v0[3]), pk(v1[0], v1[1]), pk(v1[2], v1[3])};
      op[i] = q;
    }
  }
  STAMP(24);
  STAMP_FLUSH();
  UNDEF_RA();
  }
  PINS(wv);
  if (wv >= 4) {
    STAMP(0);
    const int hw = wv - 4;
    W1_SETUP();
    const rsrc_t ws = mkrs(p.ws, WS_END);
    const int vy = six ? 4 * (lane >> 2) * (F_ * 2) + (ft0 + 4) * 32 + (lane & 3) * 16 : OOB;
    const u32 ax = ((lane >> 1) & 3) * SLOT + (lane >> 3) * 512 + (hw & 1) * 256 + (lane & 1) * 128 + (lane & 7) * 16;
#define PUT_W1(src, a) do { const u32 a_ = (a); _Pragma("unroll") for (int i = 0; i < 8; ++i) lds_w128((lp_t)(size_t)(a_ ^ (i * 16)), src[i]); } while (0)
    v4u xb[8], y0[8], y1[8];
    if (hw < 2) {
      const int T = tid - 256, m = T >> 1, vs = ((m & 0x39) | ((m & 2) << 1) | ((m & 4) >> 1)) * 32 + (T & 1) * 16 + WS_W + un * 8192;
      const u32 sh = (lane & 1) ? 0u : 16u;
#pragma unroll
      for (int i = 0; i < 8; ++i) { const u32 a = ra[i].x, b = ra[i].y, d = ra[i].z; ra[i].x = __builtin_amdgcn_alignbit(b, a, sh); ra[i].y = __builtin_amdgcn_alignbit(d, b, sh); }
      u32 ck[4];
      v4u v[4];
#pragma unroll
      for (int e = 0; e < 4; ++e) {
#pragma unroll
        for (int r = 0; r < 4; ++r) { const u32 a = ra[r][e >> 1], b = ra[4 + r][e >> 1]; v[e][r] = __builtin_amdgcn_perm(b, a, (e & 1) ? 0x07060302u : 0x05040100u); }
        st_dev(ws, vs + e * 2048, v[e]);
        ck[e] = sum4(0u, v[e]);
      }
      v2u cw = {pair_total(ck[0], ck[2], T & 1), pair_total(ck[1], ck[3], T & 1)};
      lds_w64(lptr(SCR + L_CW + T * 8), cw);
      SB();
      STAMP(1);
#define X0M 0
      if (X0M == 0) { LOAD_W1(ra, vx, X_PIECE(0)); }
    }
    STAMP(2);
    BAR();
    asm volatile("" :: "v"(cpf));
    if (X0M == 0 || hw >= 2) { transpose8(ra); PUT_W1(ra, ax + X_PIECE(0) * 512); }
    STAMP(3);
    BAR();
    if (X0M == 1 && hw < 2) { LOAD_W1(y0, vx, X_PIECE(0)); }
    LOAD_W1(xb, vx, X_PIECE(2)); LOAD_W1(ra, vx, X_PIECE(1));
    STAMP(4);
    BAR();
    BAR();
    LOAD_W1(y1, vx, X_PIECE(3));
    if (X0M == 1 && hw < 2) { transpose8(y0); PUT_W1(y0, ax + X_PIECE(0) * 512); }
    transpose8(xb); PUT_W1(xb, ax + X_PIECE(2) * 512);
    transpose8(ra); PUT_W1(ra, ax + X_PIECE(1) * 512);
    transpose8(y1); PUT_W1(y1, ax + X_PIECE(3) * 512);
    STAMP(5);
    BAR();
    LOAD_W1(y0, vy, Y_PIECE(0));
    STAMP(6);
    BAR();
    BAR();
    LOAD_W1(y1, vy, Y_PIECE(1));
    transpose8(y0); transpose8(y1);
#pragma unroll
    for (int i = 0; i < 8; ++i) {
#pragma unroll
      for (int j = 0; j < 4; ++j) { const u32 a = y0[i][j], b = y1[i][j], as = dppu<0x4e>(a), bs = dppu<0x4e>(b); y0[i][j] = (lane & 2) ? bs : a; y1[i][j] = (lane & 2) ? b : as; }
      PIN(y1[i]);
    }
    const u32 ay = (Y_PIECE((lane >> 1) & 1) + (lane >> 2)) * 512 + (hw & 1) * 256 + (lane & 1) * 144 + (lane & 4) * 8 + (lane & 2) * 32;
    PUT_W1(y0, ay + 4 * SLOT);
    const rsrc_t rwd = mkrs(p.wd, D_ * F_ * 2);
    const int vd = (lane >> 3) * (D_ * 2) + (ch & 7) * 128 + ((lane & 7) ^ ((lane >> 2) & 4)) * 16;
    int sd[4];
#pragma unroll
    for (int i = 0; i < 4; ++i) { sd[i] = tile0_of(4 * hw + i) * (16 * D_ * 2); PINS(sd[i]); }
#define DMA_WD(t, slot) do { _Pragma("unroll") for (int i = 0; i < 8; ++i) \
      __builtin_amdgcn_raw_ptr_buffer_load_lds(rwd, (LAS void*)(size_t)((slot) * SLOT + hw * 8192 + i * 1024), 16, vd, sd[i >> 1] + ((t) * 16 + (i & 1) * 8) * (D_ * 2), 0, 0); } while (0)
#define VM_WAIT(n) asm volatile("s_waitcnt vmcnt(" #n ")" ::: "memory")
    STAMP(7);
    BAR();
    PUT_W1(y1, ay);
    DMA_WD(0, 1);
    STAMP(8);
    BAR();
    DMA_WD(1, 2);
    BAR();
    DMA_WD(2, 3);
    VM_WAIT(8);
    BAR();
    DMA_WD(3, 4);
    VM_WAIT(8);
    BAR();
    if (hw < 3) DMA_WD(4, 0);
    BAR();
    if (hw < 3) DMA_WD(5, 1);
    if (hw < 3) VM_WAIT(16); else VM_WAIT(0);
    BAR();
    VM_WAIT(0);
    STAMP(9);
    BAR();
    STAMP_FLUSH();
  }

}

extern "C" void kernel_launch(void* const* d_in, const int* in_sizes, int n_in,
                              void* d_out, int out_size, void* d_ws, size_t ws_size,
                              hipStream_t stream) {
  Params p;
  p.x = (const u16*)d_in[0]; p.wr = (const u16*)d_in[1]; p.wi = (const u16*)d_in[2];
  p.wg = (const u16*)d_in[4]; p.wu = (const u16*)d_in[5]; p.wd = (const u16*)d_in[6];
  p.out = (u16*)d_out; p.ws = d_ws;
  (void)hipFuncSetAttribute((const void*)gfnet_geglu_kernel, hipFuncAttributeMaxDynamicSharedMemorySize, 5 * SLOT);
  gfnet_geglu_kernel<<<256, 512, 5 * SLOT, stream>>>(p);
}
```

```cpp
#define PROBE_I 25
#define PROBE_J 24
#define PROBE_WG 100
#include <hip/hip_runtime.h>
#include <hip/hip_bf16.h>
#include <type_traits>

typedef unsigned u32;
typedef unsigned short u16;
typedef u32 v4u __attribute__((ext_vector_type(4)));
typedef u32 v2u __attribute__((ext_vector_type(2)));
typedef float f32x2 __attribute__((ext_vector_type(2)));
typedef float f32x4 __attribute__((ext_vector_type(4)));
typedef float f32x16 __attribute__((ext_vector_type(16)));
typedef __bf16 bf16x2 __attribute__((ext_vector_type(2)));
typedef __bf16 bf16x8 __attribute__((ext_vector_type(8)));
typedef short s16x4 __attribute__((ext_vector_type(4)));
#define LAS __attribute__((address_space(3)))
#define DI __device__ __forceinline__
typedef __amdgpu_buffer_rsrc_t rsrc_t;
typedef LAS char* lp_t;

constexpr int S_ = 2048, D_ = 512, F_ = 1376, KF = 257;
constexpr int NFT = 86;
constexpr int WS_E = 0;
constexpr int WS_A = 4096;
constexpr int WS_W = WS_A + (2 << 20);
constexpr int WS_B = WS_W + (2 << 20);
constexpr int WS_L = WS_B + (2 << 20);
constexpr int WS_CA = WS_L + (2 << 20);
constexpr int WS_CB = WS_CA + (1 << 18);
constexpr int WS_S = WS_CB + (1 << 18);
constexpr int WS_H = WS_S + (1 << 17);
constexpr int HREC = 1152;
constexpr int WS_END = WS_H + 64 * NFT * HREC;
constexpr int WS_ST = WS_END;
constexpr u32 NEG2 = 0x80008000u, NEGH = 0x80000000u, NEGL = 0x8000u;
constexpr u32 SPIN_CAP = 1u << 14;
constexpr int AUX_SC1 = 16;
constexpr int OOB = 0x40000000;
constexpr int CODE_LINES = 200;
constexpr int SLOT = 32768, SCR = 4 * SLOT;
constexpr int L_X = 0, L_T1 = 8192, L_I2 = 16384, L_EX = 24576, L_CW = 24576;
constexpr int L_I3 = 8192, L_O3 = 16384, L_SO = 24576;

#define DRAIN() asm volatile("s_waitcnt vmcnt(0)" ::: "memory")
#define MEMFENCE() asm volatile("" ::: "memory")
#define PIN(x) asm volatile("" : "+v"(x))
#define PINS(x) asm volatile("" : "+s"(x))
#define SB() __builtin_amdgcn_sched_barrier(0)
#define BAR() asm volatile("s_waitcnt lgkmcnt(0)\n\ts_barrier" ::: "memory")
#define BAR_N(n) asm volatile("s_waitcnt lgkmcnt(" #n ")\n\ts_barrier" ::: "memory")

DI u32 pk(float a, float b) { f32x2 v = {a, b}; return __builtin_bit_cast(u32, __builtin_convertvector(v, bf16x2)); }
DI v4u pk4(f32x4 a, f32x4 b) { v4u r = {pk(a[0], b[0]), pk(a[1], b[1]), pk(a[2], b[2]), pk(a[3], b[3])}; return r; }
DI float asf(u32 v) { return __builtin_bit_cast(float, v); }
DI u32 asu(float v) { return __builtin_bit_cast(u32, v); }
DI float blo(u32 v) { return asf(v << 16); }
DI float bhi(u32 v) { return asf(v & 0xffff0000u); }
DI float dot2(u32 a, u32 b, float c) { return __builtin_amdgcn_fdot2_f32_bf16(__builtin_bit_cast(bf16x2, a), __builtin_bit_cast(bf16x2, b), c, false); }
DI u32 sum4(u32 m, v4u v) { return (m + v.x + v.y) + v.z + v.w; }
template <int CTRL> DI u32 dppu(u32 v) { return (u32)__builtin_amdgcn_update_dpp(0, (int)v, CTRL, 0xf, 0xf, true); }
template <int CTRL> DI float dppf(float v) { return asf(dppu<CTRL>(asu(v))); }
DI u32 pair_total(u32 s0, u32 s1, bool odd) { return (odd ? s1 : s0) + dppu<0xb1>(odd ? s0 : s1); }
DI u32 add_x16(u32 v) { const auto s = __builtin_amdgcn_permlane16_swap(v, v, false, false); return s[0] + s[1]; }
DI u32 add_x32(u32 v) { const auto s = __builtin_amdgcn_permlane32_swap(v, v, false, false); return s[0] + s[1]; }
template <int N> DI void wave_sums(float (&v)[N]) {
#define WS_STEP(C) _Pragma("unroll") for (int i = 0; i < N; ++i) v[i] += dppf<C>(v[i]);
  WS_STEP(0x111) WS_STEP(0x112) WS_STEP(0x114) WS_STEP(0x118) WS_STEP(0x142) WS_STEP(0x143)
#pragma unroll
  for (int i = 0; i < N; ++i) v[i] = asf(__builtin_amdgcn_readlane(asu(v[i]), 63));
}
DI int chk_pos(int k) { return (k & 0xc7) | ((k & 8) << 2) | ((k >> 1) & 0x18); }
DI f32x4 mm16(v4u a, v4u b, f32x4 c) { return __builtin_amdgcn_mfma_f32_16x16x32_bf16(__builtin_bit_cast(bf16x8, a), __builtin_bit_cast(bf16x8, b), c, 0, 0, 0); }
DI f32x4 mm16p(v4u a, v4u b, f32x4 c) { f32x4 d = mm16(a, b, c); PIN(d); return d; }
DI f32x16 mm32p(v4u a, v4u b, f32x16 c) { f32x16 d = __builtin_amdgcn_mfma_f32_32x32x16_bf16(__builtin_bit_cast(bf16x8, a), __builtin_bit_cast(bf16x8, b), c, 0, 0, 0); PIN(d); return d; }
DI v2u lds_tr(lp_t a) { return __builtin_bit_cast(v2u, __builtin_amdgcn_ds_read_tr16_b64_v4i16((LAS s16x4*)a)); }
DI v4u lds_tr2(lp_t a, lp_t b) { v2u x = lds_tr(a), y = lds_tr(b); v4u r = {x.x, x.y, y.x, y.y}; return r; }
DI v4u lds_r128(lp_t a) { return *(LAS v4u*)a; }
DI f32x4 lds_rf(lp_t a) { return *(LAS f32x4*)a; }
DI v2u lds_r64(lp_t a) { return *(LAS v2u*)a; }
DI void lds_w128(lp_t a, v4u v) { *(LAS v4u*)a = v; }
DI void lds_wf(lp_t a, f32x4 v) { *(LAS f32x4*)a = v; }
DI void lds_w64(lp_t a, v2u v) { *(LAS v2u*)a = v; }
DI lp_t lptr(u32 off) { lp_t q = (lp_t)(size_t)off; PIN(q); return q; }
DI rsrc_t mkrs(const void* p, u32 bytes) { return __builtin_amdgcn_make_buffer_rsrc((void*)p, 0, bytes, 0x00020000); }
DI v4u ld_dev(rsrc_t r, int v, int s) { return __builtin_amdgcn_raw_buffer_load_b128(r, v, s, AUX_SC1); }
DI u32 ld_dev1(rsrc_t r, int v, int s) { return __builtin_amdgcn_raw_buffer_load_b32(r, v, s, AUX_SC1); }
DI void st_dev(rsrc_t r, int v, v4u x) { __builtin_amdgcn_raw_buffer_store_b128(x, r, v, 0, AUX_SC1); }
DI void st_dev1(rsrc_t r, int v, u32 x) { __builtin_amdgcn_raw_buffer_store_b32(x, r, v, 0, AUX_SC1); }
DI v4u ld_in(rsrc_t r, int v, int s) { return __builtin_amdgcn_raw_buffer_load_b128(r, v, s, 0); }
DI float cosr(float rev) { return __builtin_amdgcn_cosf(rev); }
DI float sinr(float rev) { return __builtin_amdgcn_sinf(rev); }
__host__ __device__ constexpr int slot_of(int k1, int k2) { return ((k2 < 8 ? k1 + 32 * k2 : (k1 ? 32 - k1 : 16) + 32 * (15 - k2)) - 1) & 255; }
DI int tile0_of(int c) { return 4 * c + 2 * (c < 11 ? c : 11); }
template <int I, int N, class F> DI void static_for(F&& f) {
  if constexpr (I < N) { f(std::integral_constant<int, I>{}); static_for<I + 1, N>(f); }
}
DI void transpose8(v4u (&m)[8]) {
  v4u o[8];
#pragma unroll
  for (int i = 0; i < 8; ++i)
#pragma unroll
    for (int j = 0; j < 4; ++j) { const u32 a = m[2 * j][i >> 1], b = m[2 * j + 1][i >> 1]; o[i][j] = __builtin_amdgcn_perm(b, a, (i & 1) ? 0x07060302u : 0x05040100u); }
#pragma unroll
  for (int i = 0; i < 8; ++i) { m[i] = o[i]; PIN(m[i]); }
}
struct Epi { float t, lo; v4u r; u32 chk; };
template <int J> DI void epi_slice(Epi& e, const f32x16& a) {
  constexpr int v = J >> 2, ph = J & 3;
  if constexpr (J >= 32) {
    if constexpr (J < 35) { e.chk += dppu<J == 32 ? 0xb1 : J == 33 ? 0x4e : 0x128>(e.chk); PIN(e.chk); }
  } else if constexpr (ph == 0) { e.t = __builtin_fmaf(a[v] * a[v], -0.10294324f, -2.3022082f); PIN(e.t); }
  else if constexpr (ph == 1) { e.t = __builtin_amdgcn_exp2f(e.t * a[v]); PIN(e.t); }
  else if constexpr (ph == 2) { e.t = __builtin_amdgcn_rcpf(1.0f + e.t); PIN(e.t); }
  else {
    const float q = a[v] * a[v + 8] * e.t;
    if constexpr (v & 1) { u32 r = pk(e.lo, q); e.chk = v == 1 ? r : e.chk + r; PIN(r); PIN(e.chk); e.r[v >> 1] = r; } else { e.lo = q; PIN(e.lo); }
  }
}

constexpr double ct_sin(long n, long d) {
  n %= d; if (n < 0) n += d;
  if (2 * n > d) return -ct_sin(d - n, d);
  if (4 * n > d) return ct_sin(d - 2 * n, 2 * d);
  const double x = 6.283185307179586476925 * (double)n / (double)d;
  double t = x, r = x;
  for (int i = 1; i < 14; ++i) { t *= -x * x / ((2 * i) * (2 * i + 1)); r += t; }
  return r;
}
constexpr double ct_cos(long n, long d) { return ct_sin(4 * n + d, 4 * d); }
constexpr u32 ct_f32(double v) { return __builtin_bit_cast(u32, (float)v); }
constexpr u32 ct_bf(double v) { const u32 u = ct_f32(v); return (u + 0x7fffu + ((u >> 16) & 1u)) >> 16; }
constexpr u32 ct_pk(double a, double b) { return ct_bf(a) | (ct_bf(b) << 16); }
struct Tab { u32 v[6][64][4]; };
constexpr Tab make_tab() {
  Tab t = {};
  for (int l = 0; l < 64; ++l) {
    const int c = l & 15, g = l >> 4;
    for (int j = 0; j < 2; ++j) {
      const int a = 4 * g + 2 * j;
      t.v[0][l][j] = ct_pk(ct_cos(a * c, 32), ct_cos((a + 1) * c, 32));
      t.v[0][l][2 + j] = c ? ct_pk(-ct_sin(a * c, 32), -ct_sin((a + 1) * c, 32)) : ct_pk(1, -1);
    }
    for (int r = 0; r < 4; ++r) {
      const int b = 4 * g + r, sl = slot_of(b, c);
      t.v[1][l][r] = ct_f32(ct_cos(b * c, 512));
      t.v[2][l][r] = ct_f32(ct_sin(b * c, 512));
      t.v[3][l][r] = ct_pk(ct_cos(b * c, 16), ct_sin(b * c, 16));
      t.v[4][l][r] = c == 0 ? ct_pk((r & 1) ? -1 : 1, 0) : c < 8 ? ct_pk(ct_cos(b * c, 16), -ct_sin(b * c, 16)) : ct_pk(ct_cos(b * (31 - 2 * c), 32), -ct_sin(b * (31 - 2 * c), 32));
      t.v[5][l][r] = SCR + L_T1 + ((sl * 4) ^ ((sl >> 4) & 14)) * 8;
    }
  }
  return t;
}
static __device__ const __attribute__((aligned(128))) Tab g_tab = make_tab();

#define STAMP(i) do { if ((i) == PROBE_I || (i) == PROBE_J) { unsigned long long t_; asm volatile("s_memrealtime %0\n\ts_waitcnt lgkmcnt(0)" : "=s"(t_) :: "memory"); if ((i) == PROBE_I) pi_ = (u32)t_; else pj_ = (u32)t_; } } while (0)
#define STAMP_DECL u32 pi_ = 0, pj_ = 0
#define STAMP_FLUSH() do { if (wg == PROBE_WG && wv == 0) *(u32*)((char*)p.ws + WS_ST) = pj_ + (pi_ & 0u); } while (0)
#define COUNT(i, v) do { } while (0)

struct Params {
  const u16* x; const u16* wr; const u16* wi; const u16* wg; const u16* wu; const u16* wd; u16* out; void* ws;
};

__global__ void __launch_bounds__(512, 2) gfnet_geglu_kernel(Params p) {
  const int wg = blockIdx.x, tid = threadIdx.x, lane = tid & 63;
  int wv = __builtin_amdgcn_readfirstlane(tid >> 6);
  STAMP_DECL;
  STAMP(25);
  const int rg = 4 * ((wg & 7) >> 1) + (wg >> 6), ch = 8 * (wg & 1) + ((wg >> 3) & 7);
  const int un = 16 * rg + ch;
  const int ft0 = tile0_of(ch);
  const bool six = ch < 11;
#define X_PIECE(b) ((((b) ^ (rg & 1)) * 16) + ((wv >> 1) & 1) * 8)
#define Y_PIECE(b) ((2 * (b) + ((wv >> 1) & 1)) * 16)
#define LOAD_W1(dst, v, piece) _Pragma("unroll") for (int j = 0; j < 8; ++j) dst[j] = ld_in(rw1, v, (4 * (piece) + (j & 3) + 256 * (j >> 2)) * (F_ * 2))
#define W1_SETUP() const rsrc_t rw1 = mkrs((wv & 1) ? p.wu : p.wg, D_ * F_ * 2); const int vx = 4 * (lane >> 3) * (F_ * 2) + ft0 * 32 + (lane & 7) * 16
  v4u ra[8];
  u32 cpf;
#define UNDEF_RA() do { asm volatile("" : "=v"(cpf)); _Pragma("unroll") for (int i = 0; i < 8; ++i) asm volatile("" : "=v"(ra[i])); } while (0)
  if (wv >= 6) {
    unsigned long long pc0;
    asm volatile("s_getpc_b64 %0" : "=s"(pc0));
    cpf = __builtin_amdgcn_raw_buffer_load_b32(mkrs((const void*)(pc0 & ~127ull), CODE_LINES * 128), ((((wg >> 2) & 2) + (wv & 1)) * 64 + lane) * 128, 0, 0);
    SB();
    W1_SETUP();
    LOAD_W1(ra, vx, X_PIECE(0));
  } else if (wv >= 4) {
    const rsrc_t rwr = mkrs(p.wr, S_ * KF * 2), rwi = mkrs(p.wi, S_ * KF * 2);
    const int vf = (lane & 1) * (KF * 2 + 2) + ((tid & 127) >> 1) * 8;
    const int sf = (16 * (un >> 6) + 2 * (un & 7) + 256 * ((un >> 3) & 7)) * (KF * 2);
#pragma unroll
    for (int r = 0; r < 4; ++r) { ra[r] = ld_in(rwr, vf, sf + r * (64 * KF * 2)); ra[4 + r] = ld_in(rwi, vf, sf + r * (64 * KF * 2)); }
    asm volatile("" : "=v"(cpf));
  } else {
    UNDEF_RA();
  }
  SB();
  PINS(wv);

  if (wv < 4) {
  const int w = wv, c = lane & 15, g = lane >> 4;
  const float fc = (float)c, fg4 = (float)(4 * g);
  const f32x4 z = {0.f, 0.f, 0.f, 0.f};
  f32x4 twc, tws;
  v4u fbre, fbsw;
  u32 tag;
  float mean[2];
  const rsrc_t ws = mkrs(p.ws, WS_END);
  {
    v4u tb[6], xr[2];
    const rsrc_t rt = mkrs(&g_tab, sizeof(Tab));
    int vt = lane * 16;
    const int sx = (32 * (8 * (un >> 5) + 2 * w) + (un & 31)) * (D_ * 2);
#pragma unroll
    for (int i = 0; i < 6; ++i) tb[i] = ld_in(rt, vt + (i & 3) * 1024, (i >> 2) * 4096);
    SB();
    const rsrc_t rx = mkrs(p.x, S_ * D_ * 2);
    xr[0] = ld_in(rx, vt, sx); xr[1] = ld_in(rx, vt, sx + 32 * D_ * 2);
    tag = ld_dev1(ws, 0, WS_E);
    SB();
    STAMP(0);
    const u32 sc = (c & 1) ? NEG2 : 0u, sm = c < 8 ? NEGL : NEGH, p0 = c < 8 ? 0x0c0c0100u : 0x01000c0cu, p1 = c < 8 ? 0x0c0c0302u : 0x03020c0cu;
    v4u fare = {tb[0].x, tb[0].y, tb[0].x ^ sc, tb[0].y ^ sc}, faim = {tb[0].z, tb[0].w, tb[0].z ^ sc, tb[0].w ^ sc}, fbim, f0re, f0im;
    twc = __builtin_bit_cast(f32x4, tb[1]); tws = __builtin_bit_cast(f32x4, tb[2]); fbre = tb[3];
    lp_t t1w[4], t1r[2];
#pragma unroll
    for (int r = 0; r < 4; ++r) {
      const u32 a = tb[3][r], q = tb[4][r];
      fbsw[r] = __builtin_amdgcn_alignbit(a, a, 16);
      fbim[r] = fbsw[r] ^ sm;
      f0re[r] = __builtin_amdgcn_perm(q, q, p0); f0im[r] = __builtin_amdgcn_perm(q, q, p1);
      t1w[r] = lptr(tb[5][r] ^ (w * 8));
    }
#pragma unroll
    for (int i = 0; i < 2; ++i) t1r[i] = lptr(SCR + L_T1 + ((w * 128 + i * 64 + lane) ^ (2 * w + i)) * 16);
    const int sl = 64 * w + 32 * (lane & 1) + (lane >> 1);
    lp_t cwr = lptr(SCR + L_CW + sl * 4);
    int vc = chk_pos(sl) * 4 + WS_CA + un * 1024, vs = lane * 16 + WS_A + un * 8192 + w * 2048;
    float c0m = c == 0 ? 1.f / 16 : 0.f;
    lp_t xw = lptr(SCR + L_X + w * 2048 + lane * 16), xt = lptr(SCR + L_X + w * 2048 + lane * 8);
    PIN(vc); PIN(vs); PIN(c0m);
    PIN(fare); PIN(faim); PIN(fbre); PIN(fbim); PIN(f0re); PIN(f0im); PIN(twc); PIN(tws);
    STAMP(1);
    SB();
    v4u a[2];
#pragma unroll
    for (int rr = 0; rr < 2; ++rr) { lds_w128(xw + rr * 1024, xr[rr]); a[rr] = lds_tr2(xt + rr * 1024, xt + rr * 1024 + 512); }
    STAMP(2);
    float st[4] = {0.f, 0.f, 0.f, 0.f};
#pragma unroll
    for (int i = 0; i < 4; ++i)
#pragma unroll
      for (int rr = 0; rr < 2; ++rr) { const u32 v = xr[rr][i]; st[rr] = dot2(v, 0x3f803f80u, st[rr]); st[2 + rr] = dot2(v, v, st[2 + rr]); }
    SB();
    f32x4 ur[2], ui[2];
#pragma unroll
    for (int rr = 0; rr < 2; ++rr) { ur[rr] = mm16p(a[rr], fare, z); ui[rr] = mm16p(a[rr], faim, z); }
    wave_sums<4>(st);
    SB();
    v4u a2[2];
    f32x4 orr[2], oi[2], sr[2], si[2];
#pragma unroll
    for (int rr = 0; rr < 2; ++rr) {
      const f32x4 x = ur[rr] - st[rr] * c0m, y = ui[rr];
      a2[rr] = pk4(x * twc + y * tws, y * twc - x * tws);
      orr[rr] = mm16p(a2[rr], fbre, z); oi[rr] = mm16p(a2[rr], fbim, z); sr[rr] = mm16p(a2[rr], f0re, z); si[rr] = mm16p(a2[rr], f0im, z);
    }
    v4u o[2];
#pragma unroll
    for (int rr = 0; rr < 2; ++rr) {
      mean[rr] = st[rr] * (1.f / 512);
      const float rstd = __builtin_amdgcn_rsqf(fmaxf(st[2 + rr] * (1.f / 512) - mean[rr] * mean[rr], 0.f) + 1e-5f);
      orr[rr][0] = g == 0 ? sr[rr][0] : orr[rr][0]; oi[rr][0] = g == 0 ? si[rr][0] : oi[rr][0];
      o[rr] = pk4(orr[rr] * rstd, oi[rr] * rstd);
    }
#pragma unroll
    for (int r = 0; r < 4; ++r) { v2u v = {o[0][r], o[1][r]}; lds_w64(t1w[r], v); }
    tag += 1u;
    PIN(tag);
    STAMP(3);
    BAR();
    const v4u v0 = lds_r128(t1r[0]), v1 = lds_r128(t1r[1]);
    const u32 cw = *(LAS u32*)cwr;
    st_dev(ws, vs, v0);
    st_dev(ws, vs + 1024, v1);
    st_dev1(ws, vc, tag - cw - pair_total(sum4(0u, v0), sum4(0u, v1), lane & 1));
  }
  int vg = (lane >> 1) * 8192 + (lane & 1) * 16, vk = (32 * (lane & 1) + (lane >> 1)) * 1024;
  const int sg = w * (64 * 8192) + un * 32, sk = w * (64 * 1024) + chk_pos(un) * 4;
  const int sw = WS_W + w * (64 * 8192) + ((un & 3) * 64 + ((((un >> 2) & 0x39) | ((un >> 1) & 4) | ((un >> 3) & 2)))) * 32;
  v4u wq[2];
  wq[0] = ld_dev(ws, vg, sw); wq[1] = ld_dev(ws, vg, sw + 32 * 8192);
  STAMP(4);
  SB();
  v4u f2a[2][2];
  {
    v4u f1a[4], f1b[4], f2b[2][2];
    f32x4 tfc[2], tfs[2], tic[2], tis[2];
    const u32 sc = (c & 1) ? NEG2 : 0u;
    const float fj = (float)(16 * w) + fc;
#pragma unroll
    for (int r = 0; r < 4; ++r) {
#pragma unroll
      for (int ks = 0; ks < 2; ++ks) {
        const float n = (fg4 + (16 * ks + r)) * (fj * (1.f / 64));
        const float cs = cosr(n), sn = sinr(n);
        f1a[ks][r] = pk(cs, sn); f1b[ks][r] = pk(-sn, cs);
        f1a[ks + 2][r] = f1a[ks][r] ^ sc; f1b[ks + 2][r] = f1b[ks][r] ^ sc;
      }
      const float n = (fg4 + r) * (fc * (1.f / 32));
      const float cs = cosr(n), sn = sinr(n);
      const u32 a = pk(cs, sn), b = pk(-sn, cs), sr = (r & 1) ? NEG2 : 0u;
      f2a[0][0][r] = a; f2a[1][0][r] = a ^ sr; f2a[0][1][r] = a ^ sc; f2a[1][1][r] = a ^ sc ^ sr;
      f2b[0][0][r] = b; f2b[1][0][r] = b ^ sr; f2b[0][1][r] = b ^ sc; f2b[1][1][r] = b ^ sc ^ sr;
#pragma unroll
      for (int m = 0; m < 2; ++m) {
        const float nf = (fg4 + (16 * m + r)) * (fj * (1.f / 2048)), ni = (fg4 + (float)(16 * w) + r) * ((fc + 16 * m) * (1.f / 2048));
        tfc[m][r] = cosr(nf); tfs[m][r] = sinr(nf); tic[m][r] = cosr(ni); tis[m][r] = sinr(ni);
      }
    }
#pragma unroll
    for (int i = 0; i < 4; ++i) { PIN(f1a[i]); PIN(f1b[i]); PIN(f2a[i >> 1][i & 1]); PIN(f2b[i >> 1][i & 1]); }
#pragma unroll
    for (int i = 0; i < 2; ++i) { PIN(tfc[i]); PIN(tfs[i]); PIN(tic[i]); PIN(tis[i]); }
    lp_t i2w = lptr(SCR + L_I2 + w * 2048 + lane * 16), i2r = lptr(SCR + L_I2 + ((g >> 1) * 32 + c) * 32 + (g & 1) * 16);
    lp_t exw = lptr(SCR + L_EX + w * 2048 + lane * 16), exr = lptr(SCR + L_EX + lane * 16);
    int vb = ((g >> 1) * 32 + c) * 32 + (g & 1) * 16 + WS_B + un * 8192 + w * 2048, vc = chk_pos(lane) * 4 + WS_CB + un * 1024 + w * 256;
    PIN(vg); PIN(vk); PIN(vb); PIN(vc);
    STAMP(5);
    DRAIN();
    STAMP(6);
    v4u xg[2];
    f32x4 wr[2], wi[2];
    u32 spin = 0;
    for (;; ++spin) {
      MEMFENCE();
      xg[0] = ld_dev(ws, vg, WS_A + sg); xg[1] = ld_dev(ws, vg, WS_A + sg + 32 * 8192);
      const u32 k_ = ld_dev1(ws, vk, WS_CA + sk);
      SB();
      u32 s0 = sum4(0u, wq[0]), s1 = sum4(0u, wq[1]);
#pragma unroll
      for (int m = 0; m < 2; ++m) {
#pragma unroll
        for (int r = 0; r < 4; ++r) { const u32 v = wq[m][r]; wr[m][r] = blo(v); wi[m][r] = bhi(v); }
        PIN(wr[m]); PIN(wi[m]);
      }
      PIN(s0); PIN(s1);
      SB();
      lds_w128(i2w, xg[0]); lds_w128(i2w + 1024, xg[1]);
      if (__all(pair_total(sum4(s0, xg[0]), sum4(s1, xg[1]), lane & 1) + k_ == tag) || spin > SPIN_CAP) break;
      MEMFENCE();
      wq[0] = ld_dev(ws, vg, sw); wq[1] = ld_dev(ws, vg, sw + 32 * 8192);
    }
    COUNT(20, spin);
    STAMP(7);
    BAR();
    STAMP(8);
    v4u af[2][4], b2[2], a3[2];
#pragma unroll
    for (int m = 0; m < 2; ++m)
#pragma unroll
      for (int ks = 0; ks < 4; ++ks) af[m][ks] = lds_r128(i2r + ks * 2048 + m * 512);
    SB();
    f32x4 dr[2], di[2];
#pragma unroll
    for (int m = 0; m < 2; ++m)
#pragma unroll
      for (int ks = 0; ks < 4; ++ks) { dr[m] = mm16(af[m][ks], f1a[ks], ks ? dr[m] : z); di[m] = mm16(af[m][ks], f1b[ks], ks ? di[m] : z); }
#pragma unroll
    for (int m = 0; m < 2; ++m) b2[m] = pk4(dr[m] * tfc[m] + di[m] * tfs[m], di[m] * tfc[m] - dr[m] * tfs[m]);
#pragma unroll
    for (int k = 0; k < 2; ++k)
#pragma unroll
      for (int m = 0; m < 2; ++m) { dr[m] = mm16(f2a[m][k], b2[k], k ? dr[m] : z); di[m] = mm16(f2b[m][k], b2[k], k ? di[m] : z); }
#pragma unroll
    for (int m = 0; m < 2; ++m) a3[m] = pk4(dr[m] * wi[m] + di[m] * wr[m], dr[m] * wr[m] - di[m] * wi[m]);
#pragma unroll
    for (int k = 0; k < 2; ++k)
#pragma unroll
      for (int n = 0; n < 2; ++n) { di[n] = mm16(a3[k], f2a[n][k], k ? di[n] : z); dr[n] = mm16(a3[k], f2b[n][k], k ? dr[n] : z); }
#pragma unroll
    for (int n = 0; n < 2; ++n) lds_w128(exw + n * 1024, pk4(di[n] * tic[n] + dr[n] * tis[n], dr[n] * tic[n] - di[n] * tis[n]));
    STAMP(9);
    BAR();
#pragma unroll
    for (int n = 0; n < 2; ++n)
#pragma unroll
      for (int ks = 0; ks < 4; ++ks) af[n][ks] = lds_r128(exr + ks * 2048 + n * 1024);
    SB();
    u32 cs[2];
#pragma unroll
    for (int n = 0; n < 2; ++n)
#pragma unroll
      for (int ks = 0; ks < 4; ++ks) { di[n] = mm16(f1a[ks], af[n][ks], ks ? di[n] : z); dr[n] = mm16(f1b[ks], af[n][ks], ks ? dr[n] : z); }
#pragma unroll
    for (int n = 0; n < 2; ++n) {
      const v4u e = pk4(dr[n], di[n]);
      st_dev(ws, vb + n * 512, e);
      cs[n] = sum4(0u, e);
    }
    const auto sx = __builtin_amdgcn_permlane16_swap(cs[0], cs[1], false, false);
    st_dev1(ws, vc, tag - sx[0] - sx[1]);
    BAR();
    if (wg == 0 && w == 0) st_dev1(ws, WS_E, tag);
  }
  STAMP(10);
  SB();
  {
    v4u gbre, gbim, g0re, g0im, ga[2];
#pragma unroll
    for (int r = 0; r < 4; ++r) {
      gbre[r] = fbre[r] ^ (g < 2 ? NEGH : 0u);
      gbim[r] = fbsw[r] ^ (g < 2 ? 0u : NEGH);
      const float n = fc * ((31.f - 2.f * r - 2.f * fg4) * (1.f / 32));
      u32 q = pk(cosr(n), -sinr(n));
      PIN(q);
      g0re[r] = g < 2 ? gbre[r] : 0u;
      g0im[r] = g < 2 ? 0u : q;
      ga[0][r] = f2a[0][0][r] ^ NEGH; ga[1][r] = f2a[1][0][r] ^ NEGH;
    }
    twc *= 2.f / (2048.f * 512.f); tws *= 2.f / (2048.f * 512.f);
    g0re[0] = g == 0 ? ((c & 1) ? 0xbf00u : 0x3f00u) : g0re[0];
    ga[0][0] = g == 0 ? ((c & 1) ? 0xbf803f80u : 0x3f803f80u) : ga[0][0]; ga[1][0] = ga[0][0];
    PIN(gbre); PIN(gbim); PIN(g0re); PIN(g0im); PIN(ga[0]); PIN(ga[1]); PIN(twc); PIN(tws);
    f32x4 xin[2][2];
    lp_t xq = lptr(SCR + L_X + w * 2048 + (4 * c + g) * 8);
#pragma unroll
    for (int rr = 0; rr < 2; ++rr)
#pragma unroll
      for (int t = 0; t < 2; ++t) {
        const v2u v = lds_r64(xq + rr * 1024 + t * 512);
        const u32 v0 = v.x, v1 = v.y;
        f32x4 q = {blo(v0), bhi(v0), blo(v1), bhi(v1)};
        xin[rr][t] = q - mean[rr]; PIN(xin[rr][t]);
      }
    lp_t i3w = lptr(SCR + L_I3 + w * 2048 + lane * 16), i3r[4], o3w[2], o3r[2];
#pragma unroll
    for (int r = 0; r < 4; ++r) i3r[r] = lptr(SCR + L_I3 + slot_of(c, 4 * g + r) * 32 + w * 8);
#pragma unroll
    for (int i = 0; i < 2; ++i) {
      o3w[i] = lptr(SCR + L_O3 + ((4 * c + g) * 8 + ((2 * w + i) ^ (c & 7))) * 16);
      const int q = w * 128 + i * 64 + lane;
      o3r[i] = lptr(SCR + L_O3 + (q ^ ((q >> 5) & 7)) * 16);
    }
    int vs = lane * 16 + WS_L + un * 8192 + w * 2048, vt = lane < 8 ? lane * 16 + WS_S + un * 512 + w * 128 : OOB;
    PIN(vs); PIN(vt);
    lp_t sow = lptr(SCR + L_SO + tid * 16), sor = lptr(SCR + L_SO + (lane & 6) * 512 + (lane & 1) * 8);
    DRAIN();
    STAMP(11);
    u32 spin = 0;
    for (;; ++spin) {
      MEMFENCE();
      const v4u x0 = ld_dev(ws, vg, WS_B + sg), x1 = ld_dev(ws, vg, WS_B + sg + 32 * 8192);
      const u32 k_ = ld_dev1(ws, vk, WS_CB + sk);
      SB();
      lds_w128(i3w, x0); lds_w128(i3w + 1024, x1);
      if (__all(pair_total(sum4(0u, x0), sum4(0u, x1), lane & 1) + k_ == tag) || spin > SPIN_CAP) break;
    }
    COUNT(21, spin);
    STAMP(12);
    BAR();
    STAMP(13);
    v4u a[2], a2[2];
#pragma unroll
    for (int r = 0; r < 4; ++r) { const v2u v = lds_r64(i3r[r]); a[0][r] = v.x; a[1][r] = v.y; }
    f32x4 tr[2], ti[2], sr[2], si[2], h[2][2];
#pragma unroll
    for (int rr = 0; rr < 2; ++rr) { tr[rr] = mm16p(a[rr], gbre, z); ti[rr] = mm16p(a[rr], gbim, z); sr[rr] = mm16p(a[rr], g0re, z); si[rr] = mm16p(a[rr], g0im, z); }
#pragma unroll
    for (int rr = 0; rr < 2; ++rr) {
      tr[rr][0] = g == 0 ? sr[rr][0] : tr[rr][0]; ti[rr][0] = g == 0 ? si[rr][0] : ti[rr][0];
      a2[rr] = pk4(tr[rr] * twc - ti[rr] * tws, ti[rr] * twc + tr[rr] * tws);
      h[rr][0] = mm16p(a2[rr], ga[0], xin[rr][0]); h[rr][1] = mm16p(a2[rr], ga[1], xin[rr][1]);
    }
    float st[2];
#pragma unroll
    for (int rr = 0; rr < 2; ++rr) { const f32x4 q = h[rr][0] * h[rr][0] + h[rr][1] * h[rr][1]; st[rr] = (q[0] + q[2]) + (q[1] + q[3]); }
    wave_sums<2>(st);
    v4u so;
#pragma unroll
    for (int rr = 0; rr < 2; ++rr) {
      const float ve = st[rr] * (1.f / 512) + 1e-5f, rstd = __builtin_amdgcn_rsqf(ve);
      const f32x4 l0 = h[rr][0] * rstd, l1 = h[rr][1] * rstd;
      v4u l4 = {pk(l0[0], l0[1]), pk(l0[2], l0[3]), pk(l1[0], l1[1]), pk(l1[2], l1[3])};
      lds_w128(o3w[rr], l4);
      so[2 * rr] = asu(mean[rr]); so[2 * rr + 1] = asu(ve * rstd);
    }
    lds_w128(sow, so);
    STAMP(14);
    BAR();
    const v4u v0 = lds_r128(o3r[0]), v1 = lds_r128(o3r[1]);
    const v2u ms = lds_r64(sor);
    st_dev(ws, vs, v0); st_dev(ws, vs + 1024, v1);
    u32 s = sum4(sum4(0u, v0), v1);
    s += dppu<0x128>(s); s = add_x32(add_x16(s));
    v4u rec = {tag - s - ms.x - ms.y, ms.x, ms.y, tag};
    st_dev(ws, vt, rec);
    BAR();
  }
  SB();
  {
    constexpr int PD = 4;
    constexpr int LOOK = 14;
    const int kh = w & 1, rl = w >> 1, hh = lane >> 5;
    v4u ring[8], act[32], hfA[8], hfB[8], hfC[8];
    u32 hkA, hkB, hkC;
    f32x16 zz;
#pragma unroll
    for (int i = 0; i < 16; ++i) zz[i] = 0.f;
    lp_t wa[4], wb[4], wd[2][2], we[2];
#pragma unroll
    for (int t = 0; t < 4; ++t) wa[t] = lptr(t * SLOT + ((lane ^ (2 * t + ((lane >> 3) & 1))) * 16));
    {
      const int q = (lane & 15) >> 2, pp = lane & 3;
      const u32 dl = (4 * hh + q) * 128 + (q >> 1) * 64 + (pp & 1) * 32 + ((lane >> 4) & 1) * 16 + (pp >> 1) * 8;
#pragma unroll
      for (int i = 0; i < 2; ++i) {
        wd[0][i] = lptr((dl ^ (i * 64)) + SLOT + kh * 16384); wd[1][i] = lptr((dl ^ (i * 64)) + 3 * SLOT + kh * 16384);
        we[i] = lptr((dl ^ (i * 64)) + kh * 12288);
      }
    }
    auto frag = [&](auto NN) -> v4u {
      constexpr int N = NN.value;
      if constexpr (N < 64) return lds_r128(wa[N & 1] + (N >> 1) * 1024);
      else if constexpr (N < 128) return lds_r128(wa[N >> 5] + (N & 31) * 1024);
      else if constexpr (N < 192) return lds_r128(wb[(N >> 4) & 3] + (N & 31) * 1024);
      else {
        constexpr int t = N < 256 ? (N - 192) >> 4 : N < 268 ? 4 : 5, s = N < 256 ? (N & 15) : N < 268 ? N - 256 : N - 268;
        const lp_t b = (t < 4 ? wd[(t >> 1) & 1][s & 1] : we[s & 1]) + (t & 1) * SLOT + (s >> 1) * 2048;
        return lds_tr2(b, b + 1024);
      }
    };
#define FRAG(N) frag(std::integral_constant<int, (N)>{})
    const int hb = WS_H + (4 * rg + 2 * (ch >> 3) + rl) * NFT * HREC;
    int hs[8], hs4 = hb + (36 * kh + 4) * HREC;
#pragma unroll
    for (int i = 0; i < 8; ++i) { hs[i] = hb + tile0_of(8 * kh + i) * HREC; PINS(hs[i]); }
    PINS(hs4);
    int vl = lane * 16, vlz = kh ? OOB : vl, vq, vq4;
    {
      const int st = (lane & 3) + ((lane >> 1) & 4), wo = hb + 1024 + ((lane >> 4) * 8 + (lane & 4)) * 4;
      vq = wo + tile0_of(8 * kh + st) * HREC;
      vq4 = st < 6 - kh ? wo + (36 * kh + 6 * st + 4) * HREC : OOB;
    }
    int vo = WS_H + ((4 * rg + w) * NFT + ft0) * HREC + lane * 16, vk = (lane & 8) ? OOB : vo - lane * 16 + 1024 + ((lane >> 4) * 8 + (lane & 7)) * 4;
    int vo4 = six ? vo + 4 * HREC : OOB, vk4 = six ? vk + 4 * HREC : OOB;
    PIN(vl); PIN(vlz); PIN(vq); PIN(vq4); PIN(vo); PIN(vk); PIN(vo4); PIN(vk4);
#define H_LD(X, t, j) do { \
      if constexpr ((j) == 8) hk##X = ld_dev1(ws, ((t) < 4 ? vq : vq4) + ((t) & 3) * HREC, 0); \
      else if constexpr ((t) < 4) hf##X[(j) & 7] = ld_dev(ws, vl + (t) * HREC, hs[(j) & 7]); \
      else if constexpr ((j) < 6) hf##X[(j) & 7] = ld_dev(ws, ((j) == 5 ? vlz : vl) + ((t) & 3) * HREC, hs4 + 6 * (j) * HREC); } while (0)
    Epi e;
#define EPI(t, acc, J0, J1) static_for<((J0) < 0 ? 0 : (J0) > 36 ? 36 : (J0)), ((J1) < 0 ? 0 : (J1) > 36 ? 36 : (J1))>([&](auto JJ) { epi_slice<JJ.value>(e, acc); \
      if constexpr (JJ.value == 31) st_dev(ws, ((t) < 4 ? vo : vo4) + ((t) & 3) * HREC, e.r); \
      if constexpr (JJ.value == 35) st_dev1(ws, ((t) < 4 ? vk : vk4) + ((t) & 3) * HREC, tag - e.chk); })
    int va = ((lane >> 3) & 3) * 8192 + hh * 128 + (lane & 7) * 16;
    int vr = ((lane >> 3) & 3) * 512 + hh * 128 + (lane & 7) * 16;
    PIN(vr);
    const int sa = WS_L + (4 * rg + w) * 32768;
#define ACT_LD(ks) act[ks] = ld_dev(ws, va + ((ks) & 15) * 256, sa + ((ks) >> 4) * 4096)
    f32x16 tA, tB, tC;
    DRAIN();
    STAMP(15);
    u32 spin = 0;
    for (;; ++spin) {
      MEMFENCE();
      PIN(va);
      const v4u s0 = ld_dev(ws, vr, WS_S + (4 * rg + w) * 2048), s1 = ld_dev(ws, vr + 256, WS_S + (4 * rg + w) * 2048);
      SB();
      static_for<0, LOOK>([&](auto K) { ACT_LD(K.value); SB(); });
      static_for<0, PD>([&](auto K) { ring[K.value] = FRAG(K.value); });
      SB();
      if (__any(s0.w != tag || s1.w != tag) && spin <= SPIN_CAP) continue;
      u32 m = (s0.x + s0.y + s0.z) + (s1.x + s1.y + s1.z);
      static_for<0, 64>([&](auto K) {
        constexpr int n = K.value, ks = n >> 1;
        if constexpr (n & 1) tB = mm32p(ring[n & 7], act[ks], ks ? tB : zz); else tA = mm32p(ring[n & 7], act[ks], ks ? tA : zz);
        ring[(n + PD) & 7] = FRAG(n + PD);
        if constexpr (!(n & 1) && ks + LOOK < 32) ACT_LD(ks + LOOK);
        { const v4u v = act[ks]; u32 t = (n & 1) ? m + v.z + v.w : m + v.x + v.y; PIN(t); m = t; }
        SB();
      });
      if (__all(add_x32(m) == 4u * tag) || spin > SPIN_CAP) break;
    }
    COUNT(22, spin);
#pragma unroll
    for (int i = 0; i < 4; ++i) wb[i] = lptr((i < 2 ? 4 * SLOT : 0) + ((lane ^ (4 * (i & 1) + 2 * hh + ((lane >> 3) & 1))) * 16));
    BAR_N(15);
    STAMP(16);
#define G1_TILE(T, acc, ...) static_for<0, 32>([&](auto K) { constexpr int s = K.value, n = 32 * (T) + s; \
      acc = mm32p(ring[n & 7], act[s], s ? acc : zz); \
      ring[(n + PD) & 7] = FRAG(n + PD); \
      __VA_ARGS__; SB(); })
    G1_TILE(2, tC, { constexpr int j0 = (27 * s) / 16, j1 = (27 * s + 27) / 16; EPI(0, tA, j0, j1); EPI(1, tB, j0 - 36, j1 - 36); });
    BAR_N(15);
    G1_TILE(3, tA, { constexpr int j0 = (27 * s) / 16 + 18, j1 = (27 * s + 27) / 16 + 18; EPI(1, tB, j0, j1); EPI(2, tC, j0 - 36, j1 - 36); });
    BAR_N(15);
    G1_TILE(4, tB, { EPI(3, tA, (6 * (s - 2)) / 5, (6 * (s - 1)) / 5);
      if constexpr (s >= 12 && s < 30 && !(s & 1)) H_LD(A, 0, (s - 12) >> 1); });
    BAR_N(15);
    G1_TILE(5, tC, { EPI(4, tB, (6 * (s - 2)) / 5, (6 * (s - 1)) / 5);
      if constexpr (s >= 12 && s < 30 && !(s & 1)) H_LD(B, 1, (s - 12) >> 1); });
    BAR_N(15);
    STAMP(17);
    f32x16 o0, o1;
    u32 nfix = 0;
#define H_TOT(X, nf) ({ u32 m_ = hk##X; _Pragma("unroll") for (int i = 0; i < (nf); ++i) m_ = sum4(m_, hf##X[i]); \
      m_ += dppu<0xb1>(m_); m_ += dppu<0x4e>(m_); m_ += dppu<0x128>(m_); m_; })
#define H_FIX(X, t, nf, want) do { if (__builtin_expect(__any(H_TOT(X, nf) != (want)), 0)) { u32 spin_ = 0; _Pragma("nounroll") do { MEMFENCE(); nfix++; \
        static_for<0, 9>([&](auto J) { H_LD(X, t, J.value); }); } while (__any(H_TOT(X, nf) != (want)) && ++spin_ < SPIN_CAP); } SB(); } while (0)
#define G2_ROUND(X, n0, ns, ...) static_for<0, ns>([&](auto K) { constexpr int s = K.value, n = (n0) + s; \
      if constexpr (s & 1) o1 = mm32p(ring[n & 7], hf##X[s >> 1], n < 194 ? zz : o1); else o0 = mm32p(ring[n & 7], hf##X[s >> 1], n < 194 ? zz : o0); \
      if constexpr (n + PD < 280) ring[(n + PD) & 7] = FRAG(n + PD < 280 ? n + PD : 0); \
      __VA_ARGS__; SB(); })
    const u32 t8 = 8u * tag, t6 = (6u - kh) * tag;
    H_FIX(A, 0, 8, t8);
    G2_ROUND(A, 192, 16, { EPI(5, tC, (9 * s) / 4, (9 * s + 9) / 4); if constexpr (!(s & 1)) H_LD(C, 2, s >> 1); if constexpr (s == 15) H_LD(C, 2, 8); });
    BAR_N(15);
    H_FIX(B, 1, 8, t8);
    G2_ROUND(B, 208, 16, { if constexpr (!(s & 1)) H_LD(A, 3, s >> 1); if constexpr (s == 15) H_LD(A, 3, 8); });
    BAR_N(15);
    H_FIX(C, 2, 8, t8);
    G2_ROUND(C, 224, 16, { if constexpr (!(s & 1)) H_LD(B, 4, s < 12 ? s >> 1 : s == 12 ? 8 : 7); });
    const int u = 16 * rg + 8 * (ch >> 3) + 4 * rl + ((lane >> 3) & 3), col = 64 * (ch & 7) + 32 * kh + 16 * hh;
    v2u hr[4];
    v4u so;
    int vh = u * 8192 + (col & 255) * 32 + (lane & 7) * 16 + (col >> 8) * 8, vs = u * 512 + (lane & 7) * 16;
    H_FIX(A, 3, 8, t8);
    G2_ROUND(A, 240, 16, { if constexpr (s < 7) H_LD(C, 5, s < 6 ? s : 8);
      if constexpr (s >= 8 && s < 12) hr[s & 3] = __builtin_amdgcn_raw_buffer_load_b64(ws, vh + (s & 3) * 128, WS_L, AUX_SC1);
      if constexpr (s == 12) so = ld_dev(ws, vs, WS_S);
      if constexpr (s == 10) BAR_N(15); });
    H_FIX(B, 4, 6, t6);
    G2_ROUND(B, 256, 12, { });
    H_FIX(C, 5, 6, t6);
    G2_ROUND(C, 268, 12, { });
    COUNT(23, nfix);
    STAMP(18);
    lp_t rw = lptr(2 * SLOT + w * 4096 + lane * 16), rr_ = lptr(2 * SLOT + (w ^ 1) * 4096 + lane * 16);
    f32x4 f[4];
#define Q4(a, i) f32x4{a[4 * (i)], a[4 * (i) + 1], a[4 * (i) + 2], a[4 * (i) + 3]}
    if (kh) {
#pragma unroll
      for (int i = 0; i < 4; ++i) { lds_wf(rw + i * 1024, Q4(o0, i)); f[i] = Q4(o1, i); }
    } else {
#pragma unroll
      for (int i = 0; i < 4; ++i) { lds_wf(rw + i * 1024, Q4(o1, i)); f[i] = Q4(o0, i); }
    }
    {
      const u32 mb = so.y, sb = so.z;
#pragma unroll
      for (int i = 0; i < 4; ++i) { const u32 v0 = hr[i].x, v1 = hr[i].y; const f32x4 q = {blo(v0), bhi(v0), blo(v1), bhi(v1)}; f[i] += q * asf(sb) + asf(mb); }
    }
    v4u* op = (v4u*)(p.out + (32 * (8 * (u >> 5) + (lane & 7)) + (u & 31)) * D_ + col);
    BAR();
#pragma unroll
    for (int i = 0; i < 2; ++i) {
      const f32x4 v0 = f[2 * i] + lds_rf(rr_ + i * 2048), v1 = f[2 * i + 1] + lds_rf(rr_ + i * 2048 + 1024);
      v4u q = {pk(v0[0], v0[1]), pk(v0[2], v0[3]), pk(v1[0], v1[1]), pk(v1[2], v1[3])};
      op[i] = q;
    }
  }
  STAMP(24);
  STAMP_FLUSH();
  UNDEF_RA();
  }
  PINS(wv);
  if (wv >= 4) {
    STAMP(0);
    const int hw = wv - 4;
    W1_SETUP();
    const rsrc_t ws = mkrs(p.ws, WS_END);
    const int vy = six ? 4 * (lane >> 2) * (F_ * 2) + (ft0 + 4) * 32 + (lane & 3) * 16 : OOB;
    const u32 ax = ((lane >> 1) & 3) * SLOT + (lane >> 3) * 512 + (hw & 1) * 256 + (lane & 1) * 128 + (lane & 7) * 16;
#define PUT_W1(src, a) do { const u32 a_ = (a); _Pragma("unroll") for (int i = 0; i < 8; ++i) lds_w128((lp_t)(size_t)(a_ ^ (i * 16)), src[i]); } while (0)
    v4u xb[8], y0[8], y1[8];
    if (hw < 2) {
      const int T = tid - 256, m = T >> 1, vs = ((m & 0x39) | ((m & 2) << 1) | ((m & 4) >> 1)) * 32 + (T & 1) * 16 + WS_W + un * 8192;
      const u32 sh = (lane & 1) ? 0u : 16u;
#pragma unroll
      for (int i = 0; i < 8; ++i) { const u32 a = ra[i].x, b = ra[i].y, d = ra[i].z; ra[i].x = __builtin_amdgcn_alignbit(b, a, sh); ra[i].y = __builtin_amdgcn_alignbit(d, b, sh); }
      u32 ck[4];
      v4u v[4];
#pragma unroll
      for (int e = 0; e < 4; ++e) {
#pragma unroll
        for (int r = 0; r < 4; ++r) { const u32 a = ra[r][e >> 1], b = ra[4 + r][e >> 1]; v[e][r] = __builtin_amdgcn_perm(b, a, (e & 1) ? 0x07060302u : 0x05040100u); }
        st_dev(ws, vs + e * 2048, v[e]);
        ck[e] = sum4(0u, v[e]);
      }
      v2u cw = {pair_total(ck[0], ck[2], T & 1), pair_total(ck[1], ck[3], T & 1)};
      lds_w64(lptr(SCR + L_CW + T * 8), cw);
      SB();
      STAMP(1);
#define X0M 0
      if (X0M == 0) { LOAD_W1(ra, vx, X_PIECE(0)); }
    }
    STAMP(2);
    BAR();
    asm volatile("" :: "v"(cpf));
    if (X0M == 0 || hw >= 2) { transpose8(ra); PUT_W1(ra, ax + X_PIECE(0) * 512); }
    STAMP(3);
    BAR();
    if (X0M == 1 && hw < 2) { LOAD_W1(y0, vx, X_PIECE(0)); }
    LOAD_W1(xb, vx, X_PIECE(2)); LOAD_W1(ra, vx, X_PIECE(1));
    STAMP(4);
    BAR();
    BAR();
    LOAD_W1(y1, vx, X_PIECE(3));
    if (X0M == 1 && hw < 2) { transpose8(y0); PUT_W1(y0, ax + X_PIECE(0) * 512); }
    transpose8(xb); PUT_W1(xb, ax + X_PIECE(2) * 512);
    transpose8(ra); PUT_W1(ra, ax + X_PIECE(1) * 512);
    transpose8(y1); PUT_W1(y1, ax + X_PIECE(3) * 512);
    STAMP(5);
    BAR();
    LOAD_W1(y0, vy, Y_PIECE(0));
    STAMP(6);
    BAR();
    BAR();
    LOAD_W1(y1, vy, Y_PIECE(1));
    transpose8(y0); transpose8(y1);
#pragma unroll
    for (int i = 0; i < 8; ++i) {
#pragma unroll
      for (int j = 0; j < 4; ++j) { const u32 a = y0[i][j], b = y1[i][j], as = dppu<0x4e>(a), bs = dppu<0x4e>(b); y0[i][j] = (lane & 2) ? bs : a; y1[i][j] = (lane & 2) ? b : as; }
      PIN(y1[i]);
    }
    const u32 ay = (Y_PIECE((lane >> 1) & 1) + (lane >> 2)) * 512 + (hw & 1) * 256 + (lane & 1) * 144 + (lane & 4) * 8 + (lane & 2) * 32;
    PUT_W1(y0, ay + 4 * SLOT);
    const rsrc_t rwd = mkrs(p.wd, D_ * F_ * 2);
    const int vd = (lane >> 3) * (D_ * 2) + (ch & 7) * 128 + ((lane & 7) ^ ((lane >> 2) & 4)) * 16;
    int sd[4];
#pragma unroll
    for (int i = 0; i < 4; ++i) { sd[i] = tile0_of(4 * hw + i) * (16 * D_ * 2); PINS(sd[i]); }
#define DMA_WD(t, slot) do { _Pragma("unroll") for (int i = 0; i < 8; ++i) \
      __builtin_amdgcn_raw_ptr_buffer_load_lds(rwd, (LAS void*)(size_t)((slot) * SLOT + hw * 8192 + i * 1024), 16, vd, sd[i >> 1] + ((t) * 16 + (i & 1) * 8) * (D_ * 2), 0, 0); } while (0)
#define VM_WAIT(n) asm volatile("s_waitcnt vmcnt(" #n ")" ::: "memory")
    STAMP(7);
    BAR();
    PUT_W1(y1, ay);
    DMA_WD(0, 1);
    STAMP(8);
    BAR();
    DMA_WD(1, 2);
    BAR();
    DMA_WD(2, 3);
    VM_WAIT(8);
    BAR();
    DMA_WD(3, 4);
    VM_WAIT(8);
    BAR();
    if (hw < 3) DMA_WD(4, 0);
    BAR();
    if (hw < 3) DMA_WD(5, 1);
    if (hw < 3) VM_WAIT(16); else VM_WAIT(0);
    BAR();
    VM_WAIT(0);
    STAMP(9);
    BAR();
    STAMP_FLUSH();
  }

}

__global__ void aux_kernel(u32* ws, int mode) {
  const u32 now = (u32)__builtin_amdgcn_s_memrealtime();
  if (mode == 0) ws[WS_ST / 4 + 64] = now;
  if (mode == 2) { u32 d = (now - ws[WS_ST / 4]) * 4u; if (d > 20000u) d = 20000u; while ((u32)__builtin_amdgcn_s_memrealtime() - now < d) __builtin_amdgcn_s_sleep(1); }
}
extern "C" void kernel_launch(void* const* d_in, const int* in_sizes, int n_in,
                              void* d_out, int out_size, void* d_ws, size_t ws_size,
                              hipStream_t stream) {
  Params p;
  p.x = (const u16*)d_in[0]; p.wr = (const u16*)d_in[1]; p.wi = (const u16*)d_in[2];
  p.wg = (const u16*)d_in[4]; p.wu = (const u16*)d_in[5]; p.wd = (const u16*)d_in[6];
  p.out = (u16*)d_out; p.ws = d_ws;
  (void)hipFuncSetAttribute((const void*)gfnet_geglu_kernel, hipFuncAttributeMaxDynamicSharedMemorySize, 5 * SLOT);
  gfnet_geglu_kernel<<<256, 512, 5 * SLOT, stream>>>(p);
  aux_kernel<<<1, 64, 0, stream>>>((u32*)d_ws, 1);
}
```

```cpp
#define WSTAIL
#include <hip/hip_runtime.h>
#include <hip/hip_bf16.h>
#include <type_traits>

typedef unsigned u32;
typedef unsigned short u16;
typedef u32 v4u __attribute__((ext_vector_type(4)));
typedef u32 v2u __attribute__((ext_vector_type(2)));
typedef float f32x2 __attribute__((ext_vector_type(2)));
typedef float f32x4 __attribute__((ext_vector_type(4)));
typedef float f32x16 __attribute__((ext_vector_type(16)));
typedef __bf16 bf16x2 __attribute__((ext_vector_type(2)));
typedef __bf16 bf16x8 __attribute__((ext_vector_type(8)));
typedef short s16x4 __attribute__((ext_vector_type(4)));
#define LAS __attribute__((address_space(3)))
#define DI __device__ __forceinline__
typedef __amdgpu_buffer_rsrc_t rsrc_t;
typedef LAS char* lp_t;

constexpr int S_ = 2048, D_ = 512, F_ = 1376, KF = 257;
constexpr int NFT = 86;
constexpr int WS_E = 0;
constexpr int WS_A = 4096;
constexpr int WS_W = WS_A + (2 << 20);
constexpr int WS_B = WS_W + (2 << 20);
constexpr int WS_L = WS_B + (2 << 20);
constexpr int WS_CA = WS_L + (2 << 20);
constexpr int WS_CB = WS_CA + (1 << 18);
constexpr int WS_S = WS_CB + (1 << 18);
constexpr int WS_H = WS_S + (1 << 17);
constexpr int HREC = 1152;
constexpr int WS_END = WS_H + 64 * NFT * HREC;
constexpr int WS_ST = WS_END;
constexpr u32 NEG2 = 0x80008000u, NEGH = 0x80000000u, NEGL = 0x8000u;
constexpr u32 SPIN_CAP = 1u << 14;
constexpr int AUX_SC1 = 16;
#define NTAUX 2
constexpr int OOB = 0x40000000;
constexpr int CODE_LINES = 228;
constexpr int SLOT = 32768, SCR = 4 * SLOT;
constexpr int L_F = 0, L_CW = 2048, L_SO = 4096;
constexpr int L_T1 = 8192, L_X = 16384, L_I2 = 16384, L_EX = 24576, L_I3 = 8192, L_O3 = 16384;

#define DRAIN() asm volatile("s_waitcnt vmcnt(0)" ::: "memory")
#define MEMFENCE() asm volatile("" ::: "memory")
#define PIN(x) asm volatile("" : "+v"(x))
#define PINS(x) asm volatile("" : "+s"(x))
#define SB() __builtin_amdgcn_sched_barrier(0)
#define BAR() asm volatile("s_waitcnt lgkmcnt(0)\n\ts_barrier" ::: "memory")
#define BAR_N(n) asm volatile("s_waitcnt lgkmcnt(" #n ")\n\ts_barrier" ::: "memory")

DI u32 pk(float a, float b) { f32x2 v = {a, b}; return __builtin_bit_cast(u32, __builtin_convertvector(v, bf16x2)); }
DI v4u pk4(f32x4 a, f32x4 b) { v4u r = {pk(a[0], b[0]), pk(a[1], b[1]), pk(a[2], b[2]), pk(a[3], b[3])}; return r; }
DI float asf(u32 v) { return __builtin_bit_cast(float, v); }
DI u32 asu(float v) { return __builtin_bit_cast(u32, v); }
DI float blo(u32 v) { return asf(v << 16); }
DI float bhi(u32 v) { return asf(v & 0xffff0000u); }
DI float dot2(u32 a, u32 b, float c) { return __builtin_amdgcn_fdot2_f32_bf16(__builtin_bit_cast(bf16x2, a), __builtin_bit_cast(bf16x2, b), c, false); }
DI u32 sum4(u32 m, v4u v) { return (m + v.x + v.y) + v.z + v.w; }
template <int CTRL> DI u32 dppu(u32 v) { return (u32)__builtin_amdgcn_update_dpp(0, (int)v, CTRL, 0xf, 0xf, true); }
template <int CTRL> DI float dppf(float v) { return asf(dppu<CTRL>(asu(v))); }
DI u32 pair_total(u32 s0, u32 s1, bool odd) { return (odd ? s1 : s0) + dppu<0xb1>(odd ? s0 : s1); }
DI u32 add_x16(u32 v) { const auto s = __builtin_amdgcn_permlane16_swap(v, v, false, false); return s[0] + s[1]; }
DI u32 add_x32(u32 v) { const auto s = __builtin_amdgcn_permlane32_swap(v, v, false, false); return s[0] + s[1]; }
template <int N> DI void wave_sums(float (&v)[N]) {
#define WS_STEP(C) _Pragma("unroll") for (int i = 0; i < N; ++i) v[i] += dppf<C>(v[i]);
  WS_STEP(0x111) WS_STEP(0x112) WS_STEP(0x114) WS_STEP(0x118) WS_STEP(0x142) WS_STEP(0x143)
#pragma unroll
  for (int i = 0; i < N; ++i) v[i] = asf(__builtin_amdgcn_readlane(asu(v[i]), 63));
}
DI int chk_pos(int k) { return (k & 0xc7) | ((k & 8) << 2) | ((k >> 1) & 0x18); }
DI f32x4 mm16(v4u a, v4u b, f32x4 c) { return __builtin_amdgcn_mfma_f32_16x16x32_bf16(__builtin_bit_cast(bf16x8, a), __builtin_bit_cast(bf16x8, b), c, 0, 0, 0); }
DI f32x4 mm16p(v4u a, v4u b, f32x4 c) { f32x4 d = mm16(a, b, c); PIN(d); return d; }
DI f32x16 mm32p(v4u a, v4u b, f32x16 c) { f32x16 d = __builtin_amdgcn_mfma_f32_32x32x16_bf16(__builtin_bit_cast(bf16x8, a), __builtin_bit_cast(bf16x8, b), c, 0, 0, 0); PIN(d); return d; }
DI v2u lds_tr(lp_t a) { return __builtin_bit_cast(v2u, __builtin_amdgcn_ds_read_tr16_b64_v4i16((LAS s16x4*)a)); }
DI v4u lds_tr2(lp_t a, lp_t b) { v2u x = lds_tr(a), y = lds_tr(b); v4u r = {x.x, x.y, y.x, y.y}; return r; }
DI v4u lds_r128(lp_t a) { return *(LAS v4u*)a; }
DI f32x4 lds_rf(lp_t a) { return *(LAS f32x4*)a; }
DI v2u lds_r64(lp_t a) { return *(LAS v2u*)a; }
DI void lds_w128(lp_t a, v4u v) { *(LAS v4u*)a = v; }
DI void lds_wf(lp_t a, f32x4 v) { *(LAS f32x4*)a = v; }
DI void lds_w64(lp_t a, v2u v) { *(LAS v2u*)a = v; }
DI lp_t lptr(u32 off) { lp_t q = (lp_t)(size_t)off; PIN(q); return q; }
DI rsrc_t mkrs(const void* p, u32 bytes) { return __builtin_amdgcn_make_buffer_rsrc((void*)p, 0, bytes, 0x00020000); }
DI v4u ld_dev(rsrc_t r, int v, int s) { return __builtin_amdgcn_raw_buffer_load_b128(r, v, s, AUX_SC1); }
DI u32 ld_dev1(rsrc_t r, int v, int s) { return __builtin_amdgcn_raw_buffer_load_b32(r, v, s, AUX_SC1); }
DI void st_dev(rsrc_t r, int v, v4u x) { __builtin_amdgcn_raw_buffer_store_b128(x, r, v, 0, AUX_SC1); }
DI void st_dev1(rsrc_t r, int v, u32 x) { __builtin_amdgcn_raw_buffer_store_b32(x, r, v, 0, AUX_SC1); }
DI v4u ld_in(rsrc_t r, int v, int s) { return __builtin_amdgcn_raw_buffer_load_b128(r, v, s, 0); }
DI v4u ld_once(rsrc_t r, int v, int s) { return __builtin_amdgcn_raw_buffer_load_b128(r, v, s, NTAUX); }
DI u32 touch(rsrc_t r, int line) { return __builtin_amdgcn_raw_buffer_load_b32(r, line * 128, 0, 0); }
DI void flag_set(lp_t mine, u32 k) { MEMFENCE(); *(volatile LAS u32*)mine = k; MEMFENCE(); }
DI void flag_wait(lp_t theirs, u32 k) { while (!__all(*(volatile LAS u32*)theirs >= k)) { } MEMFENCE(); }
DI void flag_wait_lazy(lp_t theirs, u32 k) { while (!__all(*(volatile LAS u32*)theirs >= k)) __builtin_amdgcn_s_sleep(1); MEMFENCE(); }
DI float cosr(float rev) { return __builtin_amdgcn_cosf(rev); }
DI float sinr(float rev) { return __builtin_amdgcn_sinf(rev); }
__host__ __device__ constexpr int slot_of(int k1, int k2) { return ((k2 < 8 ? k1 + 32 * k2 : (k1 ? 32 - k1 : 16) + 32 * (15 - k2)) - 1) & 255; }
DI int tile0_of(int c) { return 4 * c + 2 * (c < 11 ? c : 11); }
template <int I, int N, class F> DI void static_for(F&& f) {
  if constexpr (I < N) { f(std::integral_constant<int, I>{}); static_for<I + 1, N>(f); }
}
DI void transpose8(v4u (&m)[8]) {
  v4u o[8];
#pragma unroll
  for (int i = 0; i < 8; ++i)
#pragma unroll
    for (int j = 0; j < 4; ++j) { const u32 a = m[2 * j][i >> 1], b = m[2 * j + 1][i >> 1]; o[i][j] = __builtin_amdgcn_perm(b, a, (i & 1) ? 0x07060302u : 0x05040100u); }
#pragma unroll
  for (int i = 0; i < 8; ++i) { m[i] = o[i]; PIN(m[i]); }
}
struct Epi { float t, lo; v4u r; u32 chk; };
template <int J> DI void epi_slice(Epi& e, const f32x16& a) {
  constexpr int v = J >> 2, ph = J & 3;
  if constexpr (J >= 32) {
    if constexpr (J < 35) { e.chk += dppu<J == 32 ? 0xb1 : J == 33 ? 0x4e : 0x128>(e.chk); PIN(e.chk); }
  } else if constexpr (ph == 0) { e.t = __builtin_fmaf(a[v] * a[v], -0.10294324f, -2.3022082f); PIN(e.t); }
  else if constexpr (ph == 1) { e.t = __builtin_amdgcn_exp2f(e.t * a[v]); PIN(e.t); }
  else if constexpr (ph == 2) { e.t = __builtin_amdgcn_rcpf(1.0f + e.t); PIN(e.t); }
  else {
    const float q = a[v] * a[v + 8] * e.t;
    if constexpr (v & 1) { u32 r = pk(e.lo, q); e.chk = v == 1 ? r : e.chk + r; PIN(r); PIN(e.chk); e.r[v >> 1] = r; } else { e.lo = q; PIN(e.lo); }
  }
}

constexpr double ct_sin(long n, long d) {
  n %= d; if (n < 0) n += d;
  if (2 * n > d) return -ct_sin(d - n, d);
  if (4 * n > d) return ct_sin(d - 2 * n, 2 * d);
  const double x = 6.283185307179586476925 * (double)n / (double)d;
  double t = x, r = x;
  for (int i = 1; i < 14; ++i) { t *= -x * x / ((2 * i) * (2 * i + 1)); r += t; }
  return r;
}
constexpr double ct_cos(long n, long d) { return ct_sin(4 * n + d, 4 * d); }
constexpr u32 ct_f32(double v) { return __builtin_bit_cast(u32, (float)v); }
constexpr u32 ct_bf(double v) { const u32 u = ct_f32(v); return (u + 0x7fffu + ((u >> 16) & 1u)) >> 16; }
constexpr u32 ct_pk(double a, double b) { return ct_bf(a) | (ct_bf(b) << 16); }
struct Tab { u32 v[6][64][4]; };
constexpr Tab make_tab() {
  Tab t = {};
  for (int l = 0; l < 64; ++l) {
    const int c = l & 15, g = l >> 4;
    for (int j = 0; j < 2; ++j) {
      const int a = 4 * g + 2 * j;
      t.v[0][l][j] = ct_pk(ct_cos(a * c, 32), ct_cos((a + 1) * c, 32));
      t.v[0][l][2 + j] = c ? ct_pk(-ct_sin(a * c, 32), -ct_sin((a + 1) * c, 32)) : ct_pk(1, -1);
    }
    for (int r = 0; r < 4; ++r) {
      const int b = 4 * g + r, sl = slot_of(b, c);
      t.v[1][l][r] = ct_f32(ct_cos(b * c, 512));
      t.v[2][l][r] = ct_f32(ct_sin(b * c, 512));
      t.v[3][l][r] = ct_pk(ct_cos(b * c, 16), ct_sin(b * c, 16));
      t.v[4][l][r] = c == 0 ? ct_pk((r & 1) ? -1 : 1, 0) : c < 8 ? ct_pk(ct_cos(b * c, 16), -ct_sin(b * c, 16)) : ct_pk(ct_cos(b * (31 - 2 * c), 32), -ct_sin(b * (31 - 2 * c), 32));
      t.v[5][l][r] = SCR + L_T1 + ((sl * 4) ^ ((sl >> 4) & 14)) * 8;
    }
  }
  return t;
}
static __device__ const __attribute__((aligned(128))) Tab g_tab = make_tab();

#define STAMP(i) do { } while (0)
#define STAMP_DECL do { } while (0)
#define STAMP_FLUSH() do { } while (0)
#define COUNT(i, v) do { } while (0)

struct Params {
  const u16* x; const u16* wr; const u16* wi; const u16* wg; const u16* wu; const u16* wd; u16* out; void* ws;
};

__global__ void __launch_bounds__(512, 2) gfnet_geglu_kernel(Params p) {
  const int wg = blockIdx.x, tid = threadIdx.x, lane = tid & 63;
  int wv = __builtin_amdgcn_readfirstlane(tid >> 6);
  STAMP_DECL;
  const int rg = 4 * ((wg & 7) >> 1) + (wg >> 6), ch = 8 * (wg & 1) + ((wg >> 3) & 7);
  const int un = 16 * rg + ch;
  const int ft0 = tile0_of(ch);
  const bool six = ch < 11;
#define X_PIECE(b) ((b) * 16)
#define Y_PIECE(b) ((2 * (b) + ((wv >> 1) & 1)) * 16)
#define LOAD_W1(dst, v, piece) _Pragma("unroll") for (int j = 0; j < 8; ++j) dst[j] = ld_in(rw1, v, (4 * (piece) + (j & 3) + 256 * (j >> 2)) * (F_ * 2))
#define W1_SETUP() const rsrc_t rw1 = mkrs((wv & 1) ? p.wu : p.wg, D_ * F_ * 2); const int vx = 4 * (lane >> 3) * (F_ * 2) + ft0 * 32 + (lane & 7) * 16
  v4u ra[8];
  u32 cpf;
#define UNDEF_RA() do { asm volatile("" : "=v"(cpf)); _Pragma("unroll") for (int i = 0; i < 8; ++i) asm volatile("" : "=v"(ra[i])); } while (0)
  UNDEF_RA();
  if (wv < 4) {
    const int vt = lane * 16;
    int sx = (32 * (8 * (un >> 5) + 2 * wv) + (un & 31)) * (D_ * 2), sy = sx + 32 * D_ * 2;
    const rsrc_t rt = mkrs(&g_tab, sizeof(Tab));
    ra[2] = ld_in(rt, vt, 0); ra[3] = ld_in(rt, vt + 1024, 0);
    PINS(sx); PINS(sy);
    SB();
    const rsrc_t rx = mkrs(p.x, S_ * D_ * 2);
    ra[0] = ld_once(rx, vt, sx); ra[1] = ld_once(rx, vt, sy);
    SB();
#pragma unroll
    for (int i = 0; i < 4; ++i) ra[4 + i] = ld_in(rt, vt + i * 1024, 2048);
    cpf = ld_dev1(mkrs(p.ws, WS_END), 0, WS_E);
    STAMP(0);
  }
  PINS(wv);
  if (wv >= 6) {
    unsigned long long pc0;
    asm volatile("s_getpc_b64 %0" : "=s"(pc0));
    cpf = touch(mkrs((const void*)(pc0 & ~127ull), CODE_LINES * 128), lane < 8 ? (((wg >> 3) ^ (wv & 1)) * 8 + lane) : 4096);
  }
  PINS(wv);
  if ((wv >> 1) == 2) {
    const rsrc_t rwr = mkrs(p.wr, S_ * KF * 2), rwi = mkrs(p.wi, S_ * KF * 2);
    const int vf = (lane & 1) * (KF * 2 + 2) + ((tid & 127) >> 1) * 8;
    const int sf = (16 * (un >> 6) + 2 * (un & 7) + 256 * ((un >> 3) & 7)) * (KF * 2);
#pragma unroll
    for (int r = 0; r < 4; ++r) { ra[r] = ld_once(rwr, vf, sf + r * (64 * KF * 2)); ra[4 + r] = ld_once(rwi, vf, sf + r * (64 * KF * 2)); }
  }
  const lp_t fmine = lptr(SCR + L_F + tid * 4);
  *(volatile LAS u32*)fmine = 0;
  BAR();
  PINS(wv);
  if (wv >= 6) {
#define X0SLEEP 2
    asm volatile("" :: "v"(cpf));
    __builtin_amdgcn_s_sleep(X0SLEEP);
    cpf = touch(mkrs((wv & 1) ? p.wu : p.wg, D_ * F_ * 2), lane < 43 ? wg * 43 + lane : 16384);
  }
  PINS(wv);
  if ((wv >> 1) == 2) {
    const rsrc_t ws = mkrs(p.ws, WS_END);
    const int T = tid - 256, m = T >> 1, vs = ((m & 0x39) | ((m & 2) << 1) | ((m & 4) >> 1)) * 32 + (T & 1) * 16 + WS_W + un * 8192;
    const u32 sh = (lane & 1) ? 0u : 16u;
    u32 al[8][2], ck[4];
#pragma unroll
    for (int i = 0; i < 8; ++i) { const u32 a = ra[i].x, b = ra[i].y, d = ra[i].z; al[i][0] = __builtin_amdgcn_alignbit(b, a, sh); al[i][1] = __builtin_amdgcn_alignbit(d, b, sh); }
    v4u v[4];
#pragma unroll
    for (int e = 0; e < 4; ++e) {
#pragma unroll
      for (int r = 0; r < 4; ++r) v[e][r] = __builtin_amdgcn_perm(al[4 + r][e >> 1], al[r][e >> 1], (e & 1) ? 0x07060302u : 0x05040100u);
      ck[e] = sum4(0u, v[e]);
    }
    v2u cw = {pair_total(ck[0], ck[2], T & 1), pair_total(ck[1], ck[3], T & 1)};
    lds_w64(lptr(SCR + L_CW + T * 8), cw);
    flag_set(fmine, 1);
#pragma unroll
    for (int e = 0; e < 4; ++e) st_dev(ws, vs + e * 2048, v[e]);
    UNDEF_RA();
    STAMP(1);
  }
  SB();
  PINS(wv);

  if (wv < 4) {
  const int w = wv, c = lane & 15, g = lane >> 4;
  const float fc = (float)c, fg4 = (float)(4 * g);
  const f32x4 z = {0.f, 0.f, 0.f, 0.f};
  f32x4 twc, tws;
  v4u fbre, fbsw;
  u32 tag;
  float mean[2];
  const rsrc_t ws = mkrs(p.ws, WS_END);
  const lp_t fall = lptr(SCR + L_F + (lane & 3) * 256 + lane * 4);
  v2u xk[2][2];
  {
    const v4u xr[2] = {ra[0], ra[1]}, tb[6] = {ra[2], ra[3], ra[4], ra[5], ra[6], ra[7]};
    tag = cpf;
    const u32 sc = (c & 1) ? NEG2 : 0u, sm = c < 8 ? NEGL : NEGH, p0 = c < 8 ? 0x0c0c0100u : 0x01000c0cu, p1 = c < 8 ? 0x0c0c0302u : 0x03020c0cu;
    v4u fare = {tb[0].x, tb[0].y, tb[0].x ^ sc, tb[0].y ^ sc}, faim = {tb[0].z, tb[0].w, tb[0].z ^ sc, tb[0].w ^ sc}, fbim, f0re, f0im;
    twc = __builtin_bit_cast(f32x4, tb[1]); tws = __builtin_bit_cast(f32x4, tb[2]); fbre = tb[3];
    lp_t t1w[4], t1r[2];
#pragma unroll
    for (int r = 0; r < 4; ++r) {
      const u32 a = tb[3][r], q = tb[4][r];
      fbsw[r] = __builtin_amdgcn_alignbit(a, a, 16);
      fbim[r] = fbsw[r] ^ sm;
      f0re[r] = __builtin_amdgcn_perm(q, q, p0); f0im[r] = __builtin_amdgcn_perm(q, q, p1);
      t1w[r] = lptr(tb[5][r] ^ (w * 8));
    }
#pragma unroll
    for (int i = 0; i < 2; ++i) t1r[i] = lptr(SCR + L_T1 + ((w * 128 + i * 64 + lane) ^ (2 * w + i)) * 16);
    const int sl = 64 * w + 32 * (lane & 1) + (lane >> 1);
    lp_t cwr = lptr(SCR + L_CW + sl * 4);
    int vc = chk_pos(sl) * 4 + WS_CA + un * 1024, vs = lane * 16 + WS_A + un * 8192 + w * 2048;
    float c0m = c == 0 ? 1.f / 16 : 0.f;
    lp_t xw = lptr(SCR + L_X + w * 2048 + lane * 16), xt = lptr(SCR + L_X + w * 2048 + lane * 8);
    lp_t xq = lptr(SCR + L_X + w * 2048 + (4 * c + g) * 8), f6 = lptr(SCR + L_F + (lane % 6) * 256 + lane * 4);
    PIN(vc); PIN(vs); PIN(c0m);
    PIN(fare); PIN(faim); PIN(fbre); PIN(fbim); PIN(f0re); PIN(f0im); PIN(twc); PIN(tws);
    STAMP(1);
    SB();
    v4u a[2];
#pragma unroll
    for (int rr = 0; rr < 2; ++rr) { lds_w128(xw + rr * 1024, xr[rr]); a[rr] = lds_tr2(xt + rr * 1024, xt + rr * 1024 + 512); }
    STAMP(2);
    float st[4] = {0.f, 0.f, 0.f, 0.f};
#pragma unroll
    for (int i = 0; i < 4; ++i)
#pragma unroll
      for (int rr = 0; rr < 2; ++rr) { const u32 v = xr[rr][i]; st[rr] = dot2(v, 0x3f803f80u, st[rr]); st[2 + rr] = dot2(v, v, st[2 + rr]); }
    SB();
    f32x4 ur[2], ui[2];
#pragma unroll
    for (int rr = 0; rr < 2; ++rr) { ur[rr] = mm16p(a[rr], fare, z); ui[rr] = mm16p(a[rr], faim, z); }
    wave_sums<4>(st);
    SB();
    v4u a2[2];
    f32x4 orr[2], oi[2], sr[2], si[2];
#pragma unroll
    for (int rr = 0; rr < 2; ++rr) {
      const f32x4 x = ur[rr] - st[rr] * c0m, y = ui[rr];
      a2[rr] = pk4(x * twc + y * tws, y * twc - x * tws);
      orr[rr] = mm16p(a2[rr], fbre, z); oi[rr] = mm16p(a2[rr], fbim, z); sr[rr] = mm16p(a2[rr], f0re, z); si[rr] = mm16p(a2[rr], f0im, z);
    }
    v4u o[2];
#pragma unroll
    for (int rr = 0; rr < 2; ++rr) {
      mean[rr] = st[rr] * (1.f / 512);
      const float rstd = __builtin_amdgcn_rsqf(fmaxf(st[2 + rr] * (1.f / 512) - mean[rr] * mean[rr], 0.f) + 1e-5f);
      orr[rr][0] = g == 0 ? sr[rr][0] : orr[rr][0]; oi[rr][0] = g == 0 ? si[rr][0] : oi[rr][0];
      o[rr] = pk4(orr[rr] * rstd, oi[rr] * rstd);
    }
#pragma unroll
    for (int r = 0; r < 4; ++r) { v2u v = {o[0][r], o[1][r]}; lds_w64(t1w[r], v); }
    tag += 1u;
    PIN(tag);
    STAMP(3);
    flag_set(fmine, 1);
#pragma unroll
    for (int i = 0; i < 4; ++i) xk[i >> 1][i & 1] = lds_r64(xq + (i >> 1) * 1024 + (i & 1) * 512);
    flag_wait(f6, 1);
    const v4u v0 = lds_r128(t1r[0]), v1 = lds_r128(t1r[1]);
    const u32 cw = *(LAS u32*)cwr;
    st_dev(ws, vs, v0);
    st_dev(ws, vs + 1024, v1);
    st_dev1(ws, vc, tag - cw - pair_total(sum4(0u, v0), sum4(0u, v1), lane & 1));
  }
  int vg = (lane >> 1) * 8192 + (lane & 1) * 16, vk = (32 * (lane & 1) + (lane >> 1)) * 1024;
  const int sg = w * (64 * 8192) + un * 32, sk = w * (64 * 1024) + chk_pos(un) * 4;
  const int sw = WS_W + w * (64 * 8192) + ((un & 3) * 64 + ((((un >> 2) & 0x39) | ((un >> 1) & 4) | ((un >> 3) & 2)))) * 32;
  v4u wq[2];
  wq[0] = ld_dev(ws, vg, sw); wq[1] = ld_dev(ws, vg, sw + 32 * 8192);
  STAMP(4);
  SB();
  v4u f2a[2][2];
  {
    v4u f1a[4], f1b[4], f2b[2][2];
    f32x4 tfc[2], tfs[2], tic[2], tis[2];
    const u32 sc = (c & 1) ? NEG2 : 0u;
    const float fj = (float)(16 * w) + fc;
#pragma unroll
    for (int r = 0; r < 4; ++r) {
#pragma unroll
      for (int ks = 0; ks < 2; ++ks) {
        const float n = (fg4 + (16 * ks + r)) * (fj * (1.f / 64));
        const float cs = cosr(n), sn = sinr(n);
        f1a[ks][r] = pk(cs, sn); f1b[ks][r] = pk(-sn, cs);
        f1a[ks + 2][r] = f1a[ks][r] ^ sc; f1b[ks + 2][r] = f1b[ks][r] ^ sc;
      }
      const float n = (fg4 + r) * (fc * (1.f / 32));
      const float cs = cosr(n), sn = sinr(n);
      const u32 a = pk(cs, sn), b = pk(-sn, cs), sr = (r & 1) ? NEG2 : 0u;
      f2a[0][0][r] = a; f2a[1][0][r] = a ^ sr; f2a[0][1][r] = a ^ sc; f2a[1][1][r] = a ^ sc ^ sr;
      f2b[0][0][r] = b; f2b[1][0][r] = b ^ sr; f2b[0][1][r] = b ^ sc; f2b[1][1][r] = b ^ sc ^ sr;
#pragma unroll
      for (int m = 0; m < 2; ++m) {
        const float nf = (fg4 + (16 * m + r)) * (fj * (1.f / 2048)), ni = (fg4 + (float)(16 * w) + r) * ((fc + 16 * m) * (1.f / 2048));
        tfc[m][r] = cosr(nf); tfs[m][r] = sinr(nf); tic[m][r] = cosr(ni); tis[m][r] = sinr(ni);
      }
    }
#pragma unroll
    for (int i = 0; i < 4; ++i) { PIN(f1a[i]); PIN(f1b[i]); PIN(f2a[i >> 1][i & 1]); PIN(f2b[i >> 1][i & 1]); }
#pragma unroll
    for (int i = 0; i < 2; ++i) { PIN(tfc[i]); PIN(tfs[i]); PIN(tic[i]); PIN(tis[i]); }
    lp_t i2w = lptr(SCR + L_I2 + w * 2048 + lane * 16), i2r = lptr(SCR + L_I2 + ((g >> 1) * 32 + c) * 32 + (g & 1) * 16);
    lp_t exw = lptr(SCR + L_EX + w * 2048 + lane * 16), exr = lptr(SCR + L_EX + lane * 16);
    int vb = ((g >> 1) * 32 + c) * 32 + (g & 1) * 16 + WS_B + un * 8192 + w * 2048, vc = chk_pos(lane) * 4 + WS_CB + un * 1024 + w * 256;
    PIN(vg); PIN(vk); PIN(vb); PIN(vc);
    STAMP(5);
    DRAIN();
    STAMP(6);
    v4u xg[2];
    f32x4 wr[2], wi[2];
    u32 spin = 0;
    for (;; ++spin) {
      MEMFENCE();
      xg[0] = ld_dev(ws, vg, WS_A + sg); xg[1] = ld_dev(ws, vg, WS_A + sg + 32 * 8192);
      const u32 k_ = ld_dev1(ws, vk, WS_CA + sk);
      SB();
      u32 s0 = sum4(0u, wq[0]), s1 = sum4(0u, wq[1]);
#pragma unroll
      for (int m = 0; m < 2; ++m) {
#pragma unroll
        for (int r = 0; r < 4; ++r) { const u32 v = wq[m][r]; wr[m][r] = blo(v); wi[m][r] = bhi(v); }
        PIN(wr[m]); PIN(wi[m]);
      }
      PIN(s0); PIN(s1);
      SB();
      lds_w128(i2w, xg[0]); lds_w128(i2w + 1024, xg[1]);
      if (__all(pair_total(sum4(s0, xg[0]), sum4(s1, xg[1]), lane & 1) + k_ == tag) || spin > SPIN_CAP) break;
      MEMFENCE();
      wq[0] = ld_dev(ws, vg, sw); wq[1] = ld_dev(ws, vg, sw + 32 * 8192);
    }
    COUNT(20, spin);
    STAMP(7);
    flag_set(fmine, 2); flag_wait(fall, 2);
    STAMP(8);
    v4u af[2][4], b2[2], a3[2];
#pragma unroll
    for (int m = 0; m < 2; ++m)
#pragma unroll
      for (int ks = 0; ks < 4; ++ks) af[m][ks] = lds_r128(i2r + ks * 2048 + m * 512);
    SB();
    f32x4 dr[2], di[2];
#pragma unroll
    for (int m = 0; m < 2; ++m)
#pragma unroll
      for (int ks = 0; ks < 4; ++ks) { dr[m] = mm16(af[m][ks], f1a[ks], ks ? dr[m] : z); di[m] = mm16(af[m][ks], f1b[ks], ks ? di[m] : z); }
#pragma unroll
    for (int m = 0; m < 2; ++m) b2[m] = pk4(dr[m] * tfc[m] + di[m] * tfs[m], di[m] * tfc[m] - dr[m] * tfs[m]);
#pragma unroll
    for (int k = 0; k < 2; ++k)
#pragma unroll
      for (int m = 0; m < 2; ++m) { dr[m] = mm16(f2a[m][k], b2[k], k ? dr[m] : z); di[m] = mm16(f2b[m][k], b2[k], k ? di[m] : z); }
#pragma unroll
    for (int m = 0; m < 2; ++m) a3[m] = pk4(dr[m] * wi[m] + di[m] * wr[m], dr[m] * wr[m] - di[m] * wi[m]);
#pragma unroll
    for (int k = 0; k < 2; ++k)
#pragma unroll
      for (int n = 0; n < 2; ++n) { di[n] = mm16(a3[k], f2a[n][k], k ? di[n] : z); dr[n] = mm16(a3[k], f2b[n][k], k ? dr[n] : z); }
#pragma unroll
    for (int n = 0; n < 2; ++n) lds_w128(exw + n * 1024, pk4(di[n] * tic[n] + dr[n] * tis[n], dr[n] * tic[n] - di[n] * tis[n]));
    STAMP(9);
    flag_set(fmine, 3); flag_wait(fall, 3);
#pragma unroll
    for (int n = 0; n < 2; ++n)
#pragma unroll
      for (int ks = 0; ks < 4; ++ks) af[n][ks] = lds_r128(exr + ks * 2048 + n * 1024);
    SB();
    u32 cs[2];
#pragma unroll
    for (int n = 0; n < 2; ++n)
#pragma unroll
      for (int ks = 0; ks < 4; ++ks) { di[n] = mm16(f1a[ks], af[n][ks], ks ? di[n] : z); dr[n] = mm16(f1b[ks], af[n][ks], ks ? dr[n] : z); }
#pragma unroll
    for (int n = 0; n < 2; ++n) {
      const v4u e = pk4(dr[n], di[n]);
      st_dev(ws, vb + n * 512, e);
      cs[n] = sum4(0u, e);
    }
    const auto sx = __builtin_amdgcn_permlane16_swap(cs[0], cs[1], false, false);
    st_dev1(ws, vc, tag - sx[0] - sx[1]);
    flag_set(fmine, 4);
    if (wg == 0 && w == 0) st_dev1(ws, WS_E, tag);
  }
  STAMP(10);
  SB();
  {
    v4u gbre, gbim, g0re, g0im, ga[2];
#pragma unroll
    for (int r = 0; r < 4; ++r) {
      gbre[r] = fbre[r] ^ (g < 2 ? NEGH : 0u);
      gbim[r] = fbsw[r] ^ (g < 2 ? 0u : NEGH);
      const float n = fc * ((31.f - 2.f * r - 2.f * fg4) * (1.f / 32));
      u32 q = pk(cosr(n), -sinr(n));
      PIN(q);
      g0re[r] = g < 2 ? gbre[r] : 0u;
      g0im[r] = g < 2 ? 0u : q;
      ga[0][r] = f2a[0][0][r] ^ NEGH; ga[1][r] = f2a[1][0][r] ^ NEGH;
    }
    twc *= 2.f / (2048.f * 512.f); tws *= 2.f / (2048.f * 512.f);
    g0re[0] = g == 0 ? ((c & 1) ? 0xbf00u : 0x3f00u) : g0re[0];
    ga[0][0] = g == 0 ? ((c & 1) ? 0xbf803f80u : 0x3f803f80u) : ga[0][0]; ga[1][0] = ga[0][0];
    PIN(gbre); PIN(gbim); PIN(g0re); PIN(g0im); PIN(ga[0]); PIN(ga[1]); PIN(twc); PIN(tws);
    f32x4 xin[2][2];
#pragma unroll
    for (int rr = 0; rr < 2; ++rr)
#pragma unroll
      for (int t = 0; t < 2; ++t) {
        const v2u v = xk[rr][t];
        const u32 v0 = v.x, v1 = v.y;
        f32x4 q = {blo(v0), bhi(v0), blo(v1), bhi(v1)};
        xin[rr][t] = q - mean[rr]; PIN(xin[rr][t]);
      }
    lp_t i3w = lptr(SCR + L_I3 + w * 2048 + lane * 16), i3r[4], o3w[2], o3r[2];
#pragma unroll
    for (int r = 0; r < 4; ++r) i3r[r] = lptr(SCR + L_I3 + slot_of(c, 4 * g + r) * 32 + w * 8);
#pragma unroll
    for (int i = 0; i < 2; ++i) {
      o3w[i] = lptr(SCR + L_O3 + ((4 * c + g) * 8 + ((2 * w + i) ^ (c & 7))) * 16);
      const int q = w * 128 + i * 64 + lane;
      o3r[i] = lptr(SCR + L_O3 + (q ^ ((q >> 5) & 7)) * 16);
    }
    int vs = lane * 16 + WS_L + un * 8192 + w * 2048, vt = lane < 8 ? lane * 16 + WS_S + un * 512 + w * 128 : OOB;
    PIN(vs); PIN(vt);
    lp_t sow = lptr(SCR + L_SO + tid * 16), sor = lptr(SCR + L_SO + (lane & 6) * 512 + (lane & 1) * 8);
    DRAIN();
    STAMP(11);
    u32 spin = 0;
    for (;; ++spin) {
      MEMFENCE();
      const v4u x0 = ld_dev(ws, vg, WS_B + sg), x1 = ld_dev(ws, vg, WS_B + sg + 32 * 8192);
      const u32 k_ = ld_dev1(ws, vk, WS_CB + sk);
      SB();
      lds_w128(i3w, x0); lds_w128(i3w + 1024, x1);
      if (__all(pair_total(sum4(0u, x0), sum4(0u, x1), lane & 1) + k_ == tag) || spin > SPIN_CAP) break;
    }
    COUNT(21, spin);
    STAMP(12);
    flag_set(fmine, 5); flag_wait(fall, 5);
    STAMP(13);
    v4u a[2], a2[2];
#pragma unroll
    for (int r = 0; r < 4; ++r) { const v2u v = lds_r64(i3r[r]); a[0][r] = v.x; a[1][r] = v.y; }
    f32x4 tr[2], ti[2], sr[2], si[2], h[2][2];
#pragma unroll
    for (int rr = 0; rr < 2; ++rr) { tr[rr] = mm16p(a[rr], gbre, z); ti[rr] = mm16p(a[rr], gbim, z); sr[rr] = mm16p(a[rr], g0re, z); si[rr] = mm16p(a[rr], g0im, z); }
#pragma unroll
    for (int rr = 0; rr < 2; ++rr) {
      tr[rr][0] = g == 0 ? sr[rr][0] : tr[rr][0]; ti[rr][0] = g == 0 ? si[rr][0] : ti[rr][0];
      a2[rr] = pk4(tr[rr] * twc - ti[rr] * tws, ti[rr] * twc + tr[rr] * tws);
      h[rr][0] = mm16p(a2[rr], ga[0], xin[rr][0]); h[rr][1] = mm16p(a2[rr], ga[1], xin[rr][1]);
    }
    float st[2];
#pragma unroll
    for (int rr = 0; rr < 2; ++rr) { const f32x4 q = h[rr][0] * h[rr][0] + h[rr][1] * h[rr][1]; st[rr] = (q[0] + q[2]) + (q[1] + q[3]); }
    wave_sums<2>(st);
    v4u so;
#pragma unroll
    for (int rr = 0; rr < 2; ++rr) {
      const float ve = st[rr] * (1.f / 512) + 1e-5f, rstd = __builtin_amdgcn_rsqf(ve);
      const f32x4 l0 = h[rr][0] * rstd, l1 = h[rr][1] * rstd;
      v4u l4 = {pk(l0[0], l0[1]), pk(l0[2], l0[3]), pk(l1[0], l1[1]), pk(l1[2], l1[3])};
      lds_w128(o3w[rr], l4);
      so[2 * rr] = asu(mean[rr]); so[2 * rr + 1] = asu(ve * rstd);
    }
    lds_w128(sow, so);
    STAMP(14);
    flag_set(fmine, 6); flag_wait(fall, 6);
    const v4u v0 = lds_r128(o3r[0]), v1 = lds_r128(o3r[1]);
    const v2u ms = lds_r64(sor);
    st_dev(ws, vs, v0); st_dev(ws, vs + 1024, v1);
    u32 s = sum4(sum4(0u, v0), v1);
    s += dppu<0x128>(s); s = add_x32(add_x16(s));
    v4u rec = {tag - s - ms.x - ms.y, ms.x, ms.y, tag};
    st_dev(ws, vt, rec);
    BAR();
  }
  SB();
  {
    constexpr int PD = 4;
    constexpr int LOOK = 14;
    const int kh = w & 1, rl = w >> 1, hh = lane >> 5;
    v4u ring[8], act[32], hfA[8], hfB[8], hfC[8];
    u32 hkA, hkB, hkC;
    f32x16 zz;
#pragma unroll
    for (int i = 0; i < 16; ++i) zz[i] = 0.f;
    lp_t wa[4], wb[4], wd[6][2];
#pragma unroll
    for (int t = 0; t < 4; ++t) wa[t] = lptr(t * SLOT + ((lane ^ (2 * t + ((lane >> 3) & 1))) * 16));
    u32 dl = (4 * hh + ((lane & 15) >> 2)) * 128 + ((lane >> 3) & 1) * 64 + (lane & 1) * 32 + ((lane >> 4) & 1) * 16 + (lane & 2) * 4;
    PIN(dl);
#define WD_PTR(t) do { const u32 o_ = ((t) == 3 ? 4 : (t) == 4 ? 3 : (t) % 5) * SLOT + kh * ((t) < 4 ? 16384 : 12288); wd[t][0] = lptr(dl + o_); wd[t][1] = lptr((dl ^ 64) + o_); } while (0)
    auto frag = [&](auto NN) -> v4u {
      constexpr int N = NN.value;
      if constexpr (N < 64) return lds_r128(wa[N & 1] + (N >> 1) * 1024);
      else if constexpr (N < 128) return lds_r128(wa[N >> 5] + (N & 31) * 1024);
      else if constexpr (N < 192) return lds_r128(wb[(N >> 4) & 3] + (N & 31) * 1024);
      else {
        constexpr int t = N < 256 ? (N - 192) >> 4 : N < 268 ? 4 : 5, s = N < 256 ? (N & 15) : N < 268 ? N - 256 : N - 268;
        const lp_t b = wd[t][s & 1] + (s >> 1) * 2048;
        return lds_tr2(b, b + 1024);
      }
    };
#define FRAG(N) frag(std::integral_constant<int, (N)>{})
    const int hb = WS_H + (4 * rg + 2 * (ch >> 3) + rl) * NFT * HREC;
    int hs[8], hs4 = hb + (36 * kh + 4) * HREC;
#pragma unroll
    for (int i = 0; i < 8; ++i) { hs[i] = hb + tile0_of(8 * kh + i) * HREC; PINS(hs[i]); }
    PINS(hs4);
    int vl = lane * 16, vlz = kh ? OOB : vl, vq, vq4;
    {
      const int st = (lane & 3) + ((lane >> 1) & 4), wo = hb + 1024 + ((lane >> 4) * 8 + (lane & 4)) * 4;
      vq = wo + tile0_of(8 * kh + st) * HREC;
      vq4 = st < 6 - kh ? wo + (36 * kh + 6 * st + 4) * HREC : OOB;
    }
    int vo = WS_H + ((4 * rg + w) * NFT + ft0) * HREC + lane * 16, vk = (lane & 8) ? OOB : vo - lane * 16 + 1024 + ((lane >> 4) * 8 + (lane & 7)) * 4;
    int vo4 = six ? vo + 4 * HREC : OOB, vk4 = six ? vk + 4 * HREC : OOB;
    PIN(vl); PIN(vlz); PIN(vq); PIN(vq4); PIN(vo); PIN(vk); PIN(vo4); PIN(vk4);
#define H_LD(X, t, j) do { \
      if constexpr ((j) == 8) hk##X = ld_dev1(ws, ((t) < 4 ? vq : vq4) + ((t) & 3) * HREC, 0); \
      else if constexpr ((t) < 4) hf##X[(j) & 7] = ld_dev(ws, vl + (t) * HREC, hs[(j) & 7]); \
      else if constexpr ((j) < 6) hf##X[(j) & 7] = ld_dev(ws, ((j) == 5 ? vlz : vl) + ((t) & 3) * HREC, hs4 + 6 * (j) * HREC); } while (0)
    Epi e;
#define EPI(t, acc, J0, J1) static_for<((J0) < 0 ? 0 : (J0) > 36 ? 36 : (J0)), ((J1) < 0 ? 0 : (J1) > 36 ? 36 : (J1))>([&](auto JJ) { epi_slice<JJ.value>(e, acc); \
      if constexpr (JJ.value == 31) st_dev(ws, ((t) < 4 ? vo : vo4) + ((t) & 3) * HREC, e.r); \
      if constexpr (JJ.value == 35) st_dev1(ws, ((t) < 4 ? vk : vk4) + ((t) & 3) * HREC, tag - e.chk); })
    int va = ((lane >> 3) & 3) * 8192 + hh * 128 + (lane & 7) * 16;
    int vr = ((lane >> 3) & 3) * 512 + hh * 128 + (lane & 7) * 16;
    PIN(vr);
    const int sa = WS_L + (4 * rg + w) * 32768;
#define ACT_LD(ks) act[ks] = ld_dev(ws, va + ((ks) & 15) * 256, sa + ((ks) >> 4) * 4096)
    f32x16 tA, tB, tC;
    u32 tw;
    const rsrc_t rwd = mkrs(p.wd, D_ * F_ * 2);
    int vw = lane < 11 ? wg * 43 + w * 11 + lane : 16384;
    PIN(vw);
    DRAIN();
    STAMP(15);
    u32 spin = 0;
    for (;; ++spin) {
      MEMFENCE();
      PIN(va);
      const v4u s0 = ld_dev(ws, vr, WS_S + (4 * rg + w) * 2048), s1 = ld_dev(ws, vr + 256, WS_S + (4 * rg + w) * 2048);
      SB();
      static_for<0, LOOK>([&](auto K) { ACT_LD(K.value); SB(); });
      static_for<0, PD>([&](auto K) { ring[K.value] = FRAG(K.value); });
      SB();
      if (__any(s0.w != tag || s1.w != tag) && spin <= SPIN_CAP) continue;
      u32 m = (s0.x + s0.y + s0.z) + (s1.x + s1.y + s1.z);
      static_for<0, 64>([&](auto K) {
        constexpr int n = K.value, ks = n >> 1;
        if constexpr (n & 1) tB = mm32p(ring[n & 7], act[ks], ks ? tB : zz); else tA = mm32p(ring[n & 7], act[ks], ks ? tA : zz);
        ring[(n + PD) & 7] = FRAG(n + PD);
        if constexpr (!(n & 1) && ks + LOOK < 32) ACT_LD(ks + LOOK);
        if constexpr (n == 2 * (32 - LOOK)) tw = touch(rwd, vw);
        { const v4u v = act[ks]; u32 t = (n & 1) ? m + v.z + v.w : m + v.x + v.y; PIN(t); m = t; }
        SB();
      });
      if (__all(add_x32(m) == 4u * tag) || spin > SPIN_CAP) break;
    }
    COUNT(22, spin);
#pragma unroll
    for (int i = 0; i < 4; ++i) wb[i] = lptr((i < 2 ? 4 : 3) * SLOT + ((lane ^ (4 * (i & 1) + 2 * hh + ((lane >> 3) & 1))) * 16));
    BAR_N(15);
    STAMP(16);
#define G1_TILE(T, acc, ...) static_for<0, 32>([&](auto K) { constexpr int s = K.value, n = 32 * (T) + s; \
      acc = mm32p(ring[n & 7], act[s], s ? acc : zz); \
      ring[(n + PD) & 7] = FRAG(n + PD); \
      __VA_ARGS__; SB(); })
    G1_TILE(2, tC, { constexpr int j0 = (27 * s) / 16, j1 = (27 * s + 27) / 16; EPI(0, tA, j0, j1); EPI(1, tB, j0 - 36, j1 - 36); });
    BAR_N(15);
    G1_TILE(3, tA, { constexpr int j0 = (27 * s) / 16 + 18, j1 = (27 * s + 27) / 16 + 18; EPI(1, tB, j0, j1); EPI(2, tC, j0 - 36, j1 - 36);
      if constexpr (s == 26) BAR_N(15); });
    asm volatile("" :: "v"(tw));
    BAR_N(15);
    v4u hfD[8];
    u32 hkD, ms = 0;
#define H_SUM(X, nf, j) do { if constexpr ((j) >= 0 && (j) < (nf)) { ms = sum4((j) ? ms : hk##X, hf##X[(j) & 7]); u32 t_ = ms; PIN(t_); ms = t_; } \
      else if constexpr ((j) >= (nf) && (j) < (nf) + 3) { u32 t_ = ms + dppu<(j) == (nf) ? 0xb1 : (j) == (nf) + 1 ? 0x4e : 0x128>(ms); PIN(t_); ms = t_; } } while (0)
    G1_TILE(4, tB, { EPI(3, tA, (6 * (s - 2)) / 5, (6 * (s - 1)) / 5);
      if constexpr (s >= 4 && s < 22 && !(s & 1)) H_LD(A, 0, (s - 4) >> 1);
      if constexpr (s >= 22 && s < 30 && !(s & 1)) H_LD(B, 1, (s - 22) >> 1);
      if constexpr (s == 25) BAR_N(15); });
    BAR_N(15);
    G1_TILE(5, tC, { EPI(4, tB, (6 * (s - 2)) / 5, (6 * (s - 1)) / 5);
      if constexpr (s >= 4 && s < 14 && !(s & 1)) H_LD(B, 1, 4 + ((s - 4) >> 1));
      if constexpr (s >= 14 && !(s & 1)) H_LD(C, 2, (s - 14) >> 1);
      if constexpr (s & 1) H_SUM(A, 8, (s - 9) / 2 - (s < 9));
      if constexpr (s == 26) WD_PTR(0); });
    BAR_N(15);
    STAMP(17);
    f32x16 o0, o1;
    u32 nfix = 0;
#define H_TOT(X, nf) ({ u32 m_ = hk##X; _Pragma("unroll") for (int i = 0; i < (nf); ++i) m_ = sum4(m_, hf##X[i]); \
      m_ += dppu<0xb1>(m_); m_ += dppu<0x4e>(m_); m_ += dppu<0x128>(m_); m_; })
#define H_FIX(X, t, nf, want) do { if (__builtin_expect(__any(ms != (want)), 0)) { u32 spin_ = 0; _Pragma("nounroll") do { MEMFENCE(); nfix++; \
        static_for<0, 9>([&](auto J) { H_LD(X, t, J.value); }); } while (__any(H_TOT(X, nf) != (want)) && ++spin_ < SPIN_CAP); } SB(); } while (0)
#define G2_ROUND(X, n0, ns, ...) static_for<0, ns>([&](auto K) { constexpr int s = K.value, n = (n0) + s; \
      if constexpr (s & 1) o1 = mm32p(ring[n & 7], hf##X[s >> 1], n < 194 ? zz : o1); else o0 = mm32p(ring[n & 7], hf##X[s >> 1], n < 194 ? zz : o0); \
      if constexpr (n + PD < 280) ring[(n + PD) & 7] = FRAG(n + PD < 280 ? n + PD : 0); \
      __VA_ARGS__; SB(); })
    const u32 t8 = 8u * tag, t6 = (6u - kh) * tag;
    H_FIX(A, 0, 8, t8);
    G2_ROUND(A, 192, 16, { EPI(5, tC, (9 * s) / 4, (9 * s + 9) / 4); if constexpr (!(s & 1)) H_LD(D, 3, s >> 1); if constexpr (s == 15) H_LD(D, 3, 8);
      H_SUM(B, 8, s - 5); if constexpr (s == 2) WD_PTR(1); });
    BAR_N(15);
    H_FIX(B, 1, 8, t8);
    const int u = 16 * rg + 8 * (ch >> 3) + 4 * rl + ((lane >> 3) & 3), col = 64 * (ch & 7) + 32 * kh + 16 * hh;
    v2u hr[4];
    v4u so;
    int vh = u * 8192 + (col & 255) * 32 + (lane & 7) * 16 + (col >> 8) * 8, vs = u * 512 + (lane & 7) * 16;
    G2_ROUND(B, 208, 16, { H_SUM(C, 8, s - 5); if constexpr (s == 2) WD_PTR(2);
      if constexpr (s < 4) hr[s & 3] = __builtin_amdgcn_raw_buffer_load_b64(ws, vh + (s & 3) * 128, WS_L, AUX_SC1);
      if constexpr (s == 4) so = ld_dev(ws, vs, WS_S);
      if constexpr (s >= 9) H_LD(A, 4, s < 15 ? s - 9 : 8); });
    BAR_N(15);
    H_FIX(C, 2, 8, t8);
    G2_ROUND(C, 224, 16, { H_SUM(D, 8, s - 5); if constexpr (s == 2) WD_PTR(3); if constexpr (s >= 9) H_LD(B, 5, s < 15 ? s - 9 : 8); });
    H_FIX(D, 3, 8, t8);
    G2_ROUND(D, 240, 16, { H_SUM(A, 6, s - 7); if constexpr (s == 2) WD_PTR(4);
      if constexpr (s == 10) BAR_N(15); });
    H_FIX(A, 4, 6, t6);
    G2_ROUND(A, 256, 12, { H_SUM(B, 6, s - 3); if constexpr (s == 1) WD_PTR(5); });
    H_FIX(B, 5, 6, t6);
    G2_ROUND(B, 268, 12, { });
    COUNT(23, nfix);
    STAMP(18);
    lp_t rw = lptr(2 * SLOT + w * 4096 + lane * 16), rr_ = lptr(2 * SLOT + (w ^ 1) * 4096 + lane * 16);
    f32x4 f[4];
#define Q4(a, i) f32x4{a[4 * (i)], a[4 * (i) + 1], a[4 * (i) + 2], a[4 * (i) + 3]}
    if (kh) {
#pragma unroll
      for (int i = 0; i < 4; ++i) { lds_wf(rw + i * 1024, Q4(o0, i)); f[i] = Q4(o1, i); }
    } else {
#pragma unroll
      for (int i = 0; i < 4; ++i) { lds_wf(rw + i * 1024, Q4(o1, i)); f[i] = Q4(o0, i); }
    }
    {
      const u32 mb = so.y, sb = so.z;
#pragma unroll
      for (int i = 0; i < 4; ++i) { const u32 v0 = hr[i].x, v1 = hr[i].y; const f32x4 q = {blo(v0), bhi(v0), blo(v1), bhi(v1)}; f[i] += q * asf(sb) + asf(mb); }
    }
    v4u* op = (v4u*)(p.out + (32 * (8 * (u >> 5) + (lane & 7)) + (u & 31)) * D_ + col);
    BAR();
#pragma unroll
    for (int i = 0; i < 2; ++i) {
      const f32x4 v0 = f[2 * i] + lds_rf(rr_ + i * 2048), v1 = f[2 * i + 1] + lds_rf(rr_ + i * 2048 + 1024);
      v4u q = {pk(v0[0], v0[1]), pk(v0[2], v0[3]), pk(v1[0], v1[1]), pk(v1[2], v1[3])};
      op[i] = q;
    }
  }
  STAMP(24);
  STAMP_FLUSH();
  UNDEF_RA();
  }
  PINS(wv);
  if (wv >= 4) {
    STAMP(0);
    const int hw = wv - 4;
    W1_SETUP();
    const int vy = six ? 4 * (lane >> 2) * (F_ * 2) + (ft0 + 4) * 32 + (lane & 3) * 16 : OOB;
    const u32 ax = ((lane >> 1) & 3) * SLOT + (lane >> 3) * 512 + (hw & 1) * 256 + (lane & 1) * 128 + (lane & 7) * 16;
#define PUT_W1(src, a) do { const u32 a_ = (a); _Pragma("unroll") for (int i = 0; i < 8; ++i) lds_w128((lp_t)(size_t)(a_ ^ (i * 16)), src[i]); } while (0)
    v4u rb[8], y0[8], y1[8];
    const lp_t fall = lptr(SCR + L_F + (lane & 3) * 256 + lane * 4);
#define PUT_X(u, v, b) do { transpose8(u); PUT_W1(u, ax + X_PIECE(b) * 512); transpose8(v); PUT_W1(v, ax + (X_PIECE(b) + 8) * 512); } while (0)
#define LOAD_X(u, v, b) do { LOAD_W1(u, vx, X_PIECE(b)); LOAD_W1(v, vx, X_PIECE(b) + 8); } while (0)
    asm volatile("" :: "v"(cpf));
    STAMP(2);
    flag_wait_lazy(fall, 2);
    LOAD_X(ra, rb, hw >> 1);
#define XM 0
    if (XM == 0) { LOAD_W1(y0, vx, X_PIECE(2) + (hw >> 1) * 8); }
    STAMP(3);
    PUT_X(ra, rb, hw >> 1);
    if (XM == 1) { flag_wait_lazy(fall, 4); LOAD_W1(y0, vx, X_PIECE(2) + (hw >> 1) * 8); }
    transpose8(y0); PUT_W1(y0, ax + (X_PIECE(2) + (hw >> 1) * 8) * 512);
    STAMP(4);
    flag_wait_lazy(fall, 5);
    LOAD_W1(ra, vx, X_PIECE(3) + (hw >> 1) * 8);
    LOAD_W1(y0, vy, Y_PIECE(0));
    transpose8(ra); PUT_W1(ra, ax + (X_PIECE(3) + (hw >> 1) * 8) * 512);
    STAMP(5);
    BAR();
    transpose8(y0);
    const rsrc_t rwd = mkrs(p.wd, D_ * F_ * 2);
    const int vd = (lane >> 3) * (D_ * 2) + (ch & 7) * 128 + ((lane & 7) ^ ((lane >> 2) & 4)) * 16;
    int sd[4];
#pragma unroll
    for (int i = 0; i < 4; ++i) { sd[i] = tile0_of(4 * hw + i) * (16 * D_ * 2); PINS(sd[i]); }
#define DMA_WD(t, slot) do { _Pragma("unroll") for (int i = 0; i < 8; ++i) \
      __builtin_amdgcn_raw_ptr_buffer_load_lds(rwd, (LAS void*)(size_t)((slot) * SLOT + hw * 8192 + i * 1024), 16, vd, sd[i >> 1] + ((t) * 16 + (i & 1) * 8) * (D_ * 2), 0, 0); } while (0)
#define VM_WAIT(n) asm volatile("s_waitcnt vmcnt(" #n ")" ::: "memory")
    BAR();
    LOAD_W1(y1, vy, Y_PIECE(1));
    DMA_WD(0, 0);
    STAMP(6);
    BAR();
    transpose8(y1);
#pragma unroll
    for (int i = 0; i < 8; ++i) {
#pragma unroll
      for (int j = 0; j < 4; ++j) { const u32 a = y0[i][j], b = y1[i][j], as = dppu<0x4e>(a), bs = dppu<0x4e>(b); y0[i][j] = (lane & 2) ? bs : a; y1[i][j] = (lane & 2) ? b : as; }
      PIN(y1[i]);
    }
    const u32 ay = (Y_PIECE((lane >> 1) & 1) + (lane >> 2)) * 512 + (hw & 1) * 256 + (lane & 1) * 144 + (lane & 4) * 8 + (lane & 2) * 32;
    PUT_W1(y0, ay + 4 * SLOT);
    STAMP(7);
    DMA_WD(1, 1);
    BAR();
    DMA_WD(2, 2);
    STAMP(8);
    BAR();
    PUT_W1(y1, ay + 3 * SLOT);
    VM_WAIT(0);
    BAR();
    BAR();
    DMA_WD(3, 4);
    BAR();
    if (hw < 3) DMA_WD(4, 3);
    BAR();
    if (hw < 3) DMA_WD(5, 0);
    if (hw < 3) VM_WAIT(16); else VM_WAIT(0);
    BAR();
    VM_WAIT(0);
    STAMP(9);
    BAR();
    STAMP_FLUSH();
  }

}

extern "C" void kernel_launch(void* const* d_in, const int* in_sizes, int n_in,
                              void* d_out, int out_size, void* d_ws, size_t ws_size,
                              hipStream_t stream) {
  Params p;
  p.x = (const u16*)d_in[0]; p.wr = (const u16*)d_in[1]; p.wi = (const u16*)d_in[2];
  p.wg = (const u16*)d_in[4]; p.wu = (const u16*)d_in[5]; p.wd = (const u16*)d_in[6];
  p.out = (u16*)d_out; p.ws = d_ws;
  p.ws = (char*)d_ws + ((ws_size - (16u << 20)) & ~(size_t)4095);
  (void)hipFuncSetAttribute((const void*)gfnet_geglu_kernel, hipFuncAttributeMaxDynamicSharedMemorySize, 5 * SLOT);
  gfnet_geglu_kernel<<<256, 512, 5 * SLOT, stream>>>(p);
}
```

```cpp
#define TW 275
#define T1 300
#define T2 585
#define D2 7
#define D3 11
#define T3 810
#define SKSL 36
#include <hip/hip_runtime.h>
#include <hip/hip_bf16.h>
#include <type_traits>

typedef unsigned u32;
typedef unsigned short u16;
typedef u32 v4u __attribute__((ext_vector_type(4)));
typedef u32 v2u __attribute__((ext_vector_type(2)));
typedef float f32x2 __attribute__((ext_vector_type(2)));
typedef float f32x4 __attribute__((ext_vector_type(4)));
typedef __bf16 bf16x2 __attribute__((ext_vector_type(2)));
typedef __bf16 bf16x8 __attribute__((ext_vector_type(8)));
typedef short s16x4 __attribute__((ext_vector_type(4)));
#define LAS __attribute__((address_space(3)))
#define DI __device__ __forceinline__
typedef __amdgpu_buffer_rsrc_t rsrc_t;
typedef LAS char* lp_t;

constexpr int S_ = 2048, D_ = 512, F_ = 1376, KF = 257;
constexpr int NFT = 86;
constexpr int WS_E = 0;
constexpr int WS_A = 4096;
constexpr int WS_W = WS_A + (2 << 20);
constexpr int WS_B = WS_W + (2 << 20);
constexpr int WS_L = WS_B + (2 << 20);
constexpr int WS_CA = WS_L + (2 << 20);
constexpr int WS_CB = WS_CA + (1 << 18);
constexpr int WS_S = WS_CB + (1 << 18);
constexpr int WS_H = WS_S + (1 << 17);
constexpr int HREC = 1152;
constexpr int WS_END = WS_H + 64 * NFT * HREC;
constexpr int WS_ST = WS_END;
constexpr u32 NEG2 = 0x80008000u, NEGH = 0x80000000u, NEGL = 0x8000u;
constexpr u32 SPIN_CAP = 1u << 14;
constexpr int AUX_SC1 = 16;
#define NTAUX 2
constexpr int OOB = 0x40000000;
constexpr int CODE_LINES = 240;
constexpr int SLOT = 32768, SCR = 4 * SLOT;
constexpr int L_F = 0, L_CW = 2048, L_TG = 3072, L_SO = 4096;
constexpr int L_T1 = 8192, L_X = 16384, L_I2 = 16384, L_EX = 24576, L_I3 = 8192, L_O3 = 16384;

#define DRAIN() asm volatile("s_waitcnt vmcnt(0)" ::: "memory")
#define MEMFENCE() asm volatile("" ::: "memory")
#define PIN(x) asm volatile("" : "+v"(x))
#define PINS(x) asm volatile("" : "+s"(x))
#define SB() __builtin_amdgcn_sched_barrier(0)
#define BAR() asm volatile("s_waitcnt lgkmcnt(0)\n\ts_barrier" ::: "memory")
#define BAR_N(n) asm volatile("s_waitcnt lgkmcnt(" #n ")\n\ts_barrier" ::: "memory")

DI u32 pk(float a, float b) { f32x2 v = {a, b}; return __builtin_bit_cast(u32, __builtin_convertvector(v, bf16x2)); }
DI v4u pk4(f32x4 a, f32x4 b) { v4u r = {pk(a[0], b[0]), pk(a[1], b[1]), pk(a[2], b[2]), pk(a[3], b[3])}; return r; }
DI float asf(u32 v) { return __builtin_bit_cast(float, v); }
DI u32 asu(float v) { return __builtin_bit_cast(u32, v); }
DI float blo(u32 v) { return asf(v << 16); }
DI float bhi(u32 v) { return asf(v & 0xffff0000u); }
DI float dot2(u32 a, u32 b, float c) { return __builtin_amdgcn_fdot2_f32_bf16(__builtin_bit_cast(bf16x2, a), __builtin_bit_cast(bf16x2, b), c, false); }
DI u32 sum4(u32 m, v4u v) { return (m + v.x + v.y) + v.z + v.w; }
template <int CTRL> DI u32 dppu(u32 v) { return (u32)__builtin_amdgcn_update_dpp(0, (int)v, CTRL, 0xf, 0xf, true); }
template <int CTRL> DI float dppf(float v) { return asf(dppu<CTRL>(asu(v))); }
DI u32 pair_total(u32 s0, u32 s1, bool odd) { return (odd ? s1 : s0) + dppu<0xb1>(odd ? s0 : s1); }
DI u32 add_x16(u32 v) { const auto s = __builtin_amdgcn_permlane16_swap(v, v, false, false); return s[0] + s[1]; }
DI u32 add_x32(u32 v) { const auto s = __builtin_amdgcn_permlane32_swap(v, v, false, false); return s[0] + s[1]; }
template <int N> DI void wave_sums(float (&v)[N]) {
#define WS_STEP(C) _Pragma("unroll") for (int i = 0; i < N; ++i) v[i] += dppf<C>(v[i]);
  WS_STEP(0x111) WS_STEP(0x112) WS_STEP(0x114) WS_STEP(0x118) WS_STEP(0x142) WS_STEP(0x143)
#pragma unroll
  for (int i = 0; i < N; ++i) v[i] = asf(__builtin_amdgcn_readlane(asu(v[i]), 63));
}
DI v4u quad_words(u32 v) { v4u r = {v, dppu<0x55>(v), dppu<0xaa>(v), dppu<0xff>(v)}; return r; }
DI int chk_pos_a(int k) { return (k & 0xd0) | ((k & 8) << 2) | ((k & 7) << 1) | ((k >> 5) & 1); }
DI int chk_pos(int k) { return (k & 0xc7) | ((k & 8) << 2) | ((k >> 1) & 0x18); }
DI f32x4 mm16(v4u a, v4u b, f32x4 c) { return __builtin_amdgcn_mfma_f32_16x16x32_bf16(__builtin_bit_cast(bf16x8, a), __builtin_bit_cast(bf16x8, b), c, 0, 0, 0); }
DI f32x4 mm16p(v4u a, v4u b, f32x4 c) { f32x4 d = mm16(a, b, c); PIN(d); return d; }
template <bool FIRST> DI void mma(f32x4& d, v4u a, v4u b) {
  if constexpr (FIRST) asm volatile("v_mfma_f32_16x16x32_bf16 %0, %1, %2, 0" : "=&v"(d) : "v"(a), "v"(b));
  else asm volatile("v_mfma_f32_16x16x32_bf16 %0, %1, %2, %0" : "+v"(d) : "v"(a), "v"(b));
}
#define MMA_DONE() asm volatile("s_nop 7")
DI v2u lds_tr(lp_t a) { return __builtin_bit_cast(v2u, __builtin_amdgcn_ds_read_tr16_b64_v4i16((LAS s16x4*)a)); }
DI v4u lds_tr2(lp_t a, lp_t b) { v2u x = lds_tr(a), y = lds_tr(b); v4u r = {x.x, x.y, y.x, y.y}; return r; }
DI v4u lds_r128(lp_t a) { return *(LAS v4u*)a; }
DI f32x4 lds_rf(lp_t a) { return *(LAS f32x4*)a; }
DI v2u lds_r64(lp_t a) { return *(LAS v2u*)a; }
DI void lds_w128(lp_t a, v4u v) { *(LAS v4u*)a = v; }
DI void lds_wf(lp_t a, f32x4 v) { *(LAS f32x4*)a = v; }
DI void lds_w64(lp_t a, v2u v) { *(LAS v2u*)a = v; }
DI lp_t lptr(u32 off) { lp_t q = (lp_t)(size_t)off; PIN(q); return q; }
DI rsrc_t mkrs(const void* p, u32 bytes) { return __builtin_amdgcn_make_buffer_rsrc((void*)p, 0, bytes, 0x00020000); }
DI v4u ld_dev(rsrc_t r, int v, int s) { return __builtin_amdgcn_raw_buffer_load_b128(r, v, s, AUX_SC1); }
DI u32 ld_dev1(rsrc_t r, int v, int s) { return __builtin_amdgcn_raw_buffer_load_b32(r, v, s, AUX_SC1); }
DI void st_dev(rsrc_t r, int v, v4u x) { __builtin_amdgcn_raw_buffer_store_b128(x, r, v, 0, AUX_SC1); }
DI void st_dev1(rsrc_t r, int v, u32 x) { __builtin_amdgcn_raw_buffer_store_b32(x, r, v, 0, AUX_SC1); }
DI v4u ld_in(rsrc_t r, int v, int s) { return __builtin_amdgcn_raw_buffer_load_b128(r, v, s, 0); }
DI v4u ld_once(rsrc_t r, int v, int s) { return __builtin_amdgcn_raw_buffer_load_b128(r, v, s, NTAUX); }
DI u32 touch(rsrc_t r, int line) { return __builtin_amdgcn_raw_buffer_load_b32(r, line * 128, 0, 0); }
DI void flag_set(lp_t mine, u32 k) { MEMFENCE(); *(volatile LAS u32*)mine = k; MEMFENCE(); }
#define FLAG_WAIT(theirs, k, ...) do { u32 f_; _Pragma("nounroll") do { f_ = *(volatile LAS u32*)(theirs); MEMFENCE(); __VA_ARGS__; MEMFENCE(); } while (!__all(f_ >= (k))); } while (0)
DI void flag_wait_lazy(lp_t theirs, u32 k) { while (!__all(*(volatile LAS u32*)theirs >= k)) __builtin_amdgcn_s_sleep(1); MEMFENCE(); }
DI float cosr(float rev) { return __builtin_amdgcn_cosf(rev); }
DI float sinr(float rev) { return __builtin_amdgcn_sinf(rev); }
__host__ __device__ constexpr int slot_of(int k1, int k2) { return ((k2 < 8 ? k1 + 32 * k2 : (k1 ? 32 - k1 : 16) + 32 * (15 - k2)) - 1) & 255; }
DI int tile0_of(int c) { const int h = c >> 3, i = c & 7; return 44 * h + 4 * i + 2 * (i < 6 - h ? i : 6 - h); }
template <int I, int N, class F> DI void static_for(F&& f) {
  if constexpr (I < N) { f(std::integral_constant<int, I>{}); static_for<I + 1, N>(f); }
}
DI void transpose8(v4u (&m)[8]) {
  v4u o[8];
#pragma unroll
  for (int i = 0; i < 8; ++i)
#pragma unroll
    for (int j = 0; j < 4; ++j) { const u32 a = m[2 * j][i >> 1], b = m[2 * j + 1][i >> 1]; o[i][j] = __builtin_amdgcn_perm(b, a, (i & 1) ? 0x07060302u : 0x05040100u); }
#pragma unroll
  for (int i = 0; i < 8; ++i) { m[i] = o[i]; PIN(m[i]); }
}
enum { E_SQ, E_POLY, E_ARG, E_EXP, E_INC, E_RCP, E_GU, E_OUT, E_PACK, E_SWA, E_SWB, E_SUM, E_SUM2, E_ST, E_X1, E_X2, E_X8, E_CHK, E_STK };
struct EOp { int kind, v; };
struct EProg { EOp op[520]; int n, first[7]; };
constexpr EProg make_eprog() {
  EProg p = {};
  for (int t = 0; t < 6; ++t) {
    p.first[t] = p.n;
    for (int v = 0; v <= 8; ++v) {
      const int V = 8 * t + v;
      const bool head = v < 8, tail = v > 0, fin = t > 0 && t < 5;
      if (head) p.op[p.n++] = {E_SQ, V};
      if (tail) p.op[p.n++] = {E_INC, V - 1};
      if (head) p.op[p.n++] = {E_POLY, V}; else p.op[p.n++] = {E_GU, V - 1};
      if (tail) p.op[p.n++] = {E_RCP, V - 1}; else if (fin) p.op[p.n++] = {E_X1, V - 1};
      if (head) p.op[p.n++] = {E_ARG, V};
      if (tail && head) p.op[p.n++] = {E_GU, V - 1};
      if (v >= 3 && (v & 1)) p.op[p.n++] = {E_PACK, V - 2};
      if (v == 8) p.op[p.n++] = {E_SWA, V - 1};
      if (fin && v == 0) p.op[p.n++] = {E_X2, V - 1};
      if (fin && v == 1) p.op[p.n++] = {E_CHK, V - 2};
      if (head) p.op[p.n++] = {E_EXP, V};
      if (fin && v == 0) p.op[p.n++] = {E_X8, V - 1};
      if (fin && v == 1) p.op[p.n++] = {E_STK, V - 2};
      if (tail) p.op[p.n++] = {E_OUT, V - 1};
    }
    p.op[p.n++] = {E_PACK, 8 * t + 7}; p.op[p.n++] = {E_SUM, 8 * t + 7}; p.op[p.n++] = {E_SWB, 8 * t + 7}; p.op[p.n++] = {E_ST, 8 * t + 7}; p.op[p.n++] = {E_SUM2, 8 * t + 7};
    if (t >= 4) for (int k = E_X1; k <= E_STK; ++k) p.op[p.n++] = {k, 8 * t + 7};
  }
  p.first[6] = p.n;
  return p;
}
constexpr EProg kEp = make_eprog();
#define ER1 56
constexpr int epos(int n) {
  const int mid = (64 * ER1) / 16, end = kEp.first[5];
  int j = n <= 64 ? 0 : n <= 128 ? ((n - 64) * ER1) / 16 : n <= 192 ? mid + ((end - mid) * (n - 128)) / 64
        : end + ((kEp.n - end) * (n - 192)) / 8;
  for (int t = 2; t < 5; ++t) if (n <= 32 * t + 34 && j > kEp.first[t]) j = kEp.first[t];
  return j;
}
struct Epi { float t[2], q, h[3]; v4u r, k4; u32 sum, fin; };
template <int KIND, int V> DI void epi_op(Epi& e, const f32x4 (&a)[4], u32 tag) {
  constexpr int v = V & 7;
  const float gv = a[v >> 2][v & 3], uv = a[2 + (v >> 2)][v & 3];
  float& t = e.t[V & 1];
  if constexpr (KIND == E_SQ) { t = gv * gv; PIN(t); }
  else if constexpr (KIND == E_POLY) { t = __builtin_fmaf(t, -0.10294324f, -2.3022082f); PIN(t); }
  else if constexpr (KIND == E_ARG) { t = t * gv; PIN(t); }
  else if constexpr (KIND == E_EXP) { t = __builtin_amdgcn_exp2f(t); PIN(t); }
  else if constexpr (KIND == E_INC) { t = t + 1.0f; PIN(t); }
  else if constexpr (KIND == E_RCP) { t = __builtin_amdgcn_rcpf(t); PIN(t); }
  else if constexpr (KIND == E_GU) { e.q = gv * uv; PIN(e.q); }
  else if constexpr (KIND == E_OUT) { e.h[v & 1] = e.q * t; PIN(e.h[v & 1]); }
  else if constexpr (KIND == E_PACK) { u32 r = pk(e.h[0], e.h[1]); PIN(r); e.r[v >> 1] = r; }
  else if constexpr (KIND == E_SWA || KIND == E_SWB) { constexpr int i = KIND == E_SWB; const u32 x = e.r[i], y = e.r[i + 2];
    const auto q = __builtin_amdgcn_permlane16_swap(x, y, false, false); u32 q0 = q[0], q1 = q[1]; PIN(q0); PIN(q1); e.r[i] = q0; e.r[i + 2] = q1; }
  else if constexpr (KIND == E_SUM) { e.sum = e.r.x + e.r.z; PIN(e.sum); }
  else if constexpr (KIND == E_SUM2) { e.fin = e.sum + e.r.y + e.r.w; PIN(e.fin); }
  else if constexpr (KIND == E_X1) { e.fin += dppu<0xb1>(e.fin); PIN(e.fin); }
  else if constexpr (KIND == E_X2) { e.fin += dppu<0x4e>(e.fin); PIN(e.fin); }
  else if constexpr (KIND == E_X8) { e.fin += dppu<0x128>(e.fin); PIN(e.fin); }
  else if constexpr (KIND == E_CHK) { asm volatile("" : "=v"(e.k4)); e.k4.x = tag - e.fin; PIN(e.k4); }
}

constexpr double ct_sin(long n, long d) {
  n %= d; if (n < 0) n += d;
  if (2 * n > d) return -ct_sin(d - n, d);
  if (4 * n > d) return ct_sin(d - 2 * n, 2 * d);
  const double x = 6.283185307179586476925 * (double)n / (double)d;
  double t = x, r = x;
  for (int i = 1; i < 14; ++i) { t *= -x * x / ((2 * i) * (2 * i + 1)); r += t; }
  return r;
}
constexpr double ct_cos(long n, long d) { return ct_sin(4 * n + d, 4 * d); }
constexpr u32 ct_f32(double v) { return __builtin_bit_cast(u32, (float)v); }
constexpr u32 ct_bf(double v) { const u32 u = ct_f32(v); return (u + 0x7fffu + ((u >> 16) & 1u)) >> 16; }
constexpr u32 ct_pk(double a, double b) { return ct_bf(a) | (ct_bf(b) << 16); }
struct Tab { u32 v[6][64][4]; };
constexpr Tab make_tab() {
  Tab t = {};
  for (int l = 0; l < 64; ++l) {
    const int c = l & 15, g = l >> 4;
    for (int j = 0; j < 2; ++j) {
      const int a = 4 * g + 2 * j;
      t.v[0][l][j] = ct_pk(ct_cos(a * c, 32), ct_cos((a + 1) * c, 32));
      t.v[0][l][2 + j] = c ? ct_pk(-ct_sin(a * c, 32), -ct_sin((a + 1) * c, 32)) : ct_pk(1, -1);
    }
    for (int r = 0; r < 4; ++r) {
      const int b = 4 * g + r, sl = slot_of(b, c);
      t.v[1][l][r] = ct_f32(ct_cos(b * c, 512));
      t.v[2][l][r] = ct_f32(ct_sin(b * c, 512));
      t.v[3][l][r] = ct_pk(ct_cos(b * c, 16), ct_sin(b * c, 16));
      t.v[4][l][r] = c == 0 ? ct_pk((r & 1) ? -1 : 1, 0) : c < 8 ? ct_pk(ct_cos(b * c, 16), -ct_sin(b * c, 16)) : ct_pk(ct_cos(b * (31 - 2 * c), 32), -ct_sin(b * (31 - 2 * c), 32));
      t.v[5][l][r] = SCR + L_T1 + ((sl * 4) ^ ((sl >> 4) & 14)) * 8;
    }
  }
  return t;
}
static_assert(true, "");
static __device__ const __attribute__((aligned(128))) Tab g_tab = make_tab();

#define STAMP(i) do { } while (0)
#define STAMP_DECL do { } while (0)
#define STAMP_FLUSH() do { } while (0)
#define COUNT(i, v) do { } while (0)

#define FSTAMP(i) do { } while (0)
#define PACE(d, t) do { __builtin_amdgcn_s_sleep(d); while ((u32)__builtin_amdgcn_s_memrealtime() - (u32)t_entry < (t)) __builtin_amdgcn_s_sleep(1); } while (0)
#define D1 1
#define WAIT1() PACE(D1, T1)
#define WAIT2() PACE(D2, T2)
#define WAIT3() PACE(D3, T3)
struct Params {
  const u16* x; const u16* wr; const u16* wi; const u16* wg; const u16* wu; const u16* wd; u16* out; void* ws;
};

__global__ void __launch_bounds__(512, 2) gfnet_geglu_kernel(Params p) {
  const int wg = blockIdx.x, tid = threadIdx.x, lane = tid & 63;
  int wv = __builtin_amdgcn_readfirstlane(tid >> 6);
  STAMP_DECL;
  unsigned long long t_entry;
  asm volatile("s_memrealtime %0" : "=s"(t_entry));
  const int rg = 4 * ((wg & 7) >> 1) + (wg >> 6), ch = 8 * (wg & 1) + ((wg >> 3) & 7);
  const int un = 16 * rg + ch;
  const int ft0 = tile0_of(ch);
  const bool six = (ch & 7) < 6 - (ch >> 3);
#define W1SZ (D_ * F_ * 2)
#define WD_SETUP() const rsrc_t rwd = mkrs(p.wd, D_ * F_ * 2); \
    int vd = (lane >> 3) * (D_ * 2) + (ch & 7) * 128 + ((lane & 7) ^ ((lane >> 3) & 2)) * 16, vd1 = vd ^ 64, sd[4]; PIN(vd); PIN(vd1); \
    _Pragma("unroll") for (int i = 0; i < 4; ++i) { sd[i] = tile0_of(4 * (wv & 3) + i) * (16 * D_ * 2); PINS(sd[i]); }
#define DMA_WD1(t, slot, i) asm volatile("s_mov_b32 m0, %0\n\ts_nop 0\n\tbuffer_load_dwordx4 %1, %2, %3 offen lds" \
      :: "s"((slot) * SLOT + (wv & 3) * 8192 + (i) * 1024), "v"(((i) & 1) ? vd1 : vd), "s"(rwd), "s"(sd[(i) >> 1] + ((t) * 16 + ((i) & 1) * 8) * (D_ * 2)) : "memory")
#define TH2 4
#define TG 4
#define THROTTLE(i) do { if ((i) % TG == 0) asm volatile("s_waitcnt vmcnt(%0)" :: "n"(TH2) : "memory"); } while (0)
#define DMA_WD(t, slot) do { _Pragma("unroll") for (int i = 0; i < 8; ++i) { THROTTLE(i); DMA_WD1(t, slot, i); } } while (0)
#define DMA_WDS(t, slot) do { _Pragma("unroll") for (int i = 0; i < 4; ++i) DMA_WD1(t, slot, i); if (!(wv & 1)) { _Pragma("unroll") for (int i = 4; i < 8; ++i) DMA_WD1(t, slot, i); } } while (0)
#define VM_WAIT(n) asm volatile("s_waitcnt vmcnt(" #n ")" ::: "memory")
#define PDN 4
  constexpr int PD = PDN;
#define FRAG(N) frag(std::integral_constant<int, (N)>{})
#define NXT(n) (((n) + PD) ^ 1)
  v4u ra[8];
  u32 cpf;
#define UNDEF_RA() do { asm volatile("" : "=v"(cpf)); _Pragma("unroll") for (int i = 0; i < 8; ++i) asm volatile("" : "=v"(ra[i])); } while (0)
  UNDEF_RA();
  if (wv < 4) {
    const int vt = lane * 16;
    int sx = (32 * (8 * (un >> 5) + 2 * wv) + (un & 31)) * (D_ * 2), sy = sx + 32 * D_ * 2;
    const rsrc_t rt = mkrs(&g_tab, sizeof(Tab));
    ra[2] = ld_in(rt, vt, 0); ra[3] = ld_in(rt, vt + 1024, 0);
    PINS(sx); PINS(sy);
    SB();
    const rsrc_t rx = mkrs(p.x, S_ * D_ * 2);
    ra[0] = ld_once(rx, vt, sx); ra[1] = ld_once(rx, vt, sy);
    SB();
#pragma unroll
    for (int i = 0; i < 4; ++i) ra[4 + i] = ld_in(rt, vt + i * 1024, 2048);
    cpf = ld_dev1(mkrs(p.ws, WS_END), 0, WS_E);
    STAMP(0);
  }
  PINS(wv);
  if (wv >= 6) {
    unsigned long long pc0;
    asm volatile("s_getpc_b64 %0" : "=s"(pc0));
    cpf = touch(mkrs((const void*)(pc0 & ~127ull), CODE_LINES * 128), lane < 8 ? (((wg >> 3) ^ (wv & 1)) * 8 + lane) : 4096);
#define X0SLEEP 14
    __builtin_amdgcn_s_sleep(X0SLEEP);
    const rsrc_t r1 = mkrs((wv & 1) ? p.wu : p.wg, W1SZ);
    ra[0].x = touch(r1, lane < 43 ? wg * 43 + lane : 16384);
  }
  PINS(wv);
  if ((wv >> 1) == 2) {
#define WSLEEP 8
    __builtin_amdgcn_s_sleep(WSLEEP);
    const rsrc_t rwr = mkrs(p.wr, S_ * KF * 2), rwi = mkrs(p.wi, S_ * KF * 2);
    const int vf = (lane & 1) * (KF * 2 + 2) + ((tid & 127) >> 1) * 8;
    const int sf = (16 * (un >> 6) + 2 * (un & 7) + 256 * ((un >> 3) & 7)) * (KF * 2);
#pragma unroll
    for (int r = 0; r < 4; ++r) { ra[r] = ld_once(rwr, vf, sf + r * (64 * KF * 2)); ra[4 + r] = ld_once(rwi, vf, sf + r * (64 * KF * 2)); }
  }
  const lp_t fmine = lptr(SCR + L_F + tid * 4);
  *(volatile LAS u32*)fmine = 0;
  BAR();
  PINS(t_entry);
  PINS(wv);
  if ((wv >> 1) == 2) {
    const rsrc_t ws = mkrs(p.ws, WS_END);
    const int T = tid - 256, m = T >> 1, vs = ((m & 0x39) | ((m & 2) << 1) | ((m & 4) >> 1)) * 32 + (T & 1) * 16 + WS_W + un * 8192;
    const u32 sh = (lane & 1) ? 0u : 16u;
    u32 al[8][2], ck[4];
#pragma unroll
    for (int i = 0; i < 8; ++i) { const u32 a = ra[i].x, b = ra[i].y, d = ra[i].z; al[i][0] = __builtin_amdgcn_alignbit(b, a, sh); al[i][1] = __builtin_amdgcn_alignbit(d, b, sh); }
    v4u v[4];
#pragma unroll
    for (int e = 0; e < 4; ++e) {
#pragma unroll
      for (int r = 0; r < 4; ++r) v[e][r] = __builtin_amdgcn_perm(al[4 + r][e >> 1], al[r][e >> 1], (e & 1) ? 0x07060302u : 0x05040100u);
      ck[e] = sum4(0u, v[e]);
    }
    v2u cw = {pair_total(ck[0], ck[2], T & 1), pair_total(ck[1], ck[3], T & 1)};
    lds_w64(lptr(SCR + L_CW + T * 8), cw);
    flag_set(fmine, 1);
#pragma unroll
    for (int e = 0; e < 4; ++e) st_dev(ws, vs + e * 2048, v[e]);
    UNDEF_RA();
    STAMP(1);
  }
  SB();
  PINS(wv);

  if (wv < 4) {
  const int w = wv, c = lane & 15, g = lane >> 4;
  const float fc = (float)c, fg4 = (float)(4 * g);
  const f32x4 z = {0.f, 0.f, 0.f, 0.f};
  f32x4 twc, tws;
  v4u fbre, fbsw;
  u32 tag;
  float mean[2];
  const rsrc_t ws = mkrs(p.ws, WS_END);
  const lp_t fall = lptr(SCR + L_F + (lane & 3) * 256 + lane * 4);
  v2u xk[2][2];
  f32x4 tfc[2], tfs[2], tic[2], tis[2];
  const float fj = (float)(16 * w) + fc;
#pragma unroll
  for (int r = 0; r < 4; ++r) {
#pragma unroll
      for (int m = 0; m < 2; ++m) {
        const float nf = (fg4 + (16 * m + r)) * (fj * (1.f / 2048)), ni = (fg4 + (float)(16 * w) + r) * ((fc + 16 * m) * (1.f / 2048));
        tfc[m][r] = cosr(nf); tfs[m][r] = sinr(nf); tic[m][r] = cosr(ni); tis[m][r] = sinr(ni);
      }
  }
#pragma unroll
  for (int i = 0; i < 2; ++i) { PIN(tfc[i]); PIN(tfs[i]); PIN(tic[i]); PIN(tis[i]); }
  SB();
  {
    const v4u xr[2] = {ra[0], ra[1]}, tb[6] = {ra[2], ra[3], ra[4], ra[5], ra[6], ra[7]};
    tag = cpf;
    const u32 sc = (c & 1) ? NEG2 : 0u, sm = c < 8 ? NEGL : NEGH, p0 = c < 8 ? 0x0c0c0100u : 0x01000c0cu, p1 = c < 8 ? 0x0c0c0302u : 0x03020c0cu;
    v4u fare = {tb[0].x, tb[0].y, tb[0].x ^ sc, tb[0].y ^ sc}, faim = {tb[0].z, tb[0].w, tb[0].z ^ sc, tb[0].w ^ sc}, fbim, f0re, f0im;
    twc = __builtin_bit_cast(f32x4, tb[1]); tws = __builtin_bit_cast(f32x4, tb[2]); fbre = tb[3];
    lp_t t1w[4], t1r[2];
#pragma unroll
    for (int r = 0; r < 4; ++r) {
      const u32 a = tb[3][r], q = tb[4][r];
      fbsw[r] = __builtin_amdgcn_alignbit(a, a, 16);
      fbim[r] = fbsw[r] ^ sm;
      f0re[r] = __builtin_amdgcn_perm(q, q, p0); f0im[r] = __builtin_amdgcn_perm(q, q, p1);
      t1w[r] = lptr(tb[5][r] ^ (w * 8));
    }
#pragma unroll
    for (int i = 0; i < 2; ++i) t1r[i] = lptr(SCR + L_T1 + ((w * 128 + i * 64 + lane) ^ (2 * w + i)) * 16);
    const int sl = 64 * w + 32 * (lane & 1) + (lane >> 1);
    lp_t cwr = lptr(SCR + L_CW + sl * 4);
    int vc = (lane & 3) ? OOB : chk_pos_a(sl) * 4 + WS_CA + un * 1024, vs = lane * 16 + WS_A + un * 8192 + w * 2048;
    float c0m = c == 0 ? 1.f / 16 : 0.f;
    lp_t xw = lptr(SCR + L_X + w * 2048 + lane * 16), xt = lptr(SCR + L_X + w * 2048 + lane * 8);
    lp_t xq = lptr(SCR + L_X + w * 2048 + (4 * c + g) * 8), f6 = lptr(SCR + L_F + (lane % 6) * 256 + lane * 4);
    PIN(vc); PIN(vs); PIN(c0m);
    PIN(fare); PIN(faim); PIN(fbre); PIN(fbim); PIN(f0re); PIN(f0im); PIN(twc); PIN(tws);
    STAMP(1);
    SB();
    v4u a[2];
#pragma unroll
    for (int rr = 0; rr < 2; ++rr) { lds_w128(xw + rr * 1024, xr[rr]); a[rr] = lds_tr2(xt + rr * 1024, xt + rr * 1024 + 512); }
    STAMP(2);
    float st[4] = {0.f, 0.f, 0.f, 0.f};
#pragma unroll
    for (int i = 0; i < 4; ++i)
#pragma unroll
      for (int rr = 0; rr < 2; ++rr) { const u32 v = xr[rr][i]; st[rr] = dot2(v, 0x3f803f80u, st[rr]); st[2 + rr] = dot2(v, v, st[2 + rr]); }
    SB();
    f32x4 ur[2], ui[2];
#pragma unroll
    for (int rr = 0; rr < 2; ++rr) { ur[rr] = mm16p(a[rr], fare, z); ui[rr] = mm16p(a[rr], faim, z); }
    wave_sums<4>(st);
    SB();
    v4u a2[2];
    f32x4 orr[2], oi[2], sr[2], si[2];
#pragma unroll
    for (int rr = 0; rr < 2; ++rr) {
      const f32x4 x = ur[rr] - st[rr] * c0m, y = ui[rr];
      a2[rr] = pk4(x * twc + y * tws, y * twc - x * tws);
      orr[rr] = mm16p(a2[rr], fbre, z); oi[rr] = mm16p(a2[rr], fbim, z); sr[rr] = mm16p(a2[rr], f0re, z); si[rr] = mm16p(a2[rr], f0im, z);
    }
    v4u o[2];
#pragma unroll
    for (int rr = 0; rr < 2; ++rr) {
      mean[rr] = st[rr] * (1.f / 512);
      const float rstd = __builtin_amdgcn_rsqf(fmaxf(st[2 + rr] * (1.f / 512) - mean[rr] * mean[rr], 0.f) + 1e-5f);
      orr[rr][0] = g == 0 ? sr[rr][0] : orr[rr][0]; oi[rr][0] = g == 0 ? si[rr][0] : oi[rr][0];
      o[rr] = pk4(orr[rr] * rstd, oi[rr] * rstd);
    }
#pragma unroll
    for (int r = 0; r < 4; ++r) { v2u v = {o[0][r], o[1][r]}; lds_w64(t1w[r], v); }
    tag += 1u;
    PIN(tag);
    STAMP(3);
    *(LAS u32*)(fmine + L_TG) = tag;
    flag_set(fmine, 1);
#pragma unroll
    for (int i = 0; i < 4; ++i) xk[i >> 1][i & 1] = lds_r64(xq + (i >> 1) * 1024 + (i & 1) * 512);
    v4u v0, v1;
    u32 cw;
    FLAG_WAIT(f6, 1, v0 = lds_r128(t1r[0]); v1 = lds_r128(t1r[1]); cw = *(LAS u32*)cwr);
    st_dev(ws, vs, v0);
    st_dev(ws, vs + 1024, v1);
    st_dev(ws, vc, quad_words(tag - cw - pair_total(sum4(0u, v0), sum4(0u, v1), lane & 1)));
  }
  int vg = (lane >> 1) * 8192 + (lane & 1) * 16, vk = (32 * (lane & 1) + (lane >> 1)) * 1024;
  const int sg = w * (64 * 8192) + un * 32, sk = w * (64 * 1024) + chk_pos(un) * 4, ska = w * (64 * 1024) + chk_pos_a(un) * 4;
  const int sw = WS_W + w * (64 * 8192) + ((un & 3) * 64 + ((((un >> 2) & 0x39) | ((un >> 1) & 4) | ((un >> 3) & 2)))) * 32;
  v4u wq[2];
  STAMP(4);
  SB();
  v4u f2a[2][2];
  {
    v4u f1a[4], f1b[4], f2b[2][2];
    const u32 sc = (c & 1) ? NEG2 : 0u;
#pragma unroll
    for (int r = 0; r < 4; ++r) {
#pragma unroll
      for (int ks = 0; ks < 2; ++ks) {
        const float n = (fg4 + (16 * ks + r)) * (fj * (1.f / 64));
        const float cs = cosr(n), sn = sinr(n);
        f1a[ks][r] = pk(cs, sn); f1b[ks][r] = pk(-sn, cs);
        f1a[ks + 2][r] = f1a[ks][r] ^ sc; f1b[ks + 2][r] = f1b[ks][r] ^ sc;
      }
      const float n = (fg4 + r) * (fc * (1.f / 32));
      const float cs = cosr(n), sn = sinr(n);
      const u32 a = pk(cs, sn), b = pk(-sn, cs), sr = (r & 1) ? NEG2 : 0u;
      f2a[0][0][r] = a; f2a[1][0][r] = a ^ sr; f2a[0][1][r] = a ^ sc; f2a[1][1][r] = a ^ sc ^ sr;
      f2b[0][0][r] = b; f2b[1][0][r] = b ^ sr; f2b[0][1][r] = b ^ sc; f2b[1][1][r] = b ^ sc ^ sr;
    }
#pragma unroll
    for (int i = 0; i < 4; ++i) { PIN(f1a[i]); PIN(f1b[i]); PIN(f2a[i >> 1][i & 1]); PIN(f2b[i >> 1][i & 1]); }
    lp_t i2w = lptr(SCR + L_I2 + w * 2048 + lane * 16), i2r = lptr(SCR + L_I2 + ((g >> 1) * 32 + c) * 32 + (g & 1) * 16);
    lp_t exw = lptr(SCR + L_EX + w * 2048 + lane * 16), exr = lptr(SCR + L_EX + lane * 16);
    int vb = ((g >> 1) * 32 + c) * 32 + (g & 1) * 16 + WS_B + un * 8192 + w * 2048, vc = (lane & 3) ? OOB : chk_pos(lane) * 4 + WS_CB + un * 1024 + w * 256;
    PIN(vg); PIN(vk); PIN(vb); PIN(vc);
    STAMP(5);
    PACE(0, TW);
    wq[0] = ld_dev(ws, vg, sw); wq[1] = ld_dev(ws, vg, sw + 32 * 8192);
    WAIT1();
    STAMP(6);
    v4u xg[2];
    f32x4 wr[2], wi[2];
    xg[0] = ld_dev(ws, vg, WS_A + sg); xg[1] = ld_dev(ws, vg, WS_A + sg + 32 * 8192);
    u32 kw = ld_dev1(ws, vk, WS_CA + ska);
    flag_set(fmine, 2);
    SB();
#define W_UNPACK() _Pragma("unroll") for (int m = 0; m < 2; ++m) { _Pragma("unroll") for (int r = 0; r < 4; ++r) { const u32 v = wq[m][r]; wr[m][r] = blo(v); wi[m][r] = bhi(v); } PIN(wr[m]); PIN(wi[m]); }
    u32 s0 = sum4(0u, wq[0]), s1 = sum4(0u, wq[1]), spin = 0;
    W_UNPACK();
    PIN(s0); PIN(s1);
    SB();
    lds_w128(i2w, xg[0]); lds_w128(i2w + 1024, xg[1]);
    u32 bad = pair_total(sum4(s0, xg[0]), sum4(s1, xg[1]), lane & 1) + kw - tag;
    if (__any(bad != 0)) {
      _Pragma("nounroll") do {
        bad |= dppu<0xb1>(bad);
        MEMFENCE();
        if (bad) {
          xg[0] = ld_dev(ws, vg, WS_A + sg); xg[1] = ld_dev(ws, vg, WS_A + sg + 32 * 8192); kw = ld_dev1(ws, vk, WS_CA + ska);
          wq[0] = ld_dev(ws, vg, sw); wq[1] = ld_dev(ws, vg, sw + 32 * 8192);
        }
        bad = pair_total(sum4(sum4(0u, wq[0]), xg[0]), sum4(sum4(0u, wq[1]), xg[1]), lane & 1) + kw - tag;
      } while (__any(bad != 0) && ++spin < SPIN_CAP);
      lds_w128(i2w, xg[0]); lds_w128(i2w + 1024, xg[1]);
      W_UNPACK();
      ++spin;
    }
    COUNT(20, spin);
    STAMP(7);
    v4u af[2][4], b2[2], a3[2];
    flag_set(fmine, 3);
    FLAG_WAIT(fall, 3, _Pragma("unroll") for (int i = 0; i < 8; ++i) af[i >> 2][i & 3] = lds_r128(i2r + (i & 3) * 2048 + (i >> 2) * 512));
    STAMP(8);
    SB();
    f32x4 dr[2], di[2];
#pragma unroll
    for (int m = 0; m < 2; ++m)
#pragma unroll
      for (int ks = 0; ks < 4; ++ks) { dr[m] = mm16(af[m][ks], f1a[ks], ks ? dr[m] : z); di[m] = mm16(af[m][ks], f1b[ks], ks ? di[m] : z); }
#pragma unroll
    for (int m = 0; m < 2; ++m) b2[m] = pk4(dr[m] * tfc[m] + di[m] * tfs[m], di[m] * tfc[m] - dr[m] * tfs[m]);
#pragma unroll
    for (int k = 0; k < 2; ++k)
#pragma unroll
      for (int m = 0; m < 2; ++m) { dr[m] = mm16(f2a[m][k], b2[k], k ? dr[m] : z); di[m] = mm16(f2b[m][k], b2[k], k ? di[m] : z); }
#pragma unroll
    for (int m = 0; m < 2; ++m) a3[m] = pk4(dr[m] * wi[m] + di[m] * wr[m], dr[m] * wr[m] - di[m] * wi[m]);
#pragma unroll
    for (int k = 0; k < 2; ++k)
#pragma unroll
      for (int n = 0; n < 2; ++n) { di[n] = mm16(a3[k], f2a[n][k], k ? di[n] : z); dr[n] = mm16(a3[k], f2b[n][k], k ? dr[n] : z); }
#pragma unroll
    for (int n = 0; n < 2; ++n) lds_w128(exw + n * 1024, pk4(di[n] * tic[n] + dr[n] * tis[n], dr[n] * tic[n] - di[n] * tis[n]));
    STAMP(9);
    flag_set(fmine, 4);
    FLAG_WAIT(fall, 4, _Pragma("unroll") for (int i = 0; i < 8; ++i) af[i >> 2][i & 3] = lds_r128(exr + (i & 3) * 2048 + (i >> 2) * 1024));
    SB();
    u32 cs[2];
#pragma unroll
    for (int n = 0; n < 2; ++n)
#pragma unroll
      for (int ks = 0; ks < 4; ++ks) { di[n] = mm16(f1a[ks], af[n][ks], ks ? di[n] : z); dr[n] = mm16(f1b[ks], af[n][ks], ks ? dr[n] : z); }
#pragma unroll
    for (int n = 0; n < 2; ++n) {
      const v4u e = pk4(dr[n], di[n]);
      st_dev(ws, vb + n * 512, e);
      cs[n] = sum4(0u, e);
    }
    const auto sx = __builtin_amdgcn_permlane16_swap(cs[0], cs[1], false, false);
    st_dev(ws, vc, quad_words(tag - sx[0] - sx[1]));
    if (wg == 0 && w == 0) st_dev1(ws, WS_E, tag);
  }
  STAMP(10);
  SB();
  {
    v4u gbre, gbim, g0re, g0im, ga[2];
#pragma unroll
    for (int r = 0; r < 4; ++r) {
      gbre[r] = fbre[r] ^ (g < 2 ? NEGH : 0u);
      gbim[r] = fbsw[r] ^ (g < 2 ? 0u : NEGH);
      const float n = fc * ((31.f - 2.f * r - 2.f * fg4) * (1.f / 32));
      u32 q = pk(cosr(n), -sinr(n));
      PIN(q);
      g0re[r] = g < 2 ? gbre[r] : 0u;
      g0im[r] = g < 2 ? 0u : q;
      ga[0][r] = f2a[0][0][r] ^ NEGH; ga[1][r] = f2a[1][0][r] ^ NEGH;
    }
    twc *= 2.f / (2048.f * 512.f); tws *= 2.f / (2048.f * 512.f);
    g0re[0] = g == 0 ? ((c & 1) ? 0xbf00u : 0x3f00u) : g0re[0];
    ga[0][0] = g == 0 ? ((c & 1) ? 0xbf803f80u : 0x3f803f80u) : ga[0][0]; ga[1][0] = ga[0][0];
    PIN(gbre); PIN(gbim); PIN(g0re); PIN(g0im); PIN(ga[0]); PIN(ga[1]); PIN(twc); PIN(tws);
    f32x4 xin[2][2];
#pragma unroll
    for (int rr = 0; rr < 2; ++rr)
#pragma unroll
      for (int t = 0; t < 2; ++t) {
        const v2u v = xk[rr][t];
        const u32 v0 = v.x, v1 = v.y;
        f32x4 q = {blo(v0), bhi(v0), blo(v1), bhi(v1)};
        xin[rr][t] = q - mean[rr]; PIN(xin[rr][t]);
      }
    lp_t i3w = lptr(SCR + L_I3 + w * 2048 + lane * 16), i3r[4], o3w[2], o3r[2];
#pragma unroll
    for (int r = 0; r < 4; ++r) i3r[r] = lptr(SCR + L_I3 + slot_of(c, 4 * g + r) * 32 + w * 8);
#pragma unroll
    for (int i = 0; i < 2; ++i) {
      o3w[i] = lptr(SCR + L_O3 + ((4 * c + g) * 8 + ((2 * w + i) ^ (c & 7))) * 16);
      const int q = w * 128 + i * 64 + lane;
      o3r[i] = lptr(SCR + L_O3 + (q ^ ((q >> 5) & 7)) * 16);
    }
    int vs = lane * 16 + WS_L + un * 8192 + w * 2048, vt = lane < 8 ? lane * 16 + WS_S + un * 512 + w * 128 : OOB;
    PIN(vs); PIN(vt);
    lp_t sow = lptr(SCR + L_SO + tid * 16), sor = lptr(SCR + L_SO + (lane & 6) * 512 + (lane & 1) * 8);
    WAIT2();
    STAMP(11);
    v4u x0 = ld_dev(ws, vg, WS_B + sg), x1 = ld_dev(ws, vg, WS_B + sg + 32 * 8192);
    u32 kw = ld_dev1(ws, vk, WS_CB + sk), spin = 0;
    flag_set(fmine, 5);
    SB();
    lds_w128(i3w, x0); lds_w128(i3w + 1024, x1);
    u32 bad = pair_total(sum4(0u, x0), sum4(0u, x1), lane & 1) + kw - tag;
    if (__any(bad != 0)) {
      _Pragma("nounroll") do {
        bad |= dppu<0xb1>(bad);
        MEMFENCE();
        if (bad) { x0 = ld_dev(ws, vg, WS_B + sg); x1 = ld_dev(ws, vg, WS_B + sg + 32 * 8192); kw = ld_dev1(ws, vk, WS_CB + sk); }
        bad = pair_total(sum4(0u, x0), sum4(0u, x1), lane & 1) + kw - tag;
      } while (__any(bad != 0) && ++spin < SPIN_CAP);
      lds_w128(i3w, x0); lds_w128(i3w + 1024, x1);
      ++spin;
    }
    COUNT(21, spin);
    STAMP(12);
    v4u a[2], a2[2];
    v2u zr[4];
    flag_set(fmine, 6);
    FLAG_WAIT(fall, 6, _Pragma("unroll") for (int r = 0; r < 4; ++r) zr[r] = lds_r64(i3r[r]));
    STAMP(13);
#pragma unroll
    for (int r = 0; r < 4; ++r) { a[0][r] = zr[r].x; a[1][r] = zr[r].y; }
    f32x4 tr[2], ti[2], sr[2], si[2], h[2][2];
#pragma unroll
    for (int rr = 0; rr < 2; ++rr) { tr[rr] = mm16p(a[rr], gbre, z); ti[rr] = mm16p(a[rr], gbim, z); sr[rr] = mm16p(a[rr], g0re, z); si[rr] = mm16p(a[rr], g0im, z); }
#pragma unroll
    for (int rr = 0; rr < 2; ++rr) {
      tr[rr][0] = g == 0 ? sr[rr][0] : tr[rr][0]; ti[rr][0] = g == 0 ? si[rr][0] : ti[rr][0];
      a2[rr] = pk4(tr[rr] * twc - ti[rr] * tws, ti[rr] * twc + tr[rr] * tws);
      h[rr][0] = mm16p(a2[rr], ga[0], xin[rr][0]); h[rr][1] = mm16p(a2[rr], ga[1], xin[rr][1]);
    }
    float st[2];
#pragma unroll
    for (int rr = 0; rr < 2; ++rr) { const f32x4 q = h[rr][0] * h[rr][0] + h[rr][1] * h[rr][1]; st[rr] = (q[0] + q[2]) + (q[1] + q[3]); }
    wave_sums<2>(st);
    v4u so;
#pragma unroll
    for (int rr = 0; rr < 2; ++rr) {
      const float ve = st[rr] * (1.f / 512) + 1e-5f, rstd = __builtin_amdgcn_rsqf(ve);
      const f32x4 l0 = h[rr][0] * rstd, l1 = h[rr][1] * rstd;
      v4u l4 = {pk(l0[0], l0[1]), pk(l0[2], l0[3]), pk(l1[0], l1[1]), pk(l1[2], l1[3])};
      lds_w128(o3w[rr], l4);
      so[2 * rr] = asu(mean[rr]); so[2 * rr + 1] = asu(ve * rstd);
    }
    lds_w128(sow, so);
    STAMP(14);
    v4u v0, v1;
    v2u ms;
    flag_set(fmine, 7);
    FLAG_WAIT(fall, 7, v0 = lds_r128(o3r[0]); v1 = lds_r128(o3r[1]); ms = lds_r64(sor));
    st_dev(ws, vs, v0); st_dev(ws, vs + 1024, v1);
    u32 s = sum4(sum4(0u, v0), v1);
    s += dppu<0x128>(s); s = add_x32(add_x16(s));
    v4u rec = {tag - s - ms.x - ms.y, ms.x, ms.y, tag};
    st_dev(ws, vt, rec);
  }
  SB();
  {
#define LOOKN 6
    constexpr int LOOK = LOOKN;
    v4u ring[8], act[32];
    lp_t wa[4], wb[4];
#pragma unroll
    for (int t = 0; t < 4; ++t) wa[t] = lptr(t * SLOT + g * 512 + ((c ^ (2 * t + (c >> 3))) * 16));
#pragma unroll
    for (int i = 0; i < 4; ++i) wb[i] = lptr(((i & 1) ? 0 : 4 * SLOT) + ((lane >> 2) ^ (g & 1)) * 64 + (((i >> 1) ^ g) & 1) * 32 + (lane & 3) * 8);
    auto frag = [&](auto NN) -> v4u {
      constexpr int N = NN.value;
      if constexpr (N < 64) return lds_r128(wa[(N >> 1) & 1] + (N >> 2) * 2048 + (N & 1) * 256);
      else if constexpr (N < 128) return lds_r128(wa[N >> 5] + ((N & 31) >> 1) * 2048 + (N & 1) * 256);
      else { const lp_t b = wb[((N >> 4) & 2) + (N & 1)] + ((N & 31) >> 1) * 2048; return lds_tr2(b, b + 1024); }
    };
    int vo = WS_H + ((4 * rg + w) * NFT + ft0) * HREC + lane * 16, vk = (lane & 11) ? OOB : vo - lane * 16 + 1024 + ((lane >> 4) * 8 + (lane & 7)) * 4;
    int vo4 = vo + 4 * HREC, vk4 = vk + 4 * HREC;
    PIN(vo); PIN(vk); PIN(vo4); PIN(vk4);
    Epi e;
#define EPI(J0, J1) static_for<(J0), (J1)>([&](auto JJ) { constexpr int kd = kEp.op[JJ.value].kind, V = kEp.op[JJ.value].v, t = V >> 3; \
      if constexpr (kd == E_ST) st_dev(ws, (t < 4 ? vo : vo4) + (t & 3) * HREC, e.r); \
      else if constexpr (kd == E_STK) st_dev(ws, (t < 4 ? vk : vk4) + (t & 3) * HREC, e.k4); \
      else epi_op<kd, V>(e, t % 3 == 0 ? tA : t % 3 == 1 ? tB : tC, tag); \
      SB(); })
    int va0 = ((lane >> 3) & 1) * 8192 + g * 128 + (lane & 7) * 16, va1 = va0 + 16384;
    int vr = ((lane >> 3) & 1) * 512 + g * 128 + (lane & 7) * 16;
    PIN(vr);
    const int sa = WS_L + (4 * rg + w) * 32768;
#define ACT_LD(i) act[i] = ld_dev(ws, (((i) & 1) ? va1 : va0) + (((i) >> 1) & 7) * 512, sa + ((i) >> 4) * 4096)
    f32x4 tA[4], tB[4], tC[4];
    WD_SETUP();
    v4u s0, s1;
#define A_HEAD() do { MEMFENCE(); PIN(va0); PIN(va1); s0 = ld_dev(ws, vr, WS_S + (4 * rg + w) * 2048); s1 = ld_dev(ws, vr + 1024, WS_S + (4 * rg + w) * 2048); SB(); \
      static_for<0, 2 * LOOK>([&](auto K) { ACT_LD(K.value); SB(); }); } while (0)
    WAIT3();
    STAMP(15);
    u32 spin = 0;
    A_HEAD();
    BAR();
    for (;; ++spin) {
      static_for<0, PD>([&](auto K) { ring[K.value ^ 1] = FRAG(K.value ^ 1); SB(); });
      if (__builtin_expect(__any(s0.w != tag || s1.w != tag), 0)) { _Pragma("nounroll") do { A_HEAD(); } while (__any(s0.w != tag || s1.w != tag) && ++spin < SPIN_CAP); ++spin; }
      PIN(va0); PIN(va1);
      u32 m = (s0.x + s0.y + s0.z) + (s1.x + s1.y + s1.z);
      static_for<0, 64>([&](auto K) {
        constexpr int n = K.value, ks = n >> 2, q = (n & 1) * 2;
        f32x4* acc = (n & 2) ? tB : tA;
        mma<ks == 0>(acc[q], ring[n & 7], act[2 * ks]); SB();
        ring[NXT(n) & 7] = FRAG(NXT(n));
        if constexpr (!(n & 1) && ks + LOOK < 16) ACT_LD(2 * (ks + LOOK) + ((n >> 1) & 1));
        SB();
        mma<ks == 0>(acc[q + 1], ring[n & 7], act[2 * ks + 1]); SB();
        { const v4u v = act[2 * ks + ((n >> 1) & 1)]; u32 t = (n & 1) ? m + v.z + v.w : m + v.x + v.y; PIN(t); m = t; }
        SB();
      });
      if (__builtin_expect(__all(add_x32(add_x16(m)) == 8u * tag) || spin > SPIN_CAP, 1)) break;
      s0.w = ~tag;
    }
    COUNT(22, spin);
    BAR_N(15);
    STAMP(16);
#define G1_TILE(T, acc, ...) static_for<0, 32>([&](auto K) { constexpr int s = K.value, n = 32 * (T) + s, ks = s >> 1, q = (s & 1) * 2, j0 = epos(n), j1 = epos(n + 1), jm = j0 + (j1 - j0 - (T) / 4) / 2; \
      mma<ks == 0>(acc[q], ring[n & 7], act[2 * ks]); SB(); \
      if constexpr (n + PD < 192) ring[NXT(n) & 7] = FRAG(n + PD < 192 ? NXT(n) : 0); SB(); \
      EPI(j0, jm); \
      mma<ks == 0>(acc[q + 1], ring[n & 7], act[2 * ks + 1]); SB(); \
      __VA_ARGS__; EPI(jm, j1); SB(); })
#define PG2 8
#define PG3 10
    G1_TILE(2, tC, { });
    FSTAMP(26);
    G1_TILE(3, tA, { if constexpr (s == PG2) BAR_N(15); });
    FSTAMP(27);
    if (six) {
      G1_TILE(4, tB, { if constexpr (s == PG3) BAR_N(15); });
      FSTAMP(28);
      G1_TILE(5, tC, { });
      BAR_N(15);
      STAMP(17);
      MMA_DONE(); SB();
      EPI(kEp.first[5], kEp.n);
    } else {
      BAR_N(15);
      MMA_DONE(); SB();
      EPI(epos(128), kEp.first[4]);
      static_for<E_X1, E_STK>([&](auto KK) { epi_op<KK.value, 31>(e, tA, tag); });
      st_dev(ws, vk + 3 * HREC, e.k4);
      __builtin_amdgcn_s_sleep(SKSL);
      FSTAMP(28);
      BAR_N(15);
      STAMP(17);
    }
    STAMP(18);
    DMA_WDS(4, 0);
    BAR();
    DMA_WDS(5, 1);
    if (w & 1) VM_WAIT(4); else VM_WAIT(8);
    BAR();
    VM_WAIT(0);
    BAR();
  }
  STAMP(24);
  STAMP_FLUSH();
  UNDEF_RA();
  }
  PINS(wv);
  if (wv >= 4) {
    STAMP(0);
    const int hw = wv - 4;
    u32 tag;
    v4u y0[8], y1[8];
    WD_SETUP();
    {
      const rsrc_t rwg = mkrs(p.wg, W1SZ), rwu = mkrs(p.wu, W1SZ);
      const int vx = 4 * (lane >> 3) * (F_ * 2) + ft0 * 32 + (lane & 7) * 16, vx8 = vx + 32 * F_ * 2;
      const int vy = six ? 4 * (lane >> 2) * (F_ * 2) + (ft0 + 4) * 32 + (lane & 3) * 16 : OOB;
      const u32 ax = ((lane >> 1) & 3) * SLOT + (16 * hw + (lane >> 3)) * 512 + (lane & 1) * 128 + (lane & 7) * 16;
#define PUT_X(u, off) do { transpose8(u); const u32 a_ = ax + (off); _Pragma("unroll") for (int i = 0; i < 8; ++i) lds_w128((lp_t)(size_t)(a_ ^ (i * 16)), u[i]); SB(); } while (0)
      v4u rb[8], rc[8], rd[8];
      const lp_t fall = lptr(SCR + L_F + (lane & 3) * 256 + lane * 4);
      const int s0 = 64 * hw * (F_ * 2);
#define THR 3
#define NA 2
#define NB 10
#define NH 2
#define TS 170
#define F1 2
#define F2 5
#define STEP(n) do { constexpr int j = (n) & 7; const int so_ = s0 + ((j & 3) + 256 * (j >> 2)) * (F_ * 2); \
        if ((n) == NA) { flag_wait_lazy(fall, F1); cpf = touch(rwd, lane < 11 ? wg * 43 + hw * 11 + lane : 16384); } if ((n) == NB) flag_wait_lazy(fall, F2); \
        if ((n) == NA + NH) flag_wait_lazy(fall, 3); if ((n) == NB + NH) flag_wait_lazy(fall, 6); \
        asm volatile("s_waitcnt vmcnt(%0)" :: "n"(THR) : "memory"); \
        if ((n) < 8) { ra[j] = ld_in(rwg, vx, so_); rb[j] = ld_in(rwg, vx8, so_); y0[j] = ld_in(rwg, vy, so_); } \
        else { rc[j] = ld_in(rwu, vx, so_); rd[j] = ld_in(rwu, vx8, so_); y1[j] = ld_in(rwu, vy, so_); } SB(); } while (0)
      if (hw >= 2) asm volatile("" :: "v"(cpf), "v"(ra[0]));
      PACE(0, TS);
      STAMP(2);
      STEP(0); STEP(1); STEP(2); STEP(3); STEP(4); STEP(5); STEP(6); STEP(7); STEP(8); STEP(9);
      STAMP(3);
      if (NB > 10) STEP(10);
      if (NB > 11) STEP(11);
      PUT_X(ra, 0);
      PUT_X(rb, 4096);
      if (NB <= 10) STEP(10);
      if (NB <= 11) STEP(11);
      STEP(12); STEP(13); STEP(14); STEP(15);
      tag = *(LAS u32*)(fall + L_TG);
      STAMP(4);
      PUT_X(rc, 256); PUT_X(rd, 4096 + 256);
      PIN(tag);
      asm volatile("" :: "v"(cpf));
    }
    STAMP(5);
    BAR();
    const rsrc_t ws = mkrs(p.ws, WS_END);
    const int kh = hw & 1, rl = hw >> 1, g = lane >> 4;
    constexpr int PD = 4;
    const u32 ayo = hw * 8192 + g * 2048 + ((lane >> 2) & 3) * 256 + (lane & 4) * 16 + ((lane & 3) ^ ((lane >> 1) & 2)) * 16;
    const lp_t ay0[2] = {lptr(ayo + 4 * SLOT), lptr((ayo ^ 64) + 4 * SLOT)}, ay1[2] = {lptr(ayo), lptr(ayo ^ 64)}, ad = lptr(3 * SLOT + hw * 8192 + lane * 16);
#define PUT_Y(a, y, j0, j1) _Pragma("unroll") for (int j = j0; j < j1; ++j) lds_w128(a[j & 1] + (j >> 2) * 1024 + (j & 2) * 64, y[j])
    PUT_Y(ay0, y0, 0, 8);
    v4u ring[8], hfA[8], hfB[8], hfC[8], hfD[8], hfE[8];
    u32 hkA, hkB, hkC, hkD, hkE, ms = 0, nfix = 0;
    lp_t wd[3][4];
    {
      const u32 dl = (g * 1024 + ((lane >> 2) & 3) * 128 + ((lane >> 3) & 3) * 32 + (lane & 3) * 8) ^ (kh * 64);
#pragma unroll
      for (int i = 0; i < 12; ++i) wd[i >> 2][i & 3] = lptr((dl ^ ((i & 3) * 32)) + (i >> 2) * 2 * SLOT + kh * 16384);
    }
    auto frag = [&](auto NN) -> v4u {
      constexpr int N = NN.value, t = N < 64 ? N >> 4 : N < 76 ? 4 : 5, s = N < 64 ? (N & 15) : N < 76 ? N - 64 : N - 76, slot = (t + 1) % 5;
      const lp_t b = wd[slot >> 1][s & 3] + (slot & 1) * SLOT + (s >> 2) * 4096;
      return lds_tr2(b, b + 512);
    };
    const int hb = WS_H + (4 * rg + 2 * (ch >> 3) + rl) * NFT * HREC;
    int hs[4];
#pragma unroll
    for (int i = 0; i < 4; ++i) { hs[i] = hb + tile0_of(8 * kh + 2 * i) * HREC; PINS(hs[i]); }
    int vl = (g & 1) * 512 + (lane & 15) * 16 + (g >> 1) * 6 * HREC, vl3 = vl - (g >> 1) * 2 * HREC, vl4 = vl + 4 * HREC, vlz = (kh && g >= 2) ? OOB : vl4, vq, vq4;
    {
      const int st = (lane & 2) + ((lane >> 1) & 4) + (g >> 1);
      vq = hb + 1024 + ((2 * (g & 1) + (lane & 1)) * 8 + (lane & 4)) * 4 + tile0_of(8 * kh + st) * HREC;
      vq4 = st < 6 - kh ? vq + 4 * HREC : OOB;
    }
    const int u = 16 * rg + 8 * (ch >> 3) + 4 * rl + ((lane >> 3) & 1), col = 64 * (ch & 7) + 32 * kh;
    int vh = u * 8192 + (col & 255) * 32 + g * 128 + (lane & 7) * 16 + (col >> 8) * 8, vs = u * 512 + (lane & 7) * 16 + 4;
    int vout = ((32 * (8 * (u >> 5) + (lane & 7)) + (u & 31)) * D_ + col + 16 * (g & 1) + 8 * (g >> 1)) * 2;
    lp_t rw = lptr(2 * SLOT + hw * 4096 + lane * 16), rr_ = lptr(2 * SLOT + (hw ^ 1) * 4096 + lane * 16);
    u32 t8 = 8u * tag, t6 = ((kh && g >= 2) ? 4u : 6u) * tag;
    PIN(vl); PIN(vl3); PIN(vl4); PIN(vlz); PIN(vq); PIN(vq4); PIN(vh); PIN(vs); PIN(vout); PIN(t8); PIN(t6);
#define H_LD(X, t, j) do { \
      if constexpr ((j) == 8) hk##X = ld_dev1(ws, ((t) < 4 ? vq : vq4) + ((t) & 3) * HREC, 0); \
      else if constexpr ((t) < 4 || (j) < 6) hf##X[(j) & 7] = ld_dev(ws, ((t) < 4 ? ((j) < 6 ? vl : vl3) : (j) < 4 ? vl4 : vlz) + ((t) & 3) * HREC + ((j) & 1) * 256, hs[((j) >> 1) & 3]); } while (0)
#define H_LDALL(X, t) static_for<0, 9>([&](auto J) { THROTTLE(J.value); H_LD(X, t, J.value ? J.value - 1 : 8); })
#define H_SUM(X, nf, j) do { if constexpr ((j) >= 0 && (j) < (nf)) { ms = sum4((j) ? ms : hk##X, hf##X[(j) & 7]); u32 t_ = ms; PIN(t_); ms = t_; } \
      else if constexpr ((j) >= (nf) && (j) < (nf) + 3) { u32 t_ = ms + dppu<(j) == (nf) ? 0xb1 : (j) == (nf) + 1 ? 0x4e : 0x128>(ms); PIN(t_); ms = t_; } } while (0)
#define H_SUMALL(X, nf) static_for<0, (nf) + 3>([&](auto J) { H_SUM(X, nf, J.value); })
#define H_FIX(X, t, nf, want) do { if (__builtin_expect(__any(ms != (want)), 0)) { u32 spin_ = 0; _Pragma("nounroll") do { MEMFENCE(); nfix |= 1u << (4 * (t)); \
        H_LDALL(X, t); H_SUMALL(X, nf); } while (__any(ms != (want)) && ++spin_ < SPIN_CAP); } SB(); } while (0)
    v4u d2[8], d3[8];
    f32x4 o[4][2];
    v2u hr[4], so[2];
#define GRP() asm volatile("s_waitcnt vmcnt(%0)" :: "n"(TH2) : "memory")
#define RAW4(dst, t, i0) do { GRP(); _Pragma("unroll") for (int i = i0; i < i0 + 4; ++i) dst[i] = ld_in(rwd, (i & 1) ? vd1 : vd, sd[i >> 1] + ((t) * 16 + (i & 1) * 8) * (D_ * 2)); SB(); } while (0)
#define DMA4(t, slot, i0) do { GRP(); _Pragma("unroll") for (int i = i0; i < i0 + 4; ++i) DMA_WD1(t, slot, i); } while (0)
#define H4(X, t, h) do { GRP(); static_for<5 * (h), 5 + 4 * (h)>([&](auto J) { H_LD(X, t, J.value ? J.value - 1 : 8); }); SB(); } while (0)
#define PUT_D(d, off, i0, i1) do { _Pragma("unroll") for (int i = i0; i < i1; ++i) lds_w128(ad + (off) + i * 1024, d[i]); SB(); } while (0)
#define RESID(i) do { const u32 v0 = hr[i].x, v1 = hr[i].y, mb = so[(i) & 1].x, sb = so[(i) & 1].y; float q[4] = {blo(v0), bhi(v0), blo(v1), bhi(v1)}; \
      _Pragma("unroll") for (int r = 0; r < 4; ++r) { q[r] = __builtin_fmaf(q[r], asf(sb), asf(mb)); PIN(q[r]); o[(i) >> 1][(i) & 1][r] = q[r]; } PIN(o[(i) >> 1][(i) & 1]); SB(); } while (0)
#define PSLEEP 16
    __builtin_amdgcn_s_sleep(PSLEEP);
    RAW4(d2, 2, 0); RAW4(d2, 2, 4);
    STAMP(6);
    BAR();
    DMA4(0, 1, 0); PUT_Y(ay1, y1, 0, 3);
    DMA4(0, 1, 4); PUT_Y(ay1, y1, 3, 6);
    GRP();
#pragma unroll
    for (int i = 0; i < 2; ++i) so[i] = __builtin_amdgcn_raw_buffer_load_b64(ws, vs + i * 1024, WS_S, AUX_SC1);
#pragma unroll
    for (int i = 0; i < 4; ++i) hr[i] = __builtin_amdgcn_raw_buffer_load_b64(ws, vh + (i >> 1) * 512, WS_L + (i & 1) * 16384, AUX_SC1);
    PUT_Y(ay1, y1, 6, 8);
    RAW4(d3, 3, 0);
    STAMP(7);
    BAR();
    RAW4(d3, 3, 4); RESID(0); RESID(1); RESID(2); RESID(3);
    DMA4(1, 2, 0); DMA4(1, 2, 4);
    H4(A, 0, 0);
    STAMP(8);
    BAR();
    H4(A, 0, 1); PUT_D(d2, 0, 0, 3);
    H4(B, 1, 0); PUT_D(d2, 0, 3, 6);
    H4(B, 1, 1); PUT_D(d2, 0, 6, 8);
    H4(C, 2, 0); H_SUMALL(A, 8);
    STAMP(9);
    H4(C, 2, 1);
    H_FIX(A, 0, 8, t8);
    static_for<0, PD>([&](auto K) { ring[K.value ^ 1] = FRAG(K.value ^ 1); SB(); });
    STAMP(10);
    BAR();
    STAMP(11);
#define G2_ROUND(X, n0, ns, ...) static_for<0, ns>([&](auto K) { constexpr int s = K.value, n = (n0) + s, ct = s & 3, a = (s >> 2) * 2; \
      mma<n == 2 || n == 3>(o[ct][0], ring[n & 7], hf##X[a]); SB(); \
      if constexpr (n + PD < 88) ring[NXT(n) & 7] = FRAG(n + PD < 88 ? NXT(n) : 0); SB(); \
      mma<n == 2 || n == 3>(o[ct][1], ring[n & 7], hf##X[a + 1]); SB(); \
      __VA_ARGS__; SB(); })
#define L3 6
#define L4 36
#define L5 48
#define LATE() do { if constexpr (n >= L3 && n < L3 + 9) H_LD(D, 3, n > L3 ? n - L3 - 1 : 8); \
      if constexpr (n >= L4 && n < L4 + 7) H_LD(E, 4, n > L4 ? n - L4 - 1 : 8); if constexpr (n >= L5 && n < L5 + 7) H_LD(A, 5, n > L5 ? n - L5 - 1 : 8); } while (0)
    G2_ROUND(A, 0, 16, { H_SUM(B, 8, s - 5); LATE(); });
    H_FIX(B, 1, 8, t8);
    FSTAMP(14);
    G2_ROUND(B, 16, 16, { if constexpr (s < 8) lds_w128(ad + SLOT + s * 1024, d3[s & 7]); H_SUM(C, 8, s - 5); LATE(); });
    BAR_N(8);
    H_FIX(C, 2, 8, t8);
    FSTAMP(15);
    G2_ROUND(C, 32, 16, { LATE(); H_SUM(D, 8, s - 5); });
    H_FIX(D, 3, 8, t8);
    FSTAMP(16);
    G2_ROUND(D, 48, 16, { LATE(); H_SUM(E, 6, s - 7); if constexpr (s == 11) BAR_N(15); });
    H_FIX(E, 4, 6, t6);
    FSTAMP(17);
    G2_ROUND(E, 64, 12, { H_SUM(A, 6, s - 3); if constexpr (s == 7) BAR_N(15); });
    H_FIX(A, 5, 6, t6);
    FSTAMP(18);
    G2_ROUND(A, 76, 12, { });
    COUNT(23, nfix);
    STAMP(12);
    MMA_DONE(); SB();
#pragma unroll
    for (int i = 0; i < 4; ++i) lds_wf(rw + i * 1024, o[2 + (i & 1)][i >> 1]);
    BAR();
    const rsrc_t ro = mkrs(p.out, S_ * D_ * 2);
#pragma unroll
    for (int i = 0; i < 2; ++i) {
      const f32x4 v0 = o[0][i] + lds_rf(rr_ + i * 2048), v1 = o[1][i] + lds_rf(rr_ + i * 2048 + 1024);
      const auto x = __builtin_amdgcn_permlane16_swap(pk(v0[0], v0[1]), pk(v1[0], v1[1]), false, false);
      const auto y = __builtin_amdgcn_permlane16_swap(pk(v0[2], v0[3]), pk(v1[2], v1[3]), false, false);
      const v4u q = {x[0], y[0], x[1], y[1]};
      __builtin_amdgcn_raw_buffer_store_b128(q, ro, vout + i * 2048, 0, 0);
    }
    STAMP(13);
    STAMP_FLUSH();
  }
}

extern "C" void kernel_launch(void* const* d_in, const int* in_sizes, int n_in,
                              void* d_out, int out_size, void* d_ws, size_t ws_size,
                              hipStream_t stream) {
  Params p;
  p.x = (const u16*)d_in[0]; p.wr = (const u16*)d_in[1]; p.wi = (const u16*)d_in[2];
  p.wg = (const u16*)d_in[4]; p.wu = (const u16*)d_in[5]; p.wd = (const u16*)d_in[6];
  p.out = (u16*)d_out; p.ws = d_ws;
  (void)hipFuncSetAttribute((const void*)gfnet_geglu_kernel, hipFuncAttributeMaxDynamicSharedMemorySize, 5 * SLOT);
  gfnet_geglu_kernel<<<256, 512, 5 * SLOT, stream>>>(p);
}
```

```cpp
#define THR 4
#define TW 235
#define T1 265
#define T2 540
#define T3 755
#include <hip/hip_runtime.h>
#include <hip/hip_bf16.h>
#include <type_traits>

typedef unsigned u32;
typedef unsigned short u16;
typedef u32 v4u __attribute__((ext_vector_type(4)));
typedef u32 v2u __attribute__((ext_vector_type(2)));
typedef float f32x2 __attribute__((ext_vector_type(2)));
typedef float f32x4 __attribute__((ext_vector_type(4)));
typedef __bf16 bf16x2 __attribute__((ext_vector_type(2)));
typedef __bf16 bf16x8 __attribute__((ext_vector_type(8)));
typedef short s16x4 __attribute__((ext_vector_type(4)));
#define LAS __attribute__((address_space(3)))
#define DI __device__ __forceinline__
typedef __amdgpu_buffer_rsrc_t rsrc_t;
typedef LAS char* lp_t;

constexpr int S_ = 2048, D_ = 512, F_ = 1376, KF = 257;
constexpr int NFT = 86;
constexpr int WS_E = 0;
constexpr int XREC = 88 * 128;
constexpr int WS_A = 4096;
constexpr int WS_B = WS_A + 256 * XREC;
constexpr int WS_W = WS_B + 256 * XREC;
constexpr int WS_L = WS_W + (2 << 20);
constexpr int WS_T = WS_L + (2 << 20);
constexpr int WS_S = WS_T + (1 << 15);
constexpr int WS_H = WS_S + (1 << 17);
constexpr int HREC = 1152;
constexpr int WS_END = WS_H + 64 * NFT * HREC;
constexpr int WS_ST = WS_END;
constexpr u32 NEG2 = 0x80008000u, NEGH = 0x80000000u, NEGL = 0x8000u;
constexpr u32 SPIN_CAP = 1u << 14;
constexpr int AUX_SC1 = 16;
#define NTAUX 2
constexpr int OOB = 0x40000000;
constexpr int CODE_LINES = 250;
constexpr int SLOT = 32768, SCR = 4 * SLOT;
constexpr int L_F = 0, L_TG = 2048;
constexpr int L_IM = 4096, L_SO = 4096, L_X = 16384, L_I2 = 16384, L_I3 = 16384, L_EX = 24576, L_O3 = 24576;

#define DRAIN() asm volatile("s_waitcnt vmcnt(0)" ::: "memory")
#define MEMFENCE() asm volatile("" ::: "memory")
#define PIN(x) asm volatile("" : "+v"(x))
#define PINS(x) asm volatile("" : "+s"(x))
#define SB() __builtin_amdgcn_sched_barrier(0)
#define BAR() asm volatile("s_waitcnt lgkmcnt(0)\n\ts_barrier" ::: "memory")
#define BAR_N(n) asm volatile("s_waitcnt lgkmcnt(" #n ")\n\ts_barrier" ::: "memory")

DI u32 pk(float a, float b) { f32x2 v = {a, b}; return __builtin_bit_cast(u32, __builtin_convertvector(v, bf16x2)); }
DI v4u pk4(f32x4 a, f32x4 b) { v4u r = {pk(a[0], b[0]), pk(a[1], b[1]), pk(a[2], b[2]), pk(a[3], b[3])}; return r; }
DI float asf(u32 v) { return __builtin_bit_cast(float, v); }
DI u32 asu(float v) { return __builtin_bit_cast(u32, v); }
DI float blo(u32 v) { return asf(v << 16); }
DI float bhi(u32 v) { return asf(v & 0xffff0000u); }
DI float dot2(u32 a, u32 b, float c) { return __builtin_amdgcn_fdot2_f32_bf16(__builtin_bit_cast(bf16x2, a), __builtin_bit_cast(bf16x2, b), c, false); }
DI u32 sum4(u32 m, v4u v) { return (m + v.x + v.y) + v.z + v.w; }
template <int CTRL> DI u32 dppu(u32 v) { return (u32)__builtin_amdgcn_update_dpp(0, (int)v, CTRL, 0xf, 0xf, true); }
template <int CTRL> DI float dppf(float v) { return asf(dppu<CTRL>(asu(v))); }
DI u32 pair_total(u32 s0, u32 s1, bool odd) { return (odd ? s1 : s0) + dppu<0xb1>(odd ? s0 : s1); }
DI u32 add_x16(u32 v) { const auto s = __builtin_amdgcn_permlane16_swap(v, v, false, false); return s[0] + s[1]; }
DI u32 add_x32(u32 v) { const auto s = __builtin_amdgcn_permlane32_swap(v, v, false, false); return s[0] + s[1]; }
template <int N> DI void wave_sums(float (&v)[N]) {
#define WS_STEP(C) _Pragma("unroll") for (int i = 0; i < N; ++i) v[i] += dppf<C>(v[i]);
  WS_STEP(0x111) WS_STEP(0x112) WS_STEP(0x114) WS_STEP(0x118) WS_STEP(0x142) WS_STEP(0x143)
#pragma unroll
  for (int i = 0; i < N; ++i) v[i] = asf(__builtin_amdgcn_readlane(asu(v[i]), 63));
}
__host__ __device__ constexpr int rec_q(int k) { return (k & 7) | ((k >> 1) & 0x18); }
__host__ __device__ constexpr int rec_line(int k) { return 11 * ((k >> 3) & 1) + ((rec_q(k) * 11) >> 5); }
__host__ __device__ constexpr int rec_pos(int k) { return rec_q(k) - 3 * ((rec_q(k) * 11) >> 5); }
__host__ __device__ constexpr int im_unit(int k, int j) { return L_IM + (k >> 6) * 3072 + rec_line(k) * 128 + ((j ^ (rec_line(k) + 2 * (k >> 6))) & 7) * 16; }
__host__ __device__ constexpr int im_chk(int k) { return im_unit(k, 6) + 4 * rec_pos(k); }
DI void lds_add(lp_t a, u32 v) { __hip_atomic_fetch_add((LAS u32*)a, v, __ATOMIC_RELAXED, __HIP_MEMORY_SCOPE_WORKGROUP); }
DI f32x4 mm16(v4u a, v4u b, f32x4 c) { return __builtin_amdgcn_mfma_f32_16x16x32_bf16(__builtin_bit_cast(bf16x8, a), __builtin_bit_cast(bf16x8, b), c, 0, 0, 0); }
DI f32x4 mm16p(v4u a, v4u b, f32x4 c) { f32x4 d = mm16(a, b, c); PIN(d); return d; }
template <bool FIRST> DI void mma(f32x4& d, v4u a, v4u b) {
  if constexpr (FIRST) asm volatile("v_mfma_f32_16x16x32_bf16 %0, %1, %2, 0" : "=&v"(d) : "v"(a), "v"(b));
  else asm volatile("v_mfma_f32_16x16x32_bf16 %0, %1, %2, %0" : "+v"(d) : "v"(a), "v"(b));
}
#define MMA_DONE() asm volatile("s_nop 7")
DI v2u lds_tr(lp_t a) { return __builtin_bit_cast(v2u, __builtin_amdgcn_ds_read_tr16_b64_v4i16((LAS s16x4*)a)); }
DI v4u lds_tr2(lp_t a, lp_t b) { v2u x = lds_tr(a), y = lds_tr(b); v4u r = {x.x, x.y, y.x, y.y}; return r; }
DI v4u lds_r128(lp_t a) { return *(LAS v4u*)a; }
DI f32x4 lds_rf(lp_t a) { return *(LAS f32x4*)a; }
DI v2u lds_r64(lp_t a) { return *(LAS v2u*)a; }
DI void lds_w128(lp_t a, v4u v) { *(LAS v4u*)a = v; }
DI void lds_wf(lp_t a, f32x4 v) { *(LAS f32x4*)a = v; }
DI void lds_w64(lp_t a, v2u v) { *(LAS v2u*)a = v; }
DI lp_t lptr(u32 off) { lp_t q = (lp_t)(size_t)off; PIN(q); return q; }
DI rsrc_t mkrs(const void* p, u32 bytes) { return __builtin_amdgcn_make_buffer_rsrc((void*)p, 0, bytes, 0x00020000); }
DI v4u ld_dev(rsrc_t r, int v, int s) { return __builtin_amdgcn_raw_buffer_load_b128(r, v, s, AUX_SC1); }
DI u32 ld_dev1(rsrc_t r, int v, int s) { return __builtin_amdgcn_raw_buffer_load_b32(r, v, s, AUX_SC1); }
DI void st_dev(rsrc_t r, int v, v4u x) { __builtin_amdgcn_raw_buffer_store_b128(x, r, v, 0, AUX_SC1); }
DI void st_dev1(rsrc_t r, int v, u32 x) { __builtin_amdgcn_raw_buffer_store_b32(x, r, v, 0, AUX_SC1); }
DI v4u ld_in(rsrc_t r, int v, int s) { return __builtin_amdgcn_raw_buffer_load_b128(r, v, s, 0); }
DI v4u ld_once(rsrc_t r, int v, int s) { return __builtin_amdgcn_raw_buffer_load_b128(r, v, s, NTAUX); }
DI u32 touch(rsrc_t r, int line) { return __builtin_amdgcn_raw_buffer_load_b32(r, line * 128, 0, 0); }
DI void flag_set(lp_t mine, u32 k) { MEMFENCE(); *(volatile LAS u32*)mine = k; MEMFENCE(); }
#define FLAG_WAIT(theirs, k, ...) do { u32 f_; _Pragma("nounroll") do { f_ = *(volatile LAS u32*)(theirs); MEMFENCE(); __VA_ARGS__; MEMFENCE(); } while (!__all(f_ >= (k))); } while (0)
DI void flag_wait_lazy(lp_t theirs, u32 k) { while (!__all(*(volatile LAS u32*)theirs >= k)) __builtin_amdgcn_s_sleep(1); MEMFENCE(); }
DI float cosr(float rev) { return __builtin_amdgcn_cosf(rev); }
DI float sinr(float rev) { return __builtin_amdgcn_sinf(rev); }
__host__ __device__ constexpr int slot_of(int k1, int k2) { return ((k2 < 8 ? k1 + 32 * k2 : (k1 ? 32 - k1 : 16) + 32 * (15 - k2)) - 1) & 255; }
DI int tile0_of(int c) { const int h = c >> 3, i = c & 7; return 44 * h + 4 * i + 2 * (i < 6 - h ? i : 6 - h); }
template <int I, int N, class F> DI void static_for(F&& f) {
  if constexpr (I < N) { f(std::integral_constant<int, I>{}); static_for<I + 1, N>(f); }
}
DI void transpose8(v4u (&m)[8]) {
  v4u o[8];
#pragma unroll
  for (int i = 0; i < 8; ++i)
#pragma unroll
    for (int j = 0; j < 4; ++j) { const u32 a = m[2 * j][i >> 1], b = m[2 * j + 1][i >> 1]; o[i][j] = __builtin_amdgcn_perm(b, a, (i & 1) ? 0x07060302u : 0x05040100u); }
#pragma unroll
  for (int i = 0; i < 8; ++i) { m[i] = o[i]; PIN(m[i]); }
}
enum { E_SQ, E_POLY, E_ARG, E_EXP, E_INC, E_RCP, E_GU, E_OUT, E_PACK, E_SWA, E_SWB, E_SUM, E_SUM2, E_ST, E_X1, E_X2, E_X8, E_CHK, E_STK };
struct EOp { int kind, v; };
struct EProg { EOp op[520]; int n, first[7]; };
constexpr EProg make_eprog() {
  EProg p = {};
  for (int t = 0; t < 6; ++t) {
    p.first[t] = p.n;
    for (int v = 0; v <= 8; ++v) {
      const int V = 8 * t + v;
      const bool head = v < 8, tail = v > 0, fin = t > 0 && t < 5;
      if (head) p.op[p.n++] = {E_SQ, V};
      if (tail) p.op[p.n++] = {E_INC, V - 1};
      if (head) p.op[p.n++] = {E_POLY, V}; else p.op[p.n++] = {E_GU, V - 1};
      if (tail) p.op[p.n++] = {E_RCP, V - 1}; else if (fin) p.op[p.n++] = {E_X1, V - 1};
      if (head) p.op[p.n++] = {E_ARG, V};
      if (tail && head) p.op[p.n++] = {E_GU, V - 1};
      if (v >= 3 && (v & 1)) p.op[p.n++] = {E_PACK, V - 2};
      if (v == 8) p.op[p.n++] = {E_SWA, V - 1};
      if (fin && v == 0) p.op[p.n++] = {E_X2, V - 1};
      if (fin && v == 1) p.op[p.n++] = {E_CHK, V - 2};
      if (head) p.op[p.n++] = {E_EXP, V};
      if (fin && v == 0) p.op[p.n++] = {E_X8, V - 1};
      if (fin && v == 1) p.op[p.n++] = {E_STK, V - 2};
      if (tail) p.op[p.n++] = {E_OUT, V - 1};
    }
    p.op[p.n++] = {E_PACK, 8 * t + 7}; p.op[p.n++] = {E_SUM, 8 * t + 7}; p.op[p.n++] = {E_SWB, 8 * t + 7}; p.op[p.n++] = {E_ST, 8 * t + 7}; p.op[p.n++] = {E_SUM2, 8 * t + 7};
    if (t >= 4) for (int k = E_X1; k <= E_STK; ++k) p.op[p.n++] = {k, 8 * t + 7};
  }
  p.first[6] = p.n;
  return p;
}
constexpr EProg kEp = make_eprog();
#define ER1 56
constexpr int epos(int n) {
  const int mid = (64 * ER1) / 16, end = kEp.first[5];
  int j = n <= 64 ? 0 : n <= 128 ? ((n - 64) * ER1) / 16 : n <= 192 ? mid + ((end - mid) * (n - 128)) / 64
        : end + ((kEp.n - end) * (n - 192)) / 8;
  for (int t = 2; t < 5; ++t) if (n <= 32 * t + 34 && j > kEp.first[t]) j = kEp.first[t];
  return j;
}
struct Epi { float t[2], q, h[3]; v4u r, k4; u32 sum, fin; };
template <int KIND, int V> DI void epi_op(Epi& e, const f32x4 (&a)[4], u32 tag) {
  constexpr int v = V & 7;
  const float gv = a[v >> 2][v & 3], uv = a[2 + (v >> 2)][v & 3];
  float& t = e.t[V & 1];
  if constexpr (KIND == E_SQ) { t = gv * gv; PIN(t); }
  else if constexpr (KIND == E_POLY) { t = __builtin_fmaf(t, -0.10294324f, -2.3022082f); PIN(t); }
  else if constexpr (KIND == E_ARG) { t = t * gv; PIN(t); }
  else if constexpr (KIND == E_EXP) { t = __builtin_amdgcn_exp2f(t); PIN(t); }
  else if constexpr (KIND == E_INC) { t = t + 1.0f; PIN(t); }
  else if constexpr (KIND == E_RCP) { t = __builtin_amdgcn_rcpf(t); PIN(t); }
  else if constexpr (KIND == E_GU) { e.q = gv * uv; PIN(e.q); }
  else if constexpr (KIND == E_OUT) { e.h[v & 1] = e.q * t; PIN(e.h[v & 1]); }
  else if constexpr (KIND == E_PACK) { u32 r = pk(e.h[0], e.h[1]); PIN(r); e.r[v >> 1] = r; }
  else if constexpr (KIND == E_SWA || KIND == E_SWB) { constexpr int i = KIND == E_SWB; const u32 x = e.r[i], y = e.r[i + 2];
    const auto q = __builtin_amdgcn_permlane16_swap(x, y, false, false); u32 q0 = q[0], q1 = q[1]; PIN(q0); PIN(q1); e.r[i] = q0; e.r[i + 2] = q1; }
  else if constexpr (KIND == E_SUM) { e.sum = e.r.x + e.r.z; PIN(e.sum); }
  else if constexpr (KIND == E_SUM2) { e.fin = e.sum + e.r.y + e.r.w; PIN(e.fin); }
  else if constexpr (KIND == E_X1) { e.fin += dppu<0xb1>(e.fin); PIN(e.fin); }
  else if constexpr (KIND == E_X2) { e.fin += dppu<0x4e>(e.fin); PIN(e.fin); }
  else if constexpr (KIND == E_X8) { e.fin += dppu<0x128>(e.fin); PIN(e.fin); }
  else if constexpr (KIND == E_CHK) { asm volatile("" : "=v"(e.k4)); e.k4.x = tag - e.fin; PIN(e.k4); }
}

constexpr double ct_sin(long n, long d) {
  n %= d; if (n < 0) n += d;
  if (2 * n > d) return -ct_sin(d - n, d);
  if (4 * n > d) return ct_sin(d - 2 * n, 2 * d);
  const double x = 6.283185307179586476925 * (double)n / (double)d;
  double t = x, r = x;
  for (int i = 1; i < 14; ++i) { t *= -x * x / ((2 * i) * (2 * i + 1)); r += t; }
  return r;
}
constexpr double ct_cos(long n, long d) { return ct_sin(4 * n + d, 4 * d); }
constexpr u32 ct_f32(double v) { return __builtin_bit_cast(u32, (float)v); }
constexpr u32 ct_bf(double v) { const u32 u = ct_f32(v); return (u + 0x7fffu + ((u >> 16) & 1u)) >> 16; }
constexpr u32 ct_pk(double a, double b) { return ct_bf(a) | (ct_bf(b) << 16); }
struct Tab { u32 v[6][64][4]; };
constexpr Tab make_tab() {
  Tab t = {};
  for (int l = 0; l < 64; ++l) {
    const int c = l & 15, g = l >> 4;
    for (int j = 0; j < 2; ++j) {
      const int a = 4 * g + 2 * j;
      t.v[0][l][j] = ct_pk(ct_cos(a * c, 32), ct_cos((a + 1) * c, 32));
      t.v[0][l][2 + j] = c ? ct_pk(-ct_sin(a * c, 32), -ct_sin((a + 1) * c, 32)) : ct_pk(1, -1);
    }
    for (int r = 0; r < 4; ++r) {
      const int b = 4 * g + r, sl = slot_of(b, c);
      t.v[1][l][r] = ct_f32(ct_cos(b * c, 512));
      t.v[2][l][r] = ct_f32(ct_sin(b * c, 512));
      t.v[3][l][r] = ct_pk(ct_cos(b * c, 16), ct_sin(b * c, 16));
      t.v[4][l][r] = c == 0 ? ct_pk((r & 1) ? -1 : 1, 0) : c < 8 ? ct_pk(ct_cos(b * c, 16), -ct_sin(b * c, 16)) : ct_pk(ct_cos(b * (31 - 2 * c), 32), -ct_sin(b * (31 - 2 * c), 32));
      t.v[5][l][r] = im_unit(sl, 2 * rec_pos(sl)) | (im_chk(sl) << 16);
    }
  }
  return t;
}
static_assert(true, "");
static __device__ const __attribute__((aligned(128))) Tab g_tab = make_tab();

#define STAMP(i) do { } while (0)
#define STAMP_DECL do { } while (0)
#define STAMP_FLUSH() do { } while (0)
#define COUNT(i, v) do { } while (0)

#define FSTAMP(i) do { } while (0)
#define PACE(d, t) do { __builtin_amdgcn_s_sleep(d); while ((u32)__builtin_amdgcn_s_memrealtime() - t_ref < (t)) __builtin_amdgcn_s_sleep(1); } while (0)
#define RS1 0
#define RS2 10
#define RS3 0
#define D1 1
#define D2 4
#define D3 6
#define SLIPPED() (*(LAS u32*)(fmine + L_TG + 1024) = (u32)__builtin_amdgcn_s_memrealtime() - t_ref)
#define PACE2(d, t, sl) do { u32 r_ = *(LAS u32*)(fall + L_TG + 1024); r_ = max(r_, dppu<0xb1>(r_)); r_ = max(r_, dppu<0x4e>(r_)); \
      r_ = __builtin_amdgcn_readfirstlane(r_) + (sl); t_ref += r_ > (t) ? r_ - (t) : 0u; PACE(d, t); } while (0)
#define SL2 175
#define SL3 150
#define WAIT1() PACE(D1, T1)
#define WAIT2() PACE2(D2, T2, SL2)
#define WAIT3() PACE2(D3, T3, SL3)
struct Params {
  const u16* x; const u16* wr; const u16* wi; const u16* wg; const u16* wu; const u16* wd; u16* out; void* ws;
};

__global__ void __launch_bounds__(512, 2) gfnet_geglu_kernel(Params p) {
  const int wg = blockIdx.x, tid = threadIdx.x, lane = tid & 63;
  int wv = __builtin_amdgcn_readfirstlane(tid >> 6);
  STAMP_DECL;
  unsigned long long t_entry;
  asm volatile("s_memrealtime %0" : "=s"(t_entry));
  const int rg = 4 * ((wg & 7) >> 1) + (wg >> 6), ch = 8 * (wg & 1) + ((wg >> 3) & 7);
  const int un = 16 * rg + ch;
  const int ft0 = tile0_of(ch);
  const bool six = (ch & 7) < 6 - (ch >> 3);
#define W1SZ (D_ * F_ * 2)
#define WD_SETUP() const rsrc_t rwd = mkrs(p.wd, D_ * F_ * 2); \
    int vd = (lane >> 3) * (D_ * 2) + (ch & 7) * 128 + ((lane & 7) ^ ((lane >> 3) & 2)) * 16, vd1 = vd ^ 64, sd[4]; PIN(vd); PIN(vd1); \
    _Pragma("unroll") for (int i = 0; i < 4; ++i) { sd[i] = tile0_of(4 * (wv & 3) + i) * (16 * D_ * 2); PINS(sd[i]); }
#define DMA_WD1(t, slot, i) asm volatile("s_mov_b32 m0, %0\n\ts_nop 0\n\tbuffer_load_dwordx4 %1, %2, %3 offen lds" \
      :: "s"((slot) * SLOT + (wv & 3) * 8192 + (i) * 1024), "v"(((i) & 1) ? vd1 : vd), "s"(rwd), "s"(sd[(i) >> 1] + ((t) * 16 + ((i) & 1) * 8) * (D_ * 2)) : "memory")
#define TH2 4
#define TG 4
#define THROTTLE(i) do { if ((i) % TG == 0) asm volatile("s_waitcnt vmcnt(%0)" :: "n"(TH2) : "memory"); } while (0)
#define DMA_WD(t, slot) do { _Pragma("unroll") for (int i = 0; i < 8; ++i) { THROTTLE(i); DMA_WD1(t, slot, i); } } while (0)
#define DMA_WDS(t, slot) do { _Pragma("unroll") for (int i = 0; i < 4; ++i) DMA_WD1(t, slot, i); if (!(wv & 1)) { _Pragma("unroll") for (int i = 4; i < 8; ++i) DMA_WD1(t, slot, i); } } while (0)
#define VM_WAIT(n) asm volatile("s_waitcnt vmcnt(" #n ")" ::: "memory")
#define PDN 4
  constexpr int PD = PDN;
#define FRAG(N) frag(std::integral_constant<int, (N)>{})
#define NXT(n) (((n) + PD) ^ 1)
  v4u ra[8];
  u32 cpf;
#define UNDEF_RA() do { asm volatile("" : "=v"(cpf)); _Pragma("unroll") for (int i = 0; i < 8; ++i) asm volatile("" : "=v"(ra[i])); } while (0)
  UNDEF_RA();
  if (wv < 4) {
    const int vt = lane * 16;
    int sx = (32 * (8 * (un >> 5) + 2 * wv) + (un & 31)) * (D_ * 2), sy = sx + 32 * D_ * 2;
    const rsrc_t rt = mkrs(&g_tab, sizeof(Tab));
    ra[2] = ld_in(rt, vt, 0); ra[3] = ld_in(rt, vt + 1024, 0);
    PINS(sx); PINS(sy);
    SB();
    const rsrc_t rx = mkrs(p.x, S_ * D_ * 2);
    ra[0] = ld_once(rx, vt, sx); ra[1] = ld_once(rx, vt, sy);
    SB();
#pragma unroll
    for (int i = 0; i < 4; ++i) ra[4 + i] = ld_in(rt, vt + i * 1024, 2048);
    cpf = ld_dev1(mkrs(p.ws, WS_END), 0, WS_E);
    STAMP(0);
  }
  PINS(wv);
  if (wv >= 6) {
    unsigned long long pc0;
    asm volatile("s_getpc_b64 %0" : "=s"(pc0));
    cpf = touch(mkrs((const void*)(pc0 & ~127ull), CODE_LINES * 128), lane < 9 ? (((wg >> 3) ^ (wv & 1)) * 9 + lane) : 4096);
    unsigned long long te;
    asm volatile("s_waitcnt lgkmcnt(0)\n\ts_mov_b64 %0, %1" : "=s"(te) : "s"(t_entry));
    st_dev(mkrs(p.ws, WS_END), (tid >= 504 && wg < 8) ? WS_T + wg * 128 + (lane & 7) * 16 : OOB, v4u{(u32)te, (u32)te, (u32)te, (u32)te});
  }
  PINS(wv);
  if ((wv >> 1) == 2) {
#define WSLEEP 2
    __builtin_amdgcn_s_sleep(WSLEEP);
    const rsrc_t rwr = mkrs(p.wr, S_ * KF * 2), rwi = mkrs(p.wi, S_ * KF * 2);
    const int vf = (lane & 1) * (KF * 2 + 2) + ((tid & 127) >> 1) * 8;
    const int sf = (16 * (un >> 6) + 2 * (un & 7) + 256 * ((un >> 3) & 7)) * (KF * 2);
#pragma unroll
    for (int r = 0; r < 4; ++r) { ra[r] = ld_once(rwr, vf, sf + r * (64 * KF * 2)); ra[4 + r] = ld_once(rwi, vf, sf + r * (64 * KF * 2)); }
  }
  const lp_t fmine = lptr(SCR + L_F + tid * 4);
  *(volatile LAS u32*)fmine = 0;
  lds_w128(lptr(SCR + L_IM + (tid & 127) * 128 + ((6 ^ (tid + 2 * (((tid & 127) * 43) >> 10))) & 7) * 16), v4u{0u, 0u, 0u, 0u});
  BAR();
  PINS(t_entry);
  u32 t_ref = (u32)t_entry;
  v4u wa_;
  asm volatile("" : "=v"(wa_));
  PINS(wv);
  if ((wv >> 1) == 2) {
    const int T = tid - 256, m = T >> 1;
    wa_ = v4u{(u32)(((m & 0x39) | ((m & 2) << 1) | ((m & 4) >> 1)) * 32 + (T & 1) * 16 + WS_W + un * 8192), (lane & 1) ? 0u : 16u, (u32)(SCR + im_chk(2 * T)), (u32)(SCR + im_chk(2 * T + 1))};
    PIN(wa_);
  }
  PINS(wv);
  if (wv >= 6) {
#define X0SLEEP 2
    __builtin_amdgcn_s_sleep(X0SLEEP);
    const rsrc_t r1 = mkrs((wv & 1) ? p.wu : p.wg, W1SZ);
    ra[0].x = touch(r1, lane < 43 ? wg * 43 + lane : 16384);
  }
  SB();
  PINS(wv);

  if (wv < 4) {
  const int w = wv, c = lane & 15, g = lane >> 4;
  const float fc = (float)c, fg4 = (float)(4 * g);
  const f32x4 z = {0.f, 0.f, 0.f, 0.f};
  f32x4 twc, tws;
  v4u fbre, fbsw;
  u32 tag;
  float mean[2];
  const rsrc_t ws = mkrs(p.ws, WS_END);
  const lp_t fall = lptr(SCR + L_F + (lane & 3) * 256 + lane * 4);
  const lp_t imr = lptr(SCR + L_IM + w * 3072 + (lane >> 3) * 128 + ((lane ^ ((lane >> 3) + 2 * w)) & 7) * 16);
  int vx0 = lane * 16 + WS_A + un * XREC + w * (22 * 128), vx2 = lane < 48 ? vx0 + 2048 : OOB;
  PIN(vx0); PIN(vx2);
  int vq8 = (lane & 7) * 128;
  PIN(vq8);
  u32 tq;
  v2u xk[2][2];
  f32x4 tfc[2], tfs[2], tic[2], tis[2];
  const float fj = (float)(16 * w) + fc;
#pragma unroll
  for (int r = 0; r < 4; ++r) {
#pragma unroll
      for (int m = 0; m < 2; ++m) {
        const float nf = (fg4 + (16 * m + r)) * (fj * (1.f / 2048)), ni = (fg4 + (float)(16 * w) + r) * ((fc + 16 * m) * (1.f / 2048));
        tfc[m][r] = cosr(nf); tfs[m][r] = sinr(nf); tic[m][r] = cosr(ni); tis[m][r] = sinr(ni);
      }
  }
#pragma unroll
  for (int i = 0; i < 2; ++i) { PIN(tfc[i]); PIN(tfs[i]); PIN(tic[i]); PIN(tis[i]); }
  SB();
  {
    const v4u xr[2] = {ra[0], ra[1]}, tb[6] = {ra[2], ra[3], ra[4], ra[5], ra[6], ra[7]};
    const u32 sc = (c & 1) ? NEG2 : 0u, sm = c < 8 ? NEGL : NEGH, p0 = c < 8 ? 0x0c0c0100u : 0x01000c0cu, p1 = c < 8 ? 0x0c0c0302u : 0x03020c0cu;
    v4u fare = {tb[0].x, tb[0].y, tb[0].x ^ sc, tb[0].y ^ sc}, faim = {tb[0].z, tb[0].w, tb[0].z ^ sc, tb[0].w ^ sc}, fbim, f0re, f0im;
    twc = __builtin_bit_cast(f32x4, tb[1]); tws = __builtin_bit_cast(f32x4, tb[2]); fbre = tb[3];
    lp_t t1w[4], t1c[4];
#pragma unroll
    for (int r = 0; r < 4; ++r) {
      const u32 a = tb[3][r], q = tb[4][r];
      fbsw[r] = __builtin_amdgcn_alignbit(a, a, 16);
      fbim[r] = fbsw[r] ^ sm;
      f0re[r] = __builtin_amdgcn_perm(q, q, p0); f0im[r] = __builtin_amdgcn_perm(q, q, p1);
      t1w[r] = lptr((tb[5][r] & 0xffffu) ^ (SCR + w * 8)); t1c[r] = lptr((tb[5][r] >> 16) + SCR);
    }
    float c0m = c == 0 ? 1.f / 16 : 0.f;
    lp_t xw = lptr(SCR + L_X + w * 2048 + lane * 16), xt = lptr(SCR + L_X + w * 2048 + lane * 8);
    lp_t xq = lptr(SCR + L_X + w * 2048 + (4 * c + g) * 8), f6 = lptr(SCR + L_F + (lane % 6) * 256 + lane * 4);
    tag = cpf + 1u;
    u32 tag0 = w ? 0u : tag;
    PIN(c0m); PIN(tag); PIN(tag0);
    *(LAS u32*)(fmine + L_TG) = tag;
    *(LAS u32*)(fmine + L_TG + 1024) = 0;
    PIN(fare); PIN(faim); PIN(fbre); PIN(fbim); PIN(f0re); PIN(f0im); PIN(twc); PIN(tws);
    STAMP(1);
    SB();
    v4u a[2];
#pragma unroll
    for (int rr = 0; rr < 2; ++rr) { lds_w128(xw + rr * 1024, xr[rr]); a[rr] = lds_tr2(xt + rr * 1024, xt + rr * 1024 + 512); }
    tq = ld_dev1(ws, vq8, WS_T);
    STAMP(2);
    float st[4] = {0.f, 0.f, 0.f, 0.f};
#pragma unroll
    for (int i = 0; i < 4; ++i)
#pragma unroll
      for (int rr = 0; rr < 2; ++rr) { const u32 v = xr[rr][i]; st[rr] = dot2(v, 0x3f803f80u, st[rr]); st[2 + rr] = dot2(v, v, st[2 + rr]); }
    SB();
    f32x4 ur[2], ui[2];
#pragma unroll
    for (int rr = 0; rr < 2; ++rr) { ur[rr] = mm16p(a[rr], fare, z); ui[rr] = mm16p(a[rr], faim, z); }
    wave_sums<4>(st);
    SB();
    v4u a2[2];
    f32x4 orr[2], oi[2], sr[2], si[2];
#pragma unroll
    for (int rr = 0; rr < 2; ++rr) {
      const f32x4 x = ur[rr] - st[rr] * c0m, y = ui[rr];
      a2[rr] = pk4(x * twc + y * tws, y * twc - x * tws);
      orr[rr] = mm16p(a2[rr], fbre, z); oi[rr] = mm16p(a2[rr], fbim, z); sr[rr] = mm16p(a2[rr], f0re, z); si[rr] = mm16p(a2[rr], f0im, z);
    }
    v4u o[2];
#pragma unroll
    for (int rr = 0; rr < 2; ++rr) {
      mean[rr] = st[rr] * (1.f / 512);
      const float rstd = __builtin_amdgcn_rsqf(fmaxf(st[2 + rr] * (1.f / 512) - mean[rr] * mean[rr], 0.f) + 1e-5f);
      orr[rr][0] = g == 0 ? sr[rr][0] : orr[rr][0]; oi[rr][0] = g == 0 ? si[rr][0] : oi[rr][0];
      o[rr] = pk4(orr[rr] * rstd, oi[rr] * rstd);
    }
#pragma unroll
    for (int r = 0; r < 4; ++r) { v2u v = {o[0][r], o[1][r]}; lds_w64(t1w[r], v); lds_add(t1c[r], v.x + v.y + tag0); }
    STAMP(3);
    flag_set(fmine, 1);
    v4u v0, v1, v2;
    FLAG_WAIT(f6, 1, v0 = lds_r128(imr); v1 = lds_r128(imr + 1024); v2 = lds_r128(imr + 2048));
    st_dev(ws, vx0, v0); st_dev(ws, vx0 + 1024, v1); st_dev(ws, vx2, v2);
    flag_set(fmine, 2);
#pragma unroll
    for (int i = 0; i < 4; ++i) xk[i >> 1][i & 1] = lds_r64(xq + (i >> 1) * 1024 + (i & 1) * 512);
  }
  int vge, vgo, vg = (lane >> 1) * 8192 + (lane & 1) * 16;
  {
    const int pay = (lane >> 1) * XREC + rec_pos(un) * 32 + (lane & 1) * 16, chk = (lane & 1) ? OOB : ((lane >> 1) ^ 1) * XREC + 96 + rec_pos(un) * 4;
    vge = (lane & 2) ? chk : pay; vgo = (lane & 2) ? pay : chk;
  }
  const int sg = w * (64 * XREC) + (22 * (un >> 6) + rec_line(un)) * 128;
#define GATHER(BUF) do { xl[0] = ld_dev(ws, vge, BUF + sg); xl[1] = ld_dev(ws, vgo, BUF + sg); xl[2] = ld_dev(ws, vge, BUF + sg + 32 * XREC); xl[3] = ld_dev(ws, vgo, BUF + sg + 32 * XREC); } while (0)
#define VERIFY(extra) ({ u32 k_ = 0; _Pragma("unroll") for (int m = 0; m < 2; ++m) { _Pragma("unroll") for (int i = 0; i < 4; ++i) xg[m][i] = (lane & 2) ? xl[2 * m + 1][i] : xl[2 * m][i]; \
      k_ += (lane & 2) ? xl[2 * m].x : xl[2 * m + 1].x; lds_w128(iw + m * 1024, xg[m]); } \
      u32 t_ = sum4(sum4((extra) - k_, xg[0]), xg[1]); t_ += dppu<0xb1>(t_); t_ += dppu<0x4e>(t_); t_; })
  const lp_t iw = lptr(SCR + L_I2 + w * 2048 + lane * 16);
  const int sw = WS_W + w * (64 * 8192) + ((un & 3) * 64 + ((((un >> 2) & 0x39) | ((un >> 1) & 4) | ((un >> 3) & 2)))) * 32;
  v4u wq[2];
  STAMP(4);
  SB();
  v4u f2a[2][2];
  {
    v4u f1a[4], f1b[4], f2b[2][2];
    const u32 sc = (c & 1) ? NEG2 : 0u;
#pragma unroll
    for (int r = 0; r < 4; ++r) {
#pragma unroll
      for (int ks = 0; ks < 2; ++ks) {
        const float n = (fg4 + (16 * ks + r)) * (fj * (1.f / 64));
        const float cs = cosr(n), sn = sinr(n);
        f1a[ks][r] = pk(cs, sn); f1b[ks][r] = pk(-sn, cs);
        f1a[ks + 2][r] = f1a[ks][r] ^ sc; f1b[ks + 2][r] = f1b[ks][r] ^ sc;
      }
      const float n = (fg4 + r) * (fc * (1.f / 32));
      const float cs = cosr(n), sn = sinr(n);
      const u32 a = pk(cs, sn), b = pk(-sn, cs), sr = (r & 1) ? NEG2 : 0u;
      f2a[0][0][r] = a; f2a[1][0][r] = a ^ sr; f2a[0][1][r] = a ^ sc; f2a[1][1][r] = a ^ sc ^ sr;
      f2b[0][0][r] = b; f2b[1][0][r] = b ^ sr; f2b[0][1][r] = b ^ sc; f2b[1][1][r] = b ^ sc ^ sr;
    }
#pragma unroll
    for (int i = 0; i < 4; ++i) { PIN(f1a[i]); PIN(f1b[i]); PIN(f2a[i >> 1][i & 1]); PIN(f2b[i >> 1][i & 1]); }
    lp_t i2r = lptr(SCR + L_I2 + ((g >> 1) * 32 + c) * 32 + (g & 1) * 16);
    lp_t exw = lptr(SCR + L_EX + w * 2048 + lane * 16), exr = lptr(SCR + L_EX + lane * 16);
    lp_t imw[2], imc = lptr(SCR + im_chk(64 * w + lane));
#pragma unroll
    for (int n = 0; n < 2; ++n) { const int k = 64 * w + 32 * (g >> 1) + 16 * n + c; imw[n] = lptr(SCR + im_unit(k, 2 * rec_pos(k) + (g & 1))); }
    vx0 += WS_B - WS_A; vx2 += WS_B - WS_A;
    PIN(vg); PIN(vge); PIN(vgo); PIN(vx0); PIN(vx2);
    tq -= t_ref; tq = tq < 150u ? tq : 0u;
    tq = max(tq, dppu<0xb1>(tq)); tq = max(tq, dppu<0x4e>(tq)); tq = max(tq, dppu<0x141>(tq));
    t_ref += __builtin_amdgcn_readfirstlane(tq);
    STAMP(5);
    PACE(0, TW);
    wq[0] = ld_dev(ws, vg, sw); wq[1] = ld_dev(ws, vg, sw + 32 * 8192);
    WAIT1();
    STAMP(6);
    v4u xl[4], xg[2];
    f32x4 wr[2], wi[2];
    GATHER(WS_A);
    flag_set(fmine, 3);
    SB();
#define W_UNPACK() _Pragma("unroll") for (int m = 0; m < 2; ++m) { _Pragma("unroll") for (int r = 0; r < 4; ++r) { const u32 v = wq[m][r]; wr[m][r] = blo(v); wi[m][r] = bhi(v); } PIN(wr[m]); PIN(wi[m]); }
    u32 s0 = sum4(sum4(tag, wq[0]), wq[1]), spin = 0;
    W_UNPACK();
    PIN(s0);
    SB();
    u32 bad = VERIFY(s0);
    if (__any(bad != 0)) {
      _Pragma("nounroll") do {
        __builtin_amdgcn_s_sleep(RS1);
        MEMFENCE();
        if (bad) { GATHER(WS_A); wq[0] = ld_dev(ws, vg, sw); wq[1] = ld_dev(ws, vg, sw + 32 * 8192); }
        bad = VERIFY(sum4(sum4(tag, wq[0]), wq[1]));
      } while (__any(bad != 0) && ++spin < SPIN_CAP);
      W_UNPACK();
      SLIPPED();
      ++spin;
    }
    COUNT(20, spin);
    STAMP(7);
    v4u af[2][4], b2[2], a3[2];
    flag_set(fmine, 4);
    FLAG_WAIT(fall, 4, _Pragma("unroll") for (int i = 0; i < 8; ++i) af[i >> 2][i & 3] = lds_r128(i2r + (i & 3) * 2048 + (i >> 2) * 512));
    STAMP(8);
    SB();
    f32x4 dr[2], di[2];
#pragma unroll
    for (int m = 0; m < 2; ++m)
#pragma unroll
      for (int ks = 0; ks < 4; ++ks) { dr[m] = mm16(af[m][ks], f1a[ks], ks ? dr[m] : z); di[m] = mm16(af[m][ks], f1b[ks], ks ? di[m] : z); }
#pragma unroll
    for (int m = 0; m < 2; ++m) b2[m] = pk4(dr[m] * tfc[m] + di[m] * tfs[m], di[m] * tfc[m] - dr[m] * tfs[m]);
#pragma unroll
    for (int k = 0; k < 2; ++k)
#pragma unroll
      for (int m = 0; m < 2; ++m) { dr[m] = mm16(f2a[m][k], b2[k], k ? dr[m] : z); di[m] = mm16(f2b[m][k], b2[k], k ? di[m] : z); }
#pragma unroll
    for (int m = 0; m < 2; ++m) a3[m] = pk4(dr[m] * wi[m] + di[m] * wr[m], dr[m] * wr[m] - di[m] * wi[m]);
#pragma unroll
    for (int k = 0; k < 2; ++k)
#pragma unroll
      for (int n = 0; n < 2; ++n) { di[n] = mm16(a3[k], f2a[n][k], k ? di[n] : z); dr[n] = mm16(a3[k], f2b[n][k], k ? dr[n] : z); }
#pragma unroll
    for (int n = 0; n < 2; ++n) lds_w128(exw + n * 1024, pk4(di[n] * tic[n] + dr[n] * tis[n], dr[n] * tic[n] - di[n] * tis[n]));
    STAMP(9);
    flag_set(fmine, 5);
    FLAG_WAIT(fall, 5, _Pragma("unroll") for (int i = 0; i < 8; ++i) af[i >> 2][i & 3] = lds_r128(exr + (i & 3) * 2048 + (i >> 2) * 1024));
    SB();
    u32 cs[2];
#pragma unroll
    for (int n = 0; n < 2; ++n)
#pragma unroll
      for (int ks = 0; ks < 4; ++ks) { di[n] = mm16(f1a[ks], af[n][ks], ks ? di[n] : z); dr[n] = mm16(f1b[ks], af[n][ks], ks ? dr[n] : z); }
#pragma unroll
    for (int n = 0; n < 2; ++n) {
      const v4u e = pk4(dr[n], di[n]);
      lds_w128(imw[n], e);
      cs[n] = sum4(0u, e);
    }
    const auto sx = __builtin_amdgcn_permlane16_swap(cs[0], cs[1], false, false);
    *(LAS u32*)imc = tag + sx[0] + sx[1];
    MEMFENCE();
    const v4u v0 = lds_r128(imr), v1 = lds_r128(imr + 1024), v2 = lds_r128(imr + 2048);
    st_dev(ws, vx0, v0); st_dev(ws, vx0 + 1024, v1); st_dev(ws, vx2, v2);
    flag_set(fmine, 6);
    if (wg == 0 && w == 0) st_dev1(ws, WS_E, tag);
  }
  STAMP(10);
  SB();
  {
    v4u gbre, gbim, g0re, g0im, ga[2];
#pragma unroll
    for (int r = 0; r < 4; ++r) {
      gbre[r] = fbre[r] ^ (g < 2 ? NEGH : 0u);
      gbim[r] = fbsw[r] ^ (g < 2 ? 0u : NEGH);
      const float n = fc * ((31.f - 2.f * r - 2.f * fg4) * (1.f / 32));
      u32 q = pk(cosr(n), -sinr(n));
      PIN(q);
      g0re[r] = g < 2 ? gbre[r] : 0u;
      g0im[r] = g < 2 ? 0u : q;
      ga[0][r] = f2a[0][0][r] ^ NEGH; ga[1][r] = f2a[1][0][r] ^ NEGH;
    }
    twc *= 2.f / (2048.f * 512.f); tws *= 2.f / (2048.f * 512.f);
    g0re[0] = g == 0 ? ((c & 1) ? 0xbf00u : 0x3f00u) : g0re[0];
    ga[0][0] = g == 0 ? ((c & 1) ? 0xbf803f80u : 0x3f803f80u) : ga[0][0]; ga[1][0] = ga[0][0];
    PIN(gbre); PIN(gbim); PIN(g0re); PIN(g0im); PIN(ga[0]); PIN(ga[1]); PIN(twc); PIN(tws);
    f32x4 xin[2][2];
#pragma unroll
    for (int rr = 0; rr < 2; ++rr)
#pragma unroll
      for (int t = 0; t < 2; ++t) {
        const v2u v = xk[rr][t];
        const u32 v0 = v.x, v1 = v.y;
        f32x4 q = {blo(v0), bhi(v0), blo(v1), bhi(v1)};
        xin[rr][t] = q - mean[rr]; PIN(xin[rr][t]);
      }
    lp_t i3r[4], o3w[2], o3r[2];
#pragma unroll
    for (int r = 0; r < 4; ++r) i3r[r] = lptr(SCR + L_I3 + slot_of(c, 4 * g + r) * 32 + w * 8);
#pragma unroll
    for (int i = 0; i < 2; ++i) {
      o3w[i] = lptr(SCR + L_O3 + ((4 * c + g) * 8 + ((2 * w + i) ^ (c & 7))) * 16);
      const int q = w * 128 + i * 64 + lane;
      o3r[i] = lptr(SCR + L_O3 + (q ^ ((q >> 5) & 7)) * 16);
    }
    int vs = lane * 16 + WS_L + un * 8192 + w * 2048, vt = lane < 8 ? lane * 16 + WS_S + un * 512 + w * 128 : OOB;
    PIN(vs); PIN(vt);
    lp_t sow = lptr(SCR + L_SO + tid * 16), sor = lptr(SCR + L_SO + (lane & 6) * 512 + (lane & 1) * 8);
    WAIT2();
    STAMP(11);
    v4u xl[4], xg[2];
    GATHER(WS_B);
    flag_set(fmine, 7);
    SB();
    u32 bad = VERIFY(tag), spin = 0;
    if (__any(bad != 0)) {
      _Pragma("nounroll") do {
        __builtin_amdgcn_s_sleep(RS1);
        MEMFENCE();
        if (bad) GATHER(WS_B);
        bad = VERIFY(tag);
      } while (__any(bad != 0) && ++spin < SPIN_CAP);
      SLIPPED();
      ++spin;
    }
    COUNT(21, spin);
    STAMP(12);
    v4u a[2], a2[2];
    v2u zr[4];
    flag_set(fmine, 8);
    FLAG_WAIT(fall, 8, _Pragma("unroll") for (int r = 0; r < 4; ++r) zr[r] = lds_r64(i3r[r]));
    STAMP(13);
#pragma unroll
    for (int r = 0; r < 4; ++r) { a[0][r] = zr[r].x; a[1][r] = zr[r].y; }
    f32x4 tr[2], ti[2], sr[2], si[2], h[2][2];
#pragma unroll
    for (int rr = 0; rr < 2; ++rr) { tr[rr] = mm16p(a[rr], gbre, z); ti[rr] = mm16p(a[rr], gbim, z); sr[rr] = mm16p(a[rr], g0re, z); si[rr] = mm16p(a[rr], g0im, z); }
#pragma unroll
    for (int rr = 0; rr < 2; ++rr) {
      tr[rr][0] = g == 0 ? sr[rr][0] : tr[rr][0]; ti[rr][0] = g == 0 ? si[rr][0] : ti[rr][0];
      a2[rr] = pk4(tr[rr] * twc - ti[rr] * tws, ti[rr] * twc + tr[rr] * tws);
      h[rr][0] = mm16p(a2[rr], ga[0], xin[rr][0]); h[rr][1] = mm16p(a2[rr], ga[1], xin[rr][1]);
    }
    float st[2];
#pragma unroll
    for (int rr = 0; rr < 2; ++rr) { const f32x4 q = h[rr][0] * h[rr][0] + h[rr][1] * h[rr][1]; st[rr] = (q[0] + q[2]) + (q[1] + q[3]); }
    wave_sums<2>(st);
    v4u so;
#pragma unroll
    for (int rr = 0; rr < 2; ++rr) {
      const float ve = st[rr] * (1.f / 512) + 1e-5f, rstd = __builtin_amdgcn_rsqf(ve);
      const f32x4 l0 = h[rr][0] * rstd, l1 = h[rr][1] * rstd;
      v4u l4 = {pk(l0[0], l0[1]), pk(l0[2], l0[3]), pk(l1[0], l1[1]), pk(l1[2], l1[3])};
      lds_w128(o3w[rr], l4);
      so[2 * rr] = asu(mean[rr]); so[2 * rr + 1] = asu(ve * rstd);
    }
    lds_w128(sow, so);
    STAMP(14);
    v4u v0, v1;
    v2u ms;
    flag_set(fmine, 9);
    FLAG_WAIT(fall, 9, v0 = lds_r128(o3r[0]); v1 = lds_r128(o3r[1]); ms = lds_r64(sor));
    st_dev(ws, vs, v0); st_dev(ws, vs + 1024, v1);
    u32 s = sum4(sum4(0u, v0), v1);
    s += dppu<0x128>(s); s = add_x32(add_x16(s));
    v4u rec = {tag - s - ms.x - ms.y, ms.x, ms.y, tag};
    st_dev(ws, vt, rec);
    flag_set(fmine, 10);
  }
  SB();
  {
#define LOOKN 6
    constexpr int LOOK = LOOKN;
    v4u ring[8], act[32];
    lp_t wa[4], wb[4];
#pragma unroll
    for (int t = 0; t < 4; ++t) wa[t] = lptr(t * SLOT + g * 512 + ((c ^ (2 * t + (c >> 3))) * 16));
#pragma unroll
    for (int i = 0; i < 4; ++i) wb[i] = lptr(((i & 1) ? 0 : 4 * SLOT) + ((lane >> 2) ^ (g & 1)) * 64 + (((i >> 1) ^ g) & 1) * 32 + (lane & 3) * 8);
    auto frag = [&](auto NN) -> v4u {
      constexpr int N = NN.value;
      if constexpr (N < 64) return lds_r128(wa[(N >> 1) & 1] + (N >> 2) * 2048 + (N & 1) * 256);
      else if constexpr (N < 128) return lds_r128(wa[N >> 5] + ((N & 31) >> 1) * 2048 + (N & 1) * 256);
      else { const lp_t b = wb[((N >> 4) & 2) + (N & 1)] + ((N & 31) >> 1) * 2048; return lds_tr2(b, b + 1024); }
    };
    int vo = WS_H + ((4 * rg + w) * NFT + ft0) * HREC + lane * 16, vk = (lane & 11) ? OOB : vo - lane * 16 + 1024 + ((lane >> 4) * 8 + (lane & 7)) * 4;
    int vo4 = vo + 4 * HREC, vk4 = vk + 4 * HREC;
    PIN(vo); PIN(vk); PIN(vo4); PIN(vk4);
    Epi e;
#define EPI(J0, J1) static_for<(J0), (J1)>([&](auto JJ) { constexpr int kd = kEp.op[JJ.value].kind, V = kEp.op[JJ.value].v, t = V >> 3; \
      if constexpr (kd == E_ST) st_dev(ws, (t < 4 ? vo : vo4) + (t & 3) * HREC, e.r); \
      else if constexpr (kd == E_STK) st_dev(ws, (t < 4 ? vk : vk4) + (t & 3) * HREC, e.k4); \
      else epi_op<kd, V>(e, t % 3 == 0 ? tA : t % 3 == 1 ? tB : tC, tag); \
      SB(); })
    int va0 = ((lane >> 3) & 1) * 8192 + g * 128 + (lane & 7) * 16, va1 = va0 + 16384;
    int vr = ((lane >> 3) & 1) * 512 + g * 128 + (lane & 7) * 16;
    PIN(vr);
    const int sa = WS_L + (4 * rg + w) * 32768;
#define ACT_LD(i) act[i] = ld_dev(ws, (((i) & 1) ? va1 : va0) + (((i) >> 1) & 7) * 512, sa + ((i) >> 4) * 4096)
    f32x4 tA[4], tB[4], tC[4];
    WD_SETUP();
    v4u s0, s1;
#define A_HEAD() do { MEMFENCE(); PIN(va0); PIN(va1); s0 = ld_dev(ws, vr, WS_S + (4 * rg + w) * 2048); s1 = ld_dev(ws, vr + 1024, WS_S + (4 * rg + w) * 2048); SB(); \
      static_for<0, 2 * LOOK>([&](auto K) { ACT_LD(K.value); SB(); }); } while (0)
    WAIT3();
    STAMP(15);
    u32 spin = 0;
    A_HEAD();
    BAR();
    for (;; ++spin) {
      static_for<0, PD>([&](auto K) { ring[K.value ^ 1] = FRAG(K.value ^ 1); SB(); });
      if (__builtin_expect(__any(s0.w != tag || s1.w != tag), 0)) { _Pragma("nounroll") do { __builtin_amdgcn_s_sleep(RS3); A_HEAD(); } while (__any(s0.w != tag || s1.w != tag) && ++spin < SPIN_CAP); ++spin; }
      PIN(va0); PIN(va1);
      u32 m = (s0.x + s0.y + s0.z) + (s1.x + s1.y + s1.z);
      static_for<0, 64>([&](auto K) {
        constexpr int n = K.value, ks = n >> 2, q = (n & 1) * 2;
        f32x4* acc = (n & 2) ? tB : tA;
        mma<ks == 0>(acc[q], ring[n & 7], act[2 * ks]); SB();
        ring[NXT(n) & 7] = FRAG(NXT(n));
        if constexpr (!(n & 1) && ks + LOOK < 16) ACT_LD(2 * (ks + LOOK) + ((n >> 1) & 1));
        SB();
        mma<ks == 0>(acc[q + 1], ring[n & 7], act[2 * ks + 1]); SB();
        { const v4u v = act[2 * ks + ((n >> 1) & 1)]; u32 t = (n & 1) ? m + v.z + v.w : m + v.x + v.y; PIN(t); m = t; }
        SB();
      });
      if (__builtin_expect(__all(add_x32(add_x16(m)) == 8u * tag) || spin > SPIN_CAP, 1)) break;
      s0.w = ~tag;
    }
    COUNT(22, spin);
    BAR_N(15);
    STAMP(16);
#define G1_TILE(T, acc, ...) static_for<0, 32>([&](auto K) { constexpr int s = K.value, n = 32 * (T) + s, ks = s >> 1, q = (s & 1) * 2, j0 = epos(n), j1 = epos(n + 1), jm = j0 + (j1 - j0 - (T) / 4) / 2; \
      mma<ks == 0>(acc[q], ring[n & 7], act[2 * ks]); SB(); \
      if constexpr (n + PD < 192) ring[NXT(n) & 7] = FRAG(n + PD < 192 ? NXT(n) : 0); SB(); \
      EPI(j0, jm); \
      mma<ks == 0>(acc[q + 1], ring[n & 7], act[2 * ks + 1]); SB(); \
      __VA_ARGS__; EPI(jm, j1); SB(); })
#define PG2 8
#define PG3 10
    G1_TILE(2, tC, { });
    FSTAMP(26);
    G1_TILE(3, tA, { if constexpr (s == PG2) BAR_N(15); });
    FSTAMP(27);
    if (six) {
      G1_TILE(4, tB, { if constexpr (s == PG3) BAR_N(15); });
      FSTAMP(28);
      G1_TILE(5, tC, { });
      BAR_N(15);
      STAMP(17);
      MMA_DONE(); SB();
      EPI(kEp.first[5], kEp.n);
    } else {
      BAR_N(15);
      MMA_DONE(); SB();
      EPI(epos(128), kEp.first[4]);
      static_for<E_X1, E_STK>([&](auto KK) { epi_op<KK.value, 31>(e, tA, tag); });
      st_dev(ws, vk + 3 * HREC, e.k4);
#define SKSL 36
      __builtin_amdgcn_s_sleep(SKSL);
      FSTAMP(28);
      BAR_N(15);
      STAMP(17);
    }
    STAMP(18);
    DMA_WDS(4, 0);
    BAR();
    DMA_WDS(5, 1);
    if (w & 1) VM_WAIT(4); else VM_WAIT(8);
    BAR();
    VM_WAIT(0);
    BAR();
  }
  STAMP(24);
  STAMP_FLUSH();
  UNDEF_RA();
  asm volatile("" : "=v"(wa_));
  }
  PINS(wv);
  if (wv >= 4) {
    STAMP(0);
    const int hw = wv - 4;
    asm volatile("" :: "v"(cpf), "v"(ra[0]));
    if (hw < 2) {
    const rsrc_t ws = mkrs(p.ws, WS_END);
    const int T = tid - 256, vs = wa_.x;
    const u32 sh = wa_.y;
    const lp_t ca0 = (lp_t)(size_t)wa_.z, ca1 = (lp_t)(size_t)wa_.w;
    u32 al[8][2], ck[4];
#pragma unroll
    for (int i = 0; i < 8; ++i) { const u32 a = ra[i].x, b = ra[i].y, d = ra[i].z; al[i][0] = __builtin_amdgcn_alignbit(b, a, sh); al[i][1] = __builtin_amdgcn_alignbit(d, b, sh); }
    v4u v[4];
#pragma unroll
    for (int e = 0; e < 4; ++e) {
#pragma unroll
      for (int r = 0; r < 4; ++r) v[e][r] = __builtin_amdgcn_perm(al[4 + r][e >> 1], al[r][e >> 1], (e & 1) ? 0x07060302u : 0x05040100u);
      st_dev(ws, vs + e * 2048, v[e]);
      ck[e] = sum4(0u, v[e]);
    }
    lds_add(ca0, pair_total(ck[0], ck[2], T & 1)); lds_add(ca1, pair_total(ck[1], ck[3], T & 1));
    flag_set(fmine, 1);
    STAMP(1);
  }
    u32 tag;
    v4u y0[8], y1[8];
    WD_SETUP();
    {
      const rsrc_t rwg = mkrs(p.wg, W1SZ), rwu = mkrs(p.wu, W1SZ);
      const int vx = 4 * (lane >> 3) * (F_ * 2) + ft0 * 32 + (lane & 7) * 16, vx8 = vx + 32 * F_ * 2;
      const int vy = six ? 4 * (lane >> 2) * (F_ * 2) + (ft0 + 4) * 32 + (lane & 3) * 16 : OOB;
      const u32 ax = ((lane >> 1) & 3) * SLOT + (16 * hw + (lane >> 3)) * 512 + (lane & 1) * 128 + (lane & 7) * 16;
#define PUT_X(u, off) do { transpose8(u); const u32 a_ = ax + (off); _Pragma("unroll") for (int i = 0; i < 8; ++i) lds_w128((lp_t)(size_t)(a_ ^ (i * 16)), u[i]); SB(); } while (0)
      v4u rb[8], rc[8], rd[8];
      const lp_t fall = lptr(SCR + L_F + (lane & 3) * 256 + lane * 4);
      const int s0 = 64 * hw * (F_ * 2);
#define NA 2
#define NB 10
#define N0 0
#define NS 8
#define NL 99
#define NH 2
#define TS 0
#define F1 2
#define F2 5
#define STEP(n) do { constexpr int j = (n) & 7; const int so_ = s0 + ((j & 3) + 256 * (j >> 2)) * (F_ * 2); \
        if ((n) == NA) { flag_wait_lazy(fall, 3); cpf = touch(rwd, lane < 11 ? wg * 43 + hw * 11 + lane : 16384); } if ((n) == NB) flag_wait_lazy(fall, 7); \
        if ((n) == NA + NH) flag_wait_lazy(fall, 4); if ((n) == NB + NH) flag_wait_lazy(fall, 8); \
        if ((n) == N0) flag_wait_lazy(fall, 2); if ((n) == NS) flag_wait_lazy(fall, 6); if ((n) == NL) flag_wait_lazy(fall, 10); \
        if ((n) > 1) asm volatile("s_waitcnt vmcnt(%0)" :: "n"(THR) : "memory"); \
        if ((n) < 8) { ra[j] = ld_in(rwg, vx, so_); rb[j] = ld_in(rwg, vx8, so_); y0[j] = ld_in(rwg, vy, so_); } \
        else { rc[j] = ld_in(rwu, vx, so_); rd[j] = ld_in(rwu, vx8, so_); y1[j] = ld_in(rwu, vy, so_); } SB(); } while (0)
      PACE(0, TS);
      STAMP(2);
      STEP(0); STEP(1); STEP(2); STEP(3); STEP(4); STEP(5); STEP(6); STEP(7); STEP(8); STEP(9);
      STAMP(3);
      if (NB > 10) STEP(10);
      if (NB > 11) STEP(11);
      PUT_X(ra, 0);
      PUT_X(rb, 4096);
      if (NB <= 10) STEP(10);
      if (NB <= 11) STEP(11);
      STEP(12); STEP(13); STEP(14); STEP(15);
      tag = *(LAS u32*)(fall + L_TG);
      STAMP(4);
      PUT_X(rc, 256); PUT_X(rd, 4096 + 256);
      PIN(tag);
      asm volatile("" :: "v"(cpf));
    }
    STAMP(5);
    BAR();
    const rsrc_t ws = mkrs(p.ws, WS_END);
    const int kh = hw & 1, rl = hw >> 1, g = lane >> 4;
    constexpr int PD = 4;
    const u32 ayo = hw * 8192 + g * 2048 + ((lane >> 2) & 3) * 256 + (lane & 4) * 16 + ((lane & 3) ^ ((lane >> 1) & 2)) * 16;
    const lp_t ay0[2] = {lptr(ayo + 4 * SLOT), lptr((ayo ^ 64) + 4 * SLOT)}, ay1[2] = {lptr(ayo), lptr(ayo ^ 64)}, ad = lptr(3 * SLOT + hw * 8192 + lane * 16);
#define PUT_Y(a, y, j0, j1) _Pragma("unroll") for (int j = j0; j < j1; ++j) lds_w128(a[j & 1] + (j >> 2) * 1024 + (j & 2) * 64, y[j])
    PUT_Y(ay0, y0, 0, 8);
    v4u ring[8], hfA[8], hfB[8], hfC[8], hfD[8], hfE[8];
    u32 hkA, hkB, hkC, hkD, hkE, ms = 0, nfix = 0;
    lp_t wd[3][4];
    {
      const u32 dl = (g * 1024 + ((lane >> 2) & 3) * 128 + ((lane >> 3) & 3) * 32 + (lane & 3) * 8) ^ (kh * 64);
#pragma unroll
      for (int i = 0; i < 12; ++i) wd[i >> 2][i & 3] = lptr((dl ^ ((i & 3) * 32)) + (i >> 2) * 2 * SLOT + kh * 16384);
    }
    auto frag = [&](auto NN) -> v4u {
      constexpr int N = NN.value, t = N < 64 ? N >> 4 : N < 76 ? 4 : 5, s = N < 64 ? (N & 15) : N < 76 ? N - 64 : N - 76, slot = (t + 1) % 5;
      const lp_t b = wd[slot >> 1][s & 3] + (slot & 1) * SLOT + (s >> 2) * 4096;
      return lds_tr2(b, b + 512);
    };
    const int hb = WS_H + (4 * rg + 2 * (ch >> 3) + rl) * NFT * HREC;
    int hs[4];
#pragma unroll
    for (int i = 0; i < 4; ++i) { hs[i] = hb + tile0_of(8 * kh + 2 * i) * HREC; PINS(hs[i]); }
    int vl = (g & 1) * 512 + (lane & 15) * 16 + (g >> 1) * 6 * HREC, vl3 = vl - (g >> 1) * 2 * HREC, vl4 = vl + 4 * HREC, vlz = (kh && g >= 2) ? OOB : vl4, vq, vq4;
    {
      const int st = (lane & 2) + ((lane >> 1) & 4) + (g >> 1);
      vq = hb + 1024 + ((2 * (g & 1) + (lane & 1)) * 8 + (lane & 4)) * 4 + tile0_of(8 * kh + st) * HREC;
      vq4 = st < 6 - kh ? vq + 4 * HREC : OOB;
    }
    const int u = 16 * rg + 8 * (ch >> 3) + 4 * rl + ((lane >> 3) & 1), col = 64 * (ch & 7) + 32 * kh;
    int vh = u * 8192 + (col & 255) * 32 + g * 128 + (lane & 7) * 16 + (col >> 8) * 8, vs = u * 512 + (lane & 7) * 16 + 4;
    int vout = ((32 * (8 * (u >> 5) + (lane & 7)) + (u & 31)) * D_ + col + 16 * (g & 1) + 8 * (g >> 1)) * 2;
    lp_t rw = lptr(2 * SLOT + hw * 4096 + lane * 16), rr_ = lptr(2 * SLOT + (hw ^ 1) * 4096 + lane * 16);
    u32 t8 = 8u * tag, t6 = ((kh && g >= 2) ? 4u : 6u) * tag;
    PIN(vl); PIN(vl3); PIN(vl4); PIN(vlz); PIN(vq); PIN(vq4); PIN(vh); PIN(vs); PIN(vout); PIN(t8); PIN(t6);
#define H_LD(X, t, j) do { \
      if constexpr ((j) == 8) hk##X = ld_dev1(ws, ((t) < 4 ? vq : vq4) + ((t) & 3) * HREC, 0); \
      else if constexpr ((t) < 4 || (j) < 6) hf##X[(j) & 7] = ld_dev(ws, ((t) < 4 ? ((j) < 6 ? vl : vl3) : (j) < 4 ? vl4 : vlz) + ((t) & 3) * HREC + ((j) & 1) * 256, hs[((j) >> 1) & 3]); } while (0)
#define H_LDALL(X, t) static_for<0, 9>([&](auto J) { THROTTLE(J.value); H_LD(X, t, J.value ? J.value - 1 : 8); })
#define H_SUM(X, nf, j) do { if constexpr ((j) >= 0 && (j) < (nf)) { ms = sum4((j) ? ms : hk##X, hf##X[(j) & 7]); u32 t_ = ms; PIN(t_); ms = t_; } \
      else if constexpr ((j) >= (nf) && (j) < (nf) + 3) { u32 t_ = ms + dppu<(j) == (nf) ? 0xb1 : (j) == (nf) + 1 ? 0x4e : 0x128>(ms); PIN(t_); ms = t_; } } while (0)
#define H_SUMALL(X, nf) static_for<0, (nf) + 3>([&](auto J) { H_SUM(X, nf, J.value); })
#define H_FIX(X, t, nf, want) do { if (__builtin_expect(__any(ms != (want)), 0)) { u32 spin_ = 0; _Pragma("nounroll") do { __builtin_amdgcn_s_sleep(RS3); MEMFENCE(); nfix |= 1u << (4 * (t)); \
        H_LDALL(X, t); H_SUMALL(X, nf); } while (__any(ms != (want)) && ++spin_ < SPIN_CAP); } SB(); } while (0)
    v4u d2[8], d3[8];
    f32x4 o[4][2];
    v2u hr[4], so[2];
#define GRP() asm volatile("s_waitcnt vmcnt(%0)" :: "n"(TH2) : "memory")
#define RAW4(dst, t, i0) do { GRP(); _Pragma("unroll") for (int i = i0; i < i0 + 4; ++i) dst[i] = ld_in(rwd, (i & 1) ? vd1 : vd, sd[i >> 1] + ((t) * 16 + (i & 1) * 8) * (D_ * 2)); SB(); } while (0)
#define DMA4(t, slot, i0) do { GRP(); _Pragma("unroll") for (int i = i0; i < i0 + 4; ++i) DMA_WD1(t, slot, i); } while (0)
#define H4(X, t, h) do { GRP(); static_for<5 * (h), 5 + 4 * (h)>([&](auto J) { H_LD(X, t, J.value ? J.value - 1 : 8); }); SB(); } while (0)
#define PUT_D(d, off, i0, i1) do { _Pragma("unroll") for (int i = i0; i < i1; ++i) lds_w128(ad + (off) + i * 1024, d[i]); SB(); } while (0)
#define RESID(i) do { const u32 v0 = hr[i].x, v1 = hr[i].y, mb = so[(i) & 1].x, sb = so[(i) & 1].y; float q[4] = {blo(v0), bhi(v0), blo(v1), bhi(v1)}; \
      _Pragma("unroll") for (int r = 0; r < 4; ++r) { q[r] = __builtin_fmaf(q[r], asf(sb), asf(mb)); PIN(q[r]); o[(i) >> 1][(i) & 1][r] = q[r]; } PIN(o[(i) >> 1][(i) & 1]); SB(); } while (0)
#define PSLEEP 16
    __builtin_amdgcn_s_sleep(PSLEEP);
    RAW4(d2, 2, 0); RAW4(d2, 2, 4);
    STAMP(6);
    BAR();
    DMA4(0, 1, 0); PUT_Y(ay1, y1, 0, 3);
    DMA4(0, 1, 4); PUT_Y(ay1, y1, 3, 6);
    GRP();
#pragma unroll
    for (int i = 0; i < 2; ++i) so[i] = __builtin_amdgcn_raw_buffer_load_b64(ws, vs + i * 1024, WS_S, AUX_SC1);
#pragma unroll
    for (int i = 0; i < 4; ++i) hr[i] = __builtin_amdgcn_raw_buffer_load_b64(ws, vh + (i >> 1) * 512, WS_L + (i & 1) * 16384, AUX_SC1);
    PUT_Y(ay1, y1, 6, 8);
    RAW4(d3, 3, 0);
    STAMP(7);
    BAR();
    RAW4(d3, 3, 4); RESID(0); RESID(1); RESID(2); RESID(3);
    DMA4(1, 2, 0); DMA4(1, 2, 4);
    H4(A, 0, 0);
    STAMP(8);
    BAR();
    H4(A, 0, 1); PUT_D(d2, 0, 0, 3);
    H4(B, 1, 0); PUT_D(d2, 0, 3, 6);
    H4(B, 1, 1); PUT_D(d2, 0, 6, 8);
    H4(C, 2, 0); H_SUMALL(A, 8);
    STAMP(9);
    H4(C, 2, 1);
    H_FIX(A, 0, 8, t8);
    static_for<0, PD>([&](auto K) { ring[K.value ^ 1] = FRAG(K.value ^ 1); SB(); });
    STAMP(10);
    BAR();
    STAMP(11);
#define G2_ROUND(X, n0, ns, ...) static_for<0, ns>([&](auto K) { constexpr int s = K.value, n = (n0) + s, ct = s & 3, a = (s >> 2) * 2; \
      mma<n == 2 || n == 3>(o[ct][0], ring[n & 7], hf##X[a]); SB(); \
      if constexpr (n + PD < 88) ring[NXT(n) & 7] = FRAG(n + PD < 88 ? NXT(n) : 0); SB(); \
      mma<n == 2 || n == 3>(o[ct][1], ring[n & 7], hf##X[a + 1]); SB(); \
      __VA_ARGS__; SB(); })
#define L3 6
#define L4 36
#define L5 48
#define LATE() do { if constexpr (n >= L3 && n < L3 + 9) H_LD(D, 3, n > L3 ? n - L3 - 1 : 8); \
      if constexpr (n >= L4 && n < L4 + 7) H_LD(E, 4, n > L4 ? n - L4 - 1 : 8); if constexpr (n >= L5 && n < L5 + 7) H_LD(A, 5, n > L5 ? n - L5 - 1 : 8); } while (0)
    G2_ROUND(A, 0, 16, { H_SUM(B, 8, s - 5); LATE(); });
    H_FIX(B, 1, 8, t8);
    FSTAMP(14);
    G2_ROUND(B, 16, 16, { if constexpr (s < 8) lds_w128(ad + SLOT + s * 1024, d3[s & 7]); H_SUM(C, 8, s - 5); LATE(); });
    BAR_N(8);
    H_FIX(C, 2, 8, t8);
    FSTAMP(15);
    G2_ROUND(C, 32, 16, { LATE(); H_SUM(D, 8, s - 5); });
    H_FIX(D, 3, 8, t8);
    FSTAMP(16);
    G2_ROUND(D, 48, 16, { LATE(); H_SUM(E, 6, s - 7); if constexpr (s == 11) BAR_N(15); });
    H_FIX(E, 4, 6, t6);
    FSTAMP(17);
    G2_ROUND(E, 64, 12, { H_SUM(A, 6, s - 3); if constexpr (s == 7) BAR_N(15); });
    H_FIX(A, 5, 6, t6);
    FSTAMP(18);
    G2_ROUND(A, 76, 12, { });
    COUNT(23, nfix);
    STAMP(12);
    MMA_DONE(); SB();
#pragma unroll
    for (int i = 0; i < 4; ++i) lds_wf(rw + i * 1024, o[2 + (i & 1)][i >> 1]);
    BAR();
    const rsrc_t ro = mkrs(p.out, S_ * D_ * 2);
#pragma unroll
    for (int i = 0; i < 2; ++i) {
      const f32x4 v0 = o[0][i] + lds_rf(rr_ + i * 2048), v1 = o[1][i] + lds_rf(rr_ + i * 2048 + 1024);
      const auto x = __builtin_amdgcn_permlane16_swap(pk(v0[0], v0[1]), pk(v1[0], v1[1]), false, false);
      const auto y = __builtin_amdgcn_permlane16_swap(pk(v0[2], v0[3]), pk(v1[2], v1[3]), false, false);
      const v4u q = {x[0], y[0], x[1], y[1]};
      __builtin_amdgcn_raw_buffer_store_b128(q, ro, vout + i * 2048, 0, 0);
    }
    STAMP(13);
    STAMP_FLUSH();
  }
}

extern "C" void kernel_launch(void* const* d_in, const int* in_sizes, int n_in,
                              void* d_out, int out_size, void* d_ws, size_t ws_size,
                              hipStream_t stream) {
  Params p;
  p.x = (const u16*)d_in[0]; p.wr = (const u16*)d_in[1]; p.wi = (const u16*)d_in[2];
  p.wg = (const u16*)d_in[4]; p.wu = (const u16*)d_in[5]; p.wd = (const u16*)d_in[6];
  p.out = (u16*)d_out; p.ws = d_ws;
  (void)hipFuncSetAttribute((const void*)gfnet_geglu_kernel, hipFuncAttributeMaxDynamicSharedMemorySize, 5 * SLOT);
  gfnet_geglu_kernel<<<256, 512, 5 * SLOT, stream>>>(p);
}
```

```cpp
#define THR 4
#define TW 240
#define T1 270
#define T2 545
#define T3 760
#define NL 14
#define OAUX 19
#include <hip/hip_runtime.h>
#include <hip/hip_bf16.h>
#include <type_traits>

typedef unsigned u32;
typedef unsigned short u16;
typedef u32 v4u __attribute__((ext_vector_type(4)));
typedef u32 v2u __attribute__((ext_vector_type(2)));
typedef float f32x2 __attribute__((ext_vector_type(2)));
typedef float f32x4 __attribute__((ext_vector_type(4)));
typedef __bf16 bf16x2 __attribute__((ext_vector_type(2)));
typedef __bf16 bf16x8 __attribute__((ext_vector_type(8)));
typedef short s16x4 __attribute__((ext_vector_type(4)));
#define LAS __attribute__((address_space(3)))
#define DI __device__ __forceinline__
typedef __amdgpu_buffer_rsrc_t rsrc_t;
typedef LAS char* lp_t;

constexpr int S_ = 2048, D_ = 512, F_ = 1376, KF = 257;
constexpr int NFT = 86;
constexpr int WS_E = 0;
constexpr int XREC = 88 * 128;
constexpr int WS_A = 4096;
constexpr int WS_B = WS_A + 256 * XREC;
constexpr int WS_W = WS_B + 256 * XREC;
constexpr int WS_L = WS_W + (2 << 20);
constexpr int WS_T = WS_L + (2 << 20);
constexpr int WS_S = WS_T + (1 << 15);
constexpr int WS_H = WS_S + (1 << 17);
constexpr int HREC = 1152;
constexpr int WS_END = WS_H + 64 * NFT * HREC;
constexpr int WS_ST = WS_END;
constexpr u32 NEG2 = 0x80008000u, NEGH = 0x80000000u, NEGL = 0x8000u;
constexpr u32 SPIN_CAP = 1u << 14;
constexpr int AUX_SC1 = 16;
#define NTAUX 2
constexpr int OOB = 0x40000000;
constexpr int CODE_LINES = 250;
constexpr int SLOT = 32768, SCR = 4 * SLOT;
constexpr int L_F = 0, L_TG = 2048;
constexpr int L_IM = 4096, L_SO = 4096, L_X = 16384, L_I2 = 16384, L_I3 = 16384, L_EX = 24576, L_O3 = 24576;

#define DRAIN() asm volatile("s_waitcnt vmcnt(0)" ::: "memory")
#define MEMFENCE() asm volatile("" ::: "memory")
#define PIN(x) asm volatile("" : "+v"(x))
#define PINS(x) asm volatile("" : "+s"(x))
#define SB() __builtin_amdgcn_sched_barrier(0)
#define BAR() asm volatile("s_waitcnt lgkmcnt(0)\n\ts_barrier" ::: "memory")
#define BAR_N(n) asm volatile("s_waitcnt lgkmcnt(" #n ")\n\ts_barrier" ::: "memory")

DI u32 pk(float a, float b) { f32x2 v = {a, b}; return __builtin_bit_cast(u32, __builtin_convertvector(v, bf16x2)); }
DI v4u pk4(f32x4 a, f32x4 b) { v4u r = {pk(a[0], b[0]), pk(a[1], b[1]), pk(a[2], b[2]), pk(a[3], b[3])}; return r; }
DI float asf(u32 v) { return __builtin_bit_cast(float, v); }
DI u32 asu(float v) { return __builtin_bit_cast(u32, v); }
DI float blo(u32 v) { return asf(v << 16); }
DI float bhi(u32 v) { return asf(v & 0xffff0000u); }
DI float dot2(u32 a, u32 b, float c) { return __builtin_amdgcn_fdot2_f32_bf16(__builtin_bit_cast(bf16x2, a), __builtin_bit_cast(bf16x2, b), c, false); }
DI u32 sum4(u32 m, v4u v) { return (m + v.x + v.y) + v.z + v.w; }
template <int CTRL> DI u32 dppu(u32 v) { return (u32)__builtin_amdgcn_update_dpp(0, (int)v, CTRL, 0xf, 0xf, true); }
template <int CTRL> DI float dppf(float v) { return asf(dppu<CTRL>(asu(v))); }
DI u32 pair_total(u32 s0, u32 s1, bool odd) { return (odd ? s1 : s0) + dppu<0xb1>(odd ? s0 : s1); }
DI u32 add_x16(u32 v) { const auto s = __builtin_amdgcn_permlane16_swap(v, v, false, false); return s[0] + s[1]; }
DI u32 add_x32(u32 v) { const auto s = __builtin_amdgcn_permlane32_swap(v, v, false, false); return s[0] + s[1]; }
template <int N> DI void wave_sums(float (&v)[N]) {
#define WS_STEP(C) _Pragma("unroll") for (int i = 0; i < N; ++i) v[i] += dppf<C>(v[i]);
  WS_STEP(0x111) WS_STEP(0x112) WS_STEP(0x114) WS_STEP(0x118) WS_STEP(0x142) WS_STEP(0x143)
#pragma unroll
  for (int i = 0; i < N; ++i) v[i] = asf(__builtin_amdgcn_readlane(asu(v[i]), 63));
}
__host__ __device__ constexpr int rec_q(int k) { return (k & 7) | ((k >> 1) & 0x18); }
__host__ __device__ constexpr int rec_line(int k) { return 11 * ((k >> 3) & 1) + ((rec_q(k) * 11) >> 5); }
__host__ __device__ constexpr int rec_pos(int k) { return rec_q(k) - 3 * ((rec_q(k) * 11) >> 5); }
__host__ __device__ constexpr int im_unit(int k, int j) { return L_IM + (k >> 6) * 3072 + rec_line(k) * 128 + ((j ^ (rec_line(k) + 2 * (k >> 6))) & 7) * 16; }
__host__ __device__ constexpr int im_chk(int k) { return im_unit(k, 6) + 4 * rec_pos(k); }
DI void lds_add(lp_t a, u32 v) { __hip_atomic_fetch_add((LAS u32*)a, v, __ATOMIC_RELAXED, __HIP_MEMORY_SCOPE_WORKGROUP); }
DI f32x4 mm16(v4u a, v4u b, f32x4 c) { return __builtin_amdgcn_mfma_f32_16x16x32_bf16(__builtin_bit_cast(bf16x8, a), __builtin_bit_cast(bf16x8, b), c, 0, 0, 0); }
DI f32x4 mm16p(v4u a, v4u b, f32x4 c) { f32x4 d = mm16(a, b, c); PIN(d); return d; }
template <bool FIRST> DI void mma(f32x4& d, v4u a, v4u b) {
  if constexpr (FIRST) asm volatile("v_mfma_f32_16x16x32_bf16 %0, %1, %2, 0" : "=&v"(d) : "v"(a), "v"(b));
  else asm volatile("v_mfma_f32_16x16x32_bf16 %0, %1, %2, %0" : "+v"(d) : "v"(a), "v"(b));
}
#define MMA_DONE() asm volatile("s_nop 7")
DI v2u lds_tr(lp_t a) { return __builtin_bit_cast(v2u, __builtin_amdgcn_ds_read_tr16_b64_v4i16((LAS s16x4*)a)); }
DI v4u lds_tr2(lp_t a, lp_t b) { v2u x = lds_tr(a), y = lds_tr(b); v4u r = {x.x, x.y, y.x, y.y}; return r; }
DI v4u lds_r128(lp_t a) { return *(LAS v4u*)a; }
DI f32x4 lds_rf(lp_t a) { return *(LAS f32x4*)a; }
DI v2u lds_r64(lp_t a) { return *(LAS v2u*)a; }
DI void lds_w128(lp_t a, v4u v) { *(LAS v4u*)a = v; }
DI void lds_wf(lp_t a, f32x4 v) { *(LAS f32x4*)a = v; }
DI void lds_w64(lp_t a, v2u v) { *(LAS v2u*)a = v; }
DI lp_t lptr(u32 off) { lp_t q = (lp_t)(size_t)off; PIN(q); return q; }
DI rsrc_t mkrs(const void* p, u32 bytes) { return __builtin_amdgcn_make_buffer_rsrc((void*)p, 0, bytes, 0x00020000); }
DI v4u ld_dev(rsrc_t r, int v, int s) { return __builtin_amdgcn_raw_buffer_load_b128(r, v, s, AUX_SC1); }
DI u32 ld_dev1(rsrc_t r, int v, int s) { return __builtin_amdgcn_raw_buffer_load_b32(r, v, s, AUX_SC1); }
DI void st_dev(rsrc_t r, int v, v4u x) { __builtin_amdgcn_raw_buffer_store_b128(x, r, v, 0, AUX_SC1); }
DI void st_dev1(rsrc_t r, int v, u32 x) { __builtin_amdgcn_raw_buffer_store_b32(x, r, v, 0, AUX_SC1); }
DI v4u ld_in(rsrc_t r, int v, int s) { return __builtin_amdgcn_raw_buffer_load_b128(r, v, s, 0); }
DI v4u ld_once(rsrc_t r, int v, int s) { return __builtin_amdgcn_raw_buffer_load_b128(r, v, s, NTAUX); }
DI u32 touch(rsrc_t r, int line) { return __builtin_amdgcn_raw_buffer_load_b32(r, line * 128, 0, 0); }
DI void flag_set(lp_t mine, u32 k) { MEMFENCE(); *(volatile LAS u32*)mine = k; MEMFENCE(); }
#define FLAG_WAIT(theirs, k, ...) do { u32 f_; _Pragma("nounroll") do { f_ = *(volatile LAS u32*)(theirs); MEMFENCE(); __VA_ARGS__; MEMFENCE(); } while (!__all(f_ >= (k))); } while (0)
DI void flag_wait_lazy(lp_t theirs, u32 k) { while (!__all(*(volatile LAS u32*)theirs >= k)) __builtin_amdgcn_s_sleep(1); MEMFENCE(); }
DI float cosr(float rev) { return __builtin_amdgcn_cosf(rev); }
DI float sinr(float rev) { return __builtin_amdgcn_sinf(rev); }
__host__ __device__ constexpr int slot_of(int k1, int k2) { return ((k2 < 8 ? k1 + 32 * k2 : (k1 ? 32 - k1 : 16) + 32 * (15 - k2)) - 1) & 255; }
DI int tile0_of(int c) { const int h = c >> 3, i = c & 7; return 44 * h + 4 * i + 2 * (i < 6 - h ? i : 6 - h); }
template <int I, int N, class F> DI void static_for(F&& f) {
  if constexpr (I < N) { f(std::integral_constant<int, I>{}); static_for<I + 1, N>(f); }
}
DI void transpose8(v4u (&m)[8]) {
  v4u o[8];
#pragma unroll
  for (int i = 0; i < 8; ++i)
#pragma unroll
    for (int j = 0; j < 4; ++j) { const u32 a = m[2 * j][i >> 1], b = m[2 * j + 1][i >> 1]; o[i][j] = __builtin_amdgcn_perm(b, a, (i & 1) ? 0x07060302u : 0x05040100u); }
#pragma unroll
  for (int i = 0; i < 8; ++i) { m[i] = o[i]; PIN(m[i]); }
}
enum { E_SQ, E_POLY, E_ARG, E_EXP, E_INC, E_RCP, E_GU, E_OUT, E_PACK, E_SWA, E_SWB, E_SUM, E_SUM2, E_ST, E_X1, E_X2, E_X8, E_CHK, E_STK };
struct EOp { int kind, v; };
struct EProg { EOp op[520]; int n, first[7]; };
constexpr EProg make_eprog() {
  EProg p = {};
  for (int t = 0; t < 6; ++t) {
    p.first[t] = p.n;
    for (int v = 0; v <= 8; ++v) {
      const int V = 8 * t + v;
      const bool head = v < 8, tail = v > 0, fin = t > 0 && t < 5;
      if (head) p.op[p.n++] = {E_SQ, V};
      if (tail) p.op[p.n++] = {E_INC, V - 1};
      if (head) p.op[p.n++] = {E_POLY, V}; else p.op[p.n++] = {E_GU, V - 1};
      if (tail) p.op[p.n++] = {E_RCP, V - 1}; else if (fin) p.op[p.n++] = {E_X1, V - 1};
      if (head) p.op[p.n++] = {E_ARG, V};
      if (tail && head) p.op[p.n++] = {E_GU, V - 1};
      if (v >= 3 && (v & 1)) p.op[p.n++] = {E_PACK, V - 2};
      if (v == 8) p.op[p.n++] = {E_SWA, V - 1};
      if (fin && v == 0) p.op[p.n++] = {E_X2, V - 1};
      if (fin && v == 1) p.op[p.n++] = {E_CHK, V - 2};
      if (head) p.op[p.n++] = {E_EXP, V};
      if (fin && v == 0) p.op[p.n++] = {E_X8, V - 1};
      if (fin && v == 1) p.op[p.n++] = {E_STK, V - 2};
      if (tail) p.op[p.n++] = {E_OUT, V - 1};
    }
    p.op[p.n++] = {E_PACK, 8 * t + 7}; p.op[p.n++] = {E_SUM, 8 * t + 7}; p.op[p.n++] = {E_SWB, 8 * t + 7}; p.op[p.n++] = {E_ST, 8 * t + 7}; p.op[p.n++] = {E_SUM2, 8 * t + 7};
    if (t >= 4) for (int k = E_X1; k <= E_STK; ++k) p.op[p.n++] = {k, 8 * t + 7};
  }
  p.first[6] = p.n;
  return p;
}
constexpr EProg kEp = make_eprog();
#define ER1 56
constexpr int epos(int n) {
  const int mid = (64 * ER1) / 16, end = kEp.first[5];
  int j = n <= 64 ? 0 : n <= 128 ? ((n - 64) * ER1) / 16 : n <= 192 ? mid + ((end - mid) * (n - 128)) / 64
        : end + ((kEp.n - end) * (n - 192)) / 8;
  for (int t = 2; t < 5; ++t) if (n <= 32 * t + 34 && j > kEp.first[t]) j = kEp.first[t];
  return j;
}
struct Epi { float t[2], q, h[3]; v4u r, k4; u32 sum, fin; };
template <int KIND, int V> DI void epi_op(Epi& e, const f32x4 (&a)[4], u32 tag) {
  constexpr int v = V & 7;
  const float gv = a[v >> 2][v & 3], uv = a[2 + (v >> 2)][v & 3];
  float& t = e.t[V & 1];
  if constexpr (KIND == E_SQ) { t = gv * gv; PIN(t); }
  else if constexpr (KIND == E_POLY) { t = __builtin_fmaf(t, -0.10294324f, -2.3022082f); PIN(t); }
  else if constexpr (KIND == E_ARG) { t = t * gv; PIN(t); }
  else if constexpr (KIND == E_EXP) { t = __builtin_amdgcn_exp2f(t); PIN(t); }
  else if constexpr (KIND == E_INC) { t = t + 1.0f; PIN(t); }
  else if constexpr (KIND == E_RCP) { t = __builtin_amdgcn_rcpf(t); PIN(t); }
  else if constexpr (KIND == E_GU) { e.q = gv * uv; PIN(e.q); }
  else if constexpr (KIND == E_OUT) { e.h[v & 1] = e.q * t; PIN(e.h[v & 1]); }
  else if constexpr (KIND == E_PACK) { u32 r = pk(e.h[0], e.h[1]); PIN(r); e.r[v >> 1] = r; }
  else if constexpr (KIND == E_SWA || KIND == E_SWB) { constexpr int i = KIND == E_SWB; const u32 x = e.r[i], y = e.r[i + 2];
    const auto q = __builtin_amdgcn_permlane16_swap(x, y, false, false); u32 q0 = q[0], q1 = q[1]; PIN(q0); PIN(q1); e.r[i] = q0; e.r[i + 2] = q1; }
  else if constexpr (KIND == E_SUM) { e.sum = e.r.x + e.r.z; PIN(e.sum); }
  else if constexpr (KIND == E_SUM2) { e.fin = e.sum + e.r.y + e.r.w; PIN(e.fin); }
  else if constexpr (KIND == E_X1) { e.fin += dppu<0xb1>(e.fin); PIN(e.fin); }
  else if constexpr (KIND == E_X2) { e.fin += dppu<0x4e>(e.fin); PIN(e.fin); }
  else if constexpr (KIND == E_X8) { e.fin += dppu<0x128>(e.fin); PIN(e.fin); }
  else if constexpr (KIND == E_CHK) { asm volatile("" : "=v"(e.k4)); e.k4.x = tag - e.fin; PIN(e.k4); }
}

constexpr double ct_sin(long n, long d) {
  n %= d; if (n < 0) n += d;
  if (2 * n > d) return -ct_sin(d - n, d);
  if (4 * n > d) return ct_sin(d - 2 * n, 2 * d);
  const double x = 6.283185307179586476925 * (double)n / (double)d;
  double t = x, r = x;
  for (int i = 1; i < 14; ++i) { t *= -x * x / ((2 * i) * (2 * i + 1)); r += t; }
  return r;
}
constexpr double ct_cos(long n, long d) { return ct_sin(4 * n + d, 4 * d); }
constexpr u32 ct_f32(double v) { return __builtin_bit_cast(u32, (float)v); }
constexpr u32 ct_bf(double v) { const u32 u = ct_f32(v); return (u + 0x7fffu + ((u >> 16) & 1u)) >> 16; }
constexpr u32 ct_pk(double a, double b) { return ct_bf(a) | (ct_bf(b) << 16); }
struct Tab { u32 v[6][64][4]; };
constexpr Tab make_tab() {
  Tab t = {};
  for (int l = 0; l < 64; ++l) {
    const int c = l & 15, g = l >> 4;
    for (int j = 0; j < 2; ++j) {
      const int a = 4 * g + 2 * j;
      t.v[0][l][j] = ct_pk(ct_cos(a * c, 32), ct_cos((a + 1) * c, 32));
      t.v[0][l][2 + j] = c ? ct_pk(-ct_sin(a * c, 32), -ct_sin((a + 1) * c, 32)) : ct_pk(1, -1);
    }
    for (int r = 0; r < 4; ++r) {
      const int b = 4 * g + r, sl = slot_of(b, c);
      t.v[1][l][r] = ct_f32(ct_cos(b * c, 512));
      t.v[2][l][r] = ct_f32(ct_sin(b * c, 512));
      t.v[3][l][r] = ct_pk(ct_cos(b * c, 16), ct_sin(b * c, 16));
      t.v[4][l][r] = c == 0 ? ct_pk((r & 1) ? -1 : 1, 0) : c < 8 ? ct_pk(ct_cos(b * c, 16), -ct_sin(b * c, 16)) : ct_pk(ct_cos(b * (31 - 2 * c), 32), -ct_sin(b * (31 - 2 * c), 32));
      t.v[5][l][r] = im_unit(sl, 2 * rec_pos(sl)) | (im_chk(sl) << 16);
    }
  }
  return t;
}
static_assert(true, "");
static __device__ const __attribute__((aligned(128))) Tab g_tab = make_tab();

#define STAMP(i) do { } while (0)
#define STAMP_DECL do { } while (0)
#define STAMP_FLUSH() do { } while (0)
#define COUNT(i, v) do { } while (0)

#define FSTAMP(i) do { } while (0)
#define PACE(d, t) do { __builtin_amdgcn_s_sleep(d); while ((u32)__builtin_amdgcn_s_memrealtime() - t_ref < (t)) __builtin_amdgcn_s_sleep(1); } while (0)
#define RS1 0
#define RS2 10
#define RS3 0
#define D1 1
#define D2 4
#define D3 6
#define SLIPPED() (*(LAS u32*)(fmine + L_TG + 1024) = (u32)__builtin_amdgcn_s_memrealtime() - t_ref)
#define PACE2(d, t, sl) do { u32 r_ = *(LAS u32*)(fall + L_TG + 1024); r_ = max(r_, dppu<0xb1>(r_)); r_ = max(r_, dppu<0x4e>(r_)); \
      r_ = __builtin_amdgcn_readfirstlane(r_) + (sl); t_ref += r_ > (t) ? r_ - (t) : 0u; PACE(d, t); } while (0)
#define SL2 175
#define SL3 150
#define WAIT1() PACE(D1, T1)
#define WAIT2() PACE2(D2, T2, SL2)
#define WAIT3() PACE2(D3, T3, SL3)
struct Params {
  const u16* x; const u16* wr; const u16* wi; const u16* wg; const u16* wu; const u16* wd; u16* out; void* ws;
};

__global__ void __launch_bounds__(512, 2) gfnet_geglu_kernel(Params p) {
  const int wg = blockIdx.x, tid = threadIdx.x, lane = tid & 63;
  int wv = __builtin_amdgcn_readfirstlane(tid >> 6);
  STAMP_DECL;
  unsigned long long t_entry;
  asm volatile("s_memrealtime %0" : "=s"(t_entry));
  const int rg = 4 * ((wg & 7) >> 1) + (wg >> 6), ch = 8 * (wg & 1) + ((wg >> 3) & 7);
  const int un = 16 * rg + ch;
  const int ft0 = tile0_of(ch);
  const bool six = (ch & 7) < 6 - (ch >> 3);
#define W1SZ (D_ * F_ * 2)
#define WD_SETUP() const rsrc_t rwd = mkrs(p.wd, D_ * F_ * 2); \
    int vd = (lane >> 3) * (D_ * 2) + (ch & 7) * 128 + ((lane & 7) ^ ((lane >> 3) & 2)) * 16, vd1 = vd ^ 64, sd[4]; PIN(vd); PIN(vd1); \
    _Pragma("unroll") for (int i = 0; i < 4; ++i) { sd[i] = tile0_of(4 * (wv & 3) + i) * (16 * D_ * 2); PINS(sd[i]); }
#define DMA_WD1(t, slot, i) asm volatile("s_mov_b32 m0, %0\n\ts_nop 0\n\tbuffer_load_dwordx4 %1, %2, %3 offen lds" \
      :: "s"((slot) * SLOT + (wv & 3) * 8192 + (i) * 1024), "v"(((i) & 1) ? vd1 : vd), "s"(rwd), "s"(sd[(i) >> 1] + ((t) * 16 + ((i) & 1) * 8) * (D_ * 2)) : "memory")
#define TH2 4
#define TG 4
#define THROTTLE(i) do { if ((i) % TG == 0) asm volatile("s_waitcnt vmcnt(%0)" :: "n"(TH2) : "memory"); } while (0)
#define DMA_WD(t, slot) do { _Pragma("unroll") for (int i = 0; i < 8; ++i) { THROTTLE(i); DMA_WD1(t, slot, i); } } while (0)
#define DMA_WDS(t, slot) do { _Pragma("unroll") for (int i = 0; i < 4; ++i) DMA_WD1(t, slot, i); if (!(wv & 1)) { _Pragma("unroll") for (int i = 4; i < 8; ++i) DMA_WD1(t, slot, i); } } while (0)
#define VM_WAIT(n) asm volatile("s_waitcnt vmcnt(" #n ")" ::: "memory")
#define PDN 4
  constexpr int PD = PDN;
#define FRAG(N) frag(std::integral_constant<int, (N)>{})
#define NXT(n) (((n) + PD) ^ 1)
  v4u ra[8];
  u32 cpf;
#define UNDEF_RA() do { asm volatile("" : "=v"(cpf)); _Pragma("unroll") for (int i = 0; i < 8; ++i) asm volatile("" : "=v"(ra[i])); } while (0)
  UNDEF_RA();
  if (wv < 4) {
    const int vt = lane * 16;
    int sx = (32 * (8 * (un >> 5) + 2 * wv) + (un & 31)) * (D_ * 2), sy = sx + 32 * D_ * 2;
    const rsrc_t rt = mkrs(&g_tab, sizeof(Tab));
    ra[2] = ld_in(rt, vt, 0); ra[3] = ld_in(rt, vt + 1024, 0);
    PINS(sx); PINS(sy);
    SB();
    const rsrc_t rx = mkrs(p.x, S_ * D_ * 2);
    ra[0] = ld_once(rx, vt, sx); ra[1] = ld_once(rx, vt, sy);
    SB();
#pragma unroll
    for (int i = 0; i < 4; ++i) ra[4 + i] = ld_in(rt, vt + i * 1024, 2048);
    cpf = ld_dev1(mkrs(p.ws, WS_END), 0, WS_E);
    STAMP(0);
  }
  PINS(wv);
  if (wv >= 6) {
    unsigned long long pc0;
    asm volatile("s_getpc_b64 %0" : "=s"(pc0));
    cpf = touch(mkrs((const void*)(pc0 & ~127ull), CODE_LINES * 128), lane < 9 ? (((wg >> 3) ^ (wv & 1)) * 9 + lane) : 4096);
    unsigned long long te;
    asm volatile("s_waitcnt lgkmcnt(0)\n\ts_mov_b64 %0, %1" : "=s"(te) : "s"(t_entry));
    st_dev(mkrs(p.ws, WS_END), (tid >= 504 && wg < 8) ? WS_T + wg * 128 + (lane & 7) * 16 : OOB, v4u{(u32)te, (u32)te, (u32)te, (u32)te});
  }
  PINS(wv);
  if ((wv >> 1) == 2) {
#define WSLEEP 2
    __builtin_amdgcn_s_sleep(WSLEEP);
    const rsrc_t rwr = mkrs(p.wr, S_ * KF * 2), rwi = mkrs(p.wi, S_ * KF * 2);
    const int vf = (lane & 1) * (KF * 2 + 2) + ((tid & 127) >> 1) * 8;
    const int sf = (16 * (un >> 6) + 2 * (un & 7) + 256 * ((un >> 3) & 7)) * (KF * 2);
#pragma unroll
    for (int r = 0; r < 4; ++r) { ra[r] = ld_once(rwr, vf, sf + r * (64 * KF * 2)); ra[4 + r] = ld_once(rwi, vf, sf + r * (64 * KF * 2)); }
  }
  const lp_t fmine = lptr(SCR + L_F + tid * 4);
  *(volatile LAS u32*)fmine = 0;
  lds_w128(lptr(SCR + L_IM + (tid & 127) * 128 + ((6 ^ (tid + 2 * (((tid & 127) * 43) >> 10))) & 7) * 16), v4u{0u, 0u, 0u, 0u});
  BAR();
  PINS(t_entry);
  u32 t_ref = (u32)t_entry;
  v4u wa_;
  asm volatile("" : "=v"(wa_));
  PINS(wv);
  if ((wv >> 1) == 2) {
    const int T = tid - 256, m = T >> 1;
    wa_ = v4u{(u32)(((m & 0x39) | ((m & 2) << 1) | ((m & 4) >> 1)) * 32 + (T & 1) * 16 + WS_W + un * 8192), (lane & 1) ? 0u : 16u, (u32)(SCR + im_chk(2 * T)), (u32)(SCR + im_chk(2 * T + 1))};
    PIN(wa_);
  }
  PINS(wv);
  if (wv >= 6) {
#define X0SLEEP 2
    __builtin_amdgcn_s_sleep(X0SLEEP);
    const rsrc_t r1 = mkrs((wv & 1) ? p.wu : p.wg, W1SZ);
    ra[0].x = touch(r1, lane < 43 ? wg * 43 + lane : 16384);
  }
  SB();
  PINS(wv);

  if (wv < 4) {
  const int w = wv, c = lane & 15, g = lane >> 4;
  const float fc = (float)c, fg4 = (float)(4 * g);
  const f32x4 z = {0.f, 0.f, 0.f, 0.f};
  f32x4 twc, tws;
  v4u fbre, fbsw;
  u32 tag;
  float mean[2];
  const rsrc_t ws = mkrs(p.ws, WS_END);
  const lp_t fall = lptr(SCR + L_F + (lane & 3) * 256 + lane * 4);
  const lp_t imr = lptr(SCR + L_IM + w * 3072 + (lane >> 3) * 128 + ((lane ^ ((lane >> 3) + 2 * w)) & 7) * 16);
  int vx0 = lane * 16 + WS_A + un * XREC + w * (22 * 128), vx2 = lane < 48 ? vx0 + 2048 : OOB;
  PIN(vx0); PIN(vx2);
  int vq8 = (lane & 7) * 128;
  PIN(vq8);
  u32 tq;
  v2u xk[2][2];
  f32x4 tfc[2], tfs[2], tic[2], tis[2];
  const float fj = (float)(16 * w) + fc;
#pragma unroll
  for (int r = 0; r < 4; ++r) {
#pragma unroll
      for (int m = 0; m < 2; ++m) {
        const float nf = (fg4 + (16 * m + r)) * (fj * (1.f / 2048)), ni = (fg4 + (float)(16 * w) + r) * ((fc + 16 * m) * (1.f / 2048));
        tfc[m][r] = cosr(nf); tfs[m][r] = sinr(nf); tic[m][r] = cosr(ni); tis[m][r] = sinr(ni);
      }
  }
#pragma unroll
  for (int i = 0; i < 2; ++i) { PIN(tfc[i]); PIN(tfs[i]); PIN(tic[i]); PIN(tis[i]); }
  SB();
  {
    const v4u xr[2] = {ra[0], ra[1]}, tb[6] = {ra[2], ra[3], ra[4], ra[5], ra[6], ra[7]};
    const u32 sc = (c & 1) ? NEG2 : 0u, sm = c < 8 ? NEGL : NEGH, p0 = c < 8 ? 0x0c0c0100u : 0x01000c0cu, p1 = c < 8 ? 0x0c0c0302u : 0x03020c0cu;
    v4u fare = {tb[0].x, tb[0].y, tb[0].x ^ sc, tb[0].y ^ sc}, faim = {tb[0].z, tb[0].w, tb[0].z ^ sc, tb[0].w ^ sc}, fbim, f0re, f0im;
    twc = __builtin_bit_cast(f32x4, tb[1]); tws = __builtin_bit_cast(f32x4, tb[2]); fbre = tb[3];
    lp_t t1w[4], t1c[4];
#pragma unroll
    for (int r = 0; r < 4; ++r) {
      const u32 a = tb[3][r], q = tb[4][r];
      fbsw[r] = __builtin_amdgcn_alignbit(a, a, 16);
      fbim[r] = fbsw[r] ^ sm;
      f0re[r] = __builtin_amdgcn_perm(q, q, p0); f0im[r] = __builtin_amdgcn_perm(q, q, p1);
      t1w[r] = lptr((tb[5][r] & 0xffffu) ^ (SCR + w * 8)); t1c[r] = lptr((tb[5][r] >> 16) + SCR);
    }
    float c0m = c == 0 ? 1.f / 16 : 0.f;
    lp_t xw = lptr(SCR + L_X + w * 2048 + lane * 16), xt = lptr(SCR + L_X + w * 2048 + lane * 8);
    lp_t xq = lptr(SCR + L_X + w * 2048 + (4 * c + g) * 8), f6 = lptr(SCR + L_F + (lane % 6) * 256 + lane * 4);
    tag = cpf + 1u;
    u32 tag0 = w ? 0u : tag;
    PIN(c0m); PIN(tag); PIN(tag0);
    *(LAS u32*)(fmine + L_TG) = tag;
    *(LAS u32*)(fmine + L_TG + 1024) = 0;
    PIN(fare); PIN(faim); PIN(fbre); PIN(fbim); PIN(f0re); PIN(f0im); PIN(twc); PIN(tws);
    STAMP(1);
    SB();
    v4u a[2];
#pragma unroll
    for (int rr = 0; rr < 2; ++rr) { lds_w128(xw + rr * 1024, xr[rr]); a[rr] = lds_tr2(xt + rr * 1024, xt + rr * 1024 + 512); }
    tq = ld_dev1(ws, vq8, WS_T);
    STAMP(2);
    float st[4] = {0.f, 0.f, 0.f, 0.f};
#pragma unroll
    for (int i = 0; i < 4; ++i)
#pragma unroll
      for (int rr = 0; rr < 2; ++rr) { const u32 v = xr[rr][i]; st[rr] = dot2(v, 0x3f803f80u, st[rr]); st[2 + rr] = dot2(v, v, st[2 + rr]); }
    SB();
    f32x4 ur[2], ui[2];
#pragma unroll
    for (int rr = 0; rr < 2; ++rr) { ur[rr] = mm16p(a[rr], fare, z); ui[rr] = mm16p(a[rr], faim, z); }
    wave_sums<4>(st);
    SB();
    v4u a2[2];
    f32x4 orr[2], oi[2], sr[2], si[2];
#pragma unroll
    for (int rr = 0; rr < 2; ++rr) {
      const f32x4 x = ur[rr] - st[rr] * c0m, y = ui[rr];
      a2[rr] = pk4(x * twc + y * tws, y * twc - x * tws);
      orr[rr] = mm16p(a2[rr], fbre, z); oi[rr] = mm16p(a2[rr], fbim, z); sr[rr] = mm16p(a2[rr], f0re, z); si[rr] = mm16p(a2[rr], f0im, z);
    }
    v4u o[2];
#pragma unroll
    for (int rr = 0; rr < 2; ++rr) {
      mean[rr] = st[rr] * (1.f / 512);
      const float rstd = __builtin_amdgcn_rsqf(fmaxf(st[2 + rr] * (1.f / 512) - mean[rr] * mean[rr], 0.f) + 1e-5f);
      orr[rr][0] = g == 0 ? sr[rr][0] : orr[rr][0]; oi[rr][0] = g == 0 ? si[rr][0] : oi[rr][0];
      o[rr] = pk4(orr[rr] * rstd, oi[rr] * rstd);
    }
#pragma unroll
    for (int r = 0; r < 4; ++r) { v2u v = {o[0][r], o[1][r]}; lds_w64(t1w[r], v); lds_add(t1c[r], v.x + v.y + tag0); }
    STAMP(3);
    flag_set(fmine, 1);
    v4u v0, v1, v2;
    FLAG_WAIT(f6, 1, v0 = lds_r128(imr); v1 = lds_r128(imr + 1024); v2 = lds_r128(imr + 2048));
    st_dev(ws, vx0, v0); st_dev(ws, vx0 + 1024, v1); st_dev(ws, vx2, v2);
    flag_set(fmine, 2);
#pragma unroll
    for (int i = 0; i < 4; ++i) xk[i >> 1][i & 1] = lds_r64(xq + (i >> 1) * 1024 + (i & 1) * 512);
  }
  int vge, vgo, vg = (lane >> 1) * 8192 + (lane & 1) * 16;
  {
    const int pay = (lane >> 1) * XREC + rec_pos(un) * 32 + (lane & 1) * 16, chk = (lane & 1) ? OOB : ((lane >> 1) ^ 1) * XREC + 96 + rec_pos(un) * 4;
    vge = (lane & 2) ? chk : pay; vgo = (lane & 2) ? pay : chk;
  }
  const int sg = w * (64 * XREC) + (22 * (un >> 6) + rec_line(un)) * 128;
#define GATHER(BUF) do { xl[0] = ld_dev(ws, vge, BUF + sg); xl[1] = ld_dev(ws, vgo, BUF + sg); xl[2] = ld_dev(ws, vge, BUF + sg + 32 * XREC); xl[3] = ld_dev(ws, vgo, BUF + sg + 32 * XREC); } while (0)
#define VERIFY(extra) ({ u32 k_ = 0; _Pragma("unroll") for (int m = 0; m < 2; ++m) { _Pragma("unroll") for (int i = 0; i < 4; ++i) xg[m][i] = (lane & 2) ? xl[2 * m + 1][i] : xl[2 * m][i]; \
      k_ += (lane & 2) ? xl[2 * m].x : xl[2 * m + 1].x; lds_w128(iw + m * 1024, xg[m]); } \
      u32 t_ = sum4(sum4((extra) - k_, xg[0]), xg[1]); t_ += dppu<0xb1>(t_); t_ += dppu<0x4e>(t_); t_; })
  const lp_t iw = lptr(SCR + L_I2 + w * 2048 + lane * 16);
  const int sw = WS_W + w * (64 * 8192) + ((un & 3) * 64 + ((((un >> 2) & 0x39) | ((un >> 1) & 4) | ((un >> 3) & 2)))) * 32;
  v4u wq[2];
  STAMP(4);
  SB();
  v4u f2a[2][2];
  {
    v4u f1a[4], f1b[4], f2b[2][2];
    const u32 sc = (c & 1) ? NEG2 : 0u;
#pragma unroll
    for (int r = 0; r < 4; ++r) {
#pragma unroll
      for (int ks = 0; ks < 2; ++ks) {
        const float n = (fg4 + (16 * ks + r)) * (fj * (1.f / 64));
        const float cs = cosr(n), sn = sinr(n);
        f1a[ks][r] = pk(cs, sn); f1b[ks][r] = pk(-sn, cs);
        f1a[ks + 2][r] = f1a[ks][r] ^ sc; f1b[ks + 2][r] = f1b[ks][r] ^ sc;
      }
      const float n = (fg4 + r) * (fc * (1.f / 32));
      const float cs = cosr(n), sn = sinr(n);
      const u32 a = pk(cs, sn), b = pk(-sn, cs), sr = (r & 1) ? NEG2 : 0u;
      f2a[0][0][r] = a; f2a[1][0][r] = a ^ sr; f2a[0][1][r] = a ^ sc; f2a[1][1][r] = a ^ sc ^ sr;
      f2b[0][0][r] = b; f2b[1][0][r] = b ^ sr; f2b[0][1][r] = b ^ sc; f2b[1][1][r] = b ^ sc ^ sr;
    }
#pragma unroll
    for (int i = 0; i < 4; ++i) { PIN(f1a[i]); PIN(f1b[i]); PIN(f2a[i >> 1][i & 1]); PIN(f2b[i >> 1][i & 1]); }
    lp_t i2r = lptr(SCR + L_I2 + ((g >> 1) * 32 + c) * 32 + (g & 1) * 16);
    lp_t exw = lptr(SCR + L_EX + w * 2048 + lane * 16), exr = lptr(SCR + L_EX + lane * 16);
    lp_t imw[2], imc = lptr(SCR + im_chk(64 * w + lane));
#pragma unroll
    for (int n = 0; n < 2; ++n) { const int k = 64 * w + 32 * (g >> 1) + 16 * n + c; imw[n] = lptr(SCR + im_unit(k, 2 * rec_pos(k) + (g & 1))); }
    vx0 += WS_B - WS_A; vx2 += WS_B - WS_A;
    PIN(vg); PIN(vge); PIN(vgo); PIN(vx0); PIN(vx2);
    tq -= t_ref; tq = tq < 150u ? tq : 0u;
    tq = max(tq, dppu<0xb1>(tq)); tq = max(tq, dppu<0x4e>(tq)); tq = max(tq, dppu<0x141>(tq));
    t_ref += __builtin_amdgcn_readfirstlane(tq);
    STAMP(5);
    PACE(0, TW);
    wq[0] = ld_dev(ws, vg, sw); wq[1] = ld_dev(ws, vg, sw + 32 * 8192);
    WAIT1();
    STAMP(6);
    v4u xl[4], xg[2];
    f32x4 wr[2], wi[2];
    GATHER(WS_A);
    flag_set(fmine, 3);
    SB();
#define W_UNPACK() _Pragma("unroll") for (int m = 0; m < 2; ++m) { _Pragma("unroll") for (int r = 0; r < 4; ++r) { const u32 v = wq[m][r]; wr[m][r] = blo(v); wi[m][r] = bhi(v); } PIN(wr[m]); PIN(wi[m]); }
    u32 s0 = sum4(sum4(tag, wq[0]), wq[1]), spin = 0;
    W_UNPACK();
    PIN(s0);
    SB();
    u32 bad = VERIFY(s0);
    if (__any(bad != 0)) {
      _Pragma("nounroll") do {
        __builtin_amdgcn_s_sleep(RS1);
        MEMFENCE();
        if (bad) { GATHER(WS_A); wq[0] = ld_dev(ws, vg, sw); wq[1] = ld_dev(ws, vg, sw + 32 * 8192); }
        bad = VERIFY(sum4(sum4(tag, wq[0]), wq[1]));
      } while (__any(bad != 0) && ++spin < SPIN_CAP);
      W_UNPACK();
      SLIPPED();
      ++spin;
    }
    COUNT(20, spin);
    STAMP(7);
    v4u af[2][4], b2[2], a3[2];
    flag_set(fmine, 4);
    FLAG_WAIT(fall, 4, _Pragma("unroll") for (int i = 0; i < 8; ++i) af[i >> 2][i & 3] = lds_r128(i2r + (i & 3) * 2048 + (i >> 2) * 512));
    STAMP(8);
    SB();
    f32x4 dr[2], di[2];
#pragma unroll
    for (int m = 0; m < 2; ++m)
#pragma unroll
      for (int ks = 0; ks < 4; ++ks) { dr[m] = mm16(af[m][ks], f1a[ks], ks ? dr[m] : z); di[m] = mm16(af[m][ks], f1b[ks], ks ? di[m] : z); }
#pragma unroll
    for (int m = 0; m < 2; ++m) b2[m] = pk4(dr[m] * tfc[m] + di[m] * tfs[m], di[m] * tfc[m] - dr[m] * tfs[m]);
#pragma unroll
    for (int k = 0; k < 2; ++k)
#pragma unroll
      for (int m = 0; m < 2; ++m) { dr[m] = mm16(f2a[m][k], b2[k], k ? dr[m] : z); di[m] = mm16(f2b[m][k], b2[k], k ? di[m] : z); }
#pragma unroll
    for (int m = 0; m < 2; ++m) a3[m] = pk4(dr[m] * wi[m] + di[m] * wr[m], dr[m] * wr[m] - di[m] * wi[m]);
#pragma unroll
    for (int k = 0; k < 2; ++k)
#pragma unroll
      for (int n = 0; n < 2; ++n) { di[n] = mm16(a3[k], f2a[n][k], k ? di[n] : z); dr[n] = mm16(a3[k], f2b[n][k], k ? dr[n] : z); }
#pragma unroll
    for (int n = 0; n < 2; ++n) lds_w128(exw + n * 1024, pk4(di[n] * tic[n] + dr[n] * tis[n], dr[n] * tic[n] - di[n] * tis[n]));
    STAMP(9);
    flag_set(fmine, 5);
    FLAG_WAIT(fall, 5, _Pragma("unroll") for (int i = 0; i < 8; ++i) af[i >> 2][i & 3] = lds_r128(exr + (i & 3) * 2048 + (i >> 2) * 1024));
    SB();
    u32 cs[2];
#pragma unroll
    for (int n = 0; n < 2; ++n)
#pragma unroll
      for (int ks = 0; ks < 4; ++ks) { di[n] = mm16(f1a[ks], af[n][ks], ks ? di[n] : z); dr[n] = mm16(f1b[ks], af[n][ks], ks ? dr[n] : z); }
#pragma unroll
    for (int n = 0; n < 2; ++n) {
      const v4u e = pk4(dr[n], di[n]);
      lds_w128(imw[n], e);
      cs[n] = sum4(0u, e);
    }
    const auto sx = __builtin_amdgcn_permlane16_swap(cs[0], cs[1], false, false);
    *(LAS u32*)imc = tag + sx[0] + sx[1];
    MEMFENCE();
    const v4u v0 = lds_r128(imr), v1 = lds_r128(imr + 1024), v2 = lds_r128(imr + 2048);
    st_dev(ws, vx0, v0); st_dev(ws, vx0 + 1024, v1); st_dev(ws, vx2, v2);
    flag_set(fmine, 6);
    if (wg == 0 && w == 0) st_dev1(ws, WS_E, tag);
  }
  STAMP(10);
  SB();
  {
    v4u gbre, gbim, g0re, g0im, ga[2];
#pragma unroll
    for (int r = 0; r < 4; ++r) {
      gbre[r] = fbre[r] ^ (g < 2 ? NEGH : 0u);
      gbim[r] = fbsw[r] ^ (g < 2 ? 0u : NEGH);
      const float n = fc * ((31.f - 2.f * r - 2.f * fg4) * (1.f / 32));
      u32 q = pk(cosr(n), -sinr(n));
      PIN(q);
      g0re[r] = g < 2 ? gbre[r] : 0u;
      g0im[r] = g < 2 ? 0u : q;
      ga[0][r] = f2a[0][0][r] ^ NEGH; ga[1][r] = f2a[1][0][r] ^ NEGH;
    }
    twc *= 2.f / (2048.f * 512.f); tws *= 2.f / (2048.f * 512.f);
    g0re[0] = g == 0 ? ((c & 1) ? 0xbf00u : 0x3f00u) : g0re[0];
    ga[0][0] = g == 0 ? ((c & 1) ? 0xbf803f80u : 0x3f803f80u) : ga[0][0]; ga[1][0] = ga[0][0];
    PIN(gbre); PIN(gbim); PIN(g0re); PIN(g0im); PIN(ga[0]); PIN(ga[1]); PIN(twc); PIN(tws);
    f32x4 xin[2][2];
#pragma unroll
    for (int rr = 0; rr < 2; ++rr)
#pragma unroll
      for (int t = 0; t < 2; ++t) {
        const v2u v = xk[rr][t];
        const u32 v0 = v.x, v1 = v.y;
        f32x4 q = {blo(v0), bhi(v0), blo(v1), bhi(v1)};
        xin[rr][t] = q - mean[rr]; PIN(xin[rr][t]);
      }
    lp_t i3r[4], o3w[2], o3r[2];
#pragma unroll
    for (int r = 0; r < 4; ++r) i3r[r] = lptr(SCR + L_I3 + slot_of(c, 4 * g + r) * 32 + w * 8);
#pragma unroll
    for (int i = 0; i < 2; ++i) {
      o3w[i] = lptr(SCR + L_O3 + ((4 * c + g) * 8 + ((2 * w + i) ^ (c & 7))) * 16);
      const int q = w * 128 + i * 64 + lane;
      o3r[i] = lptr(SCR + L_O3 + (q ^ ((q >> 5) & 7)) * 16);
    }
    int vs = lane * 16 + WS_L + un * 8192 + w * 2048, vt = lane < 8 ? lane * 16 + WS_S + un * 512 + w * 128 : OOB;
    PIN(vs); PIN(vt);
    lp_t sow = lptr(SCR + L_SO + tid * 16), sor = lptr(SCR + L_SO + (lane & 6) * 512 + (lane & 1) * 8);
    WAIT2();
    STAMP(11);
    v4u xl[4], xg[2];
    GATHER(WS_B);
    flag_set(fmine, 7);
    SB();
    u32 bad = VERIFY(tag), spin = 0;
    if (__any(bad != 0)) {
      _Pragma("nounroll") do {
        __builtin_amdgcn_s_sleep(RS1);
        MEMFENCE();
        if (bad) GATHER(WS_B);
        bad = VERIFY(tag);
      } while (__any(bad != 0) && ++spin < SPIN_CAP);
      SLIPPED();
      ++spin;
    }
    COUNT(21, spin);
    STAMP(12);
    v4u a[2], a2[2];
    v2u zr[4];
    flag_set(fmine, 8);
    FLAG_WAIT(fall, 8, _Pragma("unroll") for (int r = 0; r < 4; ++r) zr[r] = lds_r64(i3r[r]));
    STAMP(13);
#pragma unroll
    for (int r = 0; r < 4; ++r) { a[0][r] = zr[r].x; a[1][r] = zr[r].y; }
    f32x4 tr[2], ti[2], sr[2], si[2], h[2][2];
#pragma unroll
    for (int rr = 0; rr < 2; ++rr) { tr[rr] = mm16p(a[rr], gbre, z); ti[rr] = mm16p(a[rr], gbim, z); sr[rr] = mm16p(a[rr], g0re, z); si[rr] = mm16p(a[rr], g0im, z); }
#pragma unroll
    for (int rr = 0; rr < 2; ++rr) {
      tr[rr][0] = g == 0 ? sr[rr][0] : tr[rr][0]; ti[rr][0] = g == 0 ? si[rr][0] : ti[rr][0];
      a2[rr] = pk4(tr[rr] * twc - ti[rr] * tws, ti[rr] * twc + tr[rr] * tws);
      h[rr][0] = mm16p(a2[rr], ga[0], xin[rr][0]); h[rr][1] = mm16p(a2[rr], ga[1], xin[rr][1]);
    }
    float st[2];
#pragma unroll
    for (int rr = 0; rr < 2; ++rr) { const f32x4 q = h[rr][0] * h[rr][0] + h[rr][1] * h[rr][1]; st[rr] = (q[0] + q[2]) + (q[1] + q[3]); }
    wave_sums<2>(st);
    v4u so;
#pragma unroll
    for (int rr = 0; rr < 2; ++rr) {
      const float ve = st[rr] * (1.f / 512) + 1e-5f, rstd = __builtin_amdgcn_rsqf(ve);
      const f32x4 l0 = h[rr][0] * rstd, l1 = h[rr][1] * rstd;
      v4u l4 = {pk(l0[0], l0[1]), pk(l0[2], l0[3]), pk(l1[0], l1[1]), pk(l1[2], l1[3])};
      lds_w128(o3w[rr], l4);
      so[2 * rr] = asu(mean[rr]); so[2 * rr + 1] = asu(ve * rstd);
    }
    lds_w128(sow, so);
    STAMP(14);
    v4u v0, v1;
    v2u ms;
    flag_set(fmine, 9);
    FLAG_WAIT(fall, 9, v0 = lds_r128(o3r[0]); v1 = lds_r128(o3r[1]); ms = lds_r64(sor));
    st_dev(ws, vs, v0); st_dev(ws, vs + 1024, v1);
    u32 s = sum4(sum4(0u, v0), v1);
    s += dppu<0x128>(s); s = add_x32(add_x16(s));
    v4u rec = {tag - s - ms.x - ms.y, ms.x, ms.y, tag};
    st_dev(ws, vt, rec);
    flag_set(fmine, 10);
  }
  SB();
  {
#define LOOKN 6
    constexpr int LOOK = LOOKN;
    v4u ring[8], act[32];
    lp_t wa[4], wb[4];
#pragma unroll
    for (int t = 0; t < 4; ++t) wa[t] = lptr(t * SLOT + g * 512 + ((c ^ (2 * t + (c >> 3))) * 16));
#pragma unroll
    for (int i = 0; i < 4; ++i) wb[i] = lptr(((i & 1) ? 0 : 4 * SLOT) + ((lane >> 2) ^ (g & 1)) * 64 + (((i >> 1) ^ g) & 1) * 32 + (lane & 3) * 8);
    auto frag = [&](auto NN) -> v4u {
      constexpr int N = NN.value;
      if constexpr (N < 64) return lds_r128(wa[(N >> 1) & 1] + (N >> 2) * 2048 + (N & 1) * 256);
      else if constexpr (N < 128) return lds_r128(wa[N >> 5] + ((N & 31) >> 1) * 2048 + (N & 1) * 256);
      else { const lp_t b = wb[((N >> 4) & 2) + (N & 1)] + ((N & 31) >> 1) * 2048; return lds_tr2(b, b + 1024); }
    };
    int vo = WS_H + ((4 * rg + w) * NFT + ft0) * HREC + lane * 16, vk = (lane & 11) ? OOB : vo - lane * 16 + 1024 + ((lane >> 4) * 8 + (lane & 7)) * 4;
    int vo4 = vo + 4 * HREC, vk4 = vk + 4 * HREC;
    PIN(vo); PIN(vk); PIN(vo4); PIN(vk4);
    Epi e;
#define EPI(J0, J1) static_for<(J0), (J1)>([&](auto JJ) { constexpr int kd = kEp.op[JJ.value].kind, V = kEp.op[JJ.value].v, t = V >> 3; \
      if constexpr (kd == E_ST) st_dev(ws, (t < 4 ? vo : vo4) + (t & 3) * HREC, e.r); \
      else if constexpr (kd == E_STK) st_dev(ws, (t < 4 ? vk : vk4) + (t & 3) * HREC, e.k4); \
      else epi_op<kd, V>(e, t % 3 == 0 ? tA : t % 3 == 1 ? tB : tC, tag); \
      SB(); })
    int va0 = ((lane >> 3) & 1) * 8192 + g * 128 + (lane & 7) * 16, va1 = va0 + 16384;
    int vr = ((lane >> 3) & 1) * 512 + g * 128 + (lane & 7) * 16;
    PIN(vr);
    const int sa = WS_L + (4 * rg + w) * 32768;
#define ACT_LD(i) act[i] = ld_dev(ws, (((i) & 1) ? va1 : va0) + (((i) >> 1) & 7) * 512, sa + ((i) >> 4) * 4096)
    f32x4 tA[4], tB[4], tC[4];
    WD_SETUP();
    v4u s0, s1;
#define A_HEAD() do { MEMFENCE(); PIN(va0); PIN(va1); s0 = ld_dev(ws, vr, WS_S + (4 * rg + w) * 2048); s1 = ld_dev(ws, vr + 1024, WS_S + (4 * rg + w) * 2048); SB(); \
      static_for<0, 2 * LOOK>([&](auto K) { ACT_LD(K.value); SB(); }); } while (0)
    WAIT3();
    STAMP(15);
    u32 spin = 0;
    A_HEAD();
    BAR();
    for (;; ++spin) {
      static_for<0, PD>([&](auto K) { ring[K.value ^ 1] = FRAG(K.value ^ 1); SB(); });
      if (__builtin_expect(__any(s0.w != tag || s1.w != tag), 0)) { _Pragma("nounroll") do { __builtin_amdgcn_s_sleep(RS3); A_HEAD(); } while (__any(s0.w != tag || s1.w != tag) && ++spin < SPIN_CAP); ++spin; }
      PIN(va0); PIN(va1);
      u32 m = (s0.x + s0.y + s0.z) + (s1.x + s1.y + s1.z);
      static_for<0, 64>([&](auto K) {
        constexpr int n = K.value, ks = n >> 2, q = (n & 1) * 2;
        f32x4* acc = (n & 2) ? tB : tA;
        mma<ks == 0>(acc[q], ring[n & 7], act[2 * ks]); SB();
        ring[NXT(n) & 7] = FRAG(NXT(n));
        if constexpr (!(n & 1) && ks + LOOK < 16) ACT_LD(2 * (ks + LOOK) + ((n >> 1) & 1));
        SB();
        mma<ks == 0>(acc[q + 1], ring[n & 7], act[2 * ks + 1]); SB();
        { const v4u v = act[2 * ks + ((n >> 1) & 1)]; u32 t = (n & 1) ? m + v.z + v.w : m + v.x + v.y; PIN(t); m = t; }
        SB();
      });
      if (__builtin_expect(__all(add_x32(add_x16(m)) == 8u * tag) || spin > SPIN_CAP, 1)) break;
      s0.w = ~tag;
    }
    COUNT(22, spin);
    BAR_N(15);
    STAMP(16);
#define G1_TILE(T, acc, ...) static_for<0, 32>([&](auto K) { constexpr int s = K.value, n = 32 * (T) + s, ks = s >> 1, q = (s & 1) * 2, j0 = epos(n), j1 = epos(n + 1), jm = j0 + (j1 - j0 - (T) / 4) / 2; \
      mma<ks == 0>(acc[q], ring[n & 7], act[2 * ks]); SB(); \
      if constexpr (n + PD < 192) ring[NXT(n) & 7] = FRAG(n + PD < 192 ? NXT(n) : 0); SB(); \
      EPI(j0, jm); \
      mma<ks == 0>(acc[q + 1], ring[n & 7], act[2 * ks + 1]); SB(); \
      __VA_ARGS__; EPI(jm, j1); SB(); })
#define PG2 8
#define PG3 10
    G1_TILE(2, tC, { });
    FSTAMP(26);
    G1_TILE(3, tA, { if constexpr (s == PG2) BAR_N(15); });
    FSTAMP(27);
    if (six) {
      G1_TILE(4, tB, { if constexpr (s == PG3) BAR_N(15); });
      FSTAMP(28);
      G1_TILE(5, tC, { });
      BAR_N(15);
      STAMP(17);
      MMA_DONE(); SB();
      EPI(kEp.first[5], kEp.n);
    } else {
      BAR_N(15);
      MMA_DONE(); SB();
      EPI(epos(128), kEp.first[4]);
      static_for<E_X1, E_STK>([&](auto KK) { epi_op<KK.value, 31>(e, tA, tag); });
      st_dev(ws, vk + 3 * HREC, e.k4);
#define SKSL 36
      __builtin_amdgcn_s_sleep(SKSL);
      FSTAMP(28);
      BAR_N(15);
      STAMP(17);
    }
    STAMP(18);
    DMA_WDS(4, 0);
    BAR();
    DMA_WDS(5, 1);
    if (w & 1) VM_WAIT(4); else VM_WAIT(8);
    BAR();
    VM_WAIT(0);
    BAR();
  }
  STAMP(24);
  STAMP_FLUSH();
  UNDEF_RA();
  asm volatile("" : "=v"(wa_));
  }
  PINS(wv);
  if (wv >= 4) {
    STAMP(0);
    const int hw = wv - 4;
    asm volatile("" :: "v"(cpf), "v"(ra[0]));
    if (hw < 2) {
    const rsrc_t ws = mkrs(p.ws, WS_END);
    const int T = tid - 256, vs = wa_.x;
    const u32 sh = wa_.y;
    const lp_t ca0 = (lp_t)(size_t)wa_.z, ca1 = (lp_t)(size_t)wa_.w;
    u32 al[8][2], ck[4];
#pragma unroll
    for (int i = 0; i < 8; ++i) { const u32 a = ra[i].x, b = ra[i].y, d = ra[i].z; al[i][0] = __builtin_amdgcn_alignbit(b, a, sh); al[i][1] = __builtin_amdgcn_alignbit(d, b, sh); }
    v4u v[4];
#pragma unroll
    for (int e = 0; e < 4; ++e) {
#pragma unroll
      for (int r = 0; r < 4; ++r) v[e][r] = __builtin_amdgcn_perm(al[4 + r][e >> 1], al[r][e >> 1], (e & 1) ? 0x07060302u : 0x05040100u);
      st_dev(ws, vs + e * 2048, v[e]);
      ck[e] = sum4(0u, v[e]);
    }
    lds_add(ca0, pair_total(ck[0], ck[2], T & 1)); lds_add(ca1, pair_total(ck[1], ck[3], T & 1));
    flag_set(fmine, 1);
    STAMP(1);
  }
    u32 tag;
    v4u y0[8], y1[8];
    WD_SETUP();
    {
      const rsrc_t rwg = mkrs(p.wg, W1SZ), rwu = mkrs(p.wu, W1SZ);
      const int vx = 4 * (lane >> 3) * (F_ * 2) + ft0 * 32 + (lane & 7) * 16, vx8 = vx + 32 * F_ * 2;
      const int vy = six ? 4 * (lane >> 2) * (F_ * 2) + (ft0 + 4) * 32 + (lane & 3) * 16 : OOB;
      const u32 ax = ((lane >> 1) & 3) * SLOT + (16 * hw + (lane >> 3)) * 512 + (lane & 1) * 128 + (lane & 7) * 16;
#define PUT_X(u, off) do { transpose8(u); const u32 a_ = ax + (off); _Pragma("unroll") for (int i = 0; i < 8; ++i) lds_w128((lp_t)(size_t)(a_ ^ (i * 16)), u[i]); SB(); } while (0)
      v4u rb[8], rc[8], rd[8];
      const lp_t fall = lptr(SCR + L_F + (lane & 3) * 256 + lane * 4);
      const int s0 = 64 * hw * (F_ * 2);
#define NA 2
#define NB 10
#define N0 0
#define NS 8
#define NH 2
#define TS 0
#define F1 2
#define F2 5
#define STEP(n) do { constexpr int j = (n) & 7; const int so_ = s0 + ((j & 3) + 256 * (j >> 2)) * (F_ * 2); \
        if ((n) == NA) { flag_wait_lazy(fall, 3); cpf = touch(rwd, lane < 11 ? wg * 43 + hw * 11 + lane : 16384); } if ((n) == NB) flag_wait_lazy(fall, 7); \
        if ((n) == NA + NH) flag_wait_lazy(fall, 4); if ((n) == NB + NH) flag_wait_lazy(fall, 8); \
        if ((n) == N0) flag_wait_lazy(fall, 2); if ((n) == NS) flag_wait_lazy(fall, 6); if ((n) == NL) flag_wait_lazy(fall, 10); \
        if ((n) > 1) asm volatile("s_waitcnt vmcnt(%0)" :: "n"(THR) : "memory"); \
        if ((n) < 8) { ra[j] = ld_in(rwg, vx, so_); rb[j] = ld_in(rwg, vx8, so_); y0[j] = ld_in(rwg, vy, so_); } \
        else { rc[j] = ld_in(rwu, vx, so_); rd[j] = ld_in(rwu, vx8, so_); y1[j] = ld_in(rwu, vy, so_); } SB(); } while (0)
      PACE(0, TS);
      STAMP(2);
      STEP(0); STEP(1); STEP(2); STEP(3); STEP(4); STEP(5); STEP(6); STEP(7); STEP(8); STEP(9);
      STAMP(3);
      if (NB > 10) STEP(10);
      if (NB > 11) STEP(11);
      PUT_X(ra, 0);
      PUT_X(rb, 4096);
      if (NB <= 10) STEP(10);
      if (NB <= 11) STEP(11);
      STEP(12); STEP(13); STEP(14); STEP(15);
      tag = *(LAS u32*)(fall + L_TG);
      STAMP(4);
      PUT_X(rc, 256); PUT_X(rd, 4096 + 256);
      PIN(tag);
      asm volatile("" :: "v"(cpf));
    }
    STAMP(5);
    BAR();
    const rsrc_t ws = mkrs(p.ws, WS_END);
    const int kh = hw & 1, rl = hw >> 1, g = lane >> 4;
    constexpr int PD = 4;
    const u32 ayo = hw * 8192 + g * 2048 + ((lane >> 2) & 3) * 256 + (lane & 4) * 16 + ((lane & 3) ^ ((lane >> 1) & 2)) * 16;
    const lp_t ay0[2] = {lptr(ayo + 4 * SLOT), lptr((ayo ^ 64) + 4 * SLOT)}, ay1[2] = {lptr(ayo), lptr(ayo ^ 64)}, ad = lptr(3 * SLOT + hw * 8192 + lane * 16);
#define PUT_Y(a, y, j0, j1) _Pragma("unroll") for (int j = j0; j < j1; ++j) lds_w128(a[j & 1] + (j >> 2) * 1024 + (j & 2) * 64, y[j])
    PUT_Y(ay0, y0, 0, 8);
    v4u ring[8], hfA[8], hfB[8], hfC[8], hfD[8], hfE[8];
    u32 hkA, hkB, hkC, hkD, hkE, ms = 0, nfix = 0;
    lp_t wd[3][4];
    {
      const u32 dl = (g * 1024 + ((lane >> 2) & 3) * 128 + ((lane >> 3) & 3) * 32 + (lane & 3) * 8) ^ (kh * 64);
#pragma unroll
      for (int i = 0; i < 12; ++i) wd[i >> 2][i & 3] = lptr((dl ^ ((i & 3) * 32)) + (i >> 2) * 2 * SLOT + kh * 16384);
    }
    auto frag = [&](auto NN) -> v4u {
      constexpr int N = NN.value, t = N < 64 ? N >> 4 : N < 76 ? 4 : 5, s = N < 64 ? (N & 15) : N < 76 ? N - 64 : N - 76, slot = (t + 1) % 5;
      const lp_t b = wd[slot >> 1][s & 3] + (slot & 1) * SLOT + (s >> 2) * 4096;
      return lds_tr2(b, b + 512);
    };
    const int hb = WS_H + (4 * rg + 2 * (ch >> 3) + rl) * NFT * HREC;
    int hs[4];
#pragma unroll
    for (int i = 0; i < 4; ++i) { hs[i] = hb + tile0_of(8 * kh + 2 * i) * HREC; PINS(hs[i]); }
    int vl = (g & 1) * 512 + (lane & 15) * 16 + (g >> 1) * 6 * HREC, vl3 = vl - (g >> 1) * 2 * HREC, vl4 = vl + 4 * HREC, vlz = (kh && g >= 2) ? OOB : vl4, vq, vq4;
    {
      const int st = (lane & 2) + ((lane >> 1) & 4) + (g >> 1);
      vq = hb + 1024 + ((2 * (g & 1) + (lane & 1)) * 8 + (lane & 4)) * 4 + tile0_of(8 * kh + st) * HREC;
      vq4 = st < 6 - kh ? vq + 4 * HREC : OOB;
    }
    const int u = 16 * rg + 8 * (ch >> 3) + 4 * rl + ((lane >> 3) & 1), col = 64 * (ch & 7) + 32 * kh;
    int vh = u * 8192 + (col & 255) * 32 + g * 128 + (lane & 7) * 16 + (col >> 8) * 8, vs = u * 512 + (lane & 7) * 16 + 4;
    int vout = ((32 * (8 * (u >> 5) + (lane & 7)) + (u & 31)) * D_ + col + 16 * (g & 1) + 8 * (g >> 1)) * 2;
    lp_t rw = lptr(2 * SLOT + hw * 4096 + lane * 16), rr_ = lptr(2 * SLOT + (hw ^ 1) * 4096 + lane * 16);
    u32 t8 = 8u * tag, t6 = ((kh && g >= 2) ? 4u : 6u) * tag;
    PIN(vl); PIN(vl3); PIN(vl4); PIN(vlz); PIN(vq); PIN(vq4); PIN(vh); PIN(vs); PIN(vout); PIN(t8); PIN(t6);
#define H_LD(X, t, j) do { \
      if constexpr ((j) == 8) hk##X = ld_dev1(ws, ((t) < 4 ? vq : vq4) + ((t) & 3) * HREC, 0); \
      else if constexpr ((t) < 4 || (j) < 6) hf##X[(j) & 7] = ld_dev(ws, ((t) < 4 ? ((j) < 6 ? vl : vl3) : (j) < 4 ? vl4 : vlz) + ((t) & 3) * HREC + ((j) & 1) * 256, hs[((j) >> 1) & 3]); } while (0)
#define H_LDALL(X, t) static_for<0, 9>([&](auto J) { THROTTLE(J.value); H_LD(X, t, J.value ? J.value - 1 : 8); })
#define H_SUM(X, nf, j) do { if constexpr ((j) >= 0 && (j) < (nf)) { ms = sum4((j) ? ms : hk##X, hf##X[(j) & 7]); u32 t_ = ms; PIN(t_); ms = t_; } \
      else if constexpr ((j) >= (nf) && (j) < (nf) + 3) { u32 t_ = ms + dppu<(j) == (nf) ? 0xb1 : (j) == (nf) + 1 ? 0x4e : 0x128>(ms); PIN(t_); ms = t_; } } while (0)
#define H_SUMALL(X, nf) static_for<0, (nf) + 3>([&](auto J) { H_SUM(X, nf, J.value); })
#define H_FIX(X, t, nf, want) do { if (__builtin_expect(__any(ms != (want)), 0)) { u32 spin_ = 0; _Pragma("nounroll") do { __builtin_amdgcn_s_sleep(RS3); MEMFENCE(); nfix |= 1u << (4 * (t)); \
        H_LDALL(X, t); H_SUMALL(X, nf); } while (__any(ms != (want)) && ++spin_ < SPIN_CAP); } SB(); } while (0)
    v4u d2[8], d3[8];
    f32x4 o[4][2];
    v2u hr[4], so[2];
#define GRP() asm volatile("s_waitcnt vmcnt(%0)" :: "n"(TH2) : "memory")
#define RAW4(dst, t, i0) do { GRP(); _Pragma("unroll") for (int i = i0; i < i0 + 4; ++i) dst[i] = ld_in(rwd, (i & 1) ? vd1 : vd, sd[i >> 1] + ((t) * 16 + (i & 1) * 8) * (D_ * 2)); SB(); } while (0)
#define DMA4(t, slot, i0) do { GRP(); _Pragma("unroll") for (int i = i0; i < i0 + 4; ++i) DMA_WD1(t, slot, i); } while (0)
#define H4(X, t, h) do { GRP(); static_for<5 * (h), 5 + 4 * (h)>([&](auto J) { H_LD(X, t, J.value ? J.value - 1 : 8); }); SB(); } while (0)
#define PUT_D(d, off, i0, i1) do { _Pragma("unroll") for (int i = i0; i < i1; ++i) lds_w128(ad + (off) + i * 1024, d[i]); SB(); } while (0)
#define RESID(i) do { const u32 v0 = hr[i].x, v1 = hr[i].y, mb = so[(i) & 1].x, sb = so[(i) & 1].y; float q[4] = {blo(v0), bhi(v0), blo(v1), bhi(v1)}; \
      _Pragma("unroll") for (int r = 0; r < 4; ++r) { q[r] = __builtin_fmaf(q[r], asf(sb), asf(mb)); PIN(q[r]); o[(i) >> 1][(i) & 1][r] = q[r]; } PIN(o[(i) >> 1][(i) & 1]); SB(); } while (0)
#define PSLEEP 16
    __builtin_amdgcn_s_sleep(PSLEEP);
    RAW4(d2, 2, 0); RAW4(d2, 2, 4);
    STAMP(6);
    BAR();
    DMA4(0, 1, 0); PUT_Y(ay1, y1, 0, 3);
    DMA4(0, 1, 4); PUT_Y(ay1, y1, 3, 6);
    GRP();
#pragma unroll
    for (int i = 0; i < 2; ++i) so[i] = __builtin_amdgcn_raw_buffer_load_b64(ws, vs + i * 1024, WS_S, AUX_SC1);
#pragma unroll
    for (int i = 0; i < 4; ++i) hr[i] = __builtin_amdgcn_raw_buffer_load_b64(ws, vh + (i >> 1) * 512, WS_L + (i & 1) * 16384, AUX_SC1);
    PUT_Y(ay1, y1, 6, 8);
    RAW4(d3, 3, 0);
    STAMP(7);
    BAR();
    RAW4(d3, 3, 4); RESID(0); RESID(1); RESID(2); RESID(3);
    DMA4(1, 2, 0); DMA4(1, 2, 4);
    H4(A, 0, 0);
    STAMP(8);
    BAR();
    H4(A, 0, 1); PUT_D(d2, 0, 0, 3);
    H4(B, 1, 0); PUT_D(d2, 0, 3, 6);
    H4(B, 1, 1); PUT_D(d2, 0, 6, 8);
    H4(C, 2, 0); H_SUMALL(A, 8);
    STAMP(9);
    H4(C, 2, 1);
    H_FIX(A, 0, 8, t8);
    static_for<0, PD>([&](auto K) { ring[K.value ^ 1] = FRAG(K.value ^ 1); SB(); });
    STAMP(10);
    BAR();
    STAMP(11);
#define G2_ROUND(X, n0, ns, ...) static_for<0, ns>([&](auto K) { constexpr int s = K.value, n = (n0) + s, ct = s & 3, a = (s >> 2) * 2; \
      mma<n == 2 || n == 3>(o[ct][0], ring[n & 7], hf##X[a]); SB(); \
      if constexpr (n + PD < 88) ring[NXT(n) & 7] = FRAG(n + PD < 88 ? NXT(n) : 0); SB(); \
      mma<n == 2 || n == 3>(o[ct][1], ring[n & 7], hf##X[a + 1]); SB(); \
      __VA_ARGS__; SB(); })
#define L3 6
#define L4 36
#define L5 48
#define LATE() do { if constexpr (n >= L3 && n < L3 + 9) H_LD(D, 3, n > L3 ? n - L3 - 1 : 8); \
      if constexpr (n >= L4 && n < L4 + 7) H_LD(E, 4, n > L4 ? n - L4 - 1 : 8); if constexpr (n >= L5 && n < L5 + 7) H_LD(A, 5, n > L5 ? n - L5 - 1 : 8); } while (0)
    G2_ROUND(A, 0, 16, { H_SUM(B, 8, s - 5); LATE(); });
    H_FIX(B, 1, 8, t8);
    FSTAMP(14);
    G2_ROUND(B, 16, 16, { if constexpr (s < 8) lds_w128(ad + SLOT + s * 1024, d3[s & 7]); H_SUM(C, 8, s - 5); LATE(); });
    BAR_N(8);
    H_FIX(C, 2, 8, t8);
    FSTAMP(15);
    G2_ROUND(C, 32, 16, { LATE(); H_SUM(D, 8, s - 5); });
    H_FIX(D, 3, 8, t8);
    FSTAMP(16);
    G2_ROUND(D, 48, 16, { LATE(); H_SUM(E, 6, s - 7); if constexpr (s == 11) BAR_N(15); });
    H_FIX(E, 4, 6, t6);
    FSTAMP(17);
    G2_ROUND(E, 64, 12, { H_SUM(A, 6, s - 3); if constexpr (s == 7) BAR_N(15); });
    H_FIX(A, 5, 6, t6);
    FSTAMP(18);
    G2_ROUND(A, 76, 12, { });
    COUNT(23, nfix);
    STAMP(12);
    MMA_DONE(); SB();
#pragma unroll
    for (int i = 0; i < 4; ++i) lds_wf(rw + i * 1024, o[2 + (i & 1)][i >> 1]);
    BAR();
    const rsrc_t ro = mkrs(p.out, S_ * D_ * 2);
#pragma unroll
    for (int i = 0; i < 2; ++i) {
      const f32x4 v0 = o[0][i] + lds_rf(rr_ + i * 2048), v1 = o[1][i] + lds_rf(rr_ + i * 2048 + 1024);
      const auto x = __builtin_amdgcn_permlane16_swap(pk(v0[0], v0[1]), pk(v1[0], v1[1]), false, false);
      const auto y = __builtin_amdgcn_permlane16_swap(pk(v0[2], v0[3]), pk(v1[2], v1[3]), false, false);
      const v4u q = {x[0], y[0], x[1], y[1]};
      __builtin_amdgcn_raw_buffer_store_b128(q, ro, vout + i * 2048, 0, OAUX);
    }
    STAMP(13);
    STAMP_FLUSH();
  }
}

extern "C" void kernel_launch(void* const* d_in, const int* in_sizes, int n_in,
                              void* d_out, int out_size, void* d_ws, size_t ws_size,
                              hipStream_t stream) {
  Params p;
  p.x = (const u16*)d_in[0]; p.wr = (const u16*)d_in[1]; p.wi = (const u16*)d_in[2];
  p.wg = (const u16*)d_in[4]; p.wu = (const u16*)d_in[5]; p.wd = (const u16*)d_in[6];
  p.out = (u16*)d_out; p.ws = d_ws;
  (void)hipFuncSetAttribute((const void*)gfnet_geglu_kernel, hipFuncAttributeMaxDynamicSharedMemorySize, 5 * SLOT);
  gfnet_geglu_kernel<<<256, 512, 5 * SLOT, stream>>>(p);
}
```

```cpp
#define X0SLEEP 8
#define L4 36
#define L5 50
#define T1 285
#define T2 550
#define T3 760
#include <hip/hip_runtime.h>
#include <hip/hip_bf16.h>
#include <type_traits>

typedef unsigned u32;
typedef unsigned short u16;
typedef u32 v4u __attribute__((ext_vector_type(4)));
typedef u32 v2u __attribute__((ext_vector_type(2)));
typedef float f32x2 __attribute__((ext_vector_type(2)));
typedef float f32x4 __attribute__((ext_vector_type(4)));
typedef __bf16 bf16x2 __attribute__((ext_vector_type(2)));
typedef __bf16 bf16x8 __attribute__((ext_vector_type(8)));
typedef short s16x4 __attribute__((ext_vector_type(4)));
#define LAS __attribute__((address_space(3)))
#define DI __device__ __forceinline__
typedef __amdgpu_buffer_rsrc_t rsrc_t;
typedef LAS char* lp_t;

constexpr int S_ = 2048, D_ = 512, F_ = 1376, KF = 257;
constexpr int NFT = 86;
constexpr int WS_E = 0;
constexpr int XREC = 88 * 128;
constexpr int WS_A = 4096;
constexpr int WS_B = WS_A + 256 * XREC;
constexpr int WS_W = WS_B + 256 * XREC;
constexpr int WS_L = WS_W + (2 << 20);
constexpr int WS_T = WS_L + (2 << 20);
constexpr int WS_S = WS_T + (1 << 15);
constexpr int WS_H = WS_S + (1 << 17);
constexpr int HREC = 1152;
constexpr int WS_END = WS_H + 64 * NFT * HREC;
constexpr int WS_ST = WS_END;
constexpr u32 NEG2 = 0x80008000u, NEGH = 0x80000000u, NEGL = 0x8000u;
constexpr u32 SPIN_CAP = 1u << 14;
constexpr int AUX_SC1 = 16;
#define OAUX 2
#define NTAUX 2
constexpr int OOB = 0x40000000;
constexpr int CODE_LINES = 260;
constexpr int SLOT = 32768, SCR = 4 * SLOT;
constexpr int L_F = 0, L_TG = 2048;
constexpr int L_IM = 4096, L_SO = 4096, L_X = 16384, L_I2 = 16384, L_I3 = 16384, L_EX = 24576, L_O3 = 24576;

#define DRAIN() asm volatile("s_waitcnt vmcnt(0)" ::: "memory")
#define MEMFENCE() asm volatile("" ::: "memory")
#define PIN(x) asm volatile("" : "+v"(x))
#define PINS(x) asm volatile("" : "+s"(x))
#define SB() __builtin_amdgcn_sched_barrier(0)
#define BAR() asm volatile("s_waitcnt lgkmcnt(0)\n\ts_barrier" ::: "memory")
#define BAR_N(n) asm volatile("s_waitcnt lgkmcnt(" #n ")\n\ts_barrier" ::: "memory")

DI u32 pk(float a, float b) { f32x2 v = {a, b}; return __builtin_bit_cast(u32, __builtin_convertvector(v, bf16x2)); }
DI v4u pk4(f32x4 a, f32x4 b) { v4u r = {pk(a[0], b[0]), pk(a[1], b[1]), pk(a[2], b[2]), pk(a[3], b[3])}; return r; }
DI float asf(u32 v) { return __builtin_bit_cast(float, v); }
DI u32 asu(float v) { return __builtin_bit_cast(u32, v); }
DI float blo(u32 v) { return asf(v << 16); }
DI float bhi(u32 v) { return asf(v & 0xffff0000u); }
DI float dot2(u32 a, u32 b, float c) { return __builtin_amdgcn_fdot2_f32_bf16(__builtin_bit_cast(bf16x2, a), __builtin_bit_cast(bf16x2, b), c, false); }
DI u32 sum4(u32 m, v4u v) { return (m + v.x + v.y) + v.z + v.w; }
template <int CTRL> DI u32 dppu(u32 v) { return (u32)__builtin_amdgcn_update_dpp(0, (int)v, CTRL, 0xf, 0xf, true); }
template <int CTRL> DI float dppf(float v) { return asf(dppu<CTRL>(asu(v))); }
DI u32 pair_total(u32 s0, u32 s1, bool odd) { return (odd ? s1 : s0) + dppu<0xb1>(odd ? s0 : s1); }
DI u32 add_x16(u32 v) { const auto s = __builtin_amdgcn_permlane16_swap(v, v, false, false); return s[0] + s[1]; }
DI u32 add_x32(u32 v) { const auto s = __builtin_amdgcn_permlane32_swap(v, v, false, false); return s[0] + s[1]; }
template <int N> DI void wave_sums(float (&v)[N]) {
#define WS_STEP(C) _Pragma("unroll") for (int i = 0; i < N; ++i) v[i] += dppf<C>(v[i]);
  WS_STEP(0x111) WS_STEP(0x112) WS_STEP(0x114) WS_STEP(0x118) WS_STEP(0x142) WS_STEP(0x143)
#pragma unroll
  for (int i = 0; i < N; ++i) v[i] = asf(__builtin_amdgcn_readlane(asu(v[i]), 63));
}
__host__ __device__ constexpr int rec_q(int k) { return (k & 7) | ((k >> 1) & 0x18); }
__host__ __device__ constexpr int rec_line(int k) { return 11 * ((k >> 3) & 1) + ((rec_q(k) * 11) >> 5); }
__host__ __device__ constexpr int rec_pos(int k) { return rec_q(k) - 3 * ((rec_q(k) * 11) >> 5); }
__host__ __device__ constexpr int im_unit(int k, int j) { return L_IM + (k >> 6) * 3072 + rec_line(k) * 128 + ((j ^ (rec_line(k) + 2 * (k >> 6))) & 7) * 16; }
__host__ __device__ constexpr int im_chk(int k) { return im_unit(k, 6) + 4 * rec_pos(k); }
__host__ __device__ constexpr int im_chkw(int k) { return im_unit(k, 6 + ((rec_pos(k) + 3) >> 2)) + 4 * ((rec_pos(k) + 3) & 3); }
DI void lds_add(lp_t a, u32 v) { __hip_atomic_fetch_add((LAS u32*)a, v, __ATOMIC_RELAXED, __HIP_MEMORY_SCOPE_WORKGROUP); }
DI f32x4 mm16(v4u a, v4u b, f32x4 c) { return __builtin_amdgcn_mfma_f32_16x16x32_bf16(__builtin_bit_cast(bf16x8, a), __builtin_bit_cast(bf16x8, b), c, 0, 0, 0); }
DI f32x4 mm16p(v4u a, v4u b, f32x4 c) { f32x4 d = mm16(a, b, c); PIN(d); return d; }
template <bool FIRST> DI void mma(f32x4& d, v4u a, v4u b) {
  if constexpr (FIRST) asm volatile("v_mfma_f32_16x16x32_bf16 %0, %1, %2, 0" : "=&v"(d) : "v"(a), "v"(b));
  else asm volatile("v_mfma_f32_16x16x32_bf16 %0, %1, %2, %0" : "+v"(d) : "v"(a), "v"(b));
}
#define MMA_DONE() asm volatile("s_nop 7")
DI v2u lds_tr(lp_t a) { return __builtin_bit_cast(v2u, __builtin_amdgcn_ds_read_tr16_b64_v4i16((LAS s16x4*)a)); }
DI v4u lds_tr2(lp_t a, lp_t b) { v2u x = lds_tr(a), y = lds_tr(b); v4u r = {x.x, x.y, y.x, y.y}; return r; }
DI v4u lds_r128(lp_t a) { return *(LAS v4u*)a; }
DI f32x4 lds_rf(lp_t a) { return *(LAS f32x4*)a; }
DI v2u lds_r64(lp_t a) { return *(LAS v2u*)a; }
DI void lds_w128(lp_t a, v4u v) { *(LAS v4u*)a = v; }
DI void lds_wf(lp_t a, f32x4 v) { *(LAS f32x4*)a = v; }
DI void lds_w64(lp_t a, v2u v) { *(LAS v2u*)a = v; }
DI lp_t lptr(u32 off) { lp_t q = (lp_t)(size_t)off; PIN(q); return q; }
DI rsrc_t mkrs(const void* p, u32 bytes) { return __builtin_amdgcn_make_buffer_rsrc((void*)p, 0, bytes, 0x00020000); }
DI v4u ld_dev(rsrc_t r, int v, int s) { return __builtin_amdgcn_raw_buffer_load_b128(r, v, s, AUX_SC1); }
DI u32 ld_dev1(rsrc_t r, int v, int s) { return __builtin_amdgcn_raw_buffer_load_b32(r, v, s, AUX_SC1); }
DI void st_dev(rsrc_t r, int v, v4u x) { __builtin_amdgcn_raw_buffer_store_b128(x, r, v, 0, AUX_SC1); }
DI void st_dev1(rsrc_t r, int v, u32 x) { __builtin_amdgcn_raw_buffer_store_b32(x, r, v, 0, AUX_SC1); }
DI v4u ld_in(rsrc_t r, int v, int s) { return __builtin_amdgcn_raw_buffer_load_b128(r, v, s, 0); }
DI v4u ld_once(rsrc_t r, int v, int s) { return __builtin_amdgcn_raw_buffer_load_b128(r, v, s, NTAUX); }
DI u32 touch(rsrc_t r, int line) { return __builtin_amdgcn_raw_buffer_load_b32(r, line * 128, 0, 0); }
DI void flag_set(lp_t mine, u32 k) { MEMFENCE(); *(volatile LAS u32*)mine = k; MEMFENCE(); }
#define FLAG_WAIT(theirs, k, ...) do { u32 f_; _Pragma("nounroll") do { f_ = *(volatile LAS u32*)(theirs); MEMFENCE(); __VA_ARGS__; MEMFENCE(); } while (!__all(f_ >= (k))); } while (0)
DI void flag_wait_lazy(lp_t theirs, u32 k) { while (!__all(*(volatile LAS u32*)theirs >= k)) __builtin_amdgcn_s_sleep(1); MEMFENCE(); }
DI float cosr(float rev) { return __builtin_amdgcn_cosf(rev); }
DI float sinr(float rev) { return __builtin_amdgcn_sinf(rev); }
__host__ __device__ constexpr int slot_of(int k1, int k2) { return ((k2 < 8 ? k1 + 32 * k2 : (k1 ? 32 - k1 : 16) + 32 * (15 - k2)) - 1) & 255; }
DI int tile0_of(int c) { const int h = c >> 3, i = c & 7; return 44 * h + 4 * i + 2 * (i < 6 - h ? i : 6 - h); }
template <int I, int N, class F> DI void static_for(F&& f) {
  if constexpr (I < N) { f(std::integral_constant<int, I>{}); static_for<I + 1, N>(f); }
}
DI void transpose8(v4u (&m)[8]) {
  v4u o[8];
#pragma unroll
  for (int i = 0; i < 8; ++i)
#pragma unroll
    for (int j = 0; j < 4; ++j) { const u32 a = m[2 * j][i >> 1], b = m[2 * j + 1][i >> 1]; o[i][j] = __builtin_amdgcn_perm(b, a, (i & 1) ? 0x07060302u : 0x05040100u); }
#pragma unroll
  for (int i = 0; i < 8; ++i) { m[i] = o[i]; PIN(m[i]); }
}
enum { E_SQ, E_POLY, E_ARG, E_EXP, E_INC, E_RCP, E_GU, E_OUT, E_PACK, E_SWA, E_SWB, E_SUM, E_SUM2, E_ST, E_X1, E_X2, E_X8, E_CHK, E_STK };
struct EOp { int kind, v; };
struct EProg { EOp op[520]; int n, first[7]; };
constexpr EProg make_eprog() {
  EProg p = {};
  for (int t = 0; t < 6; ++t) {
    p.first[t] = p.n;
    for (int v = 0; v <= 8; ++v) {
      const int V = 8 * t + v;
      const bool head = v < 8, tail = v > 0, fin = t > 0 && t < 5;
      if (head) p.op[p.n++] = {E_SQ, V};
      if (tail) p.op[p.n++] = {E_INC, V - 1};
      if (head) p.op[p.n++] = {E_POLY, V}; else p.op[p.n++] = {E_GU, V - 1};
      if (tail) p.op[p.n++] = {E_RCP, V - 1}; else if (fin) p.op[p.n++] = {E_X1, V - 1};
      if (head) p.op[p.n++] = {E_ARG, V};
      if (tail && head) p.op[p.n++] = {E_GU, V - 1};
      if (v >= 3 && (v & 1)) p.op[p.n++] = {E_PACK, V - 2};
      if (v == 8) p.op[p.n++] = {E_SWA, V - 1};
      if (fin && v == 0) p.op[p.n++] = {E_X2, V - 1};
      if (fin && v == 1) p.op[p.n++] = {E_CHK, V - 2};
      if (head) p.op[p.n++] = {E_EXP, V};
      if (fin && v == 0) p.op[p.n++] = {E_X8, V - 1};
      if (fin && v == 1) p.op[p.n++] = {E_STK, V - 2};
      if (tail) p.op[p.n++] = {E_OUT, V - 1};
    }
    p.op[p.n++] = {E_PACK, 8 * t + 7}; p.op[p.n++] = {E_SUM, 8 * t + 7}; p.op[p.n++] = {E_SWB, 8 * t + 7}; p.op[p.n++] = {E_ST, 8 * t + 7}; p.op[p.n++] = {E_SUM2, 8 * t + 7};
    if (t >= 4) for (int k = E_X1; k <= E_STK; ++k) p.op[p.n++] = {k, 8 * t + 7};
  }
  p.first[6] = p.n;
  return p;
}
constexpr EProg kEp = make_eprog();
#define ER1 56
constexpr int epos(int n) {
  const int mid = (64 * ER1) / 16, end = kEp.first[5];
  int j = n <= 64 ? 0 : n <= 128 ? ((n - 64) * ER1) / 16 : n <= 192 ? mid + ((end - mid) * (n - 128)) / 64
        : end + ((kEp.n - end) * (n - 192)) / 8;
  for (int t = 2; t < 5; ++t) if (n <= 32 * t + 34 && j > kEp.first[t]) j = kEp.first[t];
  return j;
}
struct Epi { float t[2], q, h[3]; v4u r, k4; u32 sum, fin; };
template <int KIND, int V> DI void epi_op(Epi& e, const f32x4 (&a)[4], u32 tag) {
  constexpr int v = V & 7;
  const float gv = a[v >> 2][v & 3], uv = a[2 + (v >> 2)][v & 3];
  float& t = e.t[V & 1];
  if constexpr (KIND == E_SQ) { t = gv * gv; PIN(t); }
  else if constexpr (KIND == E_POLY) { t = __builtin_fmaf(t, -0.10294324f, -2.3022082f); PIN(t); }
  else if constexpr (KIND == E_ARG) { t = t * gv; PIN(t); }
  else if constexpr (KIND == E_EXP) { t = __builtin_amdgcn_exp2f(t); PIN(t); }
  else if constexpr (KIND == E_INC) { t = t + 1.0f; PIN(t); }
  else if constexpr (KIND == E_RCP) { t = __builtin_amdgcn_rcpf(t); PIN(t); }
  else if constexpr (KIND == E_GU) { e.q = gv * uv; PIN(e.q); }
  else if constexpr (KIND == E_OUT) { e.h[v & 1] = e.q * t; PIN(e.h[v & 1]); }
  else if constexpr (KIND == E_PACK) { u32 r = pk(e.h[0], e.h[1]); PIN(r); e.r[v >> 1] = r; }
  else if constexpr (KIND == E_SWA || KIND == E_SWB) { constexpr int i = KIND == E_SWB; const u32 x = e.r[i], y = e.r[i + 2];
    const auto q = __builtin_amdgcn_permlane16_swap(x, y, false, false); u32 q0 = q[0], q1 = q[1]; PIN(q0); PIN(q1); e.r[i] = q0; e.r[i + 2] = q1; }
  else if constexpr (KIND == E_SUM) { e.sum = e.r.x + e.r.z; PIN(e.sum); }
  else if constexpr (KIND == E_SUM2) { e.fin = e.sum + e.r.y + e.r.w; PIN(e.fin); }
  else if constexpr (KIND == E_X1) { e.fin += dppu<0xb1>(e.fin); PIN(e.fin); }
  else if constexpr (KIND == E_X2) { e.fin += dppu<0x4e>(e.fin); PIN(e.fin); }
  else if constexpr (KIND == E_X8) { e.fin += dppu<0x128>(e.fin); PIN(e.fin); }
  else if constexpr (KIND == E_CHK) { asm volatile("" : "=v"(e.k4)); e.k4.x = tag - e.fin; PIN(e.k4); }
}

constexpr double ct_sin(long n, long d) {
  n %= d; if (n < 0) n += d;
  if (2 * n > d) return -ct_sin(d - n, d);
  if (4 * n > d) return ct_sin(d - 2 * n, 2 * d);
  const double x = 6.283185307179586476925 * (double)n / (double)d;
  double t = x, r = x;
  for (int i = 1; i < 14; ++i) { t *= -x * x / ((2 * i) * (2 * i + 1)); r += t; }
  return r;
}
constexpr double ct_cos(long n, long d) { return ct_sin(4 * n + d, 4 * d); }
constexpr u32 ct_f32(double v) { return __builtin_bit_cast(u32, (float)v); }
constexpr u32 ct_bf(double v) { const u32 u = ct_f32(v); return (u + 0x7fffu + ((u >> 16) & 1u)) >> 16; }
constexpr u32 ct_pk(double a, double b) { return ct_bf(a) | (ct_bf(b) << 16); }
struct Tab { u32 v[6][64][4]; };
constexpr Tab make_tab() {
  Tab t = {};
  for (int l = 0; l < 64; ++l) {
    const int c = l & 15, g = l >> 4;
    for (int j = 0; j < 2; ++j) {
      const int a = 4 * g + 2 * j;
      t.v[0][l][j] = ct_pk(ct_cos(a * c, 32), ct_cos((a + 1) * c, 32));
      t.v[0][l][2 + j] = c ? ct_pk(-ct_sin(a * c, 32), -ct_sin((a + 1) * c, 32)) : ct_pk(1, -1);
    }
    for (int r = 0; r < 4; ++r) {
      const int b = 4 * g + r, sl = slot_of(b, c);
      t.v[1][l][r] = ct_f32(ct_cos(b * c, 512));
      t.v[2][l][r] = ct_f32(ct_sin(b * c, 512));
      t.v[3][l][r] = ct_pk(ct_cos(b * c, 16), ct_sin(b * c, 16));
      t.v[4][l][r] = c == 0 ? ct_pk((r & 1) ? -1 : 1, 0) : c < 8 ? ct_pk(ct_cos(b * c, 16), -ct_sin(b * c, 16)) : ct_pk(ct_cos(b * (31 - 2 * c), 32), -ct_sin(b * (31 - 2 * c), 32));
      t.v[5][l][r] = im_unit(sl, 2 * rec_pos(sl)) | (im_chk(sl) << 16);
    }
  }
  return t;
}
static_assert(true, "");
static __device__ const __attribute__((aligned(128))) Tab g_tab = make_tab();

#define STAMP(i) do { } while (0)
#define STAMP_DECL do { } while (0)
#define STAMP_FLUSH() do { } while (0)
#define COUNT(i, v) do { } while (0)

#define FSTAMP(i) do { } while (0)
#define PACE(d, t) do { __builtin_amdgcn_s_sleep(d); while ((u32)__builtin_amdgcn_s_memrealtime() - t_ref < (t)) __builtin_amdgcn_s_sleep(1); } while (0)
#define RS1 0
#define RS2 10
#define RS3 0
#define D1 1
#define D2 4
#define D3 6
#define TW 240
#define SLIPPED() (*(LAS u32*)(fmine + L_TG + 1024) = (u32)__builtin_amdgcn_s_memrealtime() - t_ref)
#define PACE2(d, t, sl) do { u32 r_ = *(LAS u32*)(fall + L_TG + 1024); r_ = max(r_, dppu<0xb1>(r_)); r_ = max(r_, dppu<0x4e>(r_)); \
      r_ = __builtin_amdgcn_readfirstlane(r_) + (sl); t_ref += r_ > (t) ? r_ - (t) : 0u; PACE(d, t); } while (0)
#define SL2 175
#define SL3 150
#define WAIT1() PACE(D1, T1)
#define WAIT2() PACE2(D2, T2, SL2)
#define WAIT3() PACE2(D3, T3, SL3)
struct Params {
  const u16* x; const u16* wr; const u16* wi; const u16* wg; const u16* wu; const u16* wd; u16* out; void* ws;
};

__global__ void __launch_bounds__(512, 2) gfnet_geglu_kernel(Params p) {
  const int wg = blockIdx.x, tid = threadIdx.x, lane = tid & 63;
  int wv = __builtin_amdgcn_readfirstlane(tid >> 6);
  STAMP_DECL;
  unsigned long long t_entry;
  asm volatile("s_memrealtime %0" : "=s"(t_entry));
  const int rg = 4 * ((wg & 7) >> 1) + (wg >> 6), ch = 8 * (wg & 1) + ((wg >> 3) & 7);
  const int un = 16 * rg + ch;
  const int ft0 = tile0_of(ch);
  const bool six = (ch & 7) < 6 - (ch >> 3);
#define W1SZ (D_ * F_ * 2)
#define WD_SETUP() const rsrc_t rwd = mkrs(p.wd, D_ * F_ * 2); \
    int vd = (lane >> 3) * (D_ * 2) + (ch & 7) * 128 + ((lane & 7) ^ ((lane >> 3) & 2)) * 16, vd1 = vd ^ 64, sd[4]; PIN(vd); PIN(vd1); \
    _Pragma("unroll") for (int i = 0; i < 4; ++i) { sd[i] = tile0_of(4 * (wv & 3) + i) * (16 * D_ * 2); PINS(sd[i]); }
#define DMA_WD1(t, slot, i) asm volatile("s_mov_b32 m0, %0\n\ts_nop 0\n\tbuffer_load_dwordx4 %1, %2, %3 offen lds" \
      :: "s"((slot) * SLOT + (wv & 3) * 8192 + (i) * 1024), "v"(((i) & 1) ? vd1 : vd), "s"(rwd), "s"(sd[(i) >> 1] + ((t) * 16 + ((i) & 1) * 8) * (D_ * 2)) : "memory")
#define TH2 6
#define TG 4
#define THROTTLE(i) do { if ((i) % TG == 0) asm volatile("s_waitcnt vmcnt(%0)" :: "n"(TH2) : "memory"); } while (0)
#define DMA_WD(t, slot) do { _Pragma("unroll") for (int i = 0; i < 8; ++i) { THROTTLE(i); DMA_WD1(t, slot, i); } } while (0)
#define DMA_WDS(t, slot) do { _Pragma("unroll") for (int i = 0; i < 4; ++i) DMA_WD1(t, slot, i); if (!(wv & 1)) { _Pragma("unroll") for (int i = 4; i < 8; ++i) DMA_WD1(t, slot, i); } } while (0)
#define VM_WAIT(n) asm volatile("s_waitcnt vmcnt(" #n ")" ::: "memory")
#define PDN 4
  constexpr int PD = PDN;
#define FRAG(N) frag(std::integral_constant<int, (N)>{})
#define NXT(n) (((n) + PD) ^ 1)
  v4u ra[8];
  u32 cpf;
#define UNDEF_RA() do { asm volatile("" : "=v"(cpf)); _Pragma("unroll") for (int i = 0; i < 8; ++i) asm volatile("" : "=v"(ra[i])); } while (0)
  UNDEF_RA();
  if (wv < 4) {
    const int vt = lane * 16;
    int sx = (32 * (8 * (un >> 5) + 2 * wv) + (un & 31)) * (D_ * 2), sy = sx + 32 * D_ * 2;
    const rsrc_t rt = mkrs(&g_tab, sizeof(Tab));
    ra[2] = ld_in(rt, vt, 0); ra[3] = ld_in(rt, vt + 1024, 0);
    PINS(sx); PINS(sy);
    SB();
    const rsrc_t rx = mkrs(p.x, S_ * D_ * 2);
    ra[0] = ld_once(rx, vt, sx); ra[1] = ld_once(rx, vt, sy);
    SB();
#pragma unroll
    for (int i = 0; i < 4; ++i) ra[4 + i] = ld_in(rt, vt + i * 1024, 2048);
    cpf = ld_dev1(mkrs(p.ws, WS_END), 0, WS_E);
    STAMP(0);
  }
  PINS(wv);
  if (wv >= 6) {
    unsigned long long pc0;
    asm volatile("s_getpc_b64 %0" : "=s"(pc0));
    cpf = touch(mkrs((const void*)(pc0 & ~127ull), CODE_LINES * 128), lane < 9 ? (((wg >> 3) ^ (wv & 1)) * 9 + lane) : 4096);
    unsigned long long te;
    asm volatile("s_waitcnt lgkmcnt(0)\n\ts_mov_b64 %0, %1" : "=s"(te) : "s"(t_entry));
    st_dev(mkrs(p.ws, WS_END), (tid >= 504 && wg < 8) ? WS_T + wg * 128 + (lane & 7) * 16 : OOB, v4u{(u32)te, (u32)te, (u32)te, (u32)te});
  }
  PINS(wv);
  if ((wv >> 1) == 2) {
#define WAUX 0
#define WSLEEP 2
    __builtin_amdgcn_s_sleep(WSLEEP);
    const rsrc_t rwr = mkrs(p.wr, S_ * KF * 2), rwi = mkrs(p.wi, S_ * KF * 2);
    const int vf = (lane & 7) * (KF * 2) + (lane & 1) * 2 + ((tid & 127) >> 3) * 8;
    const int sf = (16 * (un >> 6) + 2 * (un & 4) + 256 * ((un >> 3) & 7)) * (KF * 2) + (un & 3) * 128;
#pragma unroll
    for (int r = 0; r < 4; ++r) { ra[r] = __builtin_amdgcn_raw_buffer_load_b128(rwr, vf, sf + r * (64 * KF * 2), WAUX); ra[4 + r] = __builtin_amdgcn_raw_buffer_load_b128(rwi, vf, sf + r * (64 * KF * 2), WAUX); }
  }
  const lp_t fmine = lptr(SCR + L_F + tid * 4);
  *(volatile LAS u32*)fmine = 0;
  lds_w128(lptr(SCR + L_IM + (tid & 127) * 128 + (((6 + ((tid >> 7) & 1)) ^ (tid + 2 * (((tid & 127) * 43) >> 10))) & 7) * 16), v4u{0u, 0u, 0u, 0u});
  BAR();
  PINS(t_entry);
  u32 t_ref = (u32)t_entry;
  v4u wa_;
  asm volatile("" : "=v"(wa_));
  PINS(wv);
  if ((wv >> 1) == 2) {
    const int T = tid - 256, k = 64 * (un & 3) + 4 * (T >> 3) + (T & 3);
    wa_ = v4u{(u32)((T >> 3) * 512 + (T & 7) * 16 + WS_W + un * 8192), (lane & 1) ? 0u : 16u, (u32)(SCR + im_chkw(k)), (u32)(SCR + im_chk(k))};
    PIN(wa_);
  }
  PINS(wv);
  if (wv >= 6) {
    __builtin_amdgcn_s_sleep(X0SLEEP);
    const rsrc_t r1 = mkrs((wv & 1) ? p.wu : p.wg, W1SZ);
    ra[0].x = touch(r1, lane < 43 ? wg * 43 + lane : 16384);
  }
  SB();
  PINS(wv);

  if (wv < 4) {
  const int w = wv, c = lane & 15, g = lane >> 4;
  const float fc = (float)c, fg4 = (float)(4 * g);
  const f32x4 z = {0.f, 0.f, 0.f, 0.f};
  f32x4 twc, tws;
  v4u fbre, fbsw;
  u32 tag;
  float mean[2];
  const rsrc_t ws = mkrs(p.ws, WS_END);
  const lp_t fall = lptr(SCR + L_F + (lane & 3) * 256 + lane * 4);
  const lp_t imr = lptr(SCR + L_IM + w * 3072 + (lane >> 3) * 128 + ((lane ^ ((lane >> 3) + 2 * w)) & 7) * 16);
  int vx0 = lane * 16 + WS_A + un * XREC + w * (22 * 128), vx2 = lane < 48 ? vx0 + 2048 : OOB;
  PIN(vx0); PIN(vx2);
  int vq8 = (lane & 7) * 128;
  PIN(vq8);
  u32 tq;
  v2u xk[2][2];
  f32x4 tfc[2], tfs[2], tic[2], tis[2];
  const float fj = (float)(16 * w) + fc;
#pragma unroll
  for (int r = 0; r < 4; ++r) {
#pragma unroll
      for (int m = 0; m < 2; ++m) {
        const float nf = (fg4 + (16 * m + r)) * (fj * (1.f / 2048)), ni = (fg4 + (float)(16 * w) + r) * ((fc + 16 * m) * (1.f / 2048));
        tfc[m][r] = cosr(nf); tfs[m][r] = sinr(nf); tic[m][r] = cosr(ni); tis[m][r] = sinr(ni);
      }
  }
#pragma unroll
  for (int i = 0; i < 2; ++i) { PIN(tfc[i]); PIN(tfs[i]); PIN(tic[i]); PIN(tis[i]); }
  SB();
  {
    const v4u xr[2] = {ra[0], ra[1]}, tb[6] = {ra[2], ra[3], ra[4], ra[5], ra[6], ra[7]};
    const u32 sc = (c & 1) ? NEG2 : 0u, sm = c < 8 ? NEGL : NEGH, p0 = c < 8 ? 0x0c0c0100u : 0x01000c0cu, p1 = c < 8 ? 0x0c0c0302u : 0x03020c0cu;
    v4u fare = {tb[0].x, tb[0].y, tb[0].x ^ sc, tb[0].y ^ sc}, faim = {tb[0].z, tb[0].w, tb[0].z ^ sc, tb[0].w ^ sc}, fbim, f0re, f0im;
    twc = __builtin_bit_cast(f32x4, tb[1]); tws = __builtin_bit_cast(f32x4, tb[2]); fbre = tb[3];
    lp_t t1w[4], t1c[4];
#pragma unroll
    for (int r = 0; r < 4; ++r) {
      const u32 a = tb[3][r], q = tb[4][r];
      fbsw[r] = __builtin_amdgcn_alignbit(a, a, 16);
      fbim[r] = fbsw[r] ^ sm;
      f0re[r] = __builtin_amdgcn_perm(q, q, p0); f0im[r] = __builtin_amdgcn_perm(q, q, p1);
      t1w[r] = lptr((tb[5][r] & 0xffffu) ^ (SCR + w * 8)); t1c[r] = lptr((tb[5][r] >> 16) + SCR);
    }
    float c0m = c == 0 ? 1.f / 16 : 0.f;
    lp_t xw = lptr(SCR + L_X + w * 2048 + lane * 16), xt = lptr(SCR + L_X + w * 2048 + lane * 8);
    lp_t xq = lptr(SCR + L_X + w * 2048 + (4 * c + g) * 8), f6 = lptr(SCR + L_F + (lane % 6) * 256 + lane * 4);
    tag = cpf + 1u;
    u32 tag0 = w ? 0u : tag;
    PIN(c0m); PIN(tag); PIN(tag0);
    *(LAS u32*)(fmine + L_TG) = tag;
    *(LAS u32*)(fmine + L_TG + 1024) = 0;
    PIN(fare); PIN(faim); PIN(fbre); PIN(fbim); PIN(f0re); PIN(f0im); PIN(twc); PIN(tws);
    STAMP(1);
    SB();
    v4u a[2];
#pragma unroll
    for (int rr = 0; rr < 2; ++rr) { lds_w128(xw + rr * 1024, xr[rr]); a[rr] = lds_tr2(xt + rr * 1024, xt + rr * 1024 + 512); }
    tq = ld_dev1(ws, vq8, WS_T);
    STAMP(2);
    float st[4] = {0.f, 0.f, 0.f, 0.f};
#pragma unroll
    for (int i = 0; i < 4; ++i)
#pragma unroll
      for (int rr = 0; rr < 2; ++rr) { const u32 v = xr[rr][i]; st[rr] = dot2(v, 0x3f803f80u, st[rr]); st[2 + rr] = dot2(v, v, st[2 + rr]); }
    SB();
    f32x4 ur[2], ui[2];
#pragma unroll
    for (int rr = 0; rr < 2; ++rr) { ur[rr] = mm16p(a[rr], fare, z); ui[rr] = mm16p(a[rr], faim, z); }
    wave_sums<4>(st);
    SB();
    v4u a2[2];
    f32x4 orr[2], oi[2], sr[2], si[2];
#pragma unroll
    for (int rr = 0; rr < 2; ++rr) {
      const f32x4 x = ur[rr] - st[rr] * c0m, y = ui[rr];
      a2[rr] = pk4(x * twc + y * tws, y * twc - x * tws);
      orr[rr] = mm16p(a2[rr], fbre, z); oi[rr] = mm16p(a2[rr], fbim, z); sr[rr] = mm16p(a2[rr], f0re, z); si[rr] = mm16p(a2[rr], f0im, z);
    }
    v4u o[2];
#pragma unroll
    for (int rr = 0; rr < 2; ++rr) {
      mean[rr] = st[rr] * (1.f / 512);
      const float rstd = __builtin_amdgcn_rsqf(fmaxf(st[2 + rr] * (1.f / 512) - mean[rr] * mean[rr], 0.f) + 1e-5f);
      orr[rr][0] = g == 0 ? sr[rr][0] : orr[rr][0]; oi[rr][0] = g == 0 ? si[rr][0] : oi[rr][0];
      o[rr] = pk4(orr[rr] * rstd, oi[rr] * rstd);
    }
#pragma unroll
    for (int r = 0; r < 4; ++r) { v2u v = {o[0][r], o[1][r]}; lds_w64(t1w[r], v); lds_add(t1c[r], v.x + v.y + tag0); }
    STAMP(3);
    flag_set(fmine, 1);
    v4u v0, v1, v2;
    FLAG_WAIT(f6, 1, v0 = lds_r128(imr); v1 = lds_r128(imr + 1024); v2 = lds_r128(imr + 2048));
    st_dev(ws, vx0, v0); st_dev(ws, vx0 + 1024, v1); st_dev(ws, vx2, v2);
    flag_set(fmine, 2);
#pragma unroll
    for (int i = 0; i < 4; ++i) xk[i >> 1][i & 1] = lds_r64(xq + (i >> 1) * 1024 + (i & 1) * 512);
  }
  int vge, vgo, vg = (lane >> 3) * (4 * 8192) + (lane & 7) * 16;
  {
    const int pay = (lane >> 1) * XREC + rec_pos(un) * 32 + (lane & 1) * 16, chk = (lane & 1) ? OOB : ((lane >> 1) ^ 1) * XREC + 96 + rec_pos(un) * 4;
    vge = (lane & 2) ? chk : pay; vgo = (lane & 2) ? pay : chk;
  }
  const int sg = w * (64 * XREC) + (22 * (un >> 6) + rec_line(un)) * 128;
#define GATHER(BUF) do { xl[0] = ld_dev(ws, vge, BUF + sg); xl[1] = ld_dev(ws, vgo, BUF + sg); xl[2] = ld_dev(ws, vge, BUF + sg + 32 * XREC); xl[3] = ld_dev(ws, vgo, BUF + sg + 32 * XREC); } while (0)
#define VERIFY(extra) ({ u32 k_ = 0; _Pragma("unroll") for (int m = 0; m < 2; ++m) { _Pragma("unroll") for (int i = 0; i < 4; ++i) xg[m][i] = (lane & 2) ? xl[2 * m + 1][i] : xl[2 * m][i]; \
      k_ += (lane & 2) ? xl[2 * m].x : xl[2 * m + 1].x; lds_w128(iw + m * 1024, xg[m]); } \
      u32 t_ = sum4(sum4((extra) - k_, xg[0]), xg[1]); t_ += dppu<0xb1>(t_); t_ += dppu<0x4e>(t_); t_; })
  const lp_t iw = lptr(SCR + L_I2 + w * 2048 + lane * 16);
  const int sw = WS_W + (w * 64 + (un >> 6)) * 8192 + (un & 63) * 128;
  v4u wq[2];
  STAMP(4);
  SB();
  v4u f2a[2][2];
  {
    v4u f1a[4], f1b[4], f2b[2][2];
    const u32 sc = (c & 1) ? NEG2 : 0u;
#pragma unroll
    for (int r = 0; r < 4; ++r) {
#pragma unroll
      for (int ks = 0; ks < 2; ++ks) {
        const float n = (fg4 + (16 * ks + r)) * (fj * (1.f / 64));
        const float cs = cosr(n), sn = sinr(n);
        f1a[ks][r] = pk(cs, sn); f1b[ks][r] = pk(-sn, cs);
        f1a[ks + 2][r] = f1a[ks][r] ^ sc; f1b[ks + 2][r] = f1b[ks][r] ^ sc;
      }
      const float n = (fg4 + r) * (fc * (1.f / 32));
      const float cs = cosr(n), sn = sinr(n);
      const u32 a = pk(cs, sn), b = pk(-sn, cs), sr = (r & 1) ? NEG2 : 0u;
      f2a[0][0][r] = a; f2a[1][0][r] = a ^ sr; f2a[0][1][r] = a ^ sc; f2a[1][1][r] = a ^ sc ^ sr;
      f2b[0][0][r] = b; f2b[1][0][r] = b ^ sr; f2b[0][1][r] = b ^ sc; f2b[1][1][r] = b ^ sc ^ sr;
    }
#pragma unroll
    for (int i = 0; i < 4; ++i) { PIN(f1a[i]); PIN(f1b[i]); PIN(f2a[i >> 1][i & 1]); PIN(f2b[i >> 1][i & 1]); }
    lp_t i2r = lptr(SCR + L_I2 + ((g >> 1) * 32 + c) * 32 + (g & 1) * 16);
    lp_t exw = lptr(SCR + L_EX + w * 2048 + lane * 16), exr = lptr(SCR + L_EX + lane * 16);
    lp_t imw[2], imc = lptr(SCR + im_chk(64 * w + lane));
#pragma unroll
    for (int n = 0; n < 2; ++n) { const int k = 64 * w + 32 * (g >> 1) + 16 * n + c; imw[n] = lptr(SCR + im_unit(k, 2 * rec_pos(k) + (g & 1))); }
    vx0 += WS_B - WS_A; vx2 += WS_B - WS_A;
    PIN(vg); PIN(vge); PIN(vgo); PIN(vx0); PIN(vx2);
    tq -= t_ref; tq = tq < 150u ? tq : 0u;
    tq = max(tq, dppu<0xb1>(tq)); tq = max(tq, dppu<0x4e>(tq)); tq = max(tq, dppu<0x141>(tq));
    t_ref += __builtin_amdgcn_readfirstlane(tq);
    STAMP(5);
    WAIT1();
    STAMP(6);
    v4u xl[4], xg[2];
    f32x4 wr[2], wi[2];
    GATHER(WS_A);
#define WD_ 8
    SB();
    __builtin_amdgcn_s_sleep(WD_);
    wq[0] = ld_dev(ws, vg, sw); wq[1] = ld_dev(ws, vg, sw + 32 * 8192);
    flag_set(fmine, 3);
    SB();
#define KW() (((lane & 2) ? xl[0].w : xl[1].w) + ((lane & 2) ? xl[2].w : xl[3].w))
    u32 kw = KW(), spin = 0;
    u32 bad = VERIFY(tag + kw);
    if (__any(bad != 0)) {
      _Pragma("nounroll") do {
        MEMFENCE();
        if (bad) GATHER(WS_A);
        kw = KW();
        bad = VERIFY(tag + kw);
      } while (__any(bad != 0) && ++spin < SPIN_CAP);
      SLIPPED();
      ++spin;
    }
    COUNT(20, spin);
    STAMP(7);
    v4u af[2][4], b2[2], a3[2];
    flag_set(fmine, 4);
    FLAG_WAIT(fall, 4, _Pragma("unroll") for (int i = 0; i < 8; ++i) af[i >> 2][i & 3] = lds_r128(i2r + (i & 3) * 2048 + (i >> 2) * 512));
    STAMP(8);
    SB();
    f32x4 dr[2], di[2];
#pragma unroll
    for (int m = 0; m < 2; ++m)
#pragma unroll
      for (int ks = 0; ks < 4; ++ks) { dr[m] = mm16(af[m][ks], f1a[ks], ks ? dr[m] : z); di[m] = mm16(af[m][ks], f1b[ks], ks ? di[m] : z); }
#pragma unroll
    for (int m = 0; m < 2; ++m) b2[m] = pk4(dr[m] * tfc[m] + di[m] * tfs[m], di[m] * tfc[m] - dr[m] * tfs[m]);
#pragma unroll
    for (int k = 0; k < 2; ++k)
#pragma unroll
      for (int m = 0; m < 2; ++m) { dr[m] = mm16(f2a[m][k], b2[k], k ? dr[m] : z); di[m] = mm16(f2b[m][k], b2[k], k ? di[m] : z); }
    {
#define W_BAD() ({ u32 t_ = sum4(sum4(0u - kw, wq[0]), wq[1]); t_ += dppu<0xb1>(t_); t_ += dppu<0x4e>(t_); t_ += dppu<0x141>(t_); t_; })
      u32 bw = W_BAD();
#pragma unroll
      for (int m = 0; m < 2; ++m) { PIN(dr[m]); PIN(di[m]); }
      if (__builtin_expect(__any(bw != 0), 0)) {
        u32 sp = 0;
        _Pragma("nounroll") do { MEMFENCE(); if (bw) { wq[0] = ld_dev(ws, vg, sw); wq[1] = ld_dev(ws, vg, sw + 32 * 8192); } bw = W_BAD(); } while (__any(bw != 0) && ++sp < SPIN_CAP);
      }
#pragma unroll
      for (int m = 0; m < 2; ++m)
#pragma unroll
        for (int r = 0; r < 4; ++r) { const u32 v = wq[m][r]; wr[m][r] = blo(v); wi[m][r] = bhi(v); }
    }
#pragma unroll
    for (int m = 0; m < 2; ++m) a3[m] = pk4(dr[m] * wi[m] + di[m] * wr[m], dr[m] * wr[m] - di[m] * wi[m]);
#pragma unroll
    for (int k = 0; k < 2; ++k)
#pragma unroll
      for (int n = 0; n < 2; ++n) { di[n] = mm16(a3[k], f2a[n][k], k ? di[n] : z); dr[n] = mm16(a3[k], f2b[n][k], k ? dr[n] : z); }
#pragma unroll
    for (int n = 0; n < 2; ++n) lds_w128(exw + n * 1024, pk4(di[n] * tic[n] + dr[n] * tis[n], dr[n] * tic[n] - di[n] * tis[n]));
    STAMP(9);
    flag_set(fmine, 5);
    FLAG_WAIT(fall, 5, _Pragma("unroll") for (int i = 0; i < 8; ++i) af[i >> 2][i & 3] = lds_r128(exr + (i & 3) * 2048 + (i >> 2) * 1024));
    SB();
    u32 cs[2];
#pragma unroll
    for (int n = 0; n < 2; ++n)
#pragma unroll
      for (int ks = 0; ks < 4; ++ks) { di[n] = mm16(f1a[ks], af[n][ks], ks ? di[n] : z); dr[n] = mm16(f1b[ks], af[n][ks], ks ? dr[n] : z); }
#pragma unroll
    for (int n = 0; n < 2; ++n) {
      const v4u e = pk4(dr[n], di[n]);
      lds_w128(imw[n], e);
      cs[n] = sum4(0u, e);
    }
    const auto sx = __builtin_amdgcn_permlane16_swap(cs[0], cs[1], false, false);
    *(LAS u32*)imc = tag + sx[0] + sx[1];
    MEMFENCE();
    const v4u v0 = lds_r128(imr), v1 = lds_r128(imr + 1024), v2 = lds_r128(imr + 2048);
    st_dev(ws, vx0, v0); st_dev(ws, vx0 + 1024, v1); st_dev(ws, vx2, v2);
    flag_set(fmine, 6);
    if (wg == 0 && w == 0) st_dev1(ws, WS_E, tag);
  }
  STAMP(10);
  SB();
  {
    v4u gbre, gbim, g0re, g0im, ga[2];
#pragma unroll
    for (int r = 0; r < 4; ++r) {
      gbre[r] = fbre[r] ^ (g < 2 ? NEGH : 0u);
      gbim[r] = fbsw[r] ^ (g < 2 ? 0u : NEGH);
      const float n = fc * ((31.f - 2.f * r - 2.f * fg4) * (1.f / 32));
      u32 q = pk(cosr(n), -sinr(n));
      PIN(q);
      g0re[r] = g < 2 ? gbre[r] : 0u;
      g0im[r] = g < 2 ? 0u : q;
      ga[0][r] = f2a[0][0][r] ^ NEGH; ga[1][r] = f2a[1][0][r] ^ NEGH;
    }
    twc *= 2.f / (2048.f * 512.f); tws *= 2.f / (2048.f * 512.f);
    g0re[0] = g == 0 ? ((c & 1) ? 0xbf00u : 0x3f00u) : g0re[0];
    ga[0][0] = g == 0 ? ((c & 1) ? 0xbf803f80u : 0x3f803f80u) : ga[0][0]; ga[1][0] = ga[0][0];
    PIN(gbre); PIN(gbim); PIN(g0re); PIN(g0im); PIN(ga[0]); PIN(ga[1]); PIN(twc); PIN(tws);
    f32x4 xin[2][2];
#pragma unroll
    for (int rr = 0; rr < 2; ++rr)
#pragma unroll
      for (int t = 0; t < 2; ++t) {
        const v2u v = xk[rr][t];
        const u32 v0 = v.x, v1 = v.y;
        f32x4 q = {blo(v0), bhi(v0), blo(v1), bhi(v1)};
        xin[rr][t] = q - mean[rr]; PIN(xin[rr][t]);
      }
    lp_t i3r[4], o3w[2], o3r[2];
#pragma unroll
    for (int r = 0; r < 4; ++r) i3r[r] = lptr(SCR + L_I3 + slot_of(c, 4 * g + r) * 32 + w * 8);
#pragma unroll
    for (int i = 0; i < 2; ++i) {
      o3w[i] = lptr(SCR + L_O3 + ((4 * c + g) * 8 + ((2 * w + i) ^ (c & 7))) * 16);
      const int q = w * 128 + i * 64 + lane;
      o3r[i] = lptr(SCR + L_O3 + (q ^ ((q >> 5) & 7)) * 16);
    }
    int vs = lane * 16 + WS_L + un * 8192 + w * 2048, vt = lane < 8 ? lane * 16 + WS_S + un * 512 + w * 128 : OOB;
    PIN(vs); PIN(vt);
    lp_t sow = lptr(SCR + L_SO + tid * 16), sor = lptr(SCR + L_SO + (lane & 6) * 512 + (lane & 1) * 8);
    WAIT2();
    STAMP(11);
    v4u xl[4], xg[2];
    GATHER(WS_B);
    flag_set(fmine, 7);
    SB();
    u32 bad = VERIFY(tag), spin = 0;
    if (__any(bad != 0)) {
      _Pragma("nounroll") do {
        __builtin_amdgcn_s_sleep(RS1);
        MEMFENCE();
        if (bad) GATHER(WS_B);
        bad = VERIFY(tag);
      } while (__any(bad != 0) && ++spin < SPIN_CAP);
      SLIPPED();
      ++spin;
    }
    COUNT(21, spin);
    STAMP(12);
    v4u a[2], a2[2];
    v2u zr[4];
    flag_set(fmine, 8);
    FLAG_WAIT(fall, 8, _Pragma("unroll") for (int r = 0; r < 4; ++r) zr[r] = lds_r64(i3r[r]));
    STAMP(13);
#pragma unroll
    for (int r = 0; r < 4; ++r) { a[0][r] = zr[r].x; a[1][r] = zr[r].y; }
    f32x4 tr[2], ti[2], sr[2], si[2], h[2][2];
#pragma unroll
    for (int rr = 0; rr < 2; ++rr) { tr[rr] = mm16p(a[rr], gbre, z); ti[rr] = mm16p(a[rr], gbim, z); sr[rr] = mm16p(a[rr], g0re, z); si[rr] = mm16p(a[rr], g0im, z); }
#pragma unroll
    for (int rr = 0; rr < 2; ++rr) {
      tr[rr][0] = g == 0 ? sr[rr][0] : tr[rr][0]; ti[rr][0] = g == 0 ? si[rr][0] : ti[rr][0];
      a2[rr] = pk4(tr[rr] * twc - ti[rr] * tws, ti[rr] * twc + tr[rr] * tws);
      h[rr][0] = mm16p(a2[rr], ga[0], xin[rr][0]); h[rr][1] = mm16p(a2[rr], ga[1], xin[rr][1]);
    }
    float st[2];
#pragma unroll
    for (int rr = 0; rr < 2; ++rr) { const f32x4 q = h[rr][0] * h[rr][0] + h[rr][1] * h[rr][1]; st[rr] = (q[0] + q[2]) + (q[1] + q[3]); }
    wave_sums<2>(st);
    v4u so;
#pragma unroll
    for (int rr = 0; rr < 2; ++rr) {
      const float ve = st[rr] * (1.f / 512) + 1e-5f, rstd = __builtin_amdgcn_rsqf(ve);
      const f32x4 l0 = h[rr][0] * rstd, l1 = h[rr][1] * rstd;
      v4u l4 = {pk(l0[0], l0[1]), pk(l0[2], l0[3]), pk(l1[0], l1[1]), pk(l1[2], l1[3])};
      lds_w128(o3w[rr], l4);
      so[2 * rr] = asu(mean[rr]); so[2 * rr + 1] = asu(ve * rstd);
    }
    lds_w128(sow, so);
    STAMP(14);
    v4u v0, v1;
    v2u ms;
    flag_set(fmine, 9);
    FLAG_WAIT(fall, 9, v0 = lds_r128(o3r[0]); v1 = lds_r128(o3r[1]); ms = lds_r64(sor));
    st_dev(ws, vs, v0); st_dev(ws, vs + 1024, v1);
    u32 s = sum4(sum4(0u, v0), v1);
    s += dppu<0x128>(s); s = add_x32(add_x16(s));
    v4u rec = {tag - s - ms.x - ms.y, ms.x, ms.y, tag};
    st_dev(ws, vt, rec);
    flag_set(fmine, 10);
  }
  SB();
  {
#define LOOKN 6
    constexpr int LOOK = LOOKN;
    v4u ring[8], act[32];
    lp_t wa[4], wb[4];
#pragma unroll
    for (int t = 0; t < 4; ++t) wa[t] = lptr(t * SLOT + g * 512 + ((c ^ (2 * t + (c >> 3))) * 16));
#pragma unroll
    for (int i = 0; i < 4; ++i) wb[i] = lptr(((i & 1) ? 0 : 4 * SLOT) + ((lane >> 2) ^ (g & 1)) * 64 + (((i >> 1) ^ g) & 1) * 32 + (lane & 3) * 8);
    auto frag = [&](auto NN) -> v4u {
      constexpr int N = NN.value;
      if constexpr (N < 64) return lds_r128(wa[(N >> 1) & 1] + (N >> 2) * 2048 + (N & 1) * 256);
      else if constexpr (N < 128) return lds_r128(wa[N >> 5] + ((N & 31) >> 1) * 2048 + (N & 1) * 256);
      else { const lp_t b = wb[((N >> 4) & 2) + (N & 1)] + ((N & 31) >> 1) * 2048; return lds_tr2(b, b + 1024); }
    };
    int vo = WS_H + ((4 * rg + w) * NFT + ft0) * HREC + lane * 16, vk = (lane & 11) ? OOB : vo - lane * 16 + 1024 + ((lane >> 4) * 8 + (lane & 7)) * 4;
    int vo4 = vo + 4 * HREC, vk4 = vk + 4 * HREC;
    PIN(vo); PIN(vk); PIN(vo4); PIN(vk4);
    Epi e;
#define EPI(J0, J1) static_for<(J0), (J1)>([&](auto JJ) { constexpr int kd = kEp.op[JJ.value].kind, V = kEp.op[JJ.value].v, t = V >> 3; \
      if constexpr (kd == E_ST) st_dev(ws, (t < 4 ? vo : vo4) + (t & 3) * HREC, e.r); \
      else if constexpr (kd == E_STK) st_dev(ws, (t < 4 ? vk : vk4) + (t & 3) * HREC, e.k4); \
      else epi_op<kd, V>(e, t % 3 == 0 ? tA : t % 3 == 1 ? tB : tC, tag); \
      SB(); })
    int va0 = ((lane >> 3) & 1) * 8192 + g * 128 + (lane & 7) * 16, va1 = va0 + 16384;
    int vr = ((lane >> 3) & 1) * 512 + g * 128 + (lane & 7) * 16;
    PIN(vr);
    const int sa = WS_L + (4 * rg + w) * 32768;
#define ACT_LD(i) act[i] = ld_dev(ws, (((i) & 1) ? va1 : va0) + (((i) >> 1) & 7) * 512, sa + ((i) >> 4) * 4096)
    f32x4 tA[4], tB[4], tC[4];
    WD_SETUP();
    v4u s0, s1;
#define A_HEAD() do { MEMFENCE(); PIN(va0); PIN(va1); s0 = ld_dev(ws, vr, WS_S + (4 * rg + w) * 2048); s1 = ld_dev(ws, vr + 1024, WS_S + (4 * rg + w) * 2048); SB(); \
      static_for<0, 2 * LOOK>([&](auto K) { ACT_LD(K.value); SB(); }); } while (0)
    WAIT3();
    STAMP(15);
    u32 spin = 0;
    A_HEAD();
    BAR();
    for (;; ++spin) {
      static_for<0, PD>([&](auto K) { ring[K.value ^ 1] = FRAG(K.value ^ 1); SB(); });
      if (__builtin_expect(__any(s0.w != tag || s1.w != tag), 0)) { _Pragma("nounroll") do { __builtin_amdgcn_s_sleep(RS3); A_HEAD(); } while (__any(s0.w != tag || s1.w != tag) && ++spin < SPIN_CAP); ++spin; }
      PIN(va0); PIN(va1);
      u32 m = (s0.x + s0.y + s0.z) + (s1.x + s1.y + s1.z);
      static_for<0, 64>([&](auto K) {
        constexpr int n = K.value, ks = n >> 2, q = (n & 1) * 2;
        f32x4* acc = (n & 2) ? tB : tA;
        mma<ks == 0>(acc[q], ring[n & 7], act[2 * ks]); SB();
        ring[NXT(n) & 7] = FRAG(NXT(n));
        if constexpr (!(n & 1) && ks + LOOK < 16) ACT_LD(2 * (ks + LOOK) + ((n >> 1) & 1));
        SB();
        mma<ks == 0>(acc[q + 1], ring[n & 7], act[2 * ks + 1]); SB();
        { const v4u v = act[2 * ks + ((n >> 1) & 1)]; u32 t = (n & 1) ? m + v.z + v.w : m + v.x + v.y; PIN(t); m = t; }
        SB();
      });
      if (__builtin_expect(__all(add_x32(add_x16(m)) == 8u * tag) || spin > SPIN_CAP, 1)) break;
      s0.w = ~tag;
    }
    COUNT(22, spin);
    BAR_N(15);
    STAMP(16);
#define G1_TILE(T, acc, ...) static_for<0, 32>([&](auto K) { constexpr int s = K.value, n = 32 * (T) + s, ks = s >> 1, q = (s & 1) * 2, j0 = epos(n), j1 = epos(n + 1), jm = j0 + (j1 - j0 - (T) / 4) / 2; \
      mma<ks == 0>(acc[q], ring[n & 7], act[2 * ks]); SB(); \
      if constexpr (n + PD < 192) ring[NXT(n) & 7] = FRAG(n + PD < 192 ? NXT(n) : 0); SB(); \
      EPI(j0, jm); \
      mma<ks == 0>(acc[q + 1], ring[n & 7], act[2 * ks + 1]); SB(); \
      __VA_ARGS__; EPI(jm, j1); SB(); })
#define PG2 16
#define PG4 12
#define PG3 10
    G1_TILE(2, tC, { });
    FSTAMP(26);
    G1_TILE(3, tA, { if constexpr (s == PG2) BAR_N(15); });
    FSTAMP(27);
    if (__builtin_expect(six, 1)) {
      G1_TILE(4, tB, { });
      FSTAMP(28);
      G1_TILE(5, tC, { if constexpr (s == PG4) BAR_N(15); });
      STAMP(17);
      MMA_DONE(); SB();
      EPI(kEp.first[5], kEp.n);
    } else {
      MMA_DONE(); SB();
      EPI(epos(128), kEp.first[4]);
      static_for<E_X1, E_STK>([&](auto KK) { epi_op<KK.value, 31>(e, tA, tag); });
      st_dev(ws, vk + 3 * HREC, e.k4);
#define SKSL 20
      __builtin_amdgcn_s_sleep(SKSL);
      BAR_N(15);
      FSTAMP(28);
      __builtin_amdgcn_s_sleep(SKSL + 6);
      STAMP(17);
    }
    STAMP(18);
    BAR();
#define CSL 10
    __builtin_amdgcn_s_sleep(CSL);
    DMA_WDS(4, 0);
    DMA_WDS(5, 1);
    BAR();
    if (w & 1) VM_WAIT(4); else VM_WAIT(8);
    BAR();
    VM_WAIT(0);
    BAR();
  }
  STAMP(24);
  STAMP_FLUSH();
  UNDEF_RA();
  asm volatile("" : "=v"(wa_));
  }
  PINS(wv);
  if (wv >= 4) {
    STAMP(0);
    const int hw = wv - 4;
    asm volatile("" :: "v"(cpf), "v"(ra[0]));
    if (hw < 2) {
    const rsrc_t ws = mkrs(p.ws, WS_END);
    const int T = tid - 256, vs = wa_.x;
    const u32 sh = wa_.y;
    const lp_t ca0 = (lp_t)(size_t)wa_.z, ca1 = (lp_t)(size_t)wa_.w;
    u32 al[8][2], ck[4];
#pragma unroll
    for (int i = 0; i < 8; ++i) { const u32 a = ra[i].x, b = ra[i].y, d = ra[i].z; al[i][0] = __builtin_amdgcn_alignbit(b, a, sh); al[i][1] = __builtin_amdgcn_alignbit(d, b, sh); }
    v4u v[4];
#pragma unroll
    for (int e = 0; e < 4; ++e) {
#pragma unroll
      for (int r = 0; r < 4; ++r) v[e][r] = __builtin_amdgcn_perm(al[4 + r][e >> 1], al[r][e >> 1], (e & 1) ? 0x07060302u : 0x05040100u);
      st_dev(ws, vs + e * 128, v[e]);
      ck[e] = sum4(0u, v[e]);
    }
    const u32 c0 = pair_total(ck[0], ck[1], T & 1), c1 = pair_total(ck[2], ck[3], T & 1);
    const u32 cq = ((T & 2) ? c1 : c0) + dppu<0x4e>((T & 2) ? c0 : c1);
    lds_add(ca0, cq); lds_add(ca1, cq);
    flag_set(fmine, 1);
    STAMP(1);
  }
    u32 tag;
    v4u y0[8];
    WD_SETUP();
    {
      const rsrc_t rwg = mkrs(p.wg, W1SZ), rwu = mkrs(p.wu, W1SZ);
      const int vx = 4 * (lane >> 3) * (F_ * 2) + ft0 * 32 + (lane & 7) * 16, vx8 = vx + 32 * F_ * 2;
      const int vy = six ? 4 * (lane >> 2) * (F_ * 2) + (ft0 + 4) * 32 + (lane & 3) * 16 : OOB;
      const u32 ax = ((lane >> 1) & 3) * SLOT + (16 * hw + (lane >> 3)) * 512 + (lane & 1) * 128 + (lane & 7) * 16;
#define PUT_X(u, off) do { transpose8(u); const u32 a_ = ax + (off); _Pragma("unroll") for (int i = 0; i < 8; ++i) lds_w128((lp_t)(size_t)(a_ ^ (i * 16)), u[i]); SB(); } while (0)
      v4u rb[8], rc[8], rd[8];
      const lp_t fall = lptr(SCR + L_F + (lane & 3) * 256 + lane * 4);
      const int s0 = 64 * hw * (F_ * 2);
#define THR 4
#define NA 2
#define NB 10
#define N0 0
#define NS 8
#define NL 99
#define YD 1
#define THRU 4
#define NH 2
#define TS 0
#define F1 2
#define F2 5
#define STEP(n) do { constexpr int j = (n) & 7; const int so_ = s0 + ((j & 3) + 256 * (j >> 2)) * (F_ * 2); \
        if ((n) == NA) { flag_wait_lazy(fall, 3); cpf = touch(rwd, lane < 11 ? wg * 43 + hw * 11 + lane : 16384); } if ((n) == NB) flag_wait_lazy(fall, 7); \
        if ((n) == NA + NH) flag_wait_lazy(fall, 4); if ((n) == NB + NH) flag_wait_lazy(fall, 8); \
        if ((n) == N0) flag_wait_lazy(fall, 2); if ((n) == NS) flag_wait_lazy(fall, 6); if ((n) == NL) flag_wait_lazy(fall, 10); \
        if ((n) > 1) asm volatile("s_waitcnt vmcnt(%0)" :: "n"((YD && (n) > 8) ? THRU : THR) : "memory"); \
        if ((n) < 8) { ra[j] = ld_in(rwg, vx, so_); rb[j] = ld_in(rwg, vx8, so_); y0[j] = ld_in(rwg, vy, so_); } \
        else { constexpr int j2 = (2 * (n)) & 7; const int sp_ = s0 + ((j2 & 3) + 256 * (j2 >> 2)) * (F_ * 2); \
          if ((n) < 12) { rc[j2] = ld_in(rwu, vx, sp_); rc[j2 + 1] = ld_in(rwu, vx, sp_ + F_ * 2); } else { rd[j2] = ld_in(rwu, vx8, sp_); rd[j2 + 1] = ld_in(rwu, vx8, sp_ + F_ * 2); } } SB(); } while (0)
      PACE(0, TS);
      STAMP(2);
      STEP(0); STEP(1); STEP(2); STEP(3); STEP(4); STEP(5); STEP(6); STEP(7); STEP(8); STEP(9);
      STAMP(3);
      if (NB > 10) STEP(10);
      if (NB > 11) STEP(11);
      PUT_X(ra, 0);
      PUT_X(rb, 4096);
      if (NB <= 10) STEP(10);
      if (NB <= 11) STEP(11);
      STEP(12); STEP(13); STEP(14); STEP(15);
      PUT_X(rc, 256);
      tag = *(LAS u32*)(fall + L_TG);
      STAMP(4);
      PUT_X(rd, 4096 + 256);
      PIN(tag);
      asm volatile("" :: "v"(cpf));
    }
    STAMP(5);
    BAR();
    const rsrc_t ws = mkrs(p.ws, WS_END);
    const int kh = hw & 1, rl = hw >> 1, g = lane >> 4;
    constexpr int PD = 4;
    const u32 ayo = hw * 8192 + g * 2048 + ((lane >> 2) & 3) * 256 + (lane & 4) * 16 + ((lane & 3) ^ ((lane >> 1) & 2)) * 16;
    const lp_t ay0[2] = {lptr(ayo + 4 * SLOT), lptr((ayo ^ 64) + 4 * SLOT)}, ad = lptr(3 * SLOT + hw * 8192 + lane * 16);
#define PUT_Y(a, y, j0, j1) _Pragma("unroll") for (int j = j0; j < j1; ++j) lds_w128(a[j & 1] + (j >> 2) * 1024 + (j & 2) * 64, y[j])
    PUT_Y(ay0, y0, 0, 8);
    v4u ring[8], hfA[8], hfB[8], hfC[8], hfD[8], hfE[8];
    u32 hkA, hkB, hkC, hkD, hkE, ms = 0, nfix = 0;
    lp_t wd[3][4];
    {
      const u32 dl = (g * 1024 + ((lane >> 2) & 3) * 128 + ((lane >> 3) & 3) * 32 + (lane & 3) * 8) ^ (kh * 64);
#pragma unroll
      for (int i = 0; i < 12; ++i) wd[i >> 2][i & 3] = lptr((dl ^ ((i & 3) * 32)) + (i >> 2) * 2 * SLOT + kh * 16384);
    }
    auto frag = [&](auto NN) -> v4u {
      constexpr int N = NN.value, t = N < 64 ? N >> 4 : N < 76 ? 4 : 5, s = N < 64 ? (N & 15) : N < 76 ? N - 64 : N - 76, slot = (t + 1) % 5;
      const lp_t b = wd[slot >> 1][s & 3] + (slot & 1) * SLOT + (s >> 2) * 4096;
      return lds_tr2(b, b + 512);
    };
    const int hb = WS_H + (4 * rg + 2 * (ch >> 3) + rl) * NFT * HREC;
    int hs[4];
#pragma unroll
    for (int i = 0; i < 4; ++i) { hs[i] = hb + tile0_of(8 * kh + 2 * i) * HREC; PINS(hs[i]); }
    int vl = (g & 1) * 512 + (lane & 15) * 16 + (g >> 1) * 6 * HREC, vl3 = vl - (g >> 1) * 2 * HREC, vl4 = vl + 4 * HREC, vlz = (kh && g >= 2) ? OOB : vl4, vq, vq4;
    {
      const int st = (lane & 2) + ((lane >> 1) & 4) + (g >> 1);
      vq = hb + 1024 + ((2 * (g & 1) + (lane & 1)) * 8 + (lane & 4)) * 4 + tile0_of(8 * kh + st) * HREC;
      vq4 = st < 6 - kh ? vq + 4 * HREC : OOB;
    }
    const int u = 16 * rg + 8 * (ch >> 3) + 4 * rl + ((lane >> 3) & 1), col = 64 * (ch & 7) + 32 * kh;
    int vh = u * 8192 + (col & 255) * 32 + g * 128 + (lane & 7) * 16 + (col >> 8) * 8, vs = u * 512 + (lane & 7) * 16 + 4;
    int vout = ((32 * (8 * (u >> 5) + (lane & 7)) + (u & 31)) * D_ + col + 16 * (g & 1) + 8 * (g >> 1)) * 2;
    lp_t rw = lptr(2 * SLOT + hw * 4096 + lane * 16), rr_ = lptr(2 * SLOT + (hw ^ 1) * 4096 + lane * 16);
    int vyd = ((lane >> 2) ^ (g & 1)) * (F_ * 2) + (ft0 + 4) * 32 + ((lane & 3) ^ ((lane >> 3) & 2)) * 16;
    PIN(vyd);
    u32 t8 = 8u * tag, t6 = ((kh && g >= 2) ? 4u : 6u) * tag;
    PIN(vl); PIN(vl3); PIN(vl4); PIN(vlz); PIN(vq); PIN(vq4); PIN(vh); PIN(vs); PIN(vout); PIN(t8); PIN(t6);
#define H_LD(X, t, j) do { \
      if constexpr ((j) == 8) hk##X = ld_dev1(ws, ((t) < 4 ? vq : vq4) + ((t) & 3) * HREC, 0); \
      else if constexpr ((t) < 4 || (j) < 6) hf##X[(j) & 7] = ld_dev(ws, ((t) < 4 ? ((j) < 6 ? vl : vl3) : (j) < 4 ? vl4 : vlz) + ((t) & 3) * HREC + ((j) & 1) * 256, hs[((j) >> 1) & 3]); } while (0)
#define H_LDALL(X, t) static_for<0, 9>([&](auto J) { THROTTLE(J.value); H_LD(X, t, J.value ? J.value - 1 : 8); })
#define H_SUM(X, nf, j) do { if constexpr ((j) >= 0 && (j) < (nf)) { ms = sum4((j) ? ms : hk##X, hf##X[(j) & 7]); u32 t_ = ms; PIN(t_); ms = t_; } \
      else if constexpr ((j) >= (nf) && (j) < (nf) + 3) { u32 t_ = ms + dppu<(j) == (nf) ? 0xb1 : (j) == (nf) + 1 ? 0x4e : 0x128>(ms); PIN(t_); ms = t_; } } while (0)
#define H_SUMALL(X, nf) static_for<0, (nf) + 3>([&](auto J) { H_SUM(X, nf, J.value); })
#define H_FIX(X, t, nf, want) do { if (__builtin_expect(__any(ms != (want)), 0)) { u32 spin_ = 0; _Pragma("nounroll") do { __builtin_amdgcn_s_sleep(RS3); MEMFENCE(); nfix |= 1u << (4 * (t)); \
        H_LDALL(X, t); H_SUMALL(X, nf); } while (__any(ms != (want)) && ++spin_ < SPIN_CAP); } SB(); } while (0)
    v4u d3[8];
    f32x4 o[4][2];
    v2u hr[4], so[2];
#define GRP() asm volatile("s_waitcnt vmcnt(%0)" :: "n"(TH2) : "memory")
#define RAW4(dst, t, i0) do { _Pragma("unroll") for (int i = i0; i < i0 + 4; ++i) { THROTTLE(i); dst[i] = ld_in(rwd, (i & 1) ? vd1 : vd, sd[i >> 1] + ((t) * 16 + (i & 1) * 8) * (D_ * 2)); } SB(); } while (0)
#define DMA4(t, slot, i0) do { _Pragma("unroll") for (int i = i0; i < i0 + 4; ++i) { THROTTLE(i); DMA_WD1(t, slot, i); } } while (0)
#define H4(X, t, h) do { static_for<5 * (h), 5 + 4 * (h)>([&](auto J) { if (J.value != 4) THROTTLE(J.value - 5 * (h)); H_LD(X, t, J.value ? J.value - 1 : 8); }); SB(); } while (0)
#define PUT_D(d, off, i0, i1) do { _Pragma("unroll") for (int i = i0; i < i1; ++i) lds_w128(ad + (off) + i * 1024, d[i]); SB(); } while (0)
#define RESID(i) do { const u32 v0 = hr[i].x, v1 = hr[i].y, mb = so[(i) & 1].x, sb = so[(i) & 1].y; float q[4] = {blo(v0), bhi(v0), blo(v1), bhi(v1)}; \
      _Pragma("unroll") for (int r = 0; r < 4; ++r) { q[r] = __builtin_fmaf(q[r], asf(sb), asf(mb)); PIN(q[r]); o[(i) >> 1][(i) & 1][r] = q[r]; } PIN(o[(i) >> 1][(i) & 1]); SB(); } while (0)
#define PSLEEP 16
    __builtin_amdgcn_s_sleep(PSLEEP);
#define SPRIO 3
#define SPRIO1 0
#define LOWP() __builtin_amdgcn_s_setprio(SPRIO1)
#define HIGHP() __builtin_amdgcn_s_setprio(SPRIO)
#define SPRIO2 3
    __builtin_amdgcn_s_setprio(SPRIO);
    RAW4(d3, 3, 0); RAW4(d3, 3, 4);
    STAMP(6);
    BAR();
    if (YD && six) {
      const rsrc_t rwu = mkrs(p.wu, W1SZ);
#pragma unroll
      for (int i = 0; i < 8; ++i) {
        asm volatile("s_waitcnt vmcnt(4)\n\ts_mov_b32 m0, %0\n\ts_nop 0\n\tbuffer_load_dwordx4 %1, %2, %3 offen lds"
                     :: "s"(hw * 8192 + i * 1024), "v"(vyd), "s"(rwu), "s"((16 * (4 * hw + (i >> 1)) + 256 * (i & 1)) * (F_ * 2)) : "memory");
      }
    }
#pragma unroll
    for (int i = 0; i < 2; ++i) so[i] = __builtin_amdgcn_raw_buffer_load_b64(ws, vs + i * 1024, WS_S, AUX_SC1);
#pragma unroll
    for (int i = 0; i < 4; ++i) hr[i] = __builtin_amdgcn_raw_buffer_load_b64(ws, vh + (i >> 1) * 512, WS_L + (i & 1) * 16384, AUX_SC1);
    SB();
    DMA4(0, 1, 0); DMA4(0, 1, 4);
    if (YD) VM_WAIT(8);
    STAMP(7);
    BAR();
    PIN(hr[3]);
    DMA4(1, 2, 0); DMA4(1, 2, 4); LOWP(); RESID(0); RESID(1); RESID(2); RESID(3);
    HIGHP();
    H4(A, 0, 0); H4(A, 0, 1);
#define BSL 0
    __builtin_amdgcn_s_sleep(BSL);
    H4(B, 1, 0); H4(B, 1, 1);
    PIN(hfA[7]);
    STAMP(8);
    BAR();
    DMA4(2, 3, 0); DMA4(2, 3, 4);
    LOWP();
    H_SUMALL(A, 8);
    H_FIX(A, 0, 8, t8);
    HIGHP();
    STAMP(9);
    H4(C, 2, 0); H4(C, 2, 1);
    VM_WAIT(9);
    H_SUMALL(B, 8);
    H_FIX(B, 1, 8, t8);
    static_for<0, PD>([&](auto K) { ring[K.value ^ 1] = FRAG(K.value ^ 1); SB(); });
    STAMP(10);
    LOWP();
    STAMP(11);
#define G2_ROUND(X, n0, ns, ...) static_for<0, ns>([&](auto K) { constexpr int s = K.value, n = (n0) + s, ct = s & 3, a = (s >> 2) * 2; \
      mma<n == 2 || n == 3>(o[ct][0], ring[n & 7], hf##X[a]); SB(); \
      if constexpr (n + PD < 88) ring[NXT(n) & 7] = FRAG(n + PD < 88 ? NXT(n) : 0); SB(); \
      mma<n == 2 || n == 3>(o[ct][1], ring[n & 7], hf##X[a + 1]); SB(); \
      __VA_ARGS__; SB(); })
#define PP 0
#define L3 16
#define LATE() do { if constexpr (n >= L3 && n < L3 + 9) H_LD(D, 3, n > L3 ? n - L3 - 1 : 8); \
      if constexpr (n >= L4 && n < L4 + 7) H_LD(E, 4, n > L4 ? n - L4 - 1 : 8); if constexpr (n >= L5 && n < L5 + 7) H_LD(A, 5, n > L5 ? n - L5 - 1 : 8); } while (0)
#define PG5 15
#define PS 1
#define MID() do { if constexpr (n == PG5) { BAR_N(15); __builtin_amdgcn_s_setprio(SPRIO2); } \
      if constexpr (n > PG5 && n <= PG5 + 8 * PS && (n - PG5 - 1) % PS == 0) lds_w128(ad + SLOT + ((n - PG5 - 1) / PS) * 1024, d3[((n - PG5 - 1) / PS) & 7]); \
      if constexpr (n == 43) BAR_N(8); LATE(); } while (0)
    static_assert(PG5 >= 15 && PG5 <= 27 && PG5 + 8 * PS <= 39, "");
    G2_ROUND(A, 0, 16, { MID(); });
    FSTAMP(14);
    G2_ROUND(B, 16, 16, { H_SUM(C, 8, s - 5); MID(); });
    H_FIX(C, 2, 8, t8);
    FSTAMP(15);
    G2_ROUND(C, 32, 16, { H_SUM(D, 8, s - 5); MID(); });
    H_FIX(D, 3, 8, t8);
    FSTAMP(16);
    G2_ROUND(D, 48, 16, { LATE(); H_SUM(E, 6, s - 7); if constexpr (s == 11) BAR_N(15); });
    H_FIX(E, 4, 6, t6);
    FSTAMP(17);
    G2_ROUND(E, 64, 12, { H_SUM(A, 6, s - 3); if constexpr (s == 7) BAR_N(15); });
    H_FIX(A, 5, 6, t6);
    FSTAMP(18);
    G2_ROUND(A, 76, 12, { });
    COUNT(23, nfix);
    STAMP(12);
    MMA_DONE(); SB();
#pragma unroll
    for (int i = 0; i < 4; ++i) lds_wf(rw + i * 1024, o[2 + (i & 1)][i >> 1]);
    BAR();
    const rsrc_t ro = mkrs(p.out, S_ * D_ * 2);
#pragma unroll
    for (int i = 0; i < 2; ++i) {
      const f32x4 v0 = o[0][i] + lds_rf(rr_ + i * 2048), v1 = o[1][i] + lds_rf(rr_ + i * 2048 + 1024);
      const auto x = __builtin_amdgcn_permlane16_swap(pk(v0[0], v0[1]), pk(v1[0], v1[1]), false, false);
      const auto y = __builtin_amdgcn_permlane16_swap(pk(v0[2], v0[3]), pk(v1[2], v1[3]), false, false);
      const v4u q = {x[0], y[0], x[1], y[1]};
      __builtin_amdgcn_raw_buffer_store_b128(q, ro, vout + i * 2048, 0, OAUX);
    }
    STAMP(13);
    STAMP_FLUSH();
  }
}

extern "C" void kernel_launch(void* const* d_in, const int* in_sizes, int n_in,
                              void* d_out, int out_size, void* d_ws, size_t ws_size,
                              hipStream_t stream) {
  Params p;
  p.x = (const u16*)d_in[0]; p.wr = (const u16*)d_in[1]; p.wi = (const u16*)d_in[2];
  p.wg = (const u16*)d_in[4]; p.wu = (const u16*)d_in[5]; p.wd = (const u16*)d_in[6];
  p.out = (u16*)d_out; p.ws = d_ws;
  (void)hipFuncSetAttribute((const void*)gfnet_geglu_kernel, hipFuncAttributeMaxDynamicSharedMemorySize, 5 * SLOT);
  gfnet_geglu_kernel<<<256, 512, 5 * SLOT, stream>>>(p);
}
```

```cpp
#define X0SLEEP 8
#define L4 36
#define L5 50
#define T1 285
#define T2 550
#define T3 760
#define PROBE_M 4
#include <hip/hip_runtime.h>
#include <hip/hip_bf16.h>
#include <type_traits>

typedef unsigned u32;
typedef unsigned short u16;
typedef u32 v4u __attribute__((ext_vector_type(4)));
typedef u32 v2u __attribute__((ext_vector_type(2)));
typedef float f32x2 __attribute__((ext_vector_type(2)));
typedef float f32x4 __attribute__((ext_vector_type(4)));
typedef __bf16 bf16x2 __attribute__((ext_vector_type(2)));
typedef __bf16 bf16x8 __attribute__((ext_vector_type(8)));
typedef short s16x4 __attribute__((ext_vector_type(4)));
#define LAS __attribute__((address_space(3)))
#define DI __device__ __forceinline__
typedef __amdgpu_buffer_rsrc_t rsrc_t;
typedef LAS char* lp_t;

constexpr int S_ = 2048, D_ = 512, F_ = 1376, KF = 257;
constexpr int NFT = 86;
constexpr int WS_E = 0;
constexpr int XREC = 88 * 128;
constexpr int WS_A = 4096;
constexpr int WS_B = WS_A + 256 * XREC;
constexpr int WS_W = WS_B + 256 * XREC;
constexpr int WS_L = WS_W + (2 << 20);
constexpr int WS_T = WS_L + (2 << 20);
constexpr int WS_S = WS_T + (1 << 15);
constexpr int WS_H = WS_S + (1 << 17);
constexpr int HREC = 1152;
constexpr int WS_END = WS_H + 64 * NFT * HREC;
constexpr int WS_ST = WS_END;
constexpr u32 NEG2 = 0x80008000u, NEGH = 0x80000000u, NEGL = 0x8000u;
constexpr u32 SPIN_CAP = 1u << 14;
constexpr int AUX_SC1 = 16;
#define OAUX 2
#define NTAUX 2
constexpr int OOB = 0x40000000;
constexpr int CODE_LINES = 260;
constexpr int SLOT = 32768, SCR = 4 * SLOT;
constexpr int L_F = 0, L_TG = 2048;
constexpr int L_IM = 4096, L_SO = 4096, L_X = 16384, L_I2 = 16384, L_I3 = 16384, L_EX = 24576, L_O3 = 24576;

#define DRAIN() asm volatile("s_waitcnt vmcnt(0)" ::: "memory")
#define MEMFENCE() asm volatile("" ::: "memory")
#define PIN(x) asm volatile("" : "+v"(x))
#define PINS(x) asm volatile("" : "+s"(x))
#define SB() __builtin_amdgcn_sched_barrier(0)
#define BAR() asm volatile("s_waitcnt lgkmcnt(0)\n\ts_barrier" ::: "memory")
#define BAR_N(n) asm volatile("s_waitcnt lgkmcnt(" #n ")\n\ts_barrier" ::: "memory")

DI u32 pk(float a, float b) { f32x2 v = {a, b}; return __builtin_bit_cast(u32, __builtin_convertvector(v, bf16x2)); }
DI v4u pk4(f32x4 a, f32x4 b) { v4u r = {pk(a[0], b[0]), pk(a[1], b[1]), pk(a[2], b[2]), pk(a[3], b[3])}; return r; }
DI float asf(u32 v) { return __builtin_bit_cast(float, v); }
DI u32 asu(float v) { return __builtin_bit_cast(u32, v); }
DI float blo(u32 v) { return asf(v << 16); }
DI float bhi(u32 v) { return asf(v & 0xffff0000u); }
DI float dot2(u32 a, u32 b, float c) { return __builtin_amdgcn_fdot2_f32_bf16(__builtin_bit_cast(bf16x2, a), __builtin_bit_cast(bf16x2, b), c, false); }
DI u32 sum4(u32 m, v4u v) { return (m + v.x + v.y) + v.z + v.w; }
template <int CTRL> DI u32 dppu(u32 v) { return (u32)__builtin_amdgcn_update_dpp(0, (int)v, CTRL, 0xf, 0xf, true); }
template <int CTRL> DI float dppf(float v) { return asf(dppu<CTRL>(asu(v))); }
DI u32 pair_total(u32 s0, u32 s1, bool odd) { return (odd ? s1 : s0) + dppu<0xb1>(odd ? s0 : s1); }
DI u32 add_x16(u32 v) { const auto s = __builtin_amdgcn_permlane16_swap(v, v, false, false); return s[0] + s[1]; }
DI u32 add_x32(u32 v) { const auto s = __builtin_amdgcn_permlane32_swap(v, v, false, false); return s[0] + s[1]; }
template <int N> DI void wave_sums(float (&v)[N]) {
#define WS_STEP(C) _Pragma("unroll") for (int i = 0; i < N; ++i) v[i] += dppf<C>(v[i]);
  WS_STEP(0x111) WS_STEP(0x112) WS_STEP(0x114) WS_STEP(0x118) WS_STEP(0x142) WS_STEP(0x143)
#pragma unroll
  for (int i = 0; i < N; ++i) v[i] = asf(__builtin_amdgcn_readlane(asu(v[i]), 63));
}
__host__ __device__ constexpr int rec_q(int k) { return (k & 7) | ((k >> 1) & 0x18); }
__host__ __device__ constexpr int rec_line(int k) { return 11 * ((k >> 3) & 1) + ((rec_q(k) * 11) >> 5); }
__host__ __device__ constexpr int rec_pos(int k) { return rec_q(k) - 3 * ((rec_q(k) * 11) >> 5); }
__host__ __device__ constexpr int im_unit(int k, int j) { return L_IM + (k >> 6) * 3072 + rec_line(k) * 128 + ((j ^ (rec_line(k) + 2 * (k >> 6))) & 7) * 16; }
__host__ __device__ constexpr int im_chk(int k) { return im_unit(k, 6) + 4 * rec_pos(k); }
__host__ __device__ constexpr int im_chkw(int k) { return im_unit(k, 6 + ((rec_pos(k) + 3) >> 2)) + 4 * ((rec_pos(k) + 3) & 3); }
DI void lds_add(lp_t a, u32 v) { __hip_atomic_fetch_add((LAS u32*)a, v, __ATOMIC_RELAXED, __HIP_MEMORY_SCOPE_WORKGROUP); }
DI f32x4 mm16(v4u a, v4u b, f32x4 c) { return __builtin_amdgcn_mfma_f32_16x16x32_bf16(__builtin_bit_cast(bf16x8, a), __builtin_bit_cast(bf16x8, b), c, 0, 0, 0); }
DI f32x4 mm16p(v4u a, v4u b, f32x4 c) { f32x4 d = mm16(a, b, c); PIN(d); return d; }
template <bool FIRST> DI void mma(f32x4& d, v4u a, v4u b) {
  if constexpr (FIRST) asm volatile("v_mfma_f32_16x16x32_bf16 %0, %1, %2, 0" : "=&v"(d) : "v"(a), "v"(b));
  else asm volatile("v_mfma_f32_16x16x32_bf16 %0, %1, %2, %0" : "+v"(d) : "v"(a), "v"(b));
}
#define MMA_DONE() asm volatile("s_nop 7")
DI v2u lds_tr(lp_t a) { return __builtin_bit_cast(v2u, __builtin_amdgcn_ds_read_tr16_b64_v4i16((LAS s16x4*)a)); }
DI v4u lds_tr2(lp_t a, lp_t b) { v2u x = lds_tr(a), y = lds_tr(b); v4u r = {x.x, x.y, y.x, y.y}; return r; }
DI v4u lds_r128(lp_t a) { return *(LAS v4u*)a; }
DI f32x4 lds_rf(lp_t a) { return *(LAS f32x4*)a; }
DI v2u lds_r64(lp_t a) { return *(LAS v2u*)a; }
DI void lds_w128(lp_t a, v4u v) { *(LAS v4u*)a = v; }
DI void lds_wf(lp_t a, f32x4 v) { *(LAS f32x4*)a = v; }
DI void lds_w64(lp_t a, v2u v) { *(LAS v2u*)a = v; }
DI lp_t lptr(u32 off) { lp_t q = (lp_t)(size_t)off; PIN(q); return q; }
DI rsrc_t mkrs(const void* p, u32 bytes) { return __builtin_amdgcn_make_buffer_rsrc((void*)p, 0, bytes, 0x00020000); }
DI v4u ld_dev(rsrc_t r, int v, int s) { return __builtin_amdgcn_raw_buffer_load_b128(r, v, s, AUX_SC1); }
DI u32 ld_dev1(rsrc_t r, int v, int s) { return __builtin_amdgcn_raw_buffer_load_b32(r, v, s, AUX_SC1); }
DI void st_dev(rsrc_t r, int v, v4u x) { __builtin_amdgcn_raw_buffer_store_b128(x, r, v, 0, AUX_SC1); }
DI void st_dev1(rsrc_t r, int v, u32 x) { __builtin_amdgcn_raw_buffer_store_b32(x, r, v, 0, AUX_SC1); }
DI v4u ld_in(rsrc_t r, int v, int s) { return __builtin_amdgcn_raw_buffer_load_b128(r, v, s, 0); }
DI v4u ld_once(rsrc_t r, int v, int s) { return __builtin_amdgcn_raw_buffer_load_b128(r, v, s, NTAUX); }
DI u32 touch(rsrc_t r, int line) { return __builtin_amdgcn_raw_buffer_load_b32(r, line * 128, 0, 0); }
DI void flag_set(lp_t mine, u32 k) { MEMFENCE(); *(volatile LAS u32*)mine = k; MEMFENCE(); }
#define FLAG_WAIT(theirs, k, ...) do { u32 f_; _Pragma("nounroll") do { f_ = *(volatile LAS u32*)(theirs); MEMFENCE(); __VA_ARGS__; MEMFENCE(); } while (!__all(f_ >= (k))); } while (0)
DI void flag_wait_lazy(lp_t theirs, u32 k) { while (!__all(*(volatile LAS u32*)theirs >= k)) __builtin_amdgcn_s_sleep(1); MEMFENCE(); }
DI float cosr(float rev) { return __builtin_amdgcn_cosf(rev); }
DI float sinr(float rev) { return __builtin_amdgcn_sinf(rev); }
__host__ __device__ constexpr int slot_of(int k1, int k2) { return ((k2 < 8 ? k1 + 32 * k2 : (k1 ? 32 - k1 : 16) + 32 * (15 - k2)) - 1) & 255; }
DI int tile0_of(int c) { const int h = c >> 3, i = c & 7; return 44 * h + 4 * i + 2 * (i < 6 - h ? i : 6 - h); }
template <int I, int N, class F> DI void static_for(F&& f) {
  if constexpr (I < N) { f(std::integral_constant<int, I>{}); static_for<I + 1, N>(f); }
}
DI void transpose8(v4u (&m)[8]) {
  v4u o[8];
#pragma unroll
  for (int i = 0; i < 8; ++i)
#pragma unroll
    for (int j = 0; j < 4; ++j) { const u32 a = m[2 * j][i >> 1], b = m[2 * j + 1][i >> 1]; o[i][j] = __builtin_amdgcn_perm(b, a, (i & 1) ? 0x07060302u : 0x05040100u); }
#pragma unroll
  for (int i = 0; i < 8; ++i) { m[i] = o[i]; PIN(m[i]); }
}
enum { E_SQ, E_POLY, E_ARG, E_EXP, E_INC, E_RCP, E_GU, E_OUT, E_PACK, E_SWA, E_SWB, E_SUM, E_SUM2, E_ST, E_X1, E_X2, E_X8, E_CHK, E_STK };
struct EOp { int kind, v; };
struct EProg { EOp op[520]; int n, first[7]; };
constexpr EProg make_eprog() {
  EProg p = {};
  for (int t = 0; t < 6; ++t) {
    p.first[t] = p.n;
    for (int v = 0; v <= 8; ++v) {
      const int V = 8 * t + v;
      const bool head = v < 8, tail = v > 0, fin = t > 0 && t < 5;
      if (head) p.op[p.n++] = {E_SQ, V};
      if (tail) p.op[p.n++] = {E_INC, V - 1};
      if (head) p.op[p.n++] = {E_POLY, V}; else p.op[p.n++] = {E_GU, V - 1};
      if (tail) p.op[p.n++] = {E_RCP, V - 1}; else if (fin) p.op[p.n++] = {E_X1, V - 1};
      if (head) p.op[p.n++] = {E_ARG, V};
      if (tail && head) p.op[p.n++] = {E_GU, V - 1};
      if (v >= 3 && (v & 1)) p.op[p.n++] = {E_PACK, V - 2};
      if (v == 8) p.op[p.n++] = {E_SWA, V - 1};
      if (fin && v == 0) p.op[p.n++] = {E_X2, V - 1};
      if (fin && v == 1) p.op[p.n++] = {E_CHK, V - 2};
      if (head) p.op[p.n++] = {E_EXP, V};
      if (fin && v == 0) p.op[p.n++] = {E_X8, V - 1};
      if (fin && v == 1) p.op[p.n++] = {E_STK, V - 2};
      if (tail) p.op[p.n++] = {E_OUT, V - 1};
    }
    p.op[p.n++] = {E_PACK, 8 * t + 7}; p.op[p.n++] = {E_SUM, 8 * t + 7}; p.op[p.n++] = {E_SWB, 8 * t + 7}; p.op[p.n++] = {E_ST, 8 * t + 7}; p.op[p.n++] = {E_SUM2, 8 * t + 7};
    if (t >= 4) for (int k = E_X1; k <= E_STK; ++k) p.op[p.n++] = {k, 8 * t + 7};
  }
  p.first[6] = p.n;
  return p;
}
constexpr EProg kEp = make_eprog();
#define ER1 56
constexpr int epos(int n) {
  const int mid = (64 * ER1) / 16, end = kEp.first[5];
  int j = n <= 64 ? 0 : n <= 128 ? ((n - 64) * ER1) / 16 : n <= 192 ? mid + ((end - mid) * (n - 128)) / 64
        : end + ((kEp.n - end) * (n - 192)) / 8;
  for (int t = 2; t < 5; ++t) if (n <= 32 * t + 34 && j > kEp.first[t]) j = kEp.first[t];
  return j;
}
struct Epi { float t[2], q, h[3]; v4u r, k4; u32 sum, fin; };
template <int KIND, int V> DI void epi_op(Epi& e, const f32x4 (&a)[4], u32 tag) {
  constexpr int v = V & 7;
  const float gv = a[v >> 2][v & 3], uv = a[2 + (v >> 2)][v & 3];
  float& t = e.t[V & 1];
  if constexpr (KIND == E_SQ) { t = gv * gv; PIN(t); }
  else if constexpr (KIND == E_POLY) { t = __builtin_fmaf(t, -0.10294324f, -2.3022082f); PIN(t); }
  else if constexpr (KIND == E_ARG) { t = t * gv; PIN(t); }
  else if constexpr (KIND == E_EXP) { t = __builtin_amdgcn_exp2f(t); PIN(t); }
  else if constexpr (KIND == E_INC) { t = t + 1.0f; PIN(t); }
  else if constexpr (KIND == E_RCP) { t = __builtin_amdgcn_rcpf(t); PIN(t); }
  else if constexpr (KIND == E_GU) { e.q = gv * uv; PIN(e.q); }
  else if constexpr (KIND == E_OUT) { e.h[v & 1] = e.q * t; PIN(e.h[v & 1]); }
  else if constexpr (KIND == E_PACK) { u32 r = pk(e.h[0], e.h[1]); PIN(r); e.r[v >> 1] = r; }
  else if constexpr (KIND == E_SWA || KIND == E_SWB) { constexpr int i = KIND == E_SWB; const u32 x = e.r[i], y = e.r[i + 2];
    const auto q = __builtin_amdgcn_permlane16_swap(x, y, false, false); u32 q0 = q[0], q1 = q[1]; PIN(q0); PIN(q1); e.r[i] = q0; e.r[i + 2] = q1; }
  else if constexpr (KIND == E_SUM) { e.sum = e.r.x + e.r.z; PIN(e.sum); }
  else if constexpr (KIND == E_SUM2) { e.fin = e.sum + e.r.y + e.r.w; PIN(e.fin); }
  else if constexpr (KIND == E_X1) { e.fin += dppu<0xb1>(e.fin); PIN(e.fin); }
  else if constexpr (KIND == E_X2) { e.fin += dppu<0x4e>(e.fin); PIN(e.fin); }
  else if constexpr (KIND == E_X8) { e.fin += dppu<0x128>(e.fin); PIN(e.fin); }
  else if constexpr (KIND == E_CHK) { asm volatile("" : "=v"(e.k4)); e.k4.x = tag - e.fin; PIN(e.k4); }
}

constexpr double ct_sin(long n, long d) {
  n %= d; if (n < 0) n += d;
  if (2 * n > d) return -ct_sin(d - n, d);
  if (4 * n > d) return ct_sin(d - 2 * n, 2 * d);
  const double x = 6.283185307179586476925 * (double)n / (double)d;
  double t = x, r = x;
  for (int i = 1; i < 14; ++i) { t *= -x * x / ((2 * i) * (2 * i + 1)); r += t; }
  return r;
}
constexpr double ct_cos(long n, long d) { return ct_sin(4 * n + d, 4 * d); }
constexpr u32 ct_f32(double v) { return __builtin_bit_cast(u32, (float)v); }
constexpr u32 ct_bf(double v) { const u32 u = ct_f32(v); return (u + 0x7fffu + ((u >> 16) & 1u)) >> 16; }
constexpr u32 ct_pk(double a, double b) { return ct_bf(a) | (ct_bf(b) << 16); }
struct Tab { u32 v[6][64][4]; };
constexpr Tab make_tab() {
  Tab t = {};
  for (int l = 0; l < 64; ++l) {
    const int c = l & 15, g = l >> 4;
    for (int j = 0; j < 2; ++j) {
      const int a = 4 * g + 2 * j;
      t.v[0][l][j] = ct_pk(ct_cos(a * c, 32), ct_cos((a + 1) * c, 32));
      t.v[0][l][2 + j] = c ? ct_pk(-ct_sin(a * c, 32), -ct_sin((a + 1) * c, 32)) : ct_pk(1, -1);
    }
    for (int r = 0; r < 4; ++r) {
      const int b = 4 * g + r, sl = slot_of(b, c);
      t.v[1][l][r] = ct_f32(ct_cos(b * c, 512));
      t.v[2][l][r] = ct_f32(ct_sin(b * c, 512));
      t.v[3][l][r] = ct_pk(ct_cos(b * c, 16), ct_sin(b * c, 16));
      t.v[4][l][r] = c == 0 ? ct_pk((r & 1) ? -1 : 1, 0) : c < 8 ? ct_pk(ct_cos(b * c, 16), -ct_sin(b * c, 16)) : ct_pk(ct_cos(b * (31 - 2 * c), 32), -ct_sin(b * (31 - 2 * c), 32));
      t.v[5][l][r] = im_unit(sl, 2 * rec_pos(sl)) | (im_chk(sl) << 16);
    }
  }
  return t;
}
static_assert(true, "");
static __device__ const __attribute__((aligned(128))) Tab g_tab = make_tab();

#define STAMP(i) do { if ((i) == PROBE_M && wv < 4) { pm_ = (u32)__builtin_amdgcn_s_memrealtime(); } } while (0)
#define STAMP_DECL u32 pm_ = 0
#define STAMP_FLUSH() do { if (wv < 4) { u32 d_ = 2000u + (pm_ - tb_) * 8u; if (d_ > 20000u) d_ = 20000u; while ((u32)__builtin_amdgcn_s_memrealtime() - tb_ < d_) __builtin_amdgcn_s_sleep(1); } } while (0)
#define COUNT(i, v) do { } while (0)

#define FSTAMP(i) do { } while (0)
#define PACE(d, t) do { __builtin_amdgcn_s_sleep(d); while ((u32)__builtin_amdgcn_s_memrealtime() - t_ref < (t)) __builtin_amdgcn_s_sleep(1); } while (0)
#define RS1 0
#define RS2 10
#define RS3 0
#define D1 1
#define D2 4
#define D3 6
#define TW 240
#define SLIPPED() (*(LAS u32*)(fmine + L_TG + 1024) = (u32)__builtin_amdgcn_s_memrealtime() - t_ref)
#define PACE2(d, t, sl) do { u32 r_ = *(LAS u32*)(fall + L_TG + 1024); r_ = max(r_, dppu<0xb1>(r_)); r_ = max(r_, dppu<0x4e>(r_)); \
      r_ = __builtin_amdgcn_readfirstlane(r_) + (sl); t_ref += r_ > (t) ? r_ - (t) : 0u; PACE(d, t); } while (0)
#define SL2 175
#define SL3 150
#define WAIT1() PACE(D1, T1)
#define WAIT2() PACE2(D2, T2, SL2)
#define WAIT3() PACE2(D3, T3, SL3)
struct Params {
  const u16* x; const u16* wr; const u16* wi; const u16* wg; const u16* wu; const u16* wd; u16* out; void* ws;
};

__global__ void __launch_bounds__(512, 2) gfnet_geglu_kernel(Params p) {
  const int wg = blockIdx.x, tid = threadIdx.x, lane = tid & 63;
  int wv = __builtin_amdgcn_readfirstlane(tid >> 6);
  STAMP_DECL;
  unsigned long long t_entry;
  asm volatile("s_memrealtime %0" : "=s"(t_entry));
  const int rg = 4 * ((wg & 7) >> 1) + (wg >> 6), ch = 8 * (wg & 1) + ((wg >> 3) & 7);
  const int un = 16 * rg + ch;
  const int ft0 = tile0_of(ch);
  const bool six = (ch & 7) < 6 - (ch >> 3);
#define W1SZ (D_ * F_ * 2)
#define WD_SETUP() const rsrc_t rwd = mkrs(p.wd, D_ * F_ * 2); \
    int vd = (lane >> 3) * (D_ * 2) + (ch & 7) * 128 + ((lane & 7) ^ ((lane >> 3) & 2)) * 16, vd1 = vd ^ 64, sd[4]; PIN(vd); PIN(vd1); \
    _Pragma("unroll") for (int i = 0; i < 4; ++i) { sd[i] = tile0_of(4 * (wv & 3) + i) * (16 * D_ * 2); PINS(sd[i]); }
#define DMA_WD1(t, slot, i) asm volatile("s_mov_b32 m0, %0\n\ts_nop 0\n\tbuffer_load_dwordx4 %1, %2, %3 offen lds" \
      :: "s"((slot) * SLOT + (wv & 3) * 8192 + (i) * 1024), "v"(((i) & 1) ? vd1 : vd), "s"(rwd), "s"(sd[(i) >> 1] + ((t) * 16 + ((i) & 1) * 8) * (D_ * 2)) : "memory")
#define TH2 6
#define TG 4
#define THROTTLE(i) do { if ((i) % TG == 0) asm volatile("s_waitcnt vmcnt(%0)" :: "n"(TH2) : "memory"); } while (0)
#define DMA_WD(t, slot) do { _Pragma("unroll") for (int i = 0; i < 8; ++i) { THROTTLE(i); DMA_WD1(t, slot, i); } } while (0)
#define DMA_WDS(t, slot) do { _Pragma("unroll") for (int i = 0; i < 4; ++i) DMA_WD1(t, slot, i); if (!(wv & 1)) { _Pragma("unroll") for (int i = 4; i < 8; ++i) DMA_WD1(t, slot, i); } } while (0)
#define VM_WAIT(n) asm volatile("s_waitcnt vmcnt(" #n ")" ::: "memory")
#define PDN 4
  constexpr int PD = PDN;
#define FRAG(N) frag(std::integral_constant<int, (N)>{})
#define NXT(n) (((n) + PD) ^ 1)
  v4u ra[8];
  u32 cpf;
#define UNDEF_RA() do { asm volatile("" : "=v"(cpf)); _Pragma("unroll") for (int i = 0; i < 8; ++i) asm volatile("" : "=v"(ra[i])); } while (0)
  UNDEF_RA();
  if (wv < 4) {
    const int vt = lane * 16;
    int sx = (32 * (8 * (un >> 5) + 2 * wv) + (un & 31)) * (D_ * 2), sy = sx + 32 * D_ * 2;
    const rsrc_t rt = mkrs(&g_tab, sizeof(Tab));
    ra[2] = ld_in(rt, vt, 0); ra[3] = ld_in(rt, vt + 1024, 0);
    PINS(sx); PINS(sy);
    SB();
    const rsrc_t rx = mkrs(p.x, S_ * D_ * 2);
    ra[0] = ld_once(rx, vt, sx); ra[1] = ld_once(rx, vt, sy);
    SB();
#pragma unroll
    for (int i = 0; i < 4; ++i) ra[4 + i] = ld_in(rt, vt + i * 1024, 2048);
    cpf = ld_dev1(mkrs(p.ws, WS_END), 0, WS_E);
    STAMP(0);
  }
  PINS(wv);
  if (wv >= 6) {
    unsigned long long pc0;
    asm volatile("s_getpc_b64 %0" : "=s"(pc0));
    cpf = touch(mkrs((const void*)(pc0 & ~127ull), CODE_LINES * 128), lane < 9 ? (((wg >> 3) ^ (wv & 1)) * 9 + lane) : 4096);
    unsigned long long te;
    asm volatile("s_waitcnt lgkmcnt(0)\n\ts_mov_b64 %0, %1" : "=s"(te) : "s"(t_entry));
    st_dev(mkrs(p.ws, WS_END), (tid >= 504 && wg < 8) ? WS_T + wg * 128 + (lane & 7) * 16 : OOB, v4u{(u32)te, (u32)te, (u32)te, (u32)te});
  }
  PINS(wv);
  if ((wv >> 1) == 2) {
#define WAUX 0
#define WSLEEP 2
    __builtin_amdgcn_s_sleep(WSLEEP);
    const rsrc_t rwr = mkrs(p.wr, S_ * KF * 2), rwi = mkrs(p.wi, S_ * KF * 2);
    const int vf = (lane & 7) * (KF * 2) + (lane & 1) * 2 + ((tid & 127) >> 3) * 8;
    const int sf = (16 * (un >> 6) + 2 * (un & 4) + 256 * ((un >> 3) & 7)) * (KF * 2) + (un & 3) * 128;
#pragma unroll
    for (int r = 0; r < 4; ++r) { ra[r] = __builtin_amdgcn_raw_buffer_load_b128(rwr, vf, sf + r * (64 * KF * 2), WAUX); ra[4 + r] = __builtin_amdgcn_raw_buffer_load_b128(rwi, vf, sf + r * (64 * KF * 2), WAUX); }
  }
  const lp_t fmine = lptr(SCR + L_F + tid * 4);
  *(volatile LAS u32*)fmine = 0;
  lds_w128(lptr(SCR + L_IM + (tid & 127) * 128 + (((6 + ((tid >> 7) & 1)) ^ (tid + 2 * (((tid & 127) * 43) >> 10))) & 7) * 16), v4u{0u, 0u, 0u, 0u});
  BAR();
  PINS(t_entry);
  u32 t_ref = (u32)t_entry, tb_ = 0;
  v4u wa_;
  asm volatile("" : "=v"(wa_));
  PINS(wv);
  if ((wv >> 1) == 2) {
    const int T = tid - 256, k = 64 * (un & 3) + 4 * (T >> 3) + (T & 3);
    wa_ = v4u{(u32)((T >> 3) * 512 + (T & 7) * 16 + WS_W + un * 8192), (lane & 1) ? 0u : 16u, (u32)(SCR + im_chkw(k)), (u32)(SCR + im_chk(k))};
    PIN(wa_);
  }
  PINS(wv);
  if (wv >= 6) {
    __builtin_amdgcn_s_sleep(X0SLEEP);
    const rsrc_t r1 = mkrs((wv & 1) ? p.wu : p.wg, W1SZ);
    ra[0].x = touch(r1, lane < 43 ? wg * 43 + lane : 16384);
  }
  SB();
  PINS(wv);

  if (wv < 4) {
  const int w = wv, c = lane & 15, g = lane >> 4;
  const float fc = (float)c, fg4 = (float)(4 * g);
  const f32x4 z = {0.f, 0.f, 0.f, 0.f};
  f32x4 twc, tws;
  v4u fbre, fbsw;
  u32 tag;
  float mean[2];
  const rsrc_t ws = mkrs(p.ws, WS_END);
  const lp_t fall = lptr(SCR + L_F + (lane & 3) * 256 + lane * 4);
  const lp_t imr = lptr(SCR + L_IM + w * 3072 + (lane >> 3) * 128 + ((lane ^ ((lane >> 3) + 2 * w)) & 7) * 16);
  int vx0 = lane * 16 + WS_A + un * XREC + w * (22 * 128), vx2 = lane < 48 ? vx0 + 2048 : OOB;
  PIN(vx0); PIN(vx2);
  int vq8 = (lane & 7) * 128;
  PIN(vq8);
  u32 tq;
  v2u xk[2][2];
  f32x4 tfc[2], tfs[2], tic[2], tis[2];
  const float fj = (float)(16 * w) + fc;
#pragma unroll
  for (int r = 0; r < 4; ++r) {
#pragma unroll
      for (int m = 0; m < 2; ++m) {
        const float nf = (fg4 + (16 * m + r)) * (fj * (1.f / 2048)), ni = (fg4 + (float)(16 * w) + r) * ((fc + 16 * m) * (1.f / 2048));
        tfc[m][r] = cosr(nf); tfs[m][r] = sinr(nf); tic[m][r] = cosr(ni); tis[m][r] = sinr(ni);
      }
  }
#pragma unroll
  for (int i = 0; i < 2; ++i) { PIN(tfc[i]); PIN(tfs[i]); PIN(tic[i]); PIN(tis[i]); }
  SB();
  {
    const v4u xr[2] = {ra[0], ra[1]}, tb[6] = {ra[2], ra[3], ra[4], ra[5], ra[6], ra[7]};
    const u32 sc = (c & 1) ? NEG2 : 0u, sm = c < 8 ? NEGL : NEGH, p0 = c < 8 ? 0x0c0c0100u : 0x01000c0cu, p1 = c < 8 ? 0x0c0c0302u : 0x03020c0cu;
    v4u fare = {tb[0].x, tb[0].y, tb[0].x ^ sc, tb[0].y ^ sc}, faim = {tb[0].z, tb[0].w, tb[0].z ^ sc, tb[0].w ^ sc}, fbim, f0re, f0im;
    twc = __builtin_bit_cast(f32x4, tb[1]); tws = __builtin_bit_cast(f32x4, tb[2]); fbre = tb[3];
    lp_t t1w[4], t1c[4];
#pragma unroll
    for (int r = 0; r < 4; ++r) {
      const u32 a = tb[3][r], q = tb[4][r];
      fbsw[r] = __builtin_amdgcn_alignbit(a, a, 16);
      fbim[r] = fbsw[r] ^ sm;
      f0re[r] = __builtin_amdgcn_perm(q, q, p0); f0im[r] = __builtin_amdgcn_perm(q, q, p1);
      t1w[r] = lptr((tb[5][r] & 0xffffu) ^ (SCR + w * 8)); t1c[r] = lptr((tb[5][r] >> 16) + SCR);
    }
    float c0m = c == 0 ? 1.f / 16 : 0.f;
    lp_t xw = lptr(SCR + L_X + w * 2048 + lane * 16), xt = lptr(SCR + L_X + w * 2048 + lane * 8);
    lp_t xq = lptr(SCR + L_X + w * 2048 + (4 * c + g) * 8), f6 = lptr(SCR + L_F + (lane % 6) * 256 + lane * 4);
    tag = cpf + 1u;
    u32 tag0 = w ? 0u : tag;
    PIN(c0m); PIN(tag); PIN(tag0);
    *(LAS u32*)(fmine + L_TG) = tag;
    *(LAS u32*)(fmine + L_TG + 1024) = 0;
    PIN(fare); PIN(faim); PIN(fbre); PIN(fbim); PIN(f0re); PIN(f0im); PIN(twc); PIN(tws);
    STAMP(1);
    SB();
    v4u a[2];
#pragma unroll
    for (int rr = 0; rr < 2; ++rr) { lds_w128(xw + rr * 1024, xr[rr]); a[rr] = lds_tr2(xt + rr * 1024, xt + rr * 1024 + 512); }
    tq = ld_dev1(ws, vq8, WS_T);
    STAMP(2);
    float st[4] = {0.f, 0.f, 0.f, 0.f};
#pragma unroll
    for (int i = 0; i < 4; ++i)
#pragma unroll
      for (int rr = 0; rr < 2; ++rr) { const u32 v = xr[rr][i]; st[rr] = dot2(v, 0x3f803f80u, st[rr]); st[2 + rr] = dot2(v, v, st[2 + rr]); }
    SB();
    f32x4 ur[2], ui[2];
#pragma unroll
    for (int rr = 0; rr < 2; ++rr) { ur[rr] = mm16p(a[rr], fare, z); ui[rr] = mm16p(a[rr], faim, z); }
    wave_sums<4>(st);
    SB();
    v4u a2[2];
    f32x4 orr[2], oi[2], sr[2], si[2];
#pragma unroll
    for (int rr = 0; rr < 2; ++rr) {
      const f32x4 x = ur[rr] - st[rr] * c0m, y = ui[rr];
      a2[rr] = pk4(x * twc + y * tws, y * twc - x * tws);
      orr[rr] = mm16p(a2[rr], fbre, z); oi[rr] = mm16p(a2[rr], fbim, z); sr[rr] = mm16p(a2[rr], f0re, z); si[rr] = mm16p(a2[rr], f0im, z);
    }
    v4u o[2];
#pragma unroll
    for (int rr = 0; rr < 2; ++rr) {
      mean[rr] = st[rr] * (1.f / 512);
      const float rstd = __builtin_amdgcn_rsqf(fmaxf(st[2 + rr] * (1.f / 512) - mean[rr] * mean[rr], 0.f) + 1e-5f);
      orr[rr][0] = g == 0 ? sr[rr][0] : orr[rr][0]; oi[rr][0] = g == 0 ? si[rr][0] : oi[rr][0];
      o[rr] = pk4(orr[rr] * rstd, oi[rr] * rstd);
    }
#pragma unroll
    for (int r = 0; r < 4; ++r) { v2u v = {o[0][r], o[1][r]}; lds_w64(t1w[r], v); lds_add(t1c[r], v.x + v.y + tag0); }
    STAMP(3);
    flag_set(fmine, 1);
    v4u v0, v1, v2;
    FLAG_WAIT(f6, 1, v0 = lds_r128(imr); v1 = lds_r128(imr + 1024); v2 = lds_r128(imr + 2048));
    st_dev(ws, vx0, v0); st_dev(ws, vx0 + 1024, v1); st_dev(ws, vx2, v2);
    flag_set(fmine, 2);
#pragma unroll
    for (int i = 0; i < 4; ++i) xk[i >> 1][i & 1] = lds_r64(xq + (i >> 1) * 1024 + (i & 1) * 512);
  }
  int vge, vgo, vg = (lane >> 3) * (4 * 8192) + (lane & 7) * 16;
  {
    const int pay = (lane >> 1) * XREC + rec_pos(un) * 32 + (lane & 1) * 16, chk = (lane & 1) ? OOB : ((lane >> 1) ^ 1) * XREC + 96 + rec_pos(un) * 4;
    vge = (lane & 2) ? chk : pay; vgo = (lane & 2) ? pay : chk;
  }
  const int sg = w * (64 * XREC) + (22 * (un >> 6) + rec_line(un)) * 128;
#define GATHER(BUF) do { xl[0] = ld_dev(ws, vge, BUF + sg); xl[1] = ld_dev(ws, vgo, BUF + sg); xl[2] = ld_dev(ws, vge, BUF + sg + 32 * XREC); xl[3] = ld_dev(ws, vgo, BUF + sg + 32 * XREC); } while (0)
#define VERIFY(extra) ({ u32 k_ = 0; _Pragma("unroll") for (int m = 0; m < 2; ++m) { _Pragma("unroll") for (int i = 0; i < 4; ++i) xg[m][i] = (lane & 2) ? xl[2 * m + 1][i] : xl[2 * m][i]; \
      k_ += (lane & 2) ? xl[2 * m].x : xl[2 * m + 1].x; lds_w128(iw + m * 1024, xg[m]); } \
      u32 t_ = sum4(sum4((extra) - k_, xg[0]), xg[1]); t_ += dppu<0xb1>(t_); t_ += dppu<0x4e>(t_); t_; })
  const lp_t iw = lptr(SCR + L_I2 + w * 2048 + lane * 16);
  const int sw = WS_W + (w * 64 + (un >> 6)) * 8192 + (un & 63) * 128;
  v4u wq[2];
  STAMP(4);
  SB();
  v4u f2a[2][2];
  {
    v4u f1a[4], f1b[4], f2b[2][2];
    const u32 sc = (c & 1) ? NEG2 : 0u;
#pragma unroll
    for (int r = 0; r < 4; ++r) {
#pragma unroll
      for (int ks = 0; ks < 2; ++ks) {
        const float n = (fg4 + (16 * ks + r)) * (fj * (1.f / 64));
        const float cs = cosr(n), sn = sinr(n);
        f1a[ks][r] = pk(cs, sn); f1b[ks][r] = pk(-sn, cs);
        f1a[ks + 2][r] = f1a[ks][r] ^ sc; f1b[ks + 2][r] = f1b[ks][r] ^ sc;
      }
      const float n = (fg4 + r) * (fc * (1.f / 32));
      const float cs = cosr(n), sn = sinr(n);
      const u32 a = pk(cs, sn), b = pk(-sn, cs), sr = (r & 1) ? NEG2 : 0u;
      f2a[0][0][r] = a; f2a[1][0][r] = a ^ sr; f2a[0][1][r] = a ^ sc; f2a[1][1][r] = a ^ sc ^ sr;
      f2b[0][0][r] = b; f2b[1][0][r] = b ^ sr; f2b[0][1][r] = b ^ sc; f2b[1][1][r] = b ^ sc ^ sr;
    }
#pragma unroll
    for (int i = 0; i < 4; ++i) { PIN(f1a[i]); PIN(f1b[i]); PIN(f2a[i >> 1][i & 1]); PIN(f2b[i >> 1][i & 1]); }
    lp_t i2r = lptr(SCR + L_I2 + ((g >> 1) * 32 + c) * 32 + (g & 1) * 16);
    lp_t exw = lptr(SCR + L_EX + w * 2048 + lane * 16), exr = lptr(SCR + L_EX + lane * 16);
    lp_t imw[2], imc = lptr(SCR + im_chk(64 * w + lane));
#pragma unroll
    for (int n = 0; n < 2; ++n) { const int k = 64 * w + 32 * (g >> 1) + 16 * n + c; imw[n] = lptr(SCR + im_unit(k, 2 * rec_pos(k) + (g & 1))); }
    vx0 += WS_B - WS_A; vx2 += WS_B - WS_A;
    PIN(vg); PIN(vge); PIN(vgo); PIN(vx0); PIN(vx2);
    tq -= t_ref; tq = tq < 150u ? tq : 0u;
    tq = max(tq, dppu<0xb1>(tq)); tq = max(tq, dppu<0x4e>(tq)); tq = max(tq, dppu<0x141>(tq));
    t_ref += __builtin_amdgcn_readfirstlane(tq);
    tb_ = t_ref;
    STAMP(5);
    WAIT1();
    STAMP(6);
    v4u xl[4], xg[2];
    f32x4 wr[2], wi[2];
    GATHER(WS_A);
#define WD_ 8
    SB();
    __builtin_amdgcn_s_sleep(WD_);
    wq[0] = ld_dev(ws, vg, sw); wq[1] = ld_dev(ws, vg, sw + 32 * 8192);
    flag_set(fmine, 3);
    SB();
#define KW() (((lane & 2) ? xl[0].w : xl[1].w) + ((lane & 2) ? xl[2].w : xl[3].w))
    u32 kw = KW(), spin = 0;
    u32 bad = VERIFY(tag + kw);
    if (__any(bad != 0)) {
      _Pragma("nounroll") do {
        MEMFENCE();
        if (bad) GATHER(WS_A);
        kw = KW();
        bad = VERIFY(tag + kw);
      } while (__any(bad != 0) && ++spin < SPIN_CAP);
      SLIPPED();
      ++spin;
    }
    COUNT(20, spin);
    STAMP(7);
    v4u af[2][4], b2[2], a3[2];
    flag_set(fmine, 4);
    FLAG_WAIT(fall, 4, _Pragma("unroll") for (int i = 0; i < 8; ++i) af[i >> 2][i & 3] = lds_r128(i2r + (i & 3) * 2048 + (i >> 2) * 512));
    STAMP(8);
    SB();
    f32x4 dr[2], di[2];
#pragma unroll
    for (int m = 0; m < 2; ++m)
#pragma unroll
      for (int ks = 0; ks < 4; ++ks) { dr[m] = mm16(af[m][ks], f1a[ks], ks ? dr[m] : z); di[m] = mm16(af[m][ks], f1b[ks], ks ? di[m] : z); }
#pragma unroll
    for (int m = 0; m < 2; ++m) b2[m] = pk4(dr[m] * tfc[m] + di[m] * tfs[m], di[m] * tfc[m] - dr[m] * tfs[m]);
#pragma unroll
    for (int k = 0; k < 2; ++k)
#pragma unroll
      for (int m = 0; m < 2; ++m) { dr[m] = mm16(f2a[m][k], b2[k], k ? dr[m] : z); di[m] = mm16(f2b[m][k], b2[k], k ? di[m] : z); }
    {
#define W_BAD() ({ u32 t_ = sum4(sum4(0u - kw, wq[0]), wq[1]); t_ += dppu<0xb1>(t_); t_ += dppu<0x4e>(t_); t_ += dppu<0x141>(t_); t_; })
      u32 bw = W_BAD();
#pragma unroll
      for (int m = 0; m < 2; ++m) { PIN(dr[m]); PIN(di[m]); }
      if (__builtin_expect(__any(bw != 0), 0)) {
        u32 sp = 0;
        _Pragma("nounroll") do { MEMFENCE(); if (bw) { wq[0] = ld_dev(ws, vg, sw); wq[1] = ld_dev(ws, vg, sw + 32 * 8192); } bw = W_BAD(); } while (__any(bw != 0) && ++sp < SPIN_CAP);
      }
#pragma unroll
      for (int m = 0; m < 2; ++m)
#pragma unroll
        for (int r = 0; r < 4; ++r) { const u32 v = wq[m][r]; wr[m][r] = blo(v); wi[m][r] = bhi(v); }
    }
#pragma unroll
    for (int m = 0; m < 2; ++m) a3[m] = pk4(dr[m] * wi[m] + di[m] * wr[m], dr[m] * wr[m] - di[m] * wi[m]);
#pragma unroll
    for (int k = 0; k < 2; ++k)
#pragma unroll
      for (int n = 0; n < 2; ++n) { di[n] = mm16(a3[k], f2a[n][k], k ? di[n] : z); dr[n] = mm16(a3[k], f2b[n][k], k ? dr[n] : z); }
#pragma unroll
    for (int n = 0; n < 2; ++n) lds_w128(exw + n * 1024, pk4(di[n] * tic[n] + dr[n] * tis[n], dr[n] * tic[n] - di[n] * tis[n]));
    STAMP(9);
    flag_set(fmine, 5);
    FLAG_WAIT(fall, 5, _Pragma("unroll") for (int i = 0; i < 8; ++i) af[i >> 2][i & 3] = lds_r128(exr + (i & 3) * 2048 + (i >> 2) * 1024));
    SB();
    u32 cs[2];
#pragma unroll
    for (int n = 0; n < 2; ++n)
#pragma unroll
      for (int ks = 0; ks < 4; ++ks) { di[n] = mm16(f1a[ks], af[n][ks], ks ? di[n] : z); dr[n] = mm16(f1b[ks], af[n][ks], ks ? dr[n] : z); }
#pragma unroll
    for (int n = 0; n < 2; ++n) {
      const v4u e = pk4(dr[n], di[n]);
      lds_w128(imw[n], e);
      cs[n] = sum4(0u, e);
    }
    const auto sx = __builtin_amdgcn_permlane16_swap(cs[0], cs[1], false, false);
    *(LAS u32*)imc = tag + sx[0] + sx[1];
    MEMFENCE();
    const v4u v0 = lds_r128(imr), v1 = lds_r128(imr + 1024), v2 = lds_r128(imr + 2048);
    st_dev(ws, vx0, v0); st_dev(ws, vx0 + 1024, v1); st_dev(ws, vx2, v2);
    flag_set(fmine, 6);
    if (wg == 0 && w == 0) st_dev1(ws, WS_E, tag);
  }
  STAMP(10);
  SB();
  {
    v4u gbre, gbim, g0re, g0im, ga[2];
#pragma unroll
    for (int r = 0; r < 4; ++r) {
      gbre[r] = fbre[r] ^ (g < 2 ? NEGH : 0u);
      gbim[r] = fbsw[r] ^ (g < 2 ? 0u : NEGH);
      const float n = fc * ((31.f - 2.f * r - 2.f * fg4) * (1.f / 32));
      u32 q = pk(cosr(n), -sinr(n));
      PIN(q);
      g0re[r] = g < 2 ? gbre[r] : 0u;
      g0im[r] = g < 2 ? 0u : q;
      ga[0][r] = f2a[0][0][r] ^ NEGH; ga[1][r] = f2a[1][0][r] ^ NEGH;
    }
    twc *= 2.f / (2048.f * 512.f); tws *= 2.f / (2048.f * 512.f);
    g0re[0] = g == 0 ? ((c & 1) ? 0xbf00u : 0x3f00u) : g0re[0];
    ga[0][0] = g == 0 ? ((c & 1) ? 0xbf803f80u : 0x3f803f80u) : ga[0][0]; ga[1][0] = ga[0][0];
    PIN(gbre); PIN(gbim); PIN(g0re); PIN(g0im); PIN(ga[0]); PIN(ga[1]); PIN(twc); PIN(tws);
    f32x4 xin[2][2];
#pragma unroll
    for (int rr = 0; rr < 2; ++rr)
#pragma unroll
      for (int t = 0; t < 2; ++t) {
        const v2u v = xk[rr][t];
        const u32 v0 = v.x, v1 = v.y;
        f32x4 q = {blo(v0), bhi(v0), blo(v1), bhi(v1)};
        xin[rr][t] = q - mean[rr]; PIN(xin[rr][t]);
      }
    lp_t i3r[4], o3w[2], o3r[2];
#pragma unroll
    for (int r = 0; r < 4; ++r) i3r[r] = lptr(SCR + L_I3 + slot_of(c, 4 * g + r) * 32 + w * 8);
#pragma unroll
    for (int i = 0; i < 2; ++i) {
      o3w[i] = lptr(SCR + L_O3 + ((4 * c + g) * 8 + ((2 * w + i) ^ (c & 7))) * 16);
      const int q = w * 128 + i * 64 + lane;
      o3r[i] = lptr(SCR + L_O3 + (q ^ ((q >> 5) & 7)) * 16);
    }
    int vs = lane * 16 + WS_L + un * 8192 + w * 2048, vt = lane < 8 ? lane * 16 + WS_S + un * 512 + w * 128 : OOB;
    PIN(vs); PIN(vt);
    lp_t sow = lptr(SCR + L_SO + tid * 16), sor = lptr(SCR + L_SO + (lane & 6) * 512 + (lane & 1) * 8);
    WAIT2();
    STAMP(11);
    v4u xl[4], xg[2];
    GATHER(WS_B);
    flag_set(fmine, 7);
    SB();
    u32 bad = VERIFY(tag), spin = 0;
    if (__any(bad != 0)) {
      _Pragma("nounroll") do {
        __builtin_amdgcn_s_sleep(RS1);
        MEMFENCE();
        if (bad) GATHER(WS_B);
        bad = VERIFY(tag);
      } while (__any(bad != 0) && ++spin < SPIN_CAP);
      SLIPPED();
      ++spin;
    }
    COUNT(21, spin);
    STAMP(12);
    v4u a[2], a2[2];
    v2u zr[4];
    flag_set(fmine, 8);
    FLAG_WAIT(fall, 8, _Pragma("unroll") for (int r = 0; r < 4; ++r) zr[r] = lds_r64(i3r[r]));
    STAMP(13);
#pragma unroll
    for (int r = 0; r < 4; ++r) { a[0][r] = zr[r].x; a[1][r] = zr[r].y; }
    f32x4 tr[2], ti[2], sr[2], si[2], h[2][2];
#pragma unroll
    for (int rr = 0; rr < 2; ++rr) { tr[rr] = mm16p(a[rr], gbre, z); ti[rr] = mm16p(a[rr], gbim, z); sr[rr] = mm16p(a[rr], g0re, z); si[rr] = mm16p(a[rr], g0im, z); }
#pragma unroll
    for (int rr = 0; rr < 2; ++rr) {
      tr[rr][0] = g == 0 ? sr[rr][0] : tr[rr][0]; ti[rr][0] = g == 0 ? si[rr][0] : ti[rr][0];
      a2[rr] = pk4(tr[rr] * twc - ti[rr] * tws, ti[rr] * twc + tr[rr] * tws);
      h[rr][0] = mm16p(a2[rr], ga[0], xin[rr][0]); h[rr][1] = mm16p(a2[rr], ga[1], xin[rr][1]);
    }
    float st[2];
#pragma unroll
    for (int rr = 0; rr < 2; ++rr) { const f32x4 q = h[rr][0] * h[rr][0] + h[rr][1] * h[rr][1]; st[rr] = (q[0] + q[2]) + (q[1] + q[3]); }
    wave_sums<2>(st);
    v4u so;
#pragma unroll
    for (int rr = 0; rr < 2; ++rr) {
      const float ve = st[rr] * (1.f / 512) + 1e-5f, rstd = __builtin_amdgcn_rsqf(ve);
      const f32x4 l0 = h[rr][0] * rstd, l1 = h[rr][1] * rstd;
      v4u l4 = {pk(l0[0], l0[1]), pk(l0[2], l0[3]), pk(l1[0], l1[1]), pk(l1[2], l1[3])};
      lds_w128(o3w[rr], l4);
      so[2 * rr] = asu(mean[rr]); so[2 * rr + 1] = asu(ve * rstd);
    }
    lds_w128(sow, so);
    STAMP(14);
    v4u v0, v1;
    v2u ms;
    flag_set(fmine, 9);
    FLAG_WAIT(fall, 9, v0 = lds_r128(o3r[0]); v1 = lds_r128(o3r[1]); ms = lds_r64(sor));
    st_dev(ws, vs, v0); st_dev(ws, vs + 1024, v1);
    u32 s = sum4(sum4(0u, v0), v1);
    s += dppu<0x128>(s); s = add_x32(add_x16(s));
    v4u rec = {tag - s - ms.x - ms.y, ms.x, ms.y, tag};
    st_dev(ws, vt, rec);
    flag_set(fmine, 10);
  }
  SB();
  {
#define LOOKN 6
    constexpr int LOOK = LOOKN;
    v4u ring[8], act[32];
    lp_t wa[4], wb[4];
#pragma unroll
    for (int t = 0; t < 4; ++t) wa[t] = lptr(t * SLOT + g * 512 + ((c ^ (2 * t + (c >> 3))) * 16));
#pragma unroll
    for (int i = 0; i < 4; ++i) wb[i] = lptr(((i & 1) ? 0 : 4 * SLOT) + ((lane >> 2) ^ (g & 1)) * 64 + (((i >> 1) ^ g) & 1) * 32 + (lane & 3) * 8);
    auto frag = [&](auto NN) -> v4u {
      constexpr int N = NN.value;
      if constexpr (N < 64) return lds_r128(wa[(N >> 1) & 1] + (N >> 2) * 2048 + (N & 1) * 256);
      else if constexpr (N < 128) return lds_r128(wa[N >> 5] + ((N & 31) >> 1) * 2048 + (N & 1) * 256);
      else { const lp_t b = wb[((N >> 4) & 2) + (N & 1)] + ((N & 31) >> 1) * 2048; return lds_tr2(b, b + 1024); }
    };
    int vo = WS_H + ((4 * rg + w) * NFT + ft0) * HREC + lane * 16, vk = (lane & 11) ? OOB : vo - lane * 16 + 1024 + ((lane >> 4) * 8 + (lane & 7)) * 4;
    int vo4 = vo + 4 * HREC, vk4 = vk + 4 * HREC;
    PIN(vo); PIN(vk); PIN(vo4); PIN(vk4);
    Epi e;
#define EPI(J0, J1) static_for<(J0), (J1)>([&](auto JJ) { constexpr int kd = kEp.op[JJ.value].kind, V = kEp.op[JJ.value].v, t = V >> 3; \
      if constexpr (kd == E_ST) st_dev(ws, (t < 4 ? vo : vo4) + (t & 3) * HREC, e.r); \
      else if constexpr (kd == E_STK) st_dev(ws, (t < 4 ? vk : vk4) + (t & 3) * HREC, e.k4); \
      else epi_op<kd, V>(e, t % 3 == 0 ? tA : t % 3 == 1 ? tB : tC, tag); \
      SB(); })
    int va0 = ((lane >> 3) & 1) * 8192 + g * 128 + (lane & 7) * 16, va1 = va0 + 16384;
    int vr = ((lane >> 3) & 1) * 512 + g * 128 + (lane & 7) * 16;
    PIN(vr);
    const int sa = WS_L + (4 * rg + w) * 32768;
#define ACT_LD(i) act[i] = ld_dev(ws, (((i) & 1) ? va1 : va0) + (((i) >> 1) & 7) * 512, sa + ((i) >> 4) * 4096)
    f32x4 tA[4], tB[4], tC[4];
    WD_SETUP();
    v4u s0, s1;
#define A_HEAD() do { MEMFENCE(); PIN(va0); PIN(va1); s0 = ld_dev(ws, vr, WS_S + (4 * rg + w) * 2048); s1 = ld_dev(ws, vr + 1024, WS_S + (4 * rg + w) * 2048); SB(); \
      static_for<0, 2 * LOOK>([&](auto K) { ACT_LD(K.value); SB(); }); } while (0)
    WAIT3();
    STAMP(15);
    u32 spin = 0;
    A_HEAD();
    BAR();
    for (;; ++spin) {
      static_for<0, PD>([&](auto K) { ring[K.value ^ 1] = FRAG(K.value ^ 1); SB(); });
      if (__builtin_expect(__any(s0.w != tag || s1.w != tag), 0)) { _Pragma("nounroll") do { __builtin_amdgcn_s_sleep(RS3); A_HEAD(); } while (__any(s0.w != tag || s1.w != tag) && ++spin < SPIN_CAP); ++spin; }
      PIN(va0); PIN(va1);
      u32 m = (s0.x + s0.y + s0.z) + (s1.x + s1.y + s1.z);
      static_for<0, 64>([&](auto K) {
        constexpr int n = K.value, ks = n >> 2, q = (n & 1) * 2;
        f32x4* acc = (n & 2) ? tB : tA;
        mma<ks == 0>(acc[q], ring[n & 7], act[2 * ks]); SB();
        ring[NXT(n) & 7] = FRAG(NXT(n));
        if constexpr (!(n & 1) && ks + LOOK < 16) ACT_LD(2 * (ks + LOOK) + ((n >> 1) & 1));
        SB();
        mma<ks == 0>(acc[q + 1], ring[n & 7], act[2 * ks + 1]); SB();
        { const v4u v = act[2 * ks + ((n >> 1) & 1)]; u32 t = (n & 1) ? m + v.z + v.w : m + v.x + v.y; PIN(t); m = t; }
        SB();
      });
      if (__builtin_expect(__all(add_x32(add_x16(m)) == 8u * tag) || spin > SPIN_CAP, 1)) break;
      s0.w = ~tag;
    }
    COUNT(22, spin);
    BAR_N(15);
    STAMP(16);
#define G1_TILE(T, acc, ...) static_for<0, 32>([&](auto K) { constexpr int s = K.value, n = 32 * (T) + s, ks = s >> 1, q = (s & 1) * 2, j0 = epos(n), j1 = epos(n + 1), jm = j0 + (j1 - j0 - (T) / 4) / 2; \
      mma<ks == 0>(acc[q], ring[n & 7], act[2 * ks]); SB(); \
      if constexpr (n + PD < 192) ring[NXT(n) & 7] = FRAG(n + PD < 192 ? NXT(n) : 0); SB(); \
      EPI(j0, jm); \
      mma<ks == 0>(acc[q + 1], ring[n & 7], act[2 * ks + 1]); SB(); \
      __VA_ARGS__; EPI(jm, j1); SB(); })
#define PG2 16
#define PG4 12
#define PG3 10
    G1_TILE(2, tC, { });
    FSTAMP(26);
    G1_TILE(3, tA, { if constexpr (s == PG2) BAR_N(15); });
    FSTAMP(27);
    if (__builtin_expect(six, 1)) {
      G1_TILE(4, tB, { });
      FSTAMP(28);
      G1_TILE(5, tC, { if constexpr (s == PG4) BAR_N(15); });
      STAMP(17);
      MMA_DONE(); SB();
      EPI(kEp.first[5], kEp.n);
    } else {
      MMA_DONE(); SB();
      EPI(epos(128), kEp.first[4]);
      static_for<E_X1, E_STK>([&](auto KK) { epi_op<KK.value, 31>(e, tA, tag); });
      st_dev(ws, vk + 3 * HREC, e.k4);
#define SKSL 20
      __builtin_amdgcn_s_sleep(SKSL);
      BAR_N(15);
      FSTAMP(28);
      __builtin_amdgcn_s_sleep(SKSL + 6);
      STAMP(17);
    }
    STAMP(18);
    BAR();
#define CSL 10
    __builtin_amdgcn_s_sleep(CSL);
    DMA_WDS(4, 0);
    DMA_WDS(5, 1);
    BAR();
    if (w & 1) VM_WAIT(4); else VM_WAIT(8);
    BAR();
    VM_WAIT(0);
    BAR();
  }
  STAMP(24);
  STAMP_FLUSH();
  UNDEF_RA();
  asm volatile("" : "=v"(wa_));
  }
  PINS(wv);
  if (wv >= 4) {
    STAMP(0);
    const int hw = wv - 4;
    asm volatile("" :: "v"(cpf), "v"(ra[0]));
    if (hw < 2) {
    const rsrc_t ws = mkrs(p.ws, WS_END);
    const int T = tid - 256, vs = wa_.x;
    const u32 sh = wa_.y;
    const lp_t ca0 = (lp_t)(size_t)wa_.z, ca1 = (lp_t)(size_t)wa_.w;
    u32 al[8][2], ck[4];
#pragma unroll
    for (int i = 0; i < 8; ++i) { const u32 a = ra[i].x, b = ra[i].y, d = ra[i].z; al[i][0] = __builtin_amdgcn_alignbit(b, a, sh); al[i][1] = __builtin_amdgcn_alignbit(d, b, sh); }
    v4u v[4];
#pragma unroll
    for (int e = 0; e < 4; ++e) {
#pragma unroll
      for (int r = 0; r < 4; ++r) v[e][r] = __builtin_amdgcn_perm(al[4 + r][e >> 1], al[r][e >> 1], (e & 1) ? 0x07060302u : 0x05040100u);
      st_dev(ws, vs + e * 128, v[e]);
      ck[e] = sum4(0u, v[e]);
    }
    const u32 c0 = pair_total(ck[0], ck[1], T & 1), c1 = pair_total(ck[2], ck[3], T & 1);
    const u32 cq = ((T & 2) ? c1 : c0) + dppu<0x4e>((T & 2) ? c0 : c1);
    lds_add(ca0, cq); lds_add(ca1, cq);
    flag_set(fmine, 1);
    STAMP(1);
  }
    u32 tag;
    v4u y0[8];
    WD_SETUP();
    {
      const rsrc_t rwg = mkrs(p.wg, W1SZ), rwu = mkrs(p.wu, W1SZ);
      const int vx = 4 * (lane >> 3) * (F_ * 2) + ft0 * 32 + (lane & 7) * 16, vx8 = vx + 32 * F_ * 2;
      const int vy = six ? 4 * (lane >> 2) * (F_ * 2) + (ft0 + 4) * 32 + (lane & 3) * 16 : OOB;
      const u32 ax = ((lane >> 1) & 3) * SLOT + (16 * hw + (lane >> 3)) * 512 + (lane & 1) * 128 + (lane & 7) * 16;
#define PUT_X(u, off) do { transpose8(u); const u32 a_ = ax + (off); _Pragma("unroll") for (int i = 0; i < 8; ++i) lds_w128((lp_t)(size_t)(a_ ^ (i * 16)), u[i]); SB(); } while (0)
      v4u rb[8], rc[8], rd[8];
      const lp_t fall = lptr(SCR + L_F + (lane & 3) * 256 + lane * 4);
      const int s0 = 64 * hw * (F_ * 2);
#define THR 4
#define NA 2
#define NB 10
#define N0 0
#define NS 8
#define NL 99
#define YD 1
#define THRU 4
#define NH 2
#define TS 0
#define F1 2
#define F2 5
#define STEP(n) do { constexpr int j = (n) & 7; const int so_ = s0 + ((j & 3) + 256 * (j >> 2)) * (F_ * 2); \
        if ((n) == NA) { flag_wait_lazy(fall, 3); cpf = touch(rwd, lane < 11 ? wg * 43 + hw * 11 + lane : 16384); } if ((n) == NB) flag_wait_lazy(fall, 7); \
        if ((n) == NA + NH) flag_wait_lazy(fall, 4); if ((n) == NB + NH) flag_wait_lazy(fall, 8); \
        if ((n) == N0) flag_wait_lazy(fall, 2); if ((n) == NS) flag_wait_lazy(fall, 6); if ((n) == NL) flag_wait_lazy(fall, 10); \
        if ((n) > 1) asm volatile("s_waitcnt vmcnt(%0)" :: "n"((YD && (n) > 8) ? THRU : THR) : "memory"); \
        if ((n) < 8) { ra[j] = ld_in(rwg, vx, so_); rb[j] = ld_in(rwg, vx8, so_); y0[j] = ld_in(rwg, vy, so_); } \
        else { constexpr int j2 = (2 * (n)) & 7; const int sp_ = s0 + ((j2 & 3) + 256 * (j2 >> 2)) * (F_ * 2); \
          if ((n) < 12) { rc[j2] = ld_in(rwu, vx, sp_); rc[j2 + 1] = ld_in(rwu, vx, sp_ + F_ * 2); } else { rd[j2] = ld_in(rwu, vx8, sp_); rd[j2 + 1] = ld_in(rwu, vx8, sp_ + F_ * 2); } } SB(); } while (0)
      PACE(0, TS);
      STAMP(2);
      STEP(0); STEP(1); STEP(2); STEP(3); STEP(4); STEP(5); STEP(6); STEP(7); STEP(8); STEP(9);
      STAMP(3);
      if (NB > 10) STEP(10);
      if (NB > 11) STEP(11);
      PUT_X(ra, 0);
      PUT_X(rb, 4096);
      if (NB <= 10) STEP(10);
      if (NB <= 11) STEP(11);
      STEP(12); STEP(13); STEP(14); STEP(15);
      PUT_X(rc, 256);
      tag = *(LAS u32*)(fall + L_TG);
      STAMP(4);
      PUT_X(rd, 4096 + 256);
      PIN(tag);
      asm volatile("" :: "v"(cpf));
    }
    STAMP(5);
    BAR();
    const rsrc_t ws = mkrs(p.ws, WS_END);
    const int kh = hw & 1, rl = hw >> 1, g = lane >> 4;
    constexpr int PD = 4;
    const u32 ayo = hw * 8192 + g * 2048 + ((lane >> 2) & 3) * 256 + (lane & 4) * 16 + ((lane & 3) ^ ((lane >> 1) & 2)) * 16;
    const lp_t ay0[2] = {lptr(ayo + 4 * SLOT), lptr((ayo ^ 64) + 4 * SLOT)}, ad = lptr(3 * SLOT + hw * 8192 + lane * 16);
#define PUT_Y(a, y, j0, j1) _Pragma("unroll") for (int j = j0; j < j1; ++j) lds_w128(a[j & 1] + (j >> 2) * 1024 + (j & 2) * 64, y[j])
    PUT_Y(ay0, y0, 0, 8);
    v4u ring[8], hfA[8], hfB[8], hfC[8], hfD[8], hfE[8];
    u32 hkA, hkB, hkC, hkD, hkE, ms = 0, nfix = 0;
    lp_t wd[3][4];
    {
      const u32 dl = (g * 1024 + ((lane >> 2) & 3) * 128 + ((lane >> 3) & 3) * 32 + (lane & 3) * 8) ^ (kh * 64);
#pragma unroll
      for (int i = 0; i < 12; ++i) wd[i >> 2][i & 3] = lptr((dl ^ ((i & 3) * 32)) + (i >> 2) * 2 * SLOT + kh * 16384);
    }
    auto frag = [&](auto NN) -> v4u {
      constexpr int N = NN.value, t = N < 64 ? N >> 4 : N < 76 ? 4 : 5, s = N < 64 ? (N & 15) : N < 76 ? N - 64 : N - 76, slot = (t + 1) % 5;
      const lp_t b = wd[slot >> 1][s & 3] + (slot & 1) * SLOT + (s >> 2) * 4096;
      return lds_tr2(b, b + 512);
    };
    const int hb = WS_H + (4 * rg + 2 * (ch >> 3) + rl) * NFT * HREC;
    int hs[4];
#pragma unroll
    for (int i = 0; i < 4; ++i) { hs[i] = hb + tile0_of(8 * kh + 2 * i) * HREC; PINS(hs[i]); }
    int vl = (g & 1) * 512 + (lane & 15) * 16 + (g >> 1) * 6 * HREC, vl3 = vl - (g >> 1) * 2 * HREC, vl4 = vl + 4 * HREC, vlz = (kh && g >= 2) ? OOB : vl4, vq, vq4;
    {
      const int st = (lane & 2) + ((lane >> 1) & 4) + (g >> 1);
      vq = hb + 1024 + ((2 * (g & 1) + (lane & 1)) * 8 + (lane & 4)) * 4 + tile0_of(8 * kh + st) * HREC;
      vq4 = st < 6 - kh ? vq + 4 * HREC : OOB;
    }
    const int u = 16 * rg + 8 * (ch >> 3) + 4 * rl + ((lane >> 3) & 1), col = 64 * (ch & 7) + 32 * kh;
    int vh = u * 8192 + (col & 255) * 32 + g * 128 + (lane & 7) * 16 + (col >> 8) * 8, vs = u * 512 + (lane & 7) * 16 + 4;
    int vout = ((32 * (8 * (u >> 5) + (lane & 7)) + (u & 31)) * D_ + col + 16 * (g & 1) + 8 * (g >> 1)) * 2;
    lp_t rw = lptr(2 * SLOT + hw * 4096 + lane * 16), rr_ = lptr(2 * SLOT + (hw ^ 1) * 4096 + lane * 16);
    int vyd = ((lane >> 2) ^ (g & 1)) * (F_ * 2) + (ft0 + 4) * 32 + ((lane & 3) ^ ((lane >> 3) & 2)) * 16;
    PIN(vyd);
    u32 t8 = 8u * tag, t6 = ((kh && g >= 2) ? 4u : 6u) * tag;
    PIN(vl); PIN(vl3); PIN(vl4); PIN(vlz); PIN(vq); PIN(vq4); PIN(vh); PIN(vs); PIN(vout); PIN(t8); PIN(t6);
#define H_LD(X, t, j) do { \
      if constexpr ((j) == 8) hk##X = ld_dev1(ws, ((t) < 4 ? vq : vq4) + ((t) & 3) * HREC, 0); \
      else if constexpr ((t) < 4 || (j) < 6) hf##X[(j) & 7] = ld_dev(ws, ((t) < 4 ? ((j) < 6 ? vl : vl3) : (j) < 4 ? vl4 : vlz) + ((t) & 3) * HREC + ((j) & 1) * 256, hs[((j) >> 1) & 3]); } while (0)
#define H_LDALL(X, t) static_for<0, 9>([&](auto J) { THROTTLE(J.value); H_LD(X, t, J.value ? J.value - 1 : 8); })
#define H_SUM(X, nf, j) do { if constexpr ((j) >= 0 && (j) < (nf)) { ms = sum4((j) ? ms : hk##X, hf##X[(j) & 7]); u32 t_ = ms; PIN(t_); ms = t_; } \
      else if constexpr ((j) >= (nf) && (j) < (nf) + 3) { u32 t_ = ms + dppu<(j) == (nf) ? 0xb1 : (j) == (nf) + 1 ? 0x4e : 0x128>(ms); PIN(t_); ms = t_; } } while (0)
#define H_SUMALL(X, nf) static_for<0, (nf) + 3>([&](auto J) { H_SUM(X, nf, J.value); })
#define H_FIX(X, t, nf, want) do { if (__builtin_expect(__any(ms != (want)), 0)) { u32 spin_ = 0; _Pragma("nounroll") do { __builtin_amdgcn_s_sleep(RS3); MEMFENCE(); nfix |= 1u << (4 * (t)); \
        H_LDALL(X, t); H_SUMALL(X, nf); } while (__any(ms != (want)) && ++spin_ < SPIN_CAP); } SB(); } while (0)
    v4u d3[8];
    f32x4 o[4][2];
    v2u hr[4], so[2];
#define GRP() asm volatile("s_waitcnt vmcnt(%0)" :: "n"(TH2) : "memory")
#define RAW4(dst, t, i0) do { _Pragma("unroll") for (int i = i0; i < i0 + 4; ++i) { THROTTLE(i); dst[i] = ld_in(rwd, (i & 1) ? vd1 : vd, sd[i >> 1] + ((t) * 16 + (i & 1) * 8) * (D_ * 2)); } SB(); } while (0)
#define DMA4(t, slot, i0) do { _Pragma("unroll") for (int i = i0; i < i0 + 4; ++i) { THROTTLE(i); DMA_WD1(t, slot, i); } } while (0)
#define H4(X, t, h) do { static_for<5 * (h), 5 + 4 * (h)>([&](auto J) { if (J.value != 4) THROTTLE(J.value - 5 * (h)); H_LD(X, t, J.value ? J.value - 1 : 8); }); SB(); } while (0)
#define PUT_D(d, off, i0, i1) do { _Pragma("unroll") for (int i = i0; i < i1; ++i) lds_w128(ad + (off) + i * 1024, d[i]); SB(); } while (0)
#define RESID(i) do { const u32 v0 = hr[i].x, v1 = hr[i].y, mb = so[(i) & 1].x, sb = so[(i) & 1].y; float q[4] = {blo(v0), bhi(v0), blo(v1), bhi(v1)}; \
      _Pragma("unroll") for (int r = 0; r < 4; ++r) { q[r] = __builtin_fmaf(q[r], asf(sb), asf(mb)); PIN(q[r]); o[(i) >> 1][(i) & 1][r] = q[r]; } PIN(o[(i) >> 1][(i) & 1]); SB(); } while (0)
#define PSLEEP 16
    __builtin_amdgcn_s_sleep(PSLEEP);
#define SPRIO 3
#define SPRIO1 0
#define LOWP() __builtin_amdgcn_s_setprio(SPRIO1)
#define HIGHP() __builtin_amdgcn_s_setprio(SPRIO)
#define SPRIO2 3
    __builtin_amdgcn_s_setprio(SPRIO);
    RAW4(d3, 3, 0); RAW4(d3, 3, 4);
    STAMP(6);
    BAR();
    if (YD && six) {
      const rsrc_t rwu = mkrs(p.wu, W1SZ);
#pragma unroll
      for (int i = 0; i < 8; ++i) {
        asm volatile("s_waitcnt vmcnt(4)\n\ts_mov_b32 m0, %0\n\ts_nop 0\n\tbuffer_load_dwordx4 %1, %2, %3 offen lds"
                     :: "s"(hw * 8192 + i * 1024), "v"(vyd), "s"(rwu), "s"((16 * (4 * hw + (i >> 1)) + 256 * (i & 1)) * (F_ * 2)) : "memory");
      }
    }
#pragma unroll
    for (int i = 0; i < 2; ++i) so[i] = __builtin_amdgcn_raw_buffer_load_b64(ws, vs + i * 1024, WS_S, AUX_SC1);
#pragma unroll
    for (int i = 0; i < 4; ++i) hr[i] = __builtin_amdgcn_raw_buffer_load_b64(ws, vh + (i >> 1) * 512, WS_L + (i & 1) * 16384, AUX_SC1);
    SB();
    DMA4(0, 1, 0); DMA4(0, 1, 4);
    if (YD) VM_WAIT(8);
    STAMP(7);
    BAR();
    PIN(hr[3]);
    DMA4(1, 2, 0); DMA4(1, 2, 4); LOWP(); RESID(0); RESID(1); RESID(2); RESID(3);
    HIGHP();
    H4(A, 0, 0); H4(A, 0, 1);
#define BSL 0
    __builtin_amdgcn_s_sleep(BSL);
    H4(B, 1, 0); H4(B, 1, 1);
    PIN(hfA[7]);
    STAMP(8);
    BAR();
    DMA4(2, 3, 0); DMA4(2, 3, 4);
    LOWP();
    H_SUMALL(A, 8);
    H_FIX(A, 0, 8, t8);
    HIGHP();
    STAMP(9);
    H4(C, 2, 0); H4(C, 2, 1);
    VM_WAIT(9);
    H_SUMALL(B, 8);
    H_FIX(B, 1, 8, t8);
    static_for<0, PD>([&](auto K) { ring[K.value ^ 1] = FRAG(K.value ^ 1); SB(); });
    STAMP(10);
    LOWP();
    STAMP(11);
#define G2_ROUND(X, n0, ns, ...) static_for<0, ns>([&](auto K) { constexpr int s = K.value, n = (n0) + s, ct = s & 3, a = (s >> 2) * 2; \
      mma<n == 2 || n == 3>(o[ct][0], ring[n & 7], hf##X[a]); SB(); \
      if constexpr (n + PD < 88) ring[NXT(n) & 7] = FRAG(n + PD < 88 ? NXT(n) : 0); SB(); \
      mma<n == 2 || n == 3>(o[ct][1], ring[n & 7], hf##X[a + 1]); SB(); \
      __VA_ARGS__; SB(); })
#define PP 0
#define L3 16
#define LATE() do { if constexpr (n >= L3 && n < L3 + 9) H_LD(D, 3, n > L3 ? n - L3 - 1 : 8); \
      if constexpr (n >= L4 && n < L4 + 7) H_LD(E, 4, n > L4 ? n - L4 - 1 : 8); if constexpr (n >= L5 && n < L5 + 7) H_LD(A, 5, n > L5 ? n - L5 - 1 : 8); } while (0)
#define PG5 15
#define PS 1
#define MID() do { if constexpr (n == PG5) { BAR_N(15); __builtin_amdgcn_s_setprio(SPRIO2); } \
      if constexpr (n > PG5 && n <= PG5 + 8 * PS && (n - PG5 - 1) % PS == 0) lds_w128(ad + SLOT + ((n - PG5 - 1) / PS) * 1024, d3[((n - PG5 - 1) / PS) & 7]); \
      if constexpr (n == 43) BAR_N(8); LATE(); } while (0)
    static_assert(PG5 >= 15 && PG5 <= 27 && PG5 + 8 * PS <= 39, "");
    G2_ROUND(A, 0, 16, { MID(); });
    FSTAMP(14);
    G2_ROUND(B, 16, 16, { H_SUM(C, 8, s - 5); MID(); });
    H_FIX(C, 2, 8, t8);
    FSTAMP(15);
    G2_ROUND(C, 32, 16, { H_SUM(D, 8, s - 5); MID(); });
    H_FIX(D, 3, 8, t8);
    FSTAMP(16);
    G2_ROUND(D, 48, 16, { LATE(); H_SUM(E, 6, s - 7); if constexpr (s == 11) BAR_N(15); });
    H_FIX(E, 4, 6, t6);
    FSTAMP(17);
    G2_ROUND(E, 64, 12, { H_SUM(A, 6, s - 3); if constexpr (s == 7) BAR_N(15); });
    H_FIX(A, 5, 6, t6);
    FSTAMP(18);
    G2_ROUND(A, 76, 12, { });
    COUNT(23, nfix);
    STAMP(12);
    MMA_DONE(); SB();
#pragma unroll
    for (int i = 0; i < 4; ++i) lds_wf(rw + i * 1024, o[2 + (i & 1)][i >> 1]);
    BAR();
    const rsrc_t ro = mkrs(p.out, S_ * D_ * 2);
#pragma unroll
    for (int i = 0; i < 2; ++i) {
      const f32x4 v0 = o[0][i] + lds_rf(rr_ + i * 2048), v1 = o[1][i] + lds_rf(rr_ + i * 2048 + 1024);
      const auto x = __builtin_amdgcn_permlane16_swap(pk(v0[0], v0[1]), pk(v1[0], v1[1]), false, false);
      const auto y = __builtin_amdgcn_permlane16_swap(pk(v0[2], v0[3]), pk(v1[2], v1[3]), false, false);
      const v4u q = {x[0], y[0], x[1], y[1]};
      __builtin_amdgcn_raw_buffer_store_b128(q, ro, vout + i * 2048, 0, OAUX);
    }
    STAMP(13);
    STAMP_FLUSH();
  }
}

extern "C" void kernel_launch(void* const* d_in, const int* in_sizes, int n_in,
                              void* d_out, int out_size, void* d_ws, size_t ws_size,
                              hipStream_t stream) {
  Params p;
  p.x = (const u16*)d_in[0]; p.wr = (const u16*)d_in[1]; p.wi = (const u16*)d_in[2];
  p.wg = (const u16*)d_in[4]; p.wu = (const u16*)d_in[5]; p.wd = (const u16*)d_in[6];
  p.out = (u16*)d_out; p.ws = d_ws;
  (void)hipFuncSetAttribute((const void*)gfnet_geglu_kernel, hipFuncAttributeMaxDynamicSharedMemorySize, 5 * SLOT);
  gfnet_geglu_kernel<<<256, 512, 5 * SLOT, stream>>>(p);
}
```

```cpp
#define T1 270
#define T2 515
#define T3 720
#define NB 9
#include <hip/hip_runtime.h>
#include <hip/hip_bf16.h>
#include <type_traits>

typedef unsigned u32;
typedef unsigned short u16;
typedef u32 v4u __attribute__((ext_vector_type(4)));
typedef u32 v2u __attribute__((ext_vector_type(2)));
typedef float f32x2 __attribute__((ext_vector_type(2)));
typedef float f32x4 __attribute__((ext_vector_type(4)));
typedef __bf16 bf16x2 __attribute__((ext_vector_type(2)));
typedef __bf16 bf16x8 __attribute__((ext_vector_type(8)));
typedef short s16x4 __attribute__((ext_vector_type(4)));
#define LAS __attribute__((address_space(3)))
#define DI __device__ __forceinline__
typedef __amdgpu_buffer_rsrc_t rsrc_t;
typedef LAS char* lp_t;

constexpr int S_ = 2048, D_ = 512, F_ = 1376, KF = 257;
constexpr int NFT = 86;
constexpr int WS_E = 0;
#define XPAD 1
#define LPAD 0
#define WPAD 1
constexpr int WREC = 4 * 8192 + WPAD * 128, LREC = 8192 + LPAD * 128;
constexpr int XREC = (88 + XPAD) * 128;
constexpr int WS_A = 4096;
constexpr int WS_B = WS_A + 256 * XREC;
constexpr int WS_W = WS_B + 256 * XREC;
constexpr int WS_L = WS_W + 64 * WREC;
constexpr int WS_T = WS_L + 256 * LREC;
constexpr int WS_S = WS_T + (1 << 15);
constexpr int WS_H = WS_S + (1 << 17);
constexpr int HREC = 1152;
constexpr int WS_END = WS_H + 64 * NFT * HREC;
constexpr int WS_ST = 24 << 20;
constexpr u32 NEG2 = 0x80008000u, NEGH = 0x80000000u, NEGL = 0x8000u;
constexpr u32 SPIN_CAP = 1u << 14;
constexpr int AUX_SC1 = 16;
#define OAUX 2
#define NTAUX 2
constexpr int OOB = 0x40000000;
constexpr int CODE_LINES = 260;
constexpr int SLOT = 32768, SCR = 4 * SLOT;
constexpr int L_F = 0, L_TG = 2048;
constexpr int L_IM = 4096, L_SO = 4096, L_X = 16384, L_I2 = 16384, L_I3 = 16384, L_EX = 24576, L_O3 = 24576;

#define DRAIN() asm volatile("s_waitcnt vmcnt(0)" ::: "memory")
#define MEMFENCE() asm volatile("" ::: "memory")
#define PIN(x) asm volatile("" : "+v"(x))
#define PINS(x) asm volatile("" : "+s"(x))
#define SB() __builtin_amdgcn_sched_barrier(0)
#define BAR() asm volatile("s_waitcnt lgkmcnt(0)\n\ts_barrier" ::: "memory")
#define BAR_N(n) asm volatile("s_waitcnt lgkmcnt(" #n ")\n\ts_barrier" ::: "memory")

DI u32 pk(float a, float b) { f32x2 v = {a, b}; return __builtin_bit_cast(u32, __builtin_convertvector(v, bf16x2)); }
DI v4u pk4(f32x4 a, f32x4 b) { v4u r = {pk(a[0], b[0]), pk(a[1], b[1]), pk(a[2], b[2]), pk(a[3], b[3])}; return r; }
DI float asf(u32 v) { return __builtin_bit_cast(float, v); }
DI u32 asu(float v) { return __builtin_bit_cast(u32, v); }
DI float blo(u32 v) { return asf(v << 16); }
DI float bhi(u32 v) { return asf(v & 0xffff0000u); }
DI float dot2(u32 a, u32 b, float c) { return __builtin_amdgcn_fdot2_f32_bf16(__builtin_bit_cast(bf16x2, a), __builtin_bit_cast(bf16x2, b), c, false); }
DI u32 sum4(u32 m, v4u v) { return (m + v.x + v.y) + v.z + v.w; }
template <int CTRL> DI u32 dppu(u32 v) { return (u32)__builtin_amdgcn_update_dpp(0, (int)v, CTRL, 0xf, 0xf, true); }
template <int CTRL> DI float dppf(float v) { return asf(dppu<CTRL>(asu(v))); }
DI u32 pair_total(u32 s0, u32 s1, bool odd) { return (odd ? s1 : s0) + dppu<0xb1>(odd ? s0 : s1); }
DI u32 add_x16(u32 v) { const auto s = __builtin_amdgcn_permlane16_swap(v, v, false, false); return s[0] + s[1]; }
DI u32 add_x32(u32 v) { const auto s = __builtin_amdgcn_permlane32_swap(v, v, false, false); return s[0] + s[1]; }
template <int N> DI void wave_sums(float (&v)[N]) {
#define WS_STEP(C) _Pragma("unroll") for (int i = 0; i < N; ++i) v[i] += dppf<C>(v[i]);
  WS_STEP(0x111) WS_STEP(0x112) WS_STEP(0x114) WS_STEP(0x118) WS_STEP(0x142) WS_STEP(0x143)
#pragma unroll
  for (int i = 0; i < N; ++i) v[i] = asf(__builtin_amdgcn_readlane(asu(v[i]), 63));
}
__host__ __device__ constexpr int rec_q(int k) { return (k & 7) | ((k >> 1) & 0x18); }
__host__ __device__ constexpr int rec_line(int k) { return 11 * ((k >> 3) & 1) + ((rec_q(k) * 11) >> 5); }
__host__ __device__ constexpr int rec_pos(int k) { return rec_q(k) - 3 * ((rec_q(k) * 11) >> 5); }
__host__ __device__ constexpr int im_unit(int k, int j) { return L_IM + (k >> 6) * 3072 + rec_line(k) * 128 + ((j ^ (rec_line(k) + 2 * (k >> 6))) & 7) * 16; }
__host__ __device__ constexpr int im_chk(int k) { return im_unit(k, 6) + 4 * rec_pos(k); }
__host__ __device__ constexpr int im_chkw(int k) { return im_unit(k, 6 + ((rec_pos(k) + 3) >> 2)) + 4 * ((rec_pos(k) + 3) & 3); }
DI void lds_add(lp_t a, u32 v) { __hip_atomic_fetch_add((LAS u32*)a, v, __ATOMIC_RELAXED, __HIP_MEMORY_SCOPE_WORKGROUP); }
DI f32x4 mm16(v4u a, v4u b, f32x4 c) { return __builtin_amdgcn_mfma_f32_16x16x32_bf16(__builtin_bit_cast(bf16x8, a), __builtin_bit_cast(bf16x8, b), c, 0, 0, 0); }
DI f32x4 mm16p(v4u a, v4u b, f32x4 c) { f32x4 d = mm16(a, b, c); PIN(d); return d; }
template <bool FIRST> DI void mma(f32x4& d, v4u a, v4u b) {
  if constexpr (FIRST) asm volatile("v_mfma_f32_16x16x32_bf16 %0, %1, %2, 0" : "=&v"(d) : "v"(a), "v"(b));
  else asm volatile("v_mfma_f32_16x16x32_bf16 %0, %1, %2, %0" : "+v"(d) : "v"(a), "v"(b));
}
#define MMA_DONE() asm volatile("s_nop 7")
DI v2u lds_tr(lp_t a) { return __builtin_bit_cast(v2u, __builtin_amdgcn_ds_read_tr16_b64_v4i16((LAS s16x4*)a)); }
DI v4u lds_tr2(lp_t a, lp_t b) { v2u x = lds_tr(a), y = lds_tr(b); v4u r = {x.x, x.y, y.x, y.y}; return r; }
DI v4u lds_r128(lp_t a) { return *(LAS v4u*)a; }
DI f32x4 lds_rf(lp_t a) { return *(LAS f32x4*)a; }
DI v2u lds_r64(lp_t a) { return *(LAS v2u*)a; }
DI void lds_w128(lp_t a, v4u v) { *(LAS v4u*)a = v; }
DI void lds_wf(lp_t a, f32x4 v) { *(LAS f32x4*)a = v; }
DI void lds_w64(lp_t a, v2u v) { *(LAS v2u*)a = v; }
DI lp_t lptr(u32 off) { lp_t q = (lp_t)(size_t)off; PIN(q); return q; }
DI rsrc_t mkrs(const void* p, u32 bytes) { return __builtin_amdgcn_make_buffer_rsrc((void*)p, 0, bytes, 0x00020000); }
DI v4u ld_dev(rsrc_t r, int v, int s) { return __builtin_amdgcn_raw_buffer_load_b128(r, v, s, AUX_SC1); }
DI u32 ld_dev1(rsrc_t r, int v, int s) { return __builtin_amdgcn_raw_buffer_load_b32(r, v, s, AUX_SC1); }
DI void st_dev(rsrc_t r, int v, v4u x) { __builtin_amdgcn_raw_buffer_store_b128(x, r, v, 0, AUX_SC1); }
DI void st_dev1(rsrc_t r, int v, u32 x) { __builtin_amdgcn_raw_buffer_store_b32(x, r, v, 0, AUX_SC1); }
DI v4u ld_in(rsrc_t r, int v, int s) { return __builtin_amdgcn_raw_buffer_load_b128(r, v, s, 0); }
DI v4u ld_once(rsrc_t r, int v, int s) { return __builtin_amdgcn_raw_buffer_load_b128(r, v, s, NTAUX); }
DI u32 touch(rsrc_t r, int line) { return __builtin_amdgcn_raw_buffer_load_b32(r, line * 128, 0, 0); }
DI void flag_set(lp_t mine, u32 k) { MEMFENCE(); *(volatile LAS u32*)mine = k; MEMFENCE(); }
#define FLAG_WAIT(theirs, k, ...) do { u32 f_; _Pragma("nounroll") do { f_ = *(volatile LAS u32*)(theirs); MEMFENCE(); __VA_ARGS__; MEMFENCE(); } while (!__all(f_ >= (k))); } while (0)
DI void flag_wait_lazy(lp_t theirs, u32 k) { while (!__all(*(volatile LAS u32*)theirs >= k)) __builtin_amdgcn_s_sleep(1); MEMFENCE(); }
DI float cosr(float rev) { return __builtin_amdgcn_cosf(rev); }
DI float sinr(float rev) { return __builtin_amdgcn_sinf(rev); }
__host__ __device__ constexpr int slot_of(int k1, int k2) { return ((k2 < 8 ? k1 + 32 * k2 : (k1 ? 32 - k1 : 16) + 32 * (15 - k2)) - 1) & 255; }
DI int tile0_of(int c) { const int h = c >> 3, i = c & 7; return 44 * h + 4 * i + 2 * (i < 6 - h ? i : 6 - h); }
template <int I, int N, class F> DI void static_for(F&& f) {
  if constexpr (I < N) { f(std::integral_constant<int, I>{}); static_for<I + 1, N>(f); }
}
DI void transpose8(v4u (&m)[8]) {
  v4u o[8];
#pragma unroll
  for (int i = 0; i < 8; ++i)
#pragma unroll
    for (int j = 0; j < 4; ++j) { const u32 a = m[2 * j][i >> 1], b = m[2 * j + 1][i >> 1]; o[i][j] = __builtin_amdgcn_perm(b, a, (i & 1) ? 0x07060302u : 0x05040100u); }
#pragma unroll
  for (int i = 0; i < 8; ++i) { m[i] = o[i]; PIN(m[i]); }
}
enum { E_SQ, E_POLY, E_ARG, E_EXP, E_INC, E_RCP, E_GU, E_OUT, E_PACK, E_SWA, E_SWB, E_SUM, E_SUM2, E_ST, E_X1, E_X2, E_X8, E_CHK, E_STK };
struct EOp { int kind, v; };
struct EProg { EOp op[520]; int n, first[7]; };
constexpr EProg make_eprog() {
  EProg p = {};
  for (int t = 0; t < 6; ++t) {
    p.first[t] = p.n;
    for (int v = 0; v <= 8; ++v) {
      const int V = 8 * t + v;
      const bool head = v < 8, tail = v > 0, fin = t > 0 && t < 5;
      if (head) p.op[p.n++] = {E_SQ, V};
      if (tail) p.op[p.n++] = {E_INC, V - 1};
      if (head) p.op[p.n++] = {E_POLY, V}; else p.op[p.n++] = {E_GU, V - 1};
      if (tail) p.op[p.n++] = {E_RCP, V - 1}; else if (fin) p.op[p.n++] = {E_X1, V - 1};
      if (head) p.op[p.n++] = {E_ARG, V};
      if (tail && head) p.op[p.n++] = {E_GU, V - 1};
      if (v >= 3 && (v & 1)) p.op[p.n++] = {E_PACK, V - 2};
      if (v == 8) p.op[p.n++] = {E_SWA, V - 1};
      if (fin && v == 0) p.op[p.n++] = {E_X2, V - 1};
      if (fin && v == 1) p.op[p.n++] = {E_CHK, V - 2};
      if (head) p.op[p.n++] = {E_EXP, V};
      if (fin && v == 0) p.op[p.n++] = {E_X8, V - 1};
      if (fin && v == 1) p.op[p.n++] = {E_STK, V - 2};
      if (tail) p.op[p.n++] = {E_OUT, V - 1};
    }
    p.op[p.n++] = {E_PACK, 8 * t + 7}; p.op[p.n++] = {E_SUM, 8 * t + 7}; p.op[p.n++] = {E_SWB, 8 * t + 7}; p.op[p.n++] = {E_ST, 8 * t + 7}; p.op[p.n++] = {E_SUM2, 8 * t + 7};
    if (t >= 4) for (int k = E_X1; k <= E_STK; ++k) p.op[p.n++] = {k, 8 * t + 7};
  }
  p.first[6] = p.n;
  return p;
}
constexpr EProg kEp = make_eprog();
#define ER1 56
constexpr int epos(int n) {
  const int mid = (64 * ER1) / 16, end = kEp.first[5];
  int j = n <= 64 ? 0 : n <= 128 ? ((n - 64) * ER1) / 16 : n <= 192 ? mid + ((end - mid) * (n - 128)) / 64
        : end + ((kEp.n - end) * (n - 192)) / 8;
  for (int t = 2; t < 5; ++t) if (n <= 32 * t + 34 && j > kEp.first[t]) j = kEp.first[t];
  return j;
}
struct Epi { float t[2], q, h[3]; v4u r, k4; u32 sum, fin; };
template <int KIND, int V> DI void epi_op(Epi& e, const f32x4 (&a)[4], u32 tag) {
  constexpr int v = V & 7;
  const float gv = a[v >> 2][v & 3], uv = a[2 + (v >> 2)][v & 3];
  float& t = e.t[V & 1];
  if constexpr (KIND == E_SQ) { t = gv * gv; PIN(t); }
  else if constexpr (KIND == E_POLY) { t = __builtin_fmaf(t, -0.10294324f, -2.3022082f); PIN(t); }
  else if constexpr (KIND == E_ARG) { t = t * gv; PIN(t); }
  else if constexpr (KIND == E_EXP) { t = __builtin_amdgcn_exp2f(t); PIN(t); }
  else if constexpr (KIND == E_INC) { t = t + 1.0f; PIN(t); }
  else if constexpr (KIND == E_RCP) { t = __builtin_amdgcn_rcpf(t); PIN(t); }
  else if constexpr (KIND == E_GU) { e.q = gv * uv; PIN(e.q); }
  else if constexpr (KIND == E_OUT) { e.h[v & 1] = e.q * t; PIN(e.h[v & 1]); }
  else if constexpr (KIND == E_PACK) { u32 r = pk(e.h[0], e.h[1]); PIN(r); e.r[v >> 1] = r; }
  else if constexpr (KIND == E_SWA || KIND == E_SWB) { constexpr int i = KIND == E_SWB; const u32 x = e.r[i], y = e.r[i + 2];
    const auto q = __builtin_amdgcn_permlane16_swap(x, y, false, false); u32 q0 = q[0], q1 = q[1]; PIN(q0); PIN(q1); e.r[i] = q0; e.r[i + 2] = q1; }
  else if constexpr (KIND == E_SUM) { e.sum = e.r.x + e.r.z; PIN(e.sum); }
  else if constexpr (KIND == E_SUM2) { e.fin = e.sum + e.r.y + e.r.w; PIN(e.fin); }
  else if constexpr (KIND == E_X1) { e.fin += dppu<0xb1>(e.fin); PIN(e.fin); }
  else if constexpr (KIND == E_X2) { e.fin += dppu<0x4e>(e.fin); PIN(e.fin); }
  else if constexpr (KIND == E_X8) { e.fin += dppu<0x128>(e.fin); PIN(e.fin); }
  else if constexpr (KIND == E_CHK) { asm volatile("" : "=v"(e.k4)); e.k4.x = tag - e.fin; PIN(e.k4); }
}

constexpr double ct_sin(long n, long d) {
  n %= d; if (n < 0) n += d;
  if (2 * n > d) return -ct_sin(d - n, d);
  if (4 * n > d) return ct_sin(d - 2 * n, 2 * d);
  const double x = 6.283185307179586476925 * (double)n / (double)d;
  double t = x, r = x;
  for (int i = 1; i < 14; ++i) { t *= -x * x / ((2 * i) * (2 * i + 1)); r += t; }
  return r;
}
constexpr double ct_cos(long n, long d) { return ct_sin(4 * n + d, 4 * d); }
constexpr u32 ct_f32(double v) { return __builtin_bit_cast(u32, (float)v); }
constexpr u32 ct_bf(double v) { const u32 u = ct_f32(v); return (u + 0x7fffu + ((u >> 16) & 1u)) >> 16; }
constexpr u32 ct_pk(double a, double b) { return ct_bf(a) | (ct_bf(b) << 16); }
struct Tab { u32 v[6][64][4]; };
constexpr Tab make_tab() {
  Tab t = {};
  for (int l = 0; l < 64; ++l) {
    const int c = l & 15, g = l >> 4;
    for (int j = 0; j < 2; ++j) {
      const int a = 4 * g + 2 * j;
      t.v[0][l][j] = ct_pk(ct_cos(a * c, 32), ct_cos((a + 1) * c, 32));
      t.v[0][l][2 + j] = c ? ct_pk(-ct_sin(a * c, 32), -ct_sin((a + 1) * c, 32)) : ct_pk(1, -1);
    }
    for (int r = 0; r < 4; ++r) {
      const int b = 4 * g + r, sl = slot_of(b, c);
      t.v[1][l][r] = ct_f32(ct_cos(b * c, 512));
      t.v[2][l][r] = ct_f32(ct_sin(b * c, 512));
      t.v[3][l][r] = ct_pk(ct_cos(b * c, 16), ct_sin(b * c, 16));
      t.v[4][l][r] = c == 0 ? ct_pk((r & 1) ? -1 : 1, 0) : c < 8 ? ct_pk(ct_cos(b * c, 16), -ct_sin(b * c, 16)) : ct_pk(ct_cos(b * (31 - 2 * c), 32), -ct_sin(b * (31 - 2 * c), 32));
      t.v[5][l][r] = im_unit(sl, 2 * rec_pos(sl)) | (im_chk(sl) << 16);
    }
  }
  return t;
}
static_assert(true, "");
static __device__ const __attribute__((aligned(128))) Tab g_tab = make_tab();

#define STAMP(i) do { } while (0)
#define STAMP_DECL do { } while (0)
#define STAMP_FLUSH() do { } while (0)
#define COUNT(i, v) do { } while (0)

#define FSTAMP(i) do { } while (0)
#define PACE(d, t) do { __builtin_amdgcn_s_sleep(d); while ((u32)__builtin_amdgcn_s_memrealtime() - t_ref < (t)) __builtin_amdgcn_s_sleep(1); } while (0)
#define RS1 0
#define RS2 10
#define RS3 0
#define D1 1
#define D2 4
#define D3 6
#define TW 240
#define SLIPPED() (*(LAS u32*)(fmine + L_TG + 1024) = (u32)__builtin_amdgcn_s_memrealtime() - t_ref)
#define PACE2(d, t, sl) do { u32 r_ = *(LAS u32*)(fall + L_TG + 1024); r_ = max(r_, dppu<0xb1>(r_)); r_ = max(r_, dppu<0x4e>(r_)); \
      r_ = __builtin_amdgcn_readfirstlane(r_) + (sl); t_ref += r_ > (t) ? r_ - (t) : 0u; PACE(d, t); } while (0)
#define SL2 175
#define SL3 150
#define WAIT1() PACE(D1, T1)
#define WAIT2() PACE2(D2, T2, SL2)
#define WAIT3() PACE2(D3, T3, SL3)
struct Params {
  const u16* x; const u16* wr; const u16* wi; const u16* wg; const u16* wu; const u16* wd; u16* out; void* ws;
};

__global__ void __launch_bounds__(512, 2) gfnet_geglu_kernel(Params p) {
  const int wg = blockIdx.x, tid = threadIdx.x, lane = tid & 63;
  int wv = __builtin_amdgcn_readfirstlane(tid >> 6);
  STAMP_DECL;
  unsigned long long t_entry;
  asm volatile("s_memrealtime %0" : "=s"(t_entry));
  const int rg = 4 * ((wg & 7) >> 1) + (wg >> 6), ch = 8 * (wg & 1) + ((wg >> 3) & 7);
  const int un = 16 * rg + ch;
  const int ft0 = tile0_of(ch);
  const bool six = (ch & 7) < 6 - (ch >> 3);
#define W1SZ (D_ * F_ * 2)
#define CT2 ((ch + 4 - (int)((size_t)p.wd >> 7)) & 7)
#define WD_SETUP() const rsrc_t rwd = mkrs(p.wd, D_ * F_ * 2); \
    int vd = (lane >> 3) * (D_ * 2) + CT2 * 128 + ((lane & 7) ^ ((lane >> 3) & 2)) * 16, vd1 = vd ^ 64, sd[4]; PIN(vd); PIN(vd1); \
    _Pragma("unroll") for (int i = 0; i < 4; ++i) { sd[i] = tile0_of(4 * (wv & 3) + i) * (16 * D_ * 2); PINS(sd[i]); }
#define DMA_WD1(t, slot, i) asm volatile("s_mov_b32 m0, %0\n\ts_nop 0\n\tbuffer_load_dwordx4 %1, %2, %3 offen lds" \
      :: "s"((slot) * SLOT + (wv & 3) * 8192 + (i) * 1024), "v"(((i) & 1) ? vd1 : vd), "s"(rwd), "s"(sd[(i) >> 1] + ((t) * 16 + ((i) & 1) * 8) * (D_ * 2)) : "memory")
#define TH2 6
#define TG 4
#define THROTTLE(i) do { if ((i) % TG == 0) asm volatile("s_waitcnt vmcnt(%0)" :: "n"(TH2) : "memory"); } while (0)
#define DMA_WD(t, slot) do { _Pragma("unroll") for (int i = 0; i < 8; ++i) { THROTTLE(i); DMA_WD1(t, slot, i); } } while (0)
#define DMA_WDS(t, slot) do { _Pragma("unroll") for (int i = 0; i < 4; ++i) DMA_WD1(t, slot, i); if (!(wv & 1)) { _Pragma("unroll") for (int i = 4; i < 8; ++i) DMA_WD1(t, slot, i); } } while (0)
#define VM_WAIT(n) asm volatile("s_waitcnt vmcnt(" #n ")" ::: "memory")
#define PDN 4
  constexpr int PD = PDN;
#define FRAG(N) frag(std::integral_constant<int, (N)>{})
#define NXT(n) (((n) + PD) ^ 1)
  v4u ra[8];
  u32 cpf;
#define UNDEF_RA() do { asm volatile("" : "=v"(cpf)); _Pragma("unroll") for (int i = 0; i < 8; ++i) asm volatile("" : "=v"(ra[i])); } while (0)
  UNDEF_RA();
  if (wv < 4) {
    const int vt = lane * 16;
    int sx = (32 * (8 * (un >> 5) + 2 * wv) + (un & 31)) * (D_ * 2), sy = sx + 32 * D_ * 2;
    const rsrc_t rt = mkrs(&g_tab, sizeof(Tab));
    ra[2] = ld_in(rt, vt, 0); ra[3] = ld_in(rt, vt + 1024, 0);
    PINS(sx); PINS(sy);
    SB();
    const rsrc_t rx = mkrs(p.x, S_ * D_ * 2);
    ra[0] = ld_once(rx, vt, sx); ra[1] = ld_once(rx, vt, sy);
    SB();
#pragma unroll
    for (int i = 0; i < 4; ++i) ra[4 + i] = ld_in(rt, vt + i * 1024, 2048);
    cpf = ld_dev1(mkrs(p.ws, WS_END), 0, WS_E);
    STAMP(0);
  }
  PINS(wv);
  if (wv >= 6) {
    unsigned long long pc0;
    asm volatile("s_getpc_b64 %0" : "=s"(pc0));
    cpf = touch(mkrs((const void*)(pc0 & ~127ull), CODE_LINES * 128), lane < 9 ? (((wg >> 3) ^ (wv & 1)) * 9 + lane) : 4096);
    unsigned long long te;
    asm volatile("s_waitcnt lgkmcnt(0)\n\ts_mov_b64 %0, %1" : "=s"(te) : "s"(t_entry));
    st_dev(mkrs(p.ws, WS_END), (tid >= 504 && wg < 8) ? WS_T + wg * 128 + (lane & 7) * 16 : OOB, v4u{(u32)te, (u32)te, (u32)te, (u32)te});
  }
  PINS(wv);
  if ((wv >> 1) == 2) {
#define WAUX 0
#define WSLEEP 2
    __builtin_amdgcn_s_sleep(WSLEEP);
    const rsrc_t rwr = mkrs(p.wr, S_ * KF * 2), rwi = mkrs(p.wi, S_ * KF * 2);
    const int vf = (lane & 7) * (KF * 2) + (lane & 1) * 2 + ((tid & 127) >> 3) * 8;
    const int sf = (16 * (un >> 6) + 2 * (un & 4) + 256 * ((un >> 3) & 7)) * (KF * 2) + (un & 3) * 128;
#pragma unroll
    for (int r = 0; r < 4; ++r) { ra[r] = __builtin_amdgcn_raw_buffer_load_b128(rwr, vf, sf + r * (64 * KF * 2), WAUX); ra[4 + r] = __builtin_amdgcn_raw_buffer_load_b128(rwi, vf, sf + r * (64 * KF * 2), WAUX); }
  }
  const lp_t fmine = lptr(SCR + L_F + tid * 4);
  *(volatile LAS u32*)fmine = 0;
  lds_w128(lptr(SCR + L_IM + (tid & 127) * 128 + (((6 + ((tid >> 7) & 1)) ^ (tid + 2 * (((tid & 127) * 43) >> 10))) & 7) * 16), v4u{0u, 0u, 0u, 0u});
  BAR();
  PINS(t_entry);
  u32 t_ref = (u32)t_entry, tb_ = 0;
  v4u wa_;
  asm volatile("" : "=v"(wa_));
  PINS(wv);
  if ((wv >> 1) == 2) {
    const int T = tid - 256, k = 64 * (un & 3) + 4 * (T >> 3) + (T & 3);
    wa_ = v4u{(u32)((T >> 3) * 512 + (T & 7) * 16 + WS_W + (un >> 2) * WREC + (un & 3) * 8192), (lane & 1) ? 0u : 16u, (u32)(SCR + im_chkw(k)), (u32)(SCR + im_chk(k))};
    PIN(wa_);
  }
  PINS(wv);
  if (wv >= 6) {
#define X0SLEEP 8
    __builtin_amdgcn_s_sleep(X0SLEEP);
    const rsrc_t r1 = mkrs((wv & 1) ? p.wu : p.wg, W1SZ);
    ra[0].x = touch(r1, lane < 43 ? wg * 43 + lane : 16384);
  }
  SB();
  PINS(wv);

  if (wv < 4) {
  const int w = wv, c = lane & 15, g = lane >> 4;
  const float fc = (float)c, fg4 = (float)(4 * g);
  const f32x4 z = {0.f, 0.f, 0.f, 0.f};
  f32x4 twc, tws;
  v4u fbre, fbsw;
  u32 tag;
  float mean[2];
  const rsrc_t ws = mkrs(p.ws, WS_END);
  const lp_t fall = lptr(SCR + L_F + (lane & 3) * 256 + lane * 4);
  const lp_t imr = lptr(SCR + L_IM + w * 3072 + (lane >> 3) * 128 + ((lane ^ ((lane >> 3) + 2 * w)) & 7) * 16);
  int vx0 = lane * 16 + WS_A + un * XREC + w * (22 * 128), vx2 = lane < 48 ? vx0 + 2048 : OOB;
  PIN(vx0); PIN(vx2);
  int vq8 = (lane & 7) * 128;
  PIN(vq8);
  u32 tq;
  v2u xk[2][2];
  f32x4 tfc[2], tfs[2], tic[2], tis[2];
  const float fj = (float)(16 * w) + fc;
#pragma unroll
  for (int r = 0; r < 4; ++r) {
#pragma unroll
      for (int m = 0; m < 2; ++m) {
        const float nf = (fg4 + (16 * m + r)) * (fj * (1.f / 2048)), ni = (fg4 + (float)(16 * w) + r) * ((fc + 16 * m) * (1.f / 2048));
        tfc[m][r] = cosr(nf); tfs[m][r] = sinr(nf); tic[m][r] = cosr(ni); tis[m][r] = sinr(ni);
      }
  }
#pragma unroll
  for (int i = 0; i < 2; ++i) { PIN(tfc[i]); PIN(tfs[i]); PIN(tic[i]); PIN(tis[i]); }
  SB();
  {
    const v4u xr[2] = {ra[0], ra[1]}, tb[6] = {ra[2], ra[3], ra[4], ra[5], ra[6], ra[7]};
    const u32 sc = (c & 1) ? NEG2 : 0u, sm = c < 8 ? NEGL : NEGH, p0 = c < 8 ? 0x0c0c0100u : 0x01000c0cu, p1 = c < 8 ? 0x0c0c0302u : 0x03020c0cu;
    v4u fare = {tb[0].x, tb[0].y, tb[0].x ^ sc, tb[0].y ^ sc}, faim = {tb[0].z, tb[0].w, tb[0].z ^ sc, tb[0].w ^ sc}, fbim, f0re, f0im;
    twc = __builtin_bit_cast(f32x4, tb[1]); tws = __builtin_bit_cast(f32x4, tb[2]); fbre = tb[3];
    lp_t t1w[4], t1c[4];
#pragma unroll
    for (int r = 0; r < 4; ++r) {
      const u32 a = tb[3][r], q = tb[4][r];
      fbsw[r] = __builtin_amdgcn_alignbit(a, a, 16);
      fbim[r] = fbsw[r] ^ sm;
      f0re[r] = __builtin_amdgcn_perm(q, q, p0); f0im[r] = __builtin_amdgcn_perm(q, q, p1);
      t1w[r] = lptr((tb[5][r] & 0xffffu) ^ (SCR + w * 8)); t1c[r] = lptr((tb[5][r] >> 16) + SCR);
    }
    float c0m = c == 0 ? 1.f / 16 : 0.f;
    lp_t xw = lptr(SCR + L_X + w * 2048 + lane * 16), xt = lptr(SCR + L_X + w * 2048 + lane * 8);
    lp_t xq = lptr(SCR + L_X + w * 2048 + (4 * c + g) * 8), f6 = lptr(SCR + L_F + (lane % 6) * 256 + lane * 4);
    tag = cpf + 1u;
    u32 tag0 = w ? 0u : tag;
    PIN(c0m); PIN(tag); PIN(tag0);
    *(LAS u32*)(fmine + L_TG) = tag;
    *(LAS u32*)(fmine + L_TG + 1024) = 0;
    PIN(fare); PIN(faim); PIN(fbre); PIN(fbim); PIN(f0re); PIN(f0im); PIN(twc); PIN(tws);
    STAMP(1);
    SB();
    v4u a[2];
#pragma unroll
    for (int rr = 0; rr < 2; ++rr) { lds_w128(xw + rr * 1024, xr[rr]); a[rr] = lds_tr2(xt + rr * 1024, xt + rr * 1024 + 512); }
    tq = ld_dev1(ws, vq8, WS_T);
    STAMP(2);
    float st[4] = {0.f, 0.f, 0.f, 0.f};
#pragma unroll
    for (int i = 0; i < 4; ++i)
#pragma unroll
      for (int rr = 0; rr < 2; ++rr) { const u32 v = xr[rr][i]; st[rr] = dot2(v, 0x3f803f80u, st[rr]); st[2 + rr] = dot2(v, v, st[2 + rr]); }
    SB();
    f32x4 ur[2], ui[2];
#pragma unroll
    for (int rr = 0; rr < 2; ++rr) { ur[rr] = mm16p(a[rr], fare, z); ui[rr] = mm16p(a[rr], faim, z); }
    wave_sums<4>(st);
    SB();
    v4u a2[2];
    f32x4 orr[2], oi[2], sr[2], si[2];
#pragma unroll
    for (int rr = 0; rr < 2; ++rr) {
      const f32x4 x = ur[rr] - st[rr] * c0m, y = ui[rr];
      a2[rr] = pk4(x * twc + y * tws, y * twc - x * tws);
      orr[rr] = mm16p(a2[rr], fbre, z); oi[rr] = mm16p(a2[rr], fbim, z); sr[rr] = mm16p(a2[rr], f0re, z); si[rr] = mm16p(a2[rr], f0im, z);
    }
    v4u o[2];
#pragma unroll
    for (int rr = 0; rr < 2; ++rr) {
      mean[rr] = st[rr] * (1.f / 512);
      const float rstd = __builtin_amdgcn_rsqf(fmaxf(st[2 + rr] * (1.f / 512) - mean[rr] * mean[rr], 0.f) + 1e-5f);
      orr[rr][0] = g == 0 ? sr[rr][0] : orr[rr][0]; oi[rr][0] = g == 0 ? si[rr][0] : oi[rr][0];
      o[rr] = pk4(orr[rr] * rstd, oi[rr] * rstd);
    }
#pragma unroll
    for (int r = 0; r < 4; ++r) { v2u v = {o[0][r], o[1][r]}; lds_w64(t1w[r], v); lds_add(t1c[r], v.x + v.y + tag0); }
    STAMP(3);
    flag_set(fmine, 1);
    v4u v0, v1, v2;
    FLAG_WAIT(f6, 1, v0 = lds_r128(imr); v1 = lds_r128(imr + 1024); v2 = lds_r128(imr + 2048));
    st_dev(ws, vx0, v0); st_dev(ws, vx0 + 1024, v1); st_dev(ws, vx2, v2);
    flag_set(fmine, 2);
#pragma unroll
    for (int i = 0; i < 4; ++i) xk[i >> 1][i & 1] = lds_r64(xq + (i >> 1) * 1024 + (i & 1) * 512);
  }
  int vge, vgo, vg = (lane >> 3) * WREC + (lane & 7) * 16;
  {
    const int pay = (lane >> 1) * XREC + rec_pos(un) * 32 + (lane & 1) * 16, chk = (lane & 1) ? OOB : ((lane >> 1) ^ 1) * XREC + 96 + rec_pos(un) * 4;
    vge = (lane & 2) ? chk : pay; vgo = (lane & 2) ? pay : chk;
  }
  const int sg = w * (64 * XREC) + (22 * (un >> 6) + rec_line(un)) * 128;
#define GATHER(BUF) do { xl[0] = ld_dev(ws, vge, BUF + sg); xl[1] = ld_dev(ws, vgo, BUF + sg); xl[2] = ld_dev(ws, vge, BUF + sg + 32 * XREC); xl[3] = ld_dev(ws, vgo, BUF + sg + 32 * XREC); } while (0)
#define VERIFY(extra) ({ u32 k_ = 0; _Pragma("unroll") for (int m = 0; m < 2; ++m) { _Pragma("unroll") for (int i = 0; i < 4; ++i) xg[m][i] = (lane & 2) ? xl[2 * m + 1][i] : xl[2 * m][i]; \
      k_ += (lane & 2) ? xl[2 * m].x : xl[2 * m + 1].x; lds_w128(iw + m * 1024, xg[m]); } \
      u32 t_ = sum4(sum4((extra) - k_, xg[0]), xg[1]); t_ += dppu<0xb1>(t_); t_ += dppu<0x4e>(t_); t_; })
  const lp_t iw = lptr(SCR + L_I2 + w * 2048 + lane * 16);
  const int sw = WS_W + w * 16 * WREC + un * 128;
  v4u wq[2];
  STAMP(4);
  SB();
  v4u f2a[2][2];
  {
    v4u f1a[4], f1b[4], f2b[2][2];
    const u32 sc = (c & 1) ? NEG2 : 0u;
#pragma unroll
    for (int r = 0; r < 4; ++r) {
#pragma unroll
      for (int ks = 0; ks < 2; ++ks) {
        const float n = (fg4 + (16 * ks + r)) * (fj * (1.f / 64));
        const float cs = cosr(n), sn = sinr(n);
        f1a[ks][r] = pk(cs, sn); f1b[ks][r] = pk(-sn, cs);
        f1a[ks + 2][r] = f1a[ks][r] ^ sc; f1b[ks + 2][r] = f1b[ks][r] ^ sc;
      }
      const float n = (fg4 + r) * (fc * (1.f / 32));
      const float cs = cosr(n), sn = sinr(n);
      const u32 a = pk(cs, sn), b = pk(-sn, cs), sr = (r & 1) ? NEG2 : 0u;
      f2a[0][0][r] = a; f2a[1][0][r] = a ^ sr; f2a[0][1][r] = a ^ sc; f2a[1][1][r] = a ^ sc ^ sr;
      f2b[0][0][r] = b; f2b[1][0][r] = b ^ sr; f2b[0][1][r] = b ^ sc; f2b[1][1][r] = b ^ sc ^ sr;
    }
#pragma unroll
    for (int i = 0; i < 4; ++i) { PIN(f1a[i]); PIN(f1b[i]); PIN(f2a[i >> 1][i & 1]); PIN(f2b[i >> 1][i & 1]); }
    lp_t i2r = lptr(SCR + L_I2 + ((g >> 1) * 32 + c) * 32 + (g & 1) * 16);
    lp_t exw = lptr(SCR + L_EX + w * 2048 + lane * 16), exr = lptr(SCR + L_EX + lane * 16);
    lp_t imw[2], imc = lptr(SCR + im_chk(64 * w + lane));
#pragma unroll
    for (int n = 0; n < 2; ++n) { const int k = 64 * w + 32 * (g >> 1) + 16 * n + c; imw[n] = lptr(SCR + im_unit(k, 2 * rec_pos(k) + (g & 1))); }
    vx0 += WS_B - WS_A; vx2 += WS_B - WS_A;
    PIN(vg); PIN(vge); PIN(vgo); PIN(vx0); PIN(vx2);
    tq -= t_ref; tq = tq < 150u ? tq : 0u;
    tq = max(tq, dppu<0xb1>(tq)); tq = max(tq, dppu<0x4e>(tq)); tq = max(tq, dppu<0x141>(tq));
    t_ref += __builtin_amdgcn_readfirstlane(tq);
    tb_ = t_ref;
    STAMP(5);
    WAIT1();
    STAMP(6);
    v4u xl[4], xg[2];
    f32x4 wr[2], wi[2];
    GATHER(WS_A);
#define WD_ 8
    SB();
    __builtin_amdgcn_s_sleep(WD_);
    wq[0] = ld_dev(ws, vg, sw); wq[1] = ld_dev(ws, vg, sw + 8 * WREC);
    flag_set(fmine, 3);
    SB();
#define KW() (((lane & 2) ? xl[0].w : xl[1].w) + ((lane & 2) ? xl[2].w : xl[3].w))
    u32 kw = KW(), spin = 0;
    u32 bad = VERIFY(tag + kw);
    if (__any(bad != 0)) {
      _Pragma("nounroll") do {
        MEMFENCE();
        if (bad) GATHER(WS_A);
        kw = KW();
        bad = VERIFY(tag + kw);
      } while (__any(bad != 0) && ++spin < SPIN_CAP);
      SLIPPED();
      ++spin;
    }
    COUNT(20, spin);
    STAMP(7);
    v4u af[2][4], b2[2], a3[2];
    flag_set(fmine, 4);
    FLAG_WAIT(fall, 4, _Pragma("unroll") for (int i = 0; i < 8; ++i) af[i >> 2][i & 3] = lds_r128(i2r + (i & 3) * 2048 + (i >> 2) * 512));
    STAMP(8);
    SB();
    f32x4 dr[2], di[2];
#pragma unroll
    for (int m = 0; m < 2; ++m)
#pragma unroll
      for (int ks = 0; ks < 4; ++ks) { dr[m] = mm16(af[m][ks], f1a[ks], ks ? dr[m] : z); di[m] = mm16(af[m][ks], f1b[ks], ks ? di[m] : z); }
#pragma unroll
    for (int m = 0; m < 2; ++m) b2[m] = pk4(dr[m] * tfc[m] + di[m] * tfs[m], di[m] * tfc[m] - dr[m] * tfs[m]);
#pragma unroll
    for (int k = 0; k < 2; ++k)
#pragma unroll
      for (int m = 0; m < 2; ++m) { dr[m] = mm16(f2a[m][k], b2[k], k ? dr[m] : z); di[m] = mm16(f2b[m][k], b2[k], k ? di[m] : z); }
    {
#define W_BAD() ({ u32 t_ = sum4(sum4(0u - kw, wq[0]), wq[1]); t_ += dppu<0xb1>(t_); t_ += dppu<0x4e>(t_); t_ += dppu<0x141>(t_); t_; })
      u32 bw = W_BAD();
#pragma unroll
      for (int m = 0; m < 2; ++m) { PIN(dr[m]); PIN(di[m]); }
      if (__builtin_expect(__any(bw != 0), 0)) {
        u32 sp = 0;
        _Pragma("nounroll") do { MEMFENCE(); if (bw) { wq[0] = ld_dev(ws, vg, sw); wq[1] = ld_dev(ws, vg, sw + 8 * WREC); } bw = W_BAD(); } while (__any(bw != 0) && ++sp < SPIN_CAP);
      }
#pragma unroll
      for (int m = 0; m < 2; ++m)
#pragma unroll
        for (int r = 0; r < 4; ++r) { const u32 v = wq[m][r]; wr[m][r] = blo(v); wi[m][r] = bhi(v); }
    }
#pragma unroll
    for (int m = 0; m < 2; ++m) a3[m] = pk4(dr[m] * wi[m] + di[m] * wr[m], dr[m] * wr[m] - di[m] * wi[m]);
#pragma unroll
    for (int k = 0; k < 2; ++k)
#pragma unroll
      for (int n = 0; n < 2; ++n) { di[n] = mm16(a3[k], f2a[n][k], k ? di[n] : z); dr[n] = mm16(a3[k], f2b[n][k], k ? dr[n] : z); }
#pragma unroll
    for (int n = 0; n < 2; ++n) lds_w128(exw + n * 1024, pk4(di[n] * tic[n] + dr[n] * tis[n], dr[n] * tic[n] - di[n] * tis[n]));
    STAMP(9);
    flag_set(fmine, 5);
    FLAG_WAIT(fall, 5, _Pragma("unroll") for (int i = 0; i < 8; ++i) af[i >> 2][i & 3] = lds_r128(exr + (i & 3) * 2048 + (i >> 2) * 1024));
    SB();
    u32 cs[2];
#pragma unroll
    for (int n = 0; n < 2; ++n)
#pragma unroll
      for (int ks = 0; ks < 4; ++ks) { di[n] = mm16(f1a[ks], af[n][ks], ks ? di[n] : z); dr[n] = mm16(f1b[ks], af[n][ks], ks ? dr[n] : z); }
#pragma unroll
    for (int n = 0; n < 2; ++n) {
      const v4u e = pk4(dr[n], di[n]);
      lds_w128(imw[n], e);
      cs[n] = sum4(0u, e);
    }
    const auto sx = __builtin_amdgcn_permlane16_swap(cs[0], cs[1], false, false);
    *(LAS u32*)imc = tag + sx[0] + sx[1];
    MEMFENCE();
    const v4u v0 = lds_r128(imr), v1 = lds_r128(imr + 1024), v2 = lds_r128(imr + 2048);
    st_dev(ws, vx0, v0); st_dev(ws, vx0 + 1024, v1); st_dev(ws, vx2, v2);
    flag_set(fmine, 6);
    if (wg == 0 && w == 0) st_dev1(ws, WS_E, tag);
  }
  STAMP(10);
  SB();
  {
    v4u gbre, gbim, g0re, g0im, ga[2];
#pragma unroll
    for (int r = 0; r < 4; ++r) {
      gbre[r] = fbre[r] ^ (g < 2 ? NEGH : 0u);
      gbim[r] = fbsw[r] ^ (g < 2 ? 0u : NEGH);
      const float n = fc * ((31.f - 2.f * r - 2.f * fg4) * (1.f / 32));
      u32 q = pk(cosr(n), -sinr(n));
      PIN(q);
      g0re[r] = g < 2 ? gbre[r] : 0u;
      g0im[r] = g < 2 ? 0u : q;
      ga[0][r] = f2a[0][0][r] ^ NEGH; ga[1][r] = f2a[1][0][r] ^ NEGH;
    }
    twc *= 2.f / (2048.f * 512.f); tws *= 2.f / (2048.f * 512.f);
    g0re[0] = g == 0 ? ((c & 1) ? 0xbf00u : 0x3f00u) : g0re[0];
    ga[0][0] = g == 0 ? ((c & 1) ? 0xbf803f80u : 0x3f803f80u) : ga[0][0]; ga[1][0] = ga[0][0];
    PIN(gbre); PIN(gbim); PIN(g0re); PIN(g0im); PIN(ga[0]); PIN(ga[1]); PIN(twc); PIN(tws);
    f32x4 xin[2][2];
#pragma unroll
    for (int rr = 0; rr < 2; ++rr)
#pragma unroll
      for (int t = 0; t < 2; ++t) {
        const v2u v = xk[rr][t];
        const u32 v0 = v.x, v1 = v.y;
        f32x4 q = {blo(v0), bhi(v0), blo(v1), bhi(v1)};
        xin[rr][t] = q - mean[rr]; PIN(xin[rr][t]);
      }
    lp_t i3r[4], o3w[2], o3r[2];
#pragma unroll
    for (int r = 0; r < 4; ++r) i3r[r] = lptr(SCR + L_I3 + slot_of(c, 4 * g + r) * 32 + w * 8);
#pragma unroll
    for (int i = 0; i < 2; ++i) {
      o3w[i] = lptr(SCR + L_O3 + ((4 * c + g) * 8 + ((2 * w + i) ^ (c & 7))) * 16);
      const int q = w * 128 + i * 64 + lane;
      o3r[i] = lptr(SCR + L_O3 + (q ^ ((q >> 5) & 7)) * 16);
    }
    int vs = lane * 16 + WS_L + un * LREC + w * 2048, vt = lane < 8 ? lane * 16 + WS_S + un * 512 + w * 128 : OOB;
    PIN(vs); PIN(vt);
    lp_t sow = lptr(SCR + L_SO + tid * 16), sor = lptr(SCR + L_SO + (lane & 6) * 512 + (lane & 1) * 8);
    WAIT2();
    STAMP(11);
    v4u xl[4], xg[2];
    GATHER(WS_B);
    flag_set(fmine, 7);
    SB();
    u32 bad = VERIFY(tag), spin = 0;
    if (__any(bad != 0)) {
      _Pragma("nounroll") do {
        __builtin_amdgcn_s_sleep(RS1);
        MEMFENCE();
        if (bad) GATHER(WS_B);
        bad = VERIFY(tag);
      } while (__any(bad != 0) && ++spin < SPIN_CAP);
      SLIPPED();
      ++spin;
    }
    COUNT(21, spin);
    STAMP(12);
    v4u a[2], a2[2];
    v2u zr[4];
    flag_set(fmine, 8);
    FLAG_WAIT(fall, 8, _Pragma("unroll") for (int r = 0; r < 4; ++r) zr[r] = lds_r64(i3r[r]));
    STAMP(13);
#pragma unroll
    for (int r = 0; r < 4; ++r) { a[0][r] = zr[r].x; a[1][r] = zr[r].y; }
    f32x4 tr[2], ti[2], sr[2], si[2], h[2][2];
#pragma unroll
    for (int rr = 0; rr < 2; ++rr) { tr[rr] = mm16p(a[rr], gbre, z); ti[rr] = mm16p(a[rr], gbim, z); sr[rr] = mm16p(a[rr], g0re, z); si[rr] = mm16p(a[rr], g0im, z); }
#pragma unroll
    for (int rr = 0; rr < 2; ++rr) {
      tr[rr][0] = g == 0 ? sr[rr][0] : tr[rr][0]; ti[rr][0] = g == 0 ? si[rr][0] : ti[rr][0];
      a2[rr] = pk4(tr[rr] * twc - ti[rr] * tws, ti[rr] * twc + tr[rr] * tws);
      h[rr][0] = mm16p(a2[rr], ga[0], xin[rr][0]); h[rr][1] = mm16p(a2[rr], ga[1], xin[rr][1]);
    }
    float st[2];
#pragma unroll
    for (int rr = 0; rr < 2; ++rr) { const f32x4 q = h[rr][0] * h[rr][0] + h[rr][1] * h[rr][1]; st[rr] = (q[0] + q[2]) + (q[1] + q[3]); }
    wave_sums<2>(st);
    v4u so;
#pragma unroll
    for (int rr = 0; rr < 2; ++rr) {
      const float ve = st[rr] * (1.f / 512) + 1e-5f, rstd = __builtin_amdgcn_rsqf(ve);
      const f32x4 l0 = h[rr][0] * rstd, l1 = h[rr][1] * rstd;
      v4u l4 = {pk(l0[0], l0[1]), pk(l0[2], l0[3]), pk(l1[0], l1[1]), pk(l1[2], l1[3])};
      lds_w128(o3w[rr], l4);
      so[2 * rr] = asu(mean[rr]); so[2 * rr + 1] = asu(ve * rstd);
    }
    lds_w128(sow, so);
    STAMP(14);
    v4u v0, v1;
    v2u ms;
    flag_set(fmine, 9);
    FLAG_WAIT(fall, 9, v0 = lds_r128(o3r[0]); v1 = lds_r128(o3r[1]); ms = lds_r64(sor));
    st_dev(ws, vs, v0); st_dev(ws, vs + 1024, v1);
    u32 s = sum4(sum4(0u, v0), v1);
    s += dppu<0x128>(s); s = add_x32(add_x16(s));
    v4u rec = {tag - s - ms.x - ms.y, ms.x, ms.y, tag};
    st_dev(ws, vt, rec);
    flag_set(fmine, 10);
  }
  SB();
  {
#define LOOKN 6
    constexpr int LOOK = LOOKN;
    v4u ring[8], act[32];
    lp_t wa[4], wb[4];
#pragma unroll
    for (int t = 0; t < 4; ++t) wa[t] = lptr(t * SLOT + g * 512 + ((c ^ (2 * t + (c >> 3))) * 16));
#pragma unroll
    for (int i = 0; i < 4; ++i) wb[i] = lptr(((i & 1) ? 0 : 4 * SLOT) + ((lane >> 2) ^ (g & 1)) * 64 + (((i >> 1) ^ g) & 1) * 32 + (lane & 3) * 8);
    auto frag = [&](auto NN) -> v4u {
      constexpr int N = NN.value;
      if constexpr (N < 64) return lds_r128(wa[(N >> 1) & 1] + (N >> 2) * 2048 + (N & 1) * 256);
      else if constexpr (N < 128) return lds_r128(wa[N >> 5] + ((N & 31) >> 1) * 2048 + (N & 1) * 256);
      else { const lp_t b = wb[((N >> 4) & 2) + (N & 1)] + ((N & 31) >> 1) * 2048; return lds_tr2(b, b + 1024); }
    };
    int vo = WS_H + ((4 * rg + w) * NFT + ft0) * HREC + lane * 16, vk = (lane & 11) ? OOB : vo - lane * 16 + 1024 + ((lane >> 4) * 8 + (lane & 7)) * 4;
    int vo4 = vo + 4 * HREC, vk4 = vk + 4 * HREC;
    PIN(vo); PIN(vk); PIN(vo4); PIN(vk4);
    Epi e;
#define EPI(J0, J1) static_for<(J0), (J1)>([&](auto JJ) { constexpr int kd = kEp.op[JJ.value].kind, V = kEp.op[JJ.value].v, t = V >> 3; \
      if constexpr (kd == E_ST) st_dev(ws, (t < 4 ? vo : vo4) + (t & 3) * HREC, e.r); \
      else if constexpr (kd == E_STK) st_dev(ws, (t < 4 ? vk : vk4) + (t & 3) * HREC, e.k4); \
      else epi_op<kd, V>(e, t % 3 == 0 ? tA : t % 3 == 1 ? tB : tC, tag); \
      SB(); })
    int va0 = ((lane >> 3) & 1) * LREC + g * 128 + (lane & 7) * 16, va1 = va0 + 2 * LREC;
    int vr = ((lane >> 3) & 1) * 512 + g * 128 + (lane & 7) * 16;
    PIN(vr);
    const int sa = WS_L + (4 * rg + w) * 4 * LREC;
#define ACT_LD(i) act[i] = ld_dev(ws, (((i) & 1) ? va1 : va0) + (((i) >> 1) & 7) * 512, sa + ((i) >> 4) * 4096)
    f32x4 tA[4], tB[4], tC[4];
    WD_SETUP();
    v4u s0, s1;
#define A_HEAD() do { MEMFENCE(); PIN(va0); PIN(va1); s0 = ld_dev(ws, vr, WS_S + (4 * rg + w) * 2048); s1 = ld_dev(ws, vr + 1024, WS_S + (4 * rg + w) * 2048); SB(); \
      static_for<0, 2 * LOOK>([&](auto K) { ACT_LD(K.value); SB(); }); } while (0)
    WAIT3();
    STAMP(15);
    u32 spin = 0;
    A_HEAD();
    BAR();
    for (;; ++spin) {
      static_for<0, PD>([&](auto K) { ring[K.value ^ 1] = FRAG(K.value ^ 1); SB(); });
      if (__builtin_expect(__any(s0.w != tag || s1.w != tag), 0)) { _Pragma("nounroll") do { __builtin_amdgcn_s_sleep(RS3); A_HEAD(); } while (__any(s0.w != tag || s1.w != tag) && ++spin < SPIN_CAP); ++spin; }
      PIN(va0); PIN(va1);
      u32 m = (s0.x + s0.y + s0.z) + (s1.x + s1.y + s1.z);
      static_for<0, 64>([&](auto K) {
        constexpr int n = K.value, ks = n >> 2, q = (n & 1) * 2;
        f32x4* acc = (n & 2) ? tB : tA;
        mma<ks == 0>(acc[q], ring[n & 7], act[2 * ks]); SB();
        ring[NXT(n) & 7] = FRAG(NXT(n));
        if constexpr (!(n & 1) && ks + LOOK < 16) ACT_LD(2 * (ks + LOOK) + ((n >> 1) & 1));
        SB();
        mma<ks == 0>(acc[q + 1], ring[n & 7], act[2 * ks + 1]); SB();
        { const v4u v = act[2 * ks + ((n >> 1) & 1)]; u32 t = (n & 1) ? m + v.z + v.w : m + v.x + v.y; PIN(t); m = t; }
        SB();
      });
      if (__builtin_expect(__all(add_x32(add_x16(m)) == 8u * tag) || spin > SPIN_CAP, 1)) break;
      s0.w = ~tag;
    }
    COUNT(22, spin);
    BAR_N(15);
    STAMP(16);
#define G1_TILE(T, acc, ...) static_for<0, 32>([&](auto K) { constexpr int s = K.value, n = 32 * (T) + s, ks = s >> 1, q = (s & 1) * 2, j0 = epos(n), j1 = epos(n + 1), jm = j0 + (j1 - j0 - (T) / 4) / 2; \
      mma<ks == 0>(acc[q], ring[n & 7], act[2 * ks]); SB(); \
      if constexpr (n + PD < 192) ring[NXT(n) & 7] = FRAG(n + PD < 192 ? NXT(n) : 0); SB(); \
      EPI(j0, jm); \
      mma<ks == 0>(acc[q + 1], ring[n & 7], act[2 * ks + 1]); SB(); \
      __VA_ARGS__; EPI(jm, j1); SB(); })
#define PG2 16
#define PG4 12
#define PG3 10
    G1_TILE(2, tC, { });
    FSTAMP(26);
    G1_TILE(3, tA, { if constexpr (s == PG2) BAR_N(15); });
    FSTAMP(27);
    if (__builtin_expect(six, 1)) {
      G1_TILE(4, tB, { });
      FSTAMP(28);
      G1_TILE(5, tC, { if constexpr (s == PG4) BAR_N(15); });
      STAMP(17);
      MMA_DONE(); SB();
      EPI(kEp.first[5], kEp.n);
    } else {
      MMA_DONE(); SB();
      EPI(epos(128), kEp.first[4]);
      static_for<E_X1, E_STK>([&](auto KK) { epi_op<KK.value, 31>(e, tA, tag); });
      st_dev(ws, vk + 3 * HREC, e.k4);
#define SKSL 14
      __builtin_amdgcn_s_sleep(SKSL);
      BAR_N(15);
      FSTAMP(28);
      __builtin_amdgcn_s_sleep(SKSL + 6);
      STAMP(17);
    }
    STAMP(18);
    BAR();
#define CSL 10
    __builtin_amdgcn_s_sleep(CSL);
    DMA_WDS(4, 0);
    DMA_WDS(5, 1);
    BAR();
    if (w & 1) VM_WAIT(4); else VM_WAIT(8);
    BAR();
    VM_WAIT(0);
    BAR();
  }
  STAMP(24);
  STAMP_FLUSH();
  UNDEF_RA();
  asm volatile("" : "=v"(wa_));
  }
  PINS(wv);
  if (wv >= 4) {
    STAMP(0);
    const int hw = wv - 4;
    asm volatile("" :: "v"(cpf), "v"(ra[0]));
    if (hw < 2) {
    const rsrc_t ws = mkrs(p.ws, WS_END);
    const int T = tid - 256, vs = wa_.x;
    const u32 sh = wa_.y;
    const lp_t ca0 = (lp_t)(size_t)wa_.z, ca1 = (lp_t)(size_t)wa_.w;
    u32 al[8][2], ck[4];
#pragma unroll
    for (int i = 0; i < 8; ++i) { const u32 a = ra[i].x, b = ra[i].y, d = ra[i].z; al[i][0] = __builtin_amdgcn_alignbit(b, a, sh); al[i][1] = __builtin_amdgcn_alignbit(d, b, sh); }
    v4u v[4];
#pragma unroll
    for (int e = 0; e < 4; ++e) {
#pragma unroll
      for (int r = 0; r < 4; ++r) v[e][r] = __builtin_amdgcn_perm(al[4 + r][e >> 1], al[r][e >> 1], (e & 1) ? 0x07060302u : 0x05040100u);
      st_dev(ws, vs + e * 128, v[e]);
      ck[e] = sum4(0u, v[e]);
    }
    const u32 c0 = pair_total(ck[0], ck[1], T & 1), c1 = pair_total(ck[2], ck[3], T & 1);
    const u32 cq = ((T & 2) ? c1 : c0) + dppu<0x4e>((T & 2) ? c0 : c1);
    lds_add(ca0, cq); lds_add(ca1, cq);
    flag_set(fmine, 1);
    STAMP(1);
  }
    u32 tag;
    v4u y0[8];
    WD_SETUP();
    {
      const rsrc_t rwg = mkrs(p.wg, W1SZ), rwu = mkrs(p.wu, W1SZ);
      const int vx = 4 * (lane >> 3) * (F_ * 2) + ft0 * 32 + (lane & 7) * 16, vx8 = vx + 32 * F_ * 2;
      const int vy = six ? 4 * (lane >> 2) * (F_ * 2) + (ft0 + 4) * 32 + (lane & 3) * 16 : OOB;
      const u32 ax = ((lane >> 1) & 3) * SLOT + (16 * hw + (lane >> 3)) * 512 + (lane & 1) * 128 + (lane & 7) * 16;
#define PUT_X(u, off) do { transpose8(u); const u32 a_ = ax + (off); _Pragma("unroll") for (int i = 0; i < 8; ++i) lds_w128((lp_t)(size_t)(a_ ^ (i * 16)), u[i]); SB(); } while (0)
      v4u rb[8], rc[8], rd[8];
      const lp_t fall = lptr(SCR + L_F + (lane & 3) * 256 + lane * 4);
      const int s0 = 64 * hw * (F_ * 2);
#define THR 4
#define NA 2
#define N0 0
#define NS 7
#define NL 99
#define YD 1
#define THRU 4
#define NH 2
#define TS 0
#define F1 2
#define F2 5
#define STEP(n) do { constexpr int j = (n) & 7; const int so_ = s0 + ((j & 3) + 256 * (j >> 2)) * (F_ * 2); \
        if ((n) == NA) { flag_wait_lazy(fall, 3); cpf = touch(rwd, lane < 11 ? wg * 43 + hw * 11 + lane : 16384); } if ((n) == NB) flag_wait_lazy(fall, 7); \
        if ((n) == NA + NH) flag_wait_lazy(fall, 4); if ((n) == NB + NH) flag_wait_lazy(fall, 8); \
        if ((n) == N0) flag_wait_lazy(fall, 2); if ((n) == NS) flag_wait_lazy(fall, 6); if ((n) == NL) flag_wait_lazy(fall, 10); \
        if ((n) > 1) asm volatile("s_waitcnt vmcnt(%0)" :: "n"((YD && (n) > 8) ? THRU : THR) : "memory"); \
        if ((n) < 8) { ra[j] = ld_in(rwg, vx, so_); rb[j] = ld_in(rwg, vx8, so_); y0[j] = ld_in(rwg, vy, so_); } \
        else { constexpr int j2 = (2 * (n)) & 7; const int sp_ = s0 + ((j2 & 3) + 256 * (j2 >> 2)) * (F_ * 2); \
          if ((n) < 12) { rc[j2] = ld_in(rwu, vx, sp_); rc[j2 + 1] = ld_in(rwu, vx, sp_ + F_ * 2); } else { rd[j2] = ld_in(rwu, vx8, sp_); rd[j2 + 1] = ld_in(rwu, vx8, sp_ + F_ * 2); } } SB(); } while (0)
      PACE(0, TS);
      STAMP(2);
      STEP(0); STEP(1); STEP(2); STEP(3); STEP(4); STEP(5); STEP(6); STEP(7); STEP(8); STEP(9);
      STAMP(3);
      if (NB > 10) STEP(10);
      if (NB > 11) STEP(11);
      PUT_X(ra, 0);
      PUT_X(rb, 4096);
      if (NB <= 10) STEP(10);
      if (NB <= 11) STEP(11);
      STEP(12); STEP(13); STEP(14); STEP(15);
      PUT_X(rc, 256);
      tag = *(LAS u32*)(fall + L_TG);
      STAMP(4);
      PUT_X(rd, 4096 + 256);
      PIN(tag);
      asm volatile("" :: "v"(cpf));
    }
    STAMP(5);
    BAR();
    const rsrc_t ws = mkrs(p.ws, WS_END);
    const int kh = hw & 1, rl = hw >> 1, g = lane >> 4;
    constexpr int PD = 4;
    const u32 ayo = hw * 8192 + g * 2048 + ((lane >> 2) & 3) * 256 + (lane & 4) * 16 + ((lane & 3) ^ ((lane >> 1) & 2)) * 16;
    const lp_t ay0[2] = {lptr(ayo + 4 * SLOT), lptr((ayo ^ 64) + 4 * SLOT)}, ad = lptr(3 * SLOT + hw * 8192 + lane * 16);
#define PUT_Y(a, y, j0, j1) _Pragma("unroll") for (int j = j0; j < j1; ++j) lds_w128(a[j & 1] + (j >> 2) * 1024 + (j & 2) * 64, y[j])
    PUT_Y(ay0, y0, 0, 8);
    v4u ring[8], hfA[8], hfB[8], hfC[8], hfD[8], hfE[8];
    u32 hkA, hkB, hkC, hkD, hkE, ms = 0, nfix = 0;
    lp_t wd[3][4];
    {
      const u32 dl = (g * 1024 + ((lane >> 2) & 3) * 128 + ((lane >> 3) & 3) * 32 + (lane & 3) * 8) ^ (kh * 64);
#pragma unroll
      for (int i = 0; i < 12; ++i) wd[i >> 2][i & 3] = lptr((dl ^ ((i & 3) * 32)) + (i >> 2) * 2 * SLOT + kh * 16384);
    }
    auto frag = [&](auto NN) -> v4u {
      constexpr int N = NN.value, t = N < 64 ? N >> 4 : N < 76 ? 4 : 5, s = N < 64 ? (N & 15) : N < 76 ? N - 64 : N - 76, slot = (t + 1) % 5;
      const lp_t b = wd[slot >> 1][s & 3] + (slot & 1) * SLOT + (s >> 2) * 4096;
      return lds_tr2(b, b + 512);
    };
    const int hb = WS_H + (4 * rg + 2 * (ch >> 3) + rl) * NFT * HREC;
    int hs[4];
#pragma unroll
    for (int i = 0; i < 4; ++i) { hs[i] = hb + tile0_of(8 * kh + 2 * i) * HREC; PINS(hs[i]); }
    int vl = (g & 1) * 512 + (lane & 15) * 16 + (g >> 1) * 6 * HREC, vl3 = vl - (g >> 1) * 2 * HREC, vl4 = vl + 4 * HREC, vlz = (kh && g >= 2) ? OOB : vl4, vq, vq4;
    {
      const int st = (lane & 2) + ((lane >> 1) & 4) + (g >> 1);
      vq = hb + 1024 + ((2 * (g & 1) + (lane & 1)) * 8 + (lane & 4)) * 4 + tile0_of(8 * kh + st) * HREC;
      vq4 = st < 6 - kh ? vq + 4 * HREC : OOB;
    }
    const int u = 16 * rg + 8 * (ch >> 3) + 4 * rl + ((lane >> 3) & 1), col = 64 * CT2 + 32 * kh;
    int vh = u * LREC + (col & 255) * 32 + g * 128 + (lane & 7) * 16 + (col >> 8) * 8, vs = u * 512 + (lane & 7) * 16 + 4;
    int vout = ((32 * (8 * (u >> 5) + (lane & 7)) + (u & 31)) * D_ + col + 16 * (g & 1) + 8 * (g >> 1)) * 2;
    lp_t rw = lptr(2 * SLOT + hw * 4096 + lane * 16), rr_ = lptr(2 * SLOT + (hw ^ 1) * 4096 + lane * 16);
    int vyd = ((lane >> 2) ^ (g & 1)) * (F_ * 2) + (ft0 + 4) * 32 + ((lane & 3) ^ ((lane >> 3) & 2)) * 16;
    PIN(vyd);
    u32 t8 = 8u * tag, t6 = ((kh && g >= 2) ? 4u : 6u) * tag;
    PIN(vl); PIN(vl3); PIN(vl4); PIN(vlz); PIN(vq); PIN(vq4); PIN(vh); PIN(vs); PIN(vout); PIN(t8); PIN(t6);
#define H_LD(X, t, j) do { \
      if constexpr ((j) == 8) hk##X = ld_dev1(ws, ((t) < 4 ? vq : vq4) + ((t) & 3) * HREC, 0); \
      else if constexpr ((t) < 4 || (j) < 6) hf##X[(j) & 7] = ld_dev(ws, ((t) < 4 ? ((j) < 6 ? vl : vl3) : (j) < 4 ? vl4 : vlz) + ((t) & 3) * HREC + ((j) & 1) * 256, hs[((j) >> 1) & 3]); } while (0)
#define H_LDALL(X, t) static_for<0, 9>([&](auto J) { THROTTLE(J.value); H_LD(X, t, J.value ? J.value - 1 : 8); })
#define H_SUM(X, nf, j) do { if constexpr ((j) >= 0 && (j) < (nf)) { ms = sum4((j) ? ms : hk##X, hf##X[(j) & 7]); u32 t_ = ms; PIN(t_); ms = t_; } \
      else if constexpr ((j) >= (nf) && (j) < (nf) + 3) { u32 t_ = ms + dppu<(j) == (nf) ? 0xb1 : (j) == (nf) + 1 ? 0x4e : 0x128>(ms); PIN(t_); ms = t_; } } while (0)
#define H_SUMALL(X, nf) static_for<0, (nf) + 3>([&](auto J) { H_SUM(X, nf, J.value); })
#define H_FIX(X, t, nf, want) do { if (__builtin_expect(__any(ms != (want)), 0)) { u32 spin_ = 0; _Pragma("nounroll") do { __builtin_amdgcn_s_sleep(RS3); MEMFENCE(); nfix |= 1u << (4 * (t)); \
        H_LDALL(X, t); H_SUMALL(X, nf); } while (__any(ms != (want)) && ++spin_ < SPIN_CAP); } SB(); } while (0)
    v4u d3[8];
    f32x4 o[4][2];
    v2u hr[4], so[2];
#define GRP() asm volatile("s_waitcnt vmcnt(%0)" :: "n"(TH2) : "memory")
#define RAW4(dst, t, i0) do { _Pragma("unroll") for (int i = i0; i < i0 + 4; ++i) { THROTTLE(i); dst[i] = ld_in(rwd, (i & 1) ? vd1 : vd, sd[i >> 1] + ((t) * 16 + (i & 1) * 8) * (D_ * 2)); } SB(); } while (0)
#define DMA4(t, slot, i0) do { _Pragma("unroll") for (int i = i0; i < i0 + 4; ++i) { THROTTLE(i); DMA_WD1(t, slot, i); } } while (0)
#define H4(X, t, h) do { static_for<5 * (h), 5 + 4 * (h)>([&](auto J) { if (J.value != 4) THROTTLE(J.value - 5 * (h)); H_LD(X, t, J.value ? J.value - 1 : 8); }); SB(); } while (0)
#define PUT_D(d, off, i0, i1) do { _Pragma("unroll") for (int i = i0; i < i1; ++i) lds_w128(ad + (off) + i * 1024, d[i]); SB(); } while (0)
#define RESID(i) do { const u32 v0 = hr[i].x, v1 = hr[i].y, mb = so[(i) & 1].x, sb = so[(i) & 1].y; float q[4] = {blo(v0), bhi(v0), blo(v1), bhi(v1)}; \
      _Pragma("unroll") for (int r = 0; r < 4; ++r) { q[r] = __builtin_fmaf(q[r], asf(sb), asf(mb)); PIN(q[r]); o[(i) >> 1][(i) & 1][r] = q[r]; } PIN(o[(i) >> 1][(i) & 1]); SB(); } while (0)
#define PSLEEP 16
    __builtin_amdgcn_s_sleep(PSLEEP);
#define SPRIO 3
#define SPRIO1 0
#define LOWP() __builtin_amdgcn_s_setprio(SPRIO1)
#define HIGHP() __builtin_amdgcn_s_setprio(SPRIO)
#define SPRIO2 3
    __builtin_amdgcn_s_setprio(SPRIO);
    RAW4(d3, 3, 0); RAW4(d3, 3, 4);
    STAMP(6);
    BAR();
    if (YD && six) {
      const rsrc_t rwu = mkrs(p.wu, W1SZ);
#pragma unroll
      for (int i = 0; i < 8; ++i) {
        asm volatile("s_waitcnt vmcnt(4)\n\ts_mov_b32 m0, %0\n\ts_nop 0\n\tbuffer_load_dwordx4 %1, %2, %3 offen lds"
                     :: "s"(hw * 8192 + i * 1024), "v"(vyd), "s"(rwu), "s"((16 * (4 * hw + (i >> 1)) + 256 * (i & 1)) * (F_ * 2)) : "memory");
      }
    }
#pragma unroll
    for (int i = 0; i < 2; ++i) so[i] = __builtin_amdgcn_raw_buffer_load_b64(ws, vs + i * 1024, WS_S, AUX_SC1);
#pragma unroll
    for (int i = 0; i < 4; ++i) hr[i] = __builtin_amdgcn_raw_buffer_load_b64(ws, vh + (i >> 1) * 512, WS_L + (i & 1) * 2 * LREC, AUX_SC1);
    SB();
    DMA4(0, 1, 0); DMA4(0, 1, 4);
    if (YD) VM_WAIT(8);
    STAMP(7);
    BAR();
    PIN(hr[3]);
    DMA4(1, 2, 0); DMA4(1, 2, 4); LOWP(); RESID(0); RESID(1); RESID(2); RESID(3);
    HIGHP();
    H4(A, 0, 0); H4(A, 0, 1);
#define BSL 0
    __builtin_amdgcn_s_sleep(BSL);
    H4(B, 1, 0); H4(B, 1, 1);
    PIN(hfA[7]);
    STAMP(8);
    BAR();
    DMA4(2, 3, 0); DMA4(2, 3, 4);
    LOWP();
    H_SUMALL(A, 8);
    H_FIX(A, 0, 8, t8);
    HIGHP();
    STAMP(9);
    H4(C, 2, 0); H4(C, 2, 1);
    VM_WAIT(9);
    H_SUMALL(B, 8);
    H_FIX(B, 1, 8, t8);
    static_for<0, PD>([&](auto K) { ring[K.value ^ 1] = FRAG(K.value ^ 1); SB(); });
    STAMP(10);
    LOWP();
    STAMP(11);
#define G2_ROUND(X, n0, ns, ...) static_for<0, ns>([&](auto K) { constexpr int s = K.value, n = (n0) + s, ct = s & 3, a = (s >> 2) * 2; \
      mma<n == 2 || n == 3>(o[ct][0], ring[n & 7], hf##X[a]); SB(); \
      if constexpr (n + PD < 88) ring[NXT(n) & 7] = FRAG(n + PD < 88 ? NXT(n) : 0); SB(); \
      mma<n == 2 || n == 3>(o[ct][1], ring[n & 7], hf##X[a + 1]); SB(); \
      __VA_ARGS__; SB(); })
#define PP 0
#define L3 16
#define L4 36
#define L5 50
#define LATE() do { if constexpr (n >= L3 && n < L3 + 9) H_LD(D, 3, n > L3 ? n - L3 - 1 : 8); \
      if constexpr (n >= L4 && n < L4 + 7) H_LD(E, 4, n > L4 ? n - L4 - 1 : 8); if constexpr (n >= L5 && n < L5 + 7) H_LD(A, 5, n > L5 ? n - L5 - 1 : 8); } while (0)
#define PG5 15
#define PS 1
#define MID() do { if constexpr (n == PG5) { BAR_N(15); __builtin_amdgcn_s_setprio(SPRIO2); } \
      if constexpr (n > PG5 && n <= PG5 + 8 * PS && (n - PG5 - 1) % PS == 0) lds_w128(ad + SLOT + ((n - PG5 - 1) / PS) * 1024, d3[((n - PG5 - 1) / PS) & 7]); \
      if constexpr (n == 43) BAR_N(8); LATE(); } while (0)
    static_assert(PG5 >= 15 && PG5 <= 27 && PG5 + 8 * PS <= 39, "");
    G2_ROUND(A, 0, 16, { MID(); });
    FSTAMP(14);
    G2_ROUND(B, 16, 16, { H_SUM(C, 8, s - 5); MID(); });
    H_FIX(C, 2, 8, t8);
    FSTAMP(15);
    G2_ROUND(C, 32, 16, { H_SUM(D, 8, s - 5); MID(); });
    H_FIX(D, 3, 8, t8);
    FSTAMP(16);
    G2_ROUND(D, 48, 16, { LATE(); H_SUM(E, 6, s - 7); if constexpr (s == 11) BAR_N(15); });
    H_FIX(E, 4, 6, t6);
    FSTAMP(17);
    G2_ROUND(E, 64, 12, { H_SUM(A, 6, s - 3); if constexpr (s == 7) BAR_N(15); });
    H_FIX(A, 5, 6, t6);
    FSTAMP(18);
    G2_ROUND(A, 76, 12, { });
    COUNT(23, nfix);
    STAMP(12);
    MMA_DONE(); SB();
#pragma unroll
    for (int i = 0; i < 4; ++i) lds_wf(rw + i * 1024, o[2 + (i & 1)][i >> 1]);
    BAR();
    const rsrc_t ro = mkrs(p.out, S_ * D_ * 2);
#pragma unroll
    for (int i = 0; i < 2; ++i) {
      const f32x4 v0 = o[0][i] + lds_rf(rr_ + i * 2048), v1 = o[1][i] + lds_rf(rr_ + i * 2048 + 1024);
      const auto x = __builtin_amdgcn_permlane16_swap(pk(v0[0], v0[1]), pk(v1[0], v1[1]), false, false);
      const auto y = __builtin_amdgcn_permlane16_swap(pk(v0[2], v0[3]), pk(v1[2], v1[3]), false, false);
      const v4u q = {x[0], y[0], x[1], y[1]};
      __builtin_amdgcn_raw_buffer_store_b128(q, ro, vout + i * 2048, 0, OAUX);
    }
    STAMP(13);
    STAMP_FLUSH();
  }
}

extern "C" void kernel_launch(void* const* d_in, const int* in_sizes, int n_in,
                              void* d_out, int out_size, void* d_ws, size_t ws_size,
                              hipStream_t stream) {
  Params p;
  p.x = (const u16*)d_in[0]; p.wr = (const u16*)d_in[1]; p.wi = (const u16*)d_in[2];
  p.wg = (const u16*)d_in[4]; p.wu = (const u16*)d_in[5]; p.wd = (const u16*)d_in[6];
  p.out = (u16*)d_out; p.ws = d_ws;
  (void)hipFuncSetAttribute((const void*)gfnet_geglu_kernel, hipFuncAttributeMaxDynamicSharedMemorySize, 5 * SLOT);
  gfnet_geglu_kernel<<<256, 512, 5 * SLOT, stream>>>(p);
}
```

```cpp
#define T1 250
#define T2 515
#define T3 730
#include <hip/hip_runtime.h>
#include <hip/hip_bf16.h>
#include <type_traits>

typedef unsigned u32;
typedef unsigned short u16;
typedef u32 v4u __attribute__((ext_vector_type(4)));
typedef u32 v2u __attribute__((ext_vector_type(2)));
typedef float f32x2 __attribute__((ext_vector_type(2)));
typedef float f32x4 __attribute__((ext_vector_type(4)));
typedef __bf16 bf16x2 __attribute__((ext_vector_type(2)));
typedef __bf16 bf16x8 __attribute__((ext_vector_type(8)));
typedef short s16x4 __attribute__((ext_vector_type(4)));
#define LAS __attribute__((address_space(3)))
#define DI __device__ __forceinline__
typedef __amdgpu_buffer_rsrc_t rsrc_t;
typedef LAS char* lp_t;

constexpr int S_ = 2048, D_ = 512, F_ = 1376, KF = 257;
constexpr int NFT = 86;
constexpr int WS_E = 0;
#define XPAD 1
#define LPAD 0
#define WPAD 1
constexpr int WREC = 4 * 8192 + 1024 + WPAD * 128, LREC = 8192 + LPAD * 128;
constexpr int XREC = (88 + XPAD) * 128;
constexpr int WS_A = 4096;
constexpr int WS_B = WS_A + 256 * XREC;
constexpr int WS_W = WS_B + 256 * XREC;
constexpr int WS_L = WS_W + 64 * WREC;
constexpr int WS_T = WS_L + 256 * LREC;
constexpr int WS_S = WS_T + (1 << 15);
constexpr int WS_H = WS_S + (1 << 17);
constexpr int HREC = 1152;
constexpr int WS_END = WS_H + 64 * NFT * HREC;
constexpr int WS_ST = 24 << 20;
constexpr u32 NEG2 = 0x80008000u, NEGH = 0x80000000u, NEGL = 0x8000u;
constexpr u32 SPIN_CAP = 1u << 14;
constexpr int AUX_SC1 = 16;
#define OAUX 2
#define NTAUX 2
constexpr int OOB = 0x40000000;
constexpr int CODE_LINES = 260;
constexpr int SLOT = 32768, SCR = 4 * SLOT;
constexpr int L_F = 0, L_TG = 2048;
constexpr int L_IM = 4096, L_SO = 4096, L_X = 16384, L_I2 = 16384, L_I3 = 16384, L_EX = 24576, L_O3 = 24576;

#define DRAIN() asm volatile("s_waitcnt vmcnt(0)" ::: "memory")
#define MEMFENCE() asm volatile("" ::: "memory")
#define PIN(x) asm volatile("" : "+v"(x))
#define PINS(x) asm volatile("" : "+s"(x))
#define SB() __builtin_amdgcn_sched_barrier(0)
#define BAR() asm volatile("s_waitcnt lgkmcnt(0)\n\ts_barrier" ::: "memory")
#define BAR_N(n) asm volatile("s_waitcnt lgkmcnt(" #n ")\n\ts_barrier" ::: "memory")

DI u32 pk(float a, float b) { f32x2 v = {a, b}; return __builtin_bit_cast(u32, __builtin_convertvector(v, bf16x2)); }
DI v4u pk4(f32x4 a, f32x4 b) { v4u r = {pk(a[0], b[0]), pk(a[1], b[1]), pk(a[2], b[2]), pk(a[3], b[3])}; return r; }
DI float asf(u32 v) { return __builtin_bit_cast(float, v); }
DI u32 asu(float v) { return __builtin_bit_cast(u32, v); }
DI float blo(u32 v) { return asf(v << 16); }
DI float bhi(u32 v) { return asf(v & 0xffff0000u); }
DI float dot2(u32 a, u32 b, float c) { return __builtin_amdgcn_fdot2_f32_bf16(__builtin_bit_cast(bf16x2, a), __builtin_bit_cast(bf16x2, b), c, false); }
DI u32 sum4(u32 m, v4u v) { return (m + v.x + v.y) + v.z + v.w; }
template <int CTRL> DI u32 dppu(u32 v) { return (u32)__builtin_amdgcn_update_dpp(0, (int)v, CTRL, 0xf, 0xf, true); }
template <int CTRL> DI float dppf(float v) { return asf(dppu<CTRL>(asu(v))); }
DI u32 pair_total(u32 s0, u32 s1, bool odd) { return (odd ? s1 : s0) + dppu<0xb1>(odd ? s0 : s1); }
DI u32 add_x16(u32 v) { const auto s = __builtin_amdgcn_permlane16_swap(v, v, false, false); return s[0] + s[1]; }
DI u32 add_x32(u32 v) { const auto s = __builtin_amdgcn_permlane32_swap(v, v, false, false); return s[0] + s[1]; }
template <int N> DI void wave_sums(float (&v)[N]) {
#define WS_STEP(C) _Pragma("unroll") for (int i = 0; i < N; ++i) v[i] += dppf<C>(v[i]);
  WS_STEP(0x111) WS_STEP(0x112) WS_STEP(0x114) WS_STEP(0x118) WS_STEP(0x142) WS_STEP(0x143)
#pragma unroll
  for (int i = 0; i < N; ++i) v[i] = asf(__builtin_amdgcn_readlane(asu(v[i]), 63));
}
__host__ __device__ constexpr int rec_q(int k) { return (k & 7) | ((k >> 1) & 0x18); }
__host__ __device__ constexpr int rec_line(int k) { return 11 * ((k >> 3) & 1) + ((rec_q(k) * 11) >> 5); }
__host__ __device__ constexpr int rec_pos(int k) { return rec_q(k) - 3 * ((rec_q(k) * 11) >> 5); }
__host__ __device__ constexpr int im_unit(int k, int j) { return L_IM + (k >> 6) * 3072 + rec_line(k) * 128 + ((j ^ (rec_line(k) + 2 * (k >> 6))) & 7) * 16; }
__host__ __device__ constexpr int im_chk(int k) { return im_unit(k, 6) + 4 * rec_pos(k); }
DI void lds_add(lp_t a, u32 v) { __hip_atomic_fetch_add((LAS u32*)a, v, __ATOMIC_RELAXED, __HIP_MEMORY_SCOPE_WORKGROUP); }
DI f32x4 mm16(v4u a, v4u b, f32x4 c) { return __builtin_amdgcn_mfma_f32_16x16x32_bf16(__builtin_bit_cast(bf16x8, a), __builtin_bit_cast(bf16x8, b), c, 0, 0, 0); }
DI f32x4 mm16p(v4u a, v4u b, f32x4 c) { f32x4 d = mm16(a, b, c); PIN(d); return d; }
template <bool FIRST> DI void mma(f32x4& d, v4u a, v4u b) {
  if constexpr (FIRST) asm volatile("v_mfma_f32_16x16x32_bf16 %0, %1, %2, 0" : "=&v"(d) : "v"(a), "v"(b));
  else asm volatile("v_mfma_f32_16x16x32_bf16 %0, %1, %2, %0" : "+v"(d) : "v"(a), "v"(b));
}
#define MMA_DONE() asm volatile("s_nop 7")
DI v2u lds_tr(lp_t a) { return __builtin_bit_cast(v2u, __builtin_amdgcn_ds_read_tr16_b64_v4i16((LAS s16x4*)a)); }
DI v4u lds_tr2(lp_t a, lp_t b) { v2u x = lds_tr(a), y = lds_tr(b); v4u r = {x.x, x.y, y.x, y.y}; return r; }
DI v4u lds_r128(lp_t a) { return *(LAS v4u*)a; }
DI f32x4 lds_rf(lp_t a) { return *(LAS f32x4*)a; }
DI v2u lds_r64(lp_t a) { return *(LAS v2u*)a; }
DI void lds_w128(lp_t a, v4u v) { *(LAS v4u*)a = v; }
DI void lds_wf(lp_t a, f32x4 v) { *(LAS f32x4*)a = v; }
DI void lds_w64(lp_t a, v2u v) { *(LAS v2u*)a = v; }
DI lp_t lptr(u32 off) { lp_t q = (lp_t)(size_t)off; PIN(q); return q; }
DI rsrc_t mkrs(const void* p, u32 bytes) { return __builtin_amdgcn_make_buffer_rsrc((void*)p, 0, bytes, 0x00020000); }
DI v4u ld_dev(rsrc_t r, int v, int s) { return __builtin_amdgcn_raw_buffer_load_b128(r, v, s, AUX_SC1); }
DI u32 ld_dev1(rsrc_t r, int v, int s) { return __builtin_amdgcn_raw_buffer_load_b32(r, v, s, AUX_SC1); }
DI void st_dev(rsrc_t r, int v, v4u x) { __builtin_amdgcn_raw_buffer_store_b128(x, r, v, 0, AUX_SC1); }
DI void st_dev1(rsrc_t r, int v, u32 x) { __builtin_amdgcn_raw_buffer_store_b32(x, r, v, 0, AUX_SC1); }
DI v4u ld_in(rsrc_t r, int v, int s) { return __builtin_amdgcn_raw_buffer_load_b128(r, v, s, 0); }
DI v4u ld_once(rsrc_t r, int v, int s) { return __builtin_amdgcn_raw_buffer_load_b128(r, v, s, NTAUX); }
DI u32 touch(rsrc_t r, int line) { return __builtin_amdgcn_raw_buffer_load_b32(r, line * 128, 0, 0); }
DI void flag_set(lp_t mine, u32 k) { MEMFENCE(); *(volatile LAS u32*)mine = k; MEMFENCE(); }
#define FLAG_WAIT(theirs, k, ...) do { u32 f_; _Pragma("nounroll") do { f_ = *(volatile LAS u32*)(theirs); MEMFENCE(); __VA_ARGS__; MEMFENCE(); } while (!__all(f_ >= (k))); } while (0)
DI void flag_wait_lazy(lp_t theirs, u32 k) { while (!__all(*(volatile LAS u32*)theirs >= k)) __builtin_amdgcn_s_sleep(1); MEMFENCE(); }
DI float cosr(float rev) { return __builtin_amdgcn_cosf(rev); }
DI float sinr(float rev) { return __builtin_amdgcn_sinf(rev); }
__host__ __device__ constexpr int slot_of(int k1, int k2) { return ((k2 < 8 ? k1 + 32 * k2 : (k1 ? 32 - k1 : 16) + 32 * (15 - k2)) - 1) & 255; }
DI int tile0_of(int c) { const int h = c >> 3, i = c & 7; return 44 * h + 4 * i + 2 * (i < 6 - h ? i : 6 - h); }
template <int I, int N, class F> DI void static_for(F&& f) {
  if constexpr (I < N) { f(std::integral_constant<int, I>{}); static_for<I + 1, N>(f); }
}
DI void transpose8(v4u (&m)[8]) {
  v4u o[8];
#pragma unroll
  for (int i = 0; i < 8; ++i)
#pragma unroll
    for (int j = 0; j < 4; ++j) { const u32 a = m[2 * j][i >> 1], b = m[2 * j + 1][i >> 1]; o[i][j] = __builtin_amdgcn_perm(b, a, (i & 1) ? 0x07060302u : 0x05040100u); }
#pragma unroll
  for (int i = 0; i < 8; ++i) { m[i] = o[i]; PIN(m[i]); }
}
enum { E_SQ, E_POLY, E_ARG, E_EXP, E_INC, E_RCP, E_GU, E_OUT, E_PACK, E_SWA, E_SWB, E_SUM, E_SUM2, E_ST, E_X1, E_X2, E_X8, E_CHK, E_STK };
struct EOp { int kind, v; };
struct EProg { EOp op[520]; int n, first[7]; };
constexpr EProg make_eprog() {
  EProg p = {};
  for (int t = 0; t < 6; ++t) {
    p.first[t] = p.n;
    for (int v = 0; v <= 8; ++v) {
      const int V = 8 * t + v;
      const bool head = v < 8, tail = v > 0, fin = t > 0 && t < 5;
      if (head) p.op[p.n++] = {E_SQ, V};
      if (tail) p.op[p.n++] = {E_INC, V - 1};
      if (head) p.op[p.n++] = {E_POLY, V}; else p.op[p.n++] = {E_GU, V - 1};
      if (tail) p.op[p.n++] = {E_RCP, V - 1}; else if (fin) p.op[p.n++] = {E_X1, V - 1};
      if (head) p.op[p.n++] = {E_ARG, V};
      if (tail && head) p.op[p.n++] = {E_GU, V - 1};
      if (v >= 3 && (v & 1)) p.op[p.n++] = {E_PACK, V - 2};
      if (v == 8) p.op[p.n++] = {E_SWA, V - 1};
      if (fin && v == 0) p.op[p.n++] = {E_X2, V - 1};
      if (fin && v == 1) p.op[p.n++] = {E_CHK, V - 2};
      if (head) p.op[p.n++] = {E_EXP, V};
      if (fin && v == 0) p.op[p.n++] = {E_X8, V - 1};
      if (fin && v == 1) p.op[p.n++] = {E_STK, V - 2};
      if (tail) p.op[p.n++] = {E_OUT, V - 1};
    }
    p.op[p.n++] = {E_PACK, 8 * t + 7}; p.op[p.n++] = {E_SUM, 8 * t + 7}; p.op[p.n++] = {E_SWB, 8 * t + 7}; p.op[p.n++] = {E_ST, 8 * t + 7}; p.op[p.n++] = {E_SUM2, 8 * t + 7};
    if (t >= 4) for (int k = E_X1; k <= E_STK; ++k) p.op[p.n++] = {k, 8 * t + 7};
  }
  p.first[6] = p.n;
  return p;
}
constexpr EProg kEp = make_eprog();
#define ER1 56
constexpr int epos(int n) {
  const int mid = (64 * ER1) / 16, end = kEp.first[5];
  int j = n <= 64 ? 0 : n <= 128 ? ((n - 64) * ER1) / 16 : n <= 192 ? mid + ((end - mid) * (n - 128)) / 64
        : end + ((kEp.n - end) * (n - 192)) / 8;
  for (int t = 2; t < 5; ++t) if (n <= 32 * t + 34 && j > kEp.first[t]) j = kEp.first[t];
  return j;
}
struct Epi { float t[2], q, h[3]; v4u r, k4; u32 sum, fin; };
template <int KIND, int V> DI void epi_op(Epi& e, const f32x4 (&a)[4], u32 tag) {
  constexpr int v = V & 7;
  const float gv = a[v >> 2][v & 3], uv = a[2 + (v >> 2)][v & 3];
  float& t = e.t[V & 1];
  if constexpr (KIND == E_SQ) { t = gv * gv; PIN(t); }
  else if constexpr (KIND == E_POLY) { t = __builtin_fmaf(t, -0.10294324f, -2.3022082f); PIN(t); }
  else if constexpr (KIND == E_ARG) { t = t * gv; PIN(t); }
  else if constexpr (KIND == E_EXP) { t = __builtin_amdgcn_exp2f(t); PIN(t); }
  else if constexpr (KIND == E_INC) { t = t + 1.0f; PIN(t); }
  else if constexpr (KIND == E_RCP) { t = __builtin_amdgcn_rcpf(t); PIN(t); }
  else if constexpr (KIND == E_GU) { e.q = gv * uv; PIN(e.q); }
  else if constexpr (KIND == E_OUT) { e.h[v & 1] = e.q * t; PIN(e.h[v & 1]); }
  else if constexpr (KIND == E_PACK) { u32 r = pk(e.h[0], e.h[1]); PIN(r); e.r[v >> 1] = r; }
  else if constexpr (KIND == E_SWA || KIND == E_SWB) { constexpr int i = KIND == E_SWB; const u32 x = e.r[i], y = e.r[i + 2];
    const auto q = __builtin_amdgcn_permlane16_swap(x, y, false, false); u32 q0 = q[0], q1 = q[1]; PIN(q0); PIN(q1); e.r[i] = q0; e.r[i + 2] = q1; }
  else if constexpr (KIND == E_SUM) { e.sum = e.r.x + e.r.z; PIN(e.sum); }
  else if constexpr (KIND == E_SUM2) { e.fin = e.sum + e.r.y + e.r.w; PIN(e.fin); }
  else if constexpr (KIND == E_X1) { e.fin += dppu<0xb1>(e.fin); PIN(e.fin); }
  else if constexpr (KIND == E_X2) { e.fin += dppu<0x4e>(e.fin); PIN(e.fin); }
  else if constexpr (KIND == E_X8) { e.fin += dppu<0x128>(e.fin); PIN(e.fin); }
  else if constexpr (KIND == E_CHK) { asm volatile("" : "=v"(e.k4)); e.k4.x = tag - e.fin; PIN(e.k4); }
}

constexpr double ct_sin(long n, long d) {
  n %= d; if (n < 0) n += d;
  if (2 * n > d) return -ct_sin(d - n, d);
  if (4 * n > d) return ct_sin(d - 2 * n, 2 * d);
  const double x = 6.283185307179586476925 * (double)n / (double)d;
  double t = x, r = x;
  for (int i = 1; i < 14; ++i) { t *= -x * x / ((2 * i) * (2 * i + 1)); r += t; }
  return r;
}
constexpr double ct_cos(long n, long d) { return ct_sin(4 * n + d, 4 * d); }
constexpr u32 ct_f32(double v) { return __builtin_bit_cast(u32, (float)v); }
constexpr u32 ct_bf(double v) { const u32 u = ct_f32(v); return (u + 0x7fffu + ((u >> 16) & 1u)) >> 16; }
constexpr u32 ct_pk(double a, double b) { return ct_bf(a) | (ct_bf(b) << 16); }
struct Tab { u32 v[6][64][4]; };
constexpr Tab make_tab() {
  Tab t = {};
  for (int l = 0; l < 64; ++l) {
    const int c = l & 15, g = l >> 4;
    for (int j = 0; j < 2; ++j) {
      const int a = 4 * g + 2 * j;
      t.v[0][l][j] = ct_pk(ct_cos(a * c, 32), ct_cos((a + 1) * c, 32));
      t.v[0][l][2 + j] = c ? ct_pk(-ct_sin(a * c, 32), -ct_sin((a + 1) * c, 32)) : ct_pk(1, -1);
    }
    for (int r = 0; r < 4; ++r) {
      const int b = 4 * g + r, sl = slot_of(b, c);
      t.v[1][l][r] = ct_f32(ct_cos(b * c, 512));
      t.v[2][l][r] = ct_f32(ct_sin(b * c, 512));
      t.v[3][l][r] = ct_pk(ct_cos(b * c, 16), ct_sin(b * c, 16));
      t.v[4][l][r] = c == 0 ? ct_pk((r & 1) ? -1 : 1, 0) : c < 8 ? ct_pk(ct_cos(b * c, 16), -ct_sin(b * c, 16)) : ct_pk(ct_cos(b * (31 - 2 * c), 32), -ct_sin(b * (31 - 2 * c), 32));
      t.v[5][l][r] = im_unit(sl, 2 * rec_pos(sl)) | (im_chk(sl) << 16);
    }
  }
  return t;
}
static_assert(true, "");
static __device__ const __attribute__((aligned(128))) Tab g_tab = make_tab();

#define STAMP(i) do { } while (0)
#define STAMP_DECL do { } while (0)
#define STAMP_FLUSH() do { } while (0)
#define COUNT(i, v) do { } while (0)

#define FSTAMP(i) do { } while (0)
#define PACE(d, t) do { __builtin_amdgcn_s_sleep(d); while ((u32)__builtin_amdgcn_s_memrealtime() - t_ref < (t)) __builtin_amdgcn_s_sleep(1); } while (0)
#define RS1 0
#define RS2 10
#define RS3 0
#define D1 1
#define D2 4
#define D3 6
#define TW 240
#define SLIPPED() (*(LAS u32*)(fmine + L_TG + 1024) = (u32)__builtin_amdgcn_s_memrealtime() - t_ref)
#define PACE2(d, t, sl) do { u32 r_ = *(LAS u32*)(fall + L_TG + 1024); r_ = max(r_, dppu<0xb1>(r_)); r_ = max(r_, dppu<0x4e>(r_)); \
      r_ = __builtin_amdgcn_readfirstlane(r_) + (sl); t_ref += r_ > (t) ? r_ - (t) : 0u; PACE(d, t); } while (0)
#define SL2 175
#define SL3 150
#define WAIT1() PACE(D1, T1)
#define WAIT2() PACE2(D2, T2, SL2)
#define WAIT3() PACE2(D3, T3, SL3)
struct Params {
  const u16* x; const u16* wr; const u16* wi; const u16* wg; const u16* wu; const u16* wd; u16* out; void* ws;
};

__global__ void __launch_bounds__(512, 2) gfnet_geglu_kernel(Params p) {
  const int wg = blockIdx.x, tid = threadIdx.x, lane = tid & 63;
  int wv = __builtin_amdgcn_readfirstlane(tid >> 6);
  STAMP_DECL;
  unsigned long long t_entry;
  asm volatile("s_memrealtime %0" : "=s"(t_entry));
  const int rg = 4 * ((wg & 7) >> 1) + (wg >> 6), ch = 8 * (wg & 1) + ((wg >> 3) & 7);
  const int un = 16 * rg + ch;
  const int ft0 = tile0_of(ch);
  const bool six = (ch & 7) < 6 - (ch >> 3);
#define W1SZ (D_ * F_ * 2)
#define CT2 ((ch + 4 - (int)((size_t)p.wd >> 7)) & 7)
#define WD_SETUP() const rsrc_t rwd = mkrs(p.wd, D_ * F_ * 2); \
    int vd = (lane >> 3) * (D_ * 2) + CT2 * 128 + ((lane & 7) ^ ((lane >> 3) & 2)) * 16, vd1 = vd ^ 64, sd[4]; PIN(vd); PIN(vd1); \
    _Pragma("unroll") for (int i = 0; i < 4; ++i) { sd[i] = tile0_of(4 * (wv & 3) + i) * (16 * D_ * 2); PINS(sd[i]); }
#define DMA_WD1(t, slot, i) asm volatile("s_mov_b32 m0, %0\n\ts_nop 0\n\tbuffer_load_dwordx4 %1, %2, %3 offen lds" \
      :: "s"((slot) * SLOT + (wv & 3) * 8192 + (i) * 1024), "v"(((i) & 1) ? vd1 : vd), "s"(rwd), "s"(sd[(i) >> 1] + ((t) * 16 + ((i) & 1) * 8) * (D_ * 2)) : "memory")
#define TH2 6
#define TG 4
#define THROTTLE(i) do { if ((i) % TG == 0) asm volatile("s_waitcnt vmcnt(%0)" :: "n"(TH2) : "memory"); } while (0)
#define DMA_WD(t, slot) do { _Pragma("unroll") for (int i = 0; i < 8; ++i) { THROTTLE(i); DMA_WD1(t, slot, i); } } while (0)
#define DMA_WDS(t, slot) do { _Pragma("unroll") for (int i = 0; i < 4; ++i) DMA_WD1(t, slot, i); if (!(wv & 1)) { _Pragma("unroll") for (int i = 4; i < 8; ++i) DMA_WD1(t, slot, i); } } while (0)
#define VM_WAIT(n) asm volatile("s_waitcnt vmcnt(" #n ")" ::: "memory")
#define PDN 4
  constexpr int PD = PDN;
#define FRAG(N) frag(std::integral_constant<int, (N)>{})
#define NXT(n) (((n) + PD) ^ 1)
  v4u ra[8];
  u32 cpf;
#define UNDEF_RA() do { asm volatile("" : "=v"(cpf)); _Pragma("unroll") for (int i = 0; i < 8; ++i) asm volatile("" : "=v"(ra[i])); } while (0)
  UNDEF_RA();
  if (wv < 4) {
    const int vt = lane * 16;
    int sx = (32 * (8 * (un >> 5) + 2 * wv) + (un & 31)) * (D_ * 2), sy = sx + 32 * D_ * 2;
    const rsrc_t rt = mkrs(&g_tab, sizeof(Tab));
    ra[2] = ld_in(rt, vt, 0); ra[3] = ld_in(rt, vt + 1024, 0);
    PINS(sx); PINS(sy);
    SB();
    const rsrc_t rx = mkrs(p.x, S_ * D_ * 2);
    ra[0] = ld_once(rx, vt, sx); ra[1] = ld_once(rx, vt, sy);
    SB();
#pragma unroll
    for (int i = 0; i < 4; ++i) ra[4 + i] = ld_in(rt, vt + i * 1024, 2048);
    cpf = ld_dev1(mkrs(p.ws, WS_END), 0, WS_E);
    STAMP(0);
  }
  PINS(wv);
  if (wv >= 6) {
    unsigned long long pc0;
    asm volatile("s_getpc_b64 %0" : "=s"(pc0));
    cpf = touch(mkrs((const void*)(pc0 & ~127ull), CODE_LINES * 128), lane < 9 ? (((wg >> 3) ^ (wv & 1)) * 9 + lane) : 4096);
    unsigned long long te;
    asm volatile("s_waitcnt lgkmcnt(0)\n\ts_mov_b64 %0, %1" : "=s"(te) : "s"(t_entry));
    st_dev(mkrs(p.ws, WS_END), (tid >= 504 && wg < 8) ? WS_T + wg * 128 + (lane & 7) * 16 : OOB, v4u{(u32)te, (u32)te, (u32)te, (u32)te});
  }
  PINS(wv);
  if ((wv >> 1) == 2) {
#define WAUX 0
#define WL 1
#define WSLEEP 2
    __builtin_amdgcn_s_sleep(WSLEEP);
    const rsrc_t rwr = mkrs(p.wr, S_ * KF * 2), rwi = mkrs(p.wi, S_ * KF * 2);
    const int vf = (lane & 7) * (KF * 2) + (lane & 1) * 2 + ((tid & 127) >> 3) * 8;
    const int sf = (16 * (un >> 6) + 2 * (un & 4) + 256 * ((un >> 3) & 7)) * (KF * 2) + (un & 3) * 128;
#pragma unroll
    for (int r = 0; r < 4; ++r) { ra[r] = __builtin_amdgcn_raw_buffer_load_b128(rwr, vf, sf + r * (64 * KF * 2), WAUX); ra[4 + r] = __builtin_amdgcn_raw_buffer_load_b128(rwi, vf, sf + r * (64 * KF * 2), WAUX); }
    cpf = ld_dev1(mkrs(p.ws, WS_END), 0, WS_E);
  }
  const lp_t fmine = lptr(SCR + L_F + tid * 4);
  *(volatile LAS u32*)fmine = 0;
  lds_w128(lptr(SCR + L_IM + (tid & 127) * 128 + ((6 ^ (tid + 2 * (((tid & 127) * 43) >> 10))) & 7) * 16), v4u{0u, 0u, 0u, 0u});
  BAR();
  PINS(t_entry);
  u32 t_ref = (u32)t_entry, tb_ = 0;
  v4u wa_;
  asm volatile("" : "=v"(wa_));
  PINS(wv);
  if ((wv >> 1) == 2) {
    const int T = tid - 256, b = WS_W + (un >> 2) * WREC;
    wa_ = v4u{(u32)((T >> 3) * 512 + (T & 7) * 16 + b + (un & 3) * 8192), (lane & 1) ? 0u : 16u, (u32)((T & 7) ? OOB : b + 4 * 8192 + (un & 3) * 256 + T * 2), 0u};
    PIN(wa_);
  }
  PINS(wv);
  if (wv >= 6) {
#define X0SLEEP 8
    __builtin_amdgcn_s_sleep(X0SLEEP);
    const rsrc_t r1 = mkrs((wv & 1) ? p.wu : p.wg, W1SZ);
    ra[0].x = touch(r1, lane < 43 ? wg * 43 + lane : 16384);
  }
  SB();
  PINS(wv);

  if (wv < 4) {
  const int w = wv, c = lane & 15, g = lane >> 4;
  const float fc = (float)c, fg4 = (float)(4 * g);
  const f32x4 z = {0.f, 0.f, 0.f, 0.f};
  f32x4 twc, tws;
  v4u fbre, fbsw;
  u32 tag;
  float mean[2];
  const rsrc_t ws = mkrs(p.ws, WS_END);
  const lp_t fall = lptr(SCR + L_F + (lane & 3) * 256 + lane * 4);
  const lp_t imr = lptr(SCR + L_IM + w * 3072 + (lane >> 3) * 128 + ((lane ^ ((lane >> 3) + 2 * w)) & 7) * 16);
  int vx0 = lane * 16 + WS_A + un * XREC + w * (22 * 128), vx2 = lane < 48 ? vx0 + 2048 : OOB;
  PIN(vx0); PIN(vx2);
  int vq8 = (lane & 7) * 128;
  PIN(vq8);
  u32 tq;
  v2u xk[2][2];
  f32x4 tfc[2], tfs[2], tic[2], tis[2];
  const float fj = (float)(16 * w) + fc;
#pragma unroll
  for (int r = 0; r < 4; ++r) {
#pragma unroll
      for (int m = 0; m < 2; ++m) {
        const float nf = (fg4 + (16 * m + r)) * (fj * (1.f / 2048)), ni = (fg4 + (float)(16 * w) + r) * ((fc + 16 * m) * (1.f / 2048));
        tfc[m][r] = cosr(nf); tfs[m][r] = sinr(nf); tic[m][r] = cosr(ni); tis[m][r] = sinr(ni);
      }
  }
#pragma unroll
  for (int i = 0; i < 2; ++i) { PIN(tfc[i]); PIN(tfs[i]); PIN(tic[i]); PIN(tis[i]); }
  SB();
  {
    const v4u xr[2] = {ra[0], ra[1]}, tb[6] = {ra[2], ra[3], ra[4], ra[5], ra[6], ra[7]};
    const u32 sc = (c & 1) ? NEG2 : 0u, sm = c < 8 ? NEGL : NEGH, p0 = c < 8 ? 0x0c0c0100u : 0x01000c0cu, p1 = c < 8 ? 0x0c0c0302u : 0x03020c0cu;
    v4u fare = {tb[0].x, tb[0].y, tb[0].x ^ sc, tb[0].y ^ sc}, faim = {tb[0].z, tb[0].w, tb[0].z ^ sc, tb[0].w ^ sc}, fbim, f0re, f0im;
    twc = __builtin_bit_cast(f32x4, tb[1]); tws = __builtin_bit_cast(f32x4, tb[2]); fbre = tb[3];
    lp_t t1w[4], t1c[4];
#pragma unroll
    for (int r = 0; r < 4; ++r) {
      const u32 a = tb[3][r], q = tb[4][r];
      fbsw[r] = __builtin_amdgcn_alignbit(a, a, 16);
      fbim[r] = fbsw[r] ^ sm;
      f0re[r] = __builtin_amdgcn_perm(q, q, p0); f0im[r] = __builtin_amdgcn_perm(q, q, p1);
      t1w[r] = lptr((tb[5][r] & 0xffffu) ^ (SCR + w * 8)); t1c[r] = lptr((tb[5][r] >> 16) + SCR);
    }
    float c0m = c == 0 ? 1.f / 16 : 0.f;
    lp_t xw = lptr(SCR + L_X + w * 2048 + lane * 16), xt = lptr(SCR + L_X + w * 2048 + lane * 8);
    lp_t xq = lptr(SCR + L_X + w * 2048 + (4 * c + g) * 8);
    tag = cpf + 1u;
    u32 tag0 = w ? 0u : tag;
    PIN(c0m); PIN(tag); PIN(tag0);
    *(LAS u32*)(fmine + L_TG) = tag;
    *(LAS u32*)(fmine + L_TG + 1024) = 0;
    PIN(fare); PIN(faim); PIN(fbre); PIN(fbim); PIN(f0re); PIN(f0im); PIN(twc); PIN(tws);
    STAMP(1);
    SB();
    v4u a[2];
#pragma unroll
    for (int rr = 0; rr < 2; ++rr) { lds_w128(xw + rr * 1024, xr[rr]); a[rr] = lds_tr2(xt + rr * 1024, xt + rr * 1024 + 512); }
    tq = ld_dev1(ws, vq8, WS_T);
    STAMP(2);
    float st[4] = {0.f, 0.f, 0.f, 0.f};
#pragma unroll
    for (int i = 0; i < 4; ++i)
#pragma unroll
      for (int rr = 0; rr < 2; ++rr) { const u32 v = xr[rr][i]; st[rr] = dot2(v, 0x3f803f80u, st[rr]); st[2 + rr] = dot2(v, v, st[2 + rr]); }
    SB();
    f32x4 ur[2], ui[2];
#pragma unroll
    for (int rr = 0; rr < 2; ++rr) { ur[rr] = mm16p(a[rr], fare, z); ui[rr] = mm16p(a[rr], faim, z); }
    wave_sums<4>(st);
    SB();
    v4u a2[2];
    f32x4 orr[2], oi[2], sr[2], si[2];
#pragma unroll
    for (int rr = 0; rr < 2; ++rr) {
      const f32x4 x = ur[rr] - st[rr] * c0m, y = ui[rr];
      a2[rr] = pk4(x * twc + y * tws, y * twc - x * tws);
      orr[rr] = mm16p(a2[rr], fbre, z); oi[rr] = mm16p(a2[rr], fbim, z); sr[rr] = mm16p(a2[rr], f0re, z); si[rr] = mm16p(a2[rr], f0im, z);
    }
    v4u o[2];
#pragma unroll
    for (int rr = 0; rr < 2; ++rr) {
      mean[rr] = st[rr] * (1.f / 512);
      const float rstd = __builtin_amdgcn_rsqf(fmaxf(st[2 + rr] * (1.f / 512) - mean[rr] * mean[rr], 0.f) + 1e-5f);
      orr[rr][0] = g == 0 ? sr[rr][0] : orr[rr][0]; oi[rr][0] = g == 0 ? si[rr][0] : oi[rr][0];
      o[rr] = pk4(orr[rr] * rstd, oi[rr] * rstd);
    }
#pragma unroll
    for (int r = 0; r < 4; ++r) { v2u v = {o[0][r], o[1][r]}; lds_w64(t1w[r], v); lds_add(t1c[r], v.x + v.y + tag0); }
    STAMP(3);
    flag_set(fmine, 1);
    v4u v0, v1, v2;
    FLAG_WAIT(fall, 1, v0 = lds_r128(imr); v1 = lds_r128(imr + 1024); v2 = lds_r128(imr + 2048));
    st_dev(ws, vx0, v0); st_dev(ws, vx0 + 1024, v1); st_dev(ws, vx2, v2);
    flag_set(fmine, 2);
#pragma unroll
    for (int i = 0; i < 4; ++i) xk[i >> 1][i & 1] = lds_r64(xq + (i >> 1) * 1024 + (i & 1) * 512);
  }
  int vge, vgo, vg = (lane >> 3) * WREC + (lane & 7) * 16;
  {
    const int pay = (lane >> 1) * XREC + rec_pos(un) * 32 + (lane & 1) * 16, chk = (lane & 1) ? OOB : ((lane >> 1) ^ 1) * XREC + 96 + rec_pos(un) * 4;
    vge = (lane & 2) ? chk : pay; vgo = (lane & 2) ? pay : chk;
  }
  const int sg = w * (64 * XREC) + (22 * (un >> 6) + rec_line(un)) * 128;
#define GATHER(BUF) do { xl[0] = ld_dev(ws, vge, BUF + sg); xl[1] = ld_dev(ws, vgo, BUF + sg); xl[2] = ld_dev(ws, vge, BUF + sg + 32 * XREC); xl[3] = ld_dev(ws, vgo, BUF + sg + 32 * XREC); } while (0)
#define VERIFY(extra) ({ u32 k_ = 0; _Pragma("unroll") for (int m = 0; m < 2; ++m) { _Pragma("unroll") for (int i = 0; i < 4; ++i) xg[m][i] = (lane & 2) ? xl[2 * m + 1][i] : xl[2 * m][i]; \
      k_ += (lane & 2) ? xl[2 * m].x : xl[2 * m + 1].x; lds_w128(iw + m * 1024, xg[m]); } \
      u32 t_ = sum4(sum4((extra) - k_, xg[0]), xg[1]); t_ += dppu<0xb1>(t_); t_ += dppu<0x4e>(t_); t_; })
  const lp_t iw = lptr(SCR + L_I2 + w * 2048 + lane * 16);
  const int sw = WS_W + w * 16 * WREC + un * 128;
  v4u wq[2];
  STAMP(4);
  SB();
  v4u f2a[2][2];
  {
    v4u f1a[4], f1b[4], f2b[2][2];
    const u32 sc = (c & 1) ? NEG2 : 0u;
#pragma unroll
    for (int r = 0; r < 4; ++r) {
#pragma unroll
      for (int ks = 0; ks < 2; ++ks) {
        const float n = (fg4 + (16 * ks + r)) * (fj * (1.f / 64));
        const float cs = cosr(n), sn = sinr(n);
        f1a[ks][r] = pk(cs, sn); f1b[ks][r] = pk(-sn, cs);
        f1a[ks + 2][r] = f1a[ks][r] ^ sc; f1b[ks + 2][r] = f1b[ks][r] ^ sc;
      }
      const float n = (fg4 + r) * (fc * (1.f / 32));
      const float cs = cosr(n), sn = sinr(n);
      const u32 a = pk(cs, sn), b = pk(-sn, cs), sr = (r & 1) ? NEG2 : 0u;
      f2a[0][0][r] = a; f2a[1][0][r] = a ^ sr; f2a[0][1][r] = a ^ sc; f2a[1][1][r] = a ^ sc ^ sr;
      f2b[0][0][r] = b; f2b[1][0][r] = b ^ sr; f2b[0][1][r] = b ^ sc; f2b[1][1][r] = b ^ sc ^ sr;
    }
#pragma unroll
    for (int i = 0; i < 4; ++i) { PIN(f1a[i]); PIN(f1b[i]); PIN(f2a[i >> 1][i & 1]); PIN(f2b[i >> 1][i & 1]); }
    lp_t i2r = lptr(SCR + L_I2 + ((g >> 1) * 32 + c) * 32 + (g & 1) * 16);
    lp_t exw = lptr(SCR + L_EX + w * 2048 + lane * 16), exr = lptr(SCR + L_EX + lane * 16);
    lp_t imw[2], imc = lptr(SCR + im_chk(64 * w + lane));
#pragma unroll
    for (int n = 0; n < 2; ++n) { const int k = 64 * w + 32 * (g >> 1) + 16 * n + c; imw[n] = lptr(SCR + im_unit(k, 2 * rec_pos(k) + (g & 1))); }
    vx0 += WS_B - WS_A; vx2 += WS_B - WS_A;
    int vkw = (lane & 6) ? OOB : ((lane >> 3) + 8 * (lane & 1)) * WREC;
    PIN(vkw); PIN(vg); PIN(vge); PIN(vgo); PIN(vx0); PIN(vx2);
    tq -= t_ref; tq = tq < 150u ? tq : 0u;
    tq = max(tq, dppu<0xb1>(tq)); tq = max(tq, dppu<0x4e>(tq)); tq = max(tq, dppu<0x141>(tq));
    t_ref += __builtin_amdgcn_readfirstlane(tq);
    tb_ = t_ref;
    STAMP(5);
    WAIT1();
    STAMP(6);
    v4u xl[4], xg[2];
    f32x4 wr[2], wi[2];
    GATHER(WS_A);
#define WD_ 10
    SB();
    __builtin_amdgcn_s_sleep(WD_);
#define W_LD() do { wq[0] = ld_dev(ws, vg, sw); wq[1] = ld_dev(ws, vg, sw + 8 * WREC); kw = ld_dev1(ws, vkw, sw + 4 * 8192 - un * 124); } while (0)
    u32 kw;
    W_LD();
    flag_set(fmine, 3);
    SB();
    u32 bad = VERIFY(tag), spin = 0;
    if (__any(bad != 0)) {
      _Pragma("nounroll") do {
        MEMFENCE();
        if (bad) GATHER(WS_A);
        bad = VERIFY(tag);
      } while (__any(bad != 0) && ++spin < SPIN_CAP);
      SLIPPED();
      ++spin;
    }
    COUNT(20, spin);
    STAMP(7);
    v4u af[2][4], b2[2], a3[2];
    flag_set(fmine, 4);
    FLAG_WAIT(fall, 4, _Pragma("unroll") for (int i = 0; i < 8; ++i) af[i >> 2][i & 3] = lds_r128(i2r + (i & 3) * 2048 + (i >> 2) * 512));
    STAMP(8);
    SB();
    f32x4 dr[2], di[2];
#pragma unroll
    for (int m = 0; m < 2; ++m)
#pragma unroll
      for (int ks = 0; ks < 4; ++ks) { dr[m] = mm16(af[m][ks], f1a[ks], ks ? dr[m] : z); di[m] = mm16(af[m][ks], f1b[ks], ks ? di[m] : z); }
#pragma unroll
    for (int m = 0; m < 2; ++m) b2[m] = pk4(dr[m] * tfc[m] + di[m] * tfs[m], di[m] * tfc[m] - dr[m] * tfs[m]);
#pragma unroll
    for (int k = 0; k < 2; ++k)
#pragma unroll
      for (int m = 0; m < 2; ++m) { dr[m] = mm16(f2a[m][k], b2[k], k ? dr[m] : z); di[m] = mm16(f2b[m][k], b2[k], k ? di[m] : z); }
    {
#define W_BAD() ({ u32 t_ = sum4(sum4(0u - kw, wq[0]), wq[1]); t_ += dppu<0xb1>(t_); t_ += dppu<0x4e>(t_); t_ += dppu<0x141>(t_); t_ + 2u * tag; })
      u32 bw = W_BAD();
#pragma unroll
      for (int m = 0; m < 2; ++m) { PIN(dr[m]); PIN(di[m]); }
      if (__builtin_expect(__any(bw != 0), 0)) {
        u32 sp = 0;
        _Pragma("nounroll") do { MEMFENCE(); if (bw) W_LD(); bw = W_BAD(); } while (__any(bw != 0) && ++sp < SPIN_CAP);
        COUNT(24, 1);
      }
#pragma unroll
      for (int m = 0; m < 2; ++m)
#pragma unroll
        for (int r = 0; r < 4; ++r) { const u32 v = wq[m][r]; wr[m][r] = blo(v); wi[m][r] = bhi(v); }
    }
#pragma unroll
    for (int m = 0; m < 2; ++m) a3[m] = pk4(dr[m] * wi[m] + di[m] * wr[m], dr[m] * wr[m] - di[m] * wi[m]);
#pragma unroll
    for (int k = 0; k < 2; ++k)
#pragma unroll
      for (int n = 0; n < 2; ++n) { di[n] = mm16(a3[k], f2a[n][k], k ? di[n] : z); dr[n] = mm16(a3[k], f2b[n][k], k ? dr[n] : z); }
#pragma unroll
    for (int n = 0; n < 2; ++n) lds_w128(exw + n * 1024, pk4(di[n] * tic[n] + dr[n] * tis[n], dr[n] * tic[n] - di[n] * tis[n]));
    STAMP(9);
    flag_set(fmine, 5);
    FLAG_WAIT(fall, 5, _Pragma("unroll") for (int i = 0; i < 8; ++i) af[i >> 2][i & 3] = lds_r128(exr + (i & 3) * 2048 + (i >> 2) * 1024));
    SB();
    u32 cs[2];
#pragma unroll
    for (int n = 0; n < 2; ++n)
#pragma unroll
      for (int ks = 0; ks < 4; ++ks) { di[n] = mm16(f1a[ks], af[n][ks], ks ? di[n] : z); dr[n] = mm16(f1b[ks], af[n][ks], ks ? dr[n] : z); }
#pragma unroll
    for (int n = 0; n < 2; ++n) {
      const v4u e = pk4(dr[n], di[n]);
      lds_w128(imw[n], e);
      cs[n] = sum4(0u, e);
    }
    const auto sx = __builtin_amdgcn_permlane16_swap(cs[0], cs[1], false, false);
    *(LAS u32*)imc = tag + sx[0] + sx[1];
    MEMFENCE();
    const v4u v0 = lds_r128(imr), v1 = lds_r128(imr + 1024), v2 = lds_r128(imr + 2048);
    st_dev(ws, vx0, v0); st_dev(ws, vx0 + 1024, v1); st_dev(ws, vx2, v2);
    flag_set(fmine, 6);
  }
  STAMP(10);
  SB();
  {
    v4u gbre, gbim, g0re, g0im, ga[2];
#pragma unroll
    for (int r = 0; r < 4; ++r) {
      gbre[r] = fbre[r] ^ (g < 2 ? NEGH : 0u);
      gbim[r] = fbsw[r] ^ (g < 2 ? 0u : NEGH);
      const float n = fc * ((31.f - 2.f * r - 2.f * fg4) * (1.f / 32));
      u32 q = pk(cosr(n), -sinr(n));
      PIN(q);
      g0re[r] = g < 2 ? gbre[r] : 0u;
      g0im[r] = g < 2 ? 0u : q;
      ga[0][r] = f2a[0][0][r] ^ NEGH; ga[1][r] = f2a[1][0][r] ^ NEGH;
    }
    twc *= 2.f / (2048.f * 512.f); tws *= 2.f / (2048.f * 512.f);
    g0re[0] = g == 0 ? ((c & 1) ? 0xbf00u : 0x3f00u) : g0re[0];
    ga[0][0] = g == 0 ? ((c & 1) ? 0xbf803f80u : 0x3f803f80u) : ga[0][0]; ga[1][0] = ga[0][0];
    PIN(gbre); PIN(gbim); PIN(g0re); PIN(g0im); PIN(ga[0]); PIN(ga[1]); PIN(twc); PIN(tws);
    f32x4 xin[2][2];
#pragma unroll
    for (int rr = 0; rr < 2; ++rr)
#pragma unroll
      for (int t = 0; t < 2; ++t) {
        const v2u v = xk[rr][t];
        const u32 v0 = v.x, v1 = v.y;
        f32x4 q = {blo(v0), bhi(v0), blo(v1), bhi(v1)};
        xin[rr][t] = q - mean[rr]; PIN(xin[rr][t]);
      }
    lp_t i3r[4], o3w[2], o3r[2];
#pragma unroll
    for (int r = 0; r < 4; ++r) i3r[r] = lptr(SCR + L_I3 + slot_of(c, 4 * g + r) * 32 + w * 8);
#pragma unroll
    for (int i = 0; i < 2; ++i) {
      o3w[i] = lptr(SCR + L_O3 + ((4 * c + g) * 8 + ((2 * w + i) ^ (c & 7))) * 16);
      const int q = w * 128 + i * 64 + lane;
      o3r[i] = lptr(SCR + L_O3 + (q ^ ((q >> 5) & 7)) * 16);
    }
    int vs = lane * 16 + WS_L + un * LREC + w * 2048, vt = lane < 8 ? lane * 16 + WS_S + un * 512 + w * 128 : OOB;
    PIN(vs); PIN(vt);
    lp_t sow = lptr(SCR + L_SO + tid * 16), sor = lptr(SCR + L_SO + (lane & 6) * 512 + (lane & 1) * 8);
    WAIT2();
    STAMP(11);
    v4u xl[4], xg[2];
    GATHER(WS_B);
    flag_set(fmine, 7);
    SB();
    u32 bad = VERIFY(tag), spin = 0;
    if (__any(bad != 0)) {
      _Pragma("nounroll") do {
        __builtin_amdgcn_s_sleep(RS1);
        MEMFENCE();
        if (bad) GATHER(WS_B);
        bad = VERIFY(tag);
      } while (__any(bad != 0) && ++spin < SPIN_CAP);
      SLIPPED();
      ++spin;
    }
    COUNT(21, spin);
    STAMP(12);
    v4u a[2], a2[2];
    v2u zr[4];
    flag_set(fmine, 8);
    FLAG_WAIT(fall, 8, _Pragma("unroll") for (int r = 0; r < 4; ++r) zr[r] = lds_r64(i3r[r]));
    STAMP(13);
#pragma unroll
    for (int r = 0; r < 4; ++r) { a[0][r] = zr[r].x; a[1][r] = zr[r].y; }
    f32x4 tr[2], ti[2], sr[2], si[2], h[2][2];
#pragma unroll
    for (int rr = 0; rr < 2; ++rr) { tr[rr] = mm16p(a[rr], gbre, z); ti[rr] = mm16p(a[rr], gbim, z); sr[rr] = mm16p(a[rr], g0re, z); si[rr] = mm16p(a[rr], g0im, z); }
#pragma unroll
    for (int rr = 0; rr < 2; ++rr) {
      tr[rr][0] = g == 0 ? sr[rr][0] : tr[rr][0]; ti[rr][0] = g == 0 ? si[rr][0] : ti[rr][0];
      a2[rr] = pk4(tr[rr] * twc - ti[rr] * tws, ti[rr] * twc + tr[rr] * tws);
      h[rr][0] = mm16p(a2[rr], ga[0], xin[rr][0]); h[rr][1] = mm16p(a2[rr], ga[1], xin[rr][1]);
    }
    float st[2];
#pragma unroll
    for (int rr = 0; rr < 2; ++rr) { const f32x4 q = h[rr][0] * h[rr][0] + h[rr][1] * h[rr][1]; st[rr] = (q[0] + q[2]) + (q[1] + q[3]); }
    wave_sums<2>(st);
    v4u so;
#pragma unroll
    for (int rr = 0; rr < 2; ++rr) {
      const float ve = st[rr] * (1.f / 512) + 1e-5f, rstd = __builtin_amdgcn_rsqf(ve);
      const f32x4 l0 = h[rr][0] * rstd, l1 = h[rr][1] * rstd;
      v4u l4 = {pk(l0[0], l0[1]), pk(l0[2], l0[3]), pk(l1[0], l1[1]), pk(l1[2], l1[3])};
      lds_w128(o3w[rr], l4);
      so[2 * rr] = asu(mean[rr]); so[2 * rr + 1] = asu(ve * rstd);
    }
    lds_w128(sow, so);
    STAMP(14);
    v4u v0, v1;
    v2u ms;
    flag_set(fmine, 9);
    FLAG_WAIT(fall, 9, v0 = lds_r128(o3r[0]); v1 = lds_r128(o3r[1]); ms = lds_r64(sor));
    st_dev(ws, vs, v0); st_dev(ws, vs + 1024, v1);
    u32 s = sum4(sum4(0u, v0), v1);
    s += dppu<0x128>(s); s = add_x32(add_x16(s));
    v4u rec = {tag - s - ms.x - ms.y, ms.x, ms.y, tag};
    st_dev(ws, vt, rec);
    flag_set(fmine, 10);
    if (wg == 0 && w == 0) st_dev1(ws, WS_E, tag);
  }
  SB();
  {
#define LOOKN 6
    constexpr int LOOK = LOOKN;
    v4u ring[8], act[32];
    lp_t wa[4], wb[4];
#pragma unroll
    for (int t = 0; t < 4; ++t) wa[t] = lptr(t * SLOT + g * 512 + ((c ^ (2 * t + (c >> 3))) * 16));
#pragma unroll
    for (int i = 0; i < 4; ++i) wb[i] = lptr(((i & 1) ? 0 : 4 * SLOT) + ((lane >> 2) ^ (g & 1)) * 64 + (((i >> 1) ^ g) & 1) * 32 + (lane & 3) * 8);
    auto frag = [&](auto NN) -> v4u {
      constexpr int N = NN.value;
      if constexpr (N < 64) return lds_r128(wa[(N >> 1) & 1] + (N >> 2) * 2048 + (N & 1) * 256);
      else if constexpr (N < 128) return lds_r128(wa[N >> 5] + ((N & 31) >> 1) * 2048 + (N & 1) * 256);
      else { const lp_t b = wb[((N >> 4) & 2) + (N & 1)] + ((N & 31) >> 1) * 2048; return lds_tr2(b, b + 1024); }
    };
    int vo = WS_H + ((4 * rg + w) * NFT + ft0) * HREC + lane * 16, vk = (lane & 11) ? OOB : vo - lane * 16 + 1024 + ((lane >> 4) * 8 + (lane & 7)) * 4;
    int vo4 = vo + 4 * HREC, vk4 = vk + 4 * HREC;
    PIN(vo); PIN(vk); PIN(vo4); PIN(vk4);
    Epi e;
#define EPI(J0, J1) static_for<(J0), (J1)>([&](auto JJ) { constexpr int kd = kEp.op[JJ.value].kind, V = kEp.op[JJ.value].v, t = V >> 3; \
      if constexpr (kd == E_ST) st_dev(ws, (t < 4 ? vo : vo4) + (t & 3) * HREC, e.r); \
      else if constexpr (kd == E_STK) st_dev(ws, (t < 4 ? vk : vk4) + (t & 3) * HREC, e.k4); \
      else epi_op<kd, V>(e, t % 3 == 0 ? tA : t % 3 == 1 ? tB : tC, tag); \
      SB(); })
    int va0 = ((lane >> 3) & 1) * LREC + g * 128 + (lane & 7) * 16, va1 = va0 + 2 * LREC;
    int vr = ((lane >> 3) & 1) * 512 + g * 128 + (lane & 7) * 16;
    PIN(vr);
    const int sa = WS_L + (4 * rg + w) * 4 * LREC;
#define ACT_LD(i) act[i] = ld_dev(ws, (((i) & 1) ? va1 : va0) + (((i) >> 1) & 7) * 512, sa + ((i) >> 4) * 4096)
    f32x4 tA[4], tB[4], tC[4];
    WD_SETUP();
    v4u s0, s1;
#define A_HEAD() do { MEMFENCE(); PIN(va0); PIN(va1); s0 = ld_dev(ws, vr, WS_S + (4 * rg + w) * 2048); s1 = ld_dev(ws, vr + 1024, WS_S + (4 * rg + w) * 2048); SB(); \
      static_for<0, 2 * LOOK>([&](auto K) { ACT_LD(K.value); SB(); }); } while (0)
    WAIT3();
    STAMP(15);
    u32 spin = 0;
    A_HEAD();
    BAR();
    for (;; ++spin) {
      static_for<0, PD>([&](auto K) { ring[K.value ^ 1] = FRAG(K.value ^ 1); SB(); });
      if (__builtin_expect(__any(s0.w != tag || s1.w != tag), 0)) { _Pragma("nounroll") do { __builtin_amdgcn_s_sleep(RS3); A_HEAD(); } while (__any(s0.w != tag || s1.w != tag) && ++spin < SPIN_CAP); ++spin; }
      PIN(va0); PIN(va1);
      u32 m = (s0.x + s0.y + s0.z) + (s1.x + s1.y + s1.z);
      static_for<0, 64>([&](auto K) {
        constexpr int n = K.value, ks = n >> 2, q = (n & 1) * 2;
        f32x4* acc = (n & 2) ? tB : tA;
        mma<ks == 0>(acc[q], ring[n & 7], act[2 * ks]); SB();
        ring[NXT(n) & 7] = FRAG(NXT(n));
        if constexpr (!(n & 1) && ks + LOOK < 16) ACT_LD(2 * (ks + LOOK) + ((n >> 1) & 1));
        SB();
        mma<ks == 0>(acc[q + 1], ring[n & 7], act[2 * ks + 1]); SB();
        { const v4u v = act[2 * ks + ((n >> 1) & 1)]; u32 t = (n & 1) ? m + v.z + v.w : m + v.x + v.y; PIN(t); m = t; }
        SB();
      });
      if (__builtin_expect(__all(add_x32(add_x16(m)) == 8u * tag) || spin > SPIN_CAP, 1)) break;
      s0.w = ~tag;
    }
    COUNT(22, spin);
    BAR_N(15);
    STAMP(16);
#define G1_TILE(T, acc, ...) static_for<0, 32>([&](auto K) { constexpr int s = K.value, n = 32 * (T) + s, ks = s >> 1, q = (s & 1) * 2, j0 = epos(n), j1 = epos(n + 1), jm = j0 + (j1 - j0 - (T) / 4) / 2; \
      mma<ks == 0>(acc[q], ring[n & 7], act[2 * ks]); SB(); \
      if constexpr (n + PD < 192) ring[NXT(n) & 7] = FRAG(n + PD < 192 ? NXT(n) : 0); SB(); \
      EPI(j0, jm); \
      mma<ks == 0>(acc[q + 1], ring[n & 7], act[2 * ks + 1]); SB(); \
      __VA_ARGS__; EPI(jm, j1); SB(); })
#define PG2 16
#define PG4 12
#define PG3 10
    G1_TILE(2, tC, { });
    FSTAMP(26);
    G1_TILE(3, tA, { if constexpr (s == PG2) BAR_N(15); });
    FSTAMP(27);
    if (__builtin_expect(six, 1)) {
      G1_TILE(4, tB, { });
      FSTAMP(28);
      G1_TILE(5, tC, { if constexpr (s == PG4) BAR_N(15); });
      STAMP(17);
      MMA_DONE(); SB();
      EPI(kEp.first[5], kEp.n);
    } else {
      MMA_DONE(); SB();
      EPI(epos(128), kEp.first[4]);
      static_for<E_X1, E_STK>([&](auto KK) { epi_op<KK.value, 31>(e, tA, tag); });
      st_dev(ws, vk + 3 * HREC, e.k4);
#define SKSL 14
      __builtin_amdgcn_s_sleep(SKSL);
      BAR_N(15);
      FSTAMP(28);
      __builtin_amdgcn_s_sleep(SKSL + 6);
      STAMP(17);
    }
    STAMP(18);
    BAR();
#define CSL 10
    __builtin_amdgcn_s_sleep(CSL);
    DMA_WDS(4, 0);
    DMA_WDS(5, 1);
    BAR();
    if (w & 1) VM_WAIT(4); else VM_WAIT(8);
    BAR();
    VM_WAIT(0);
    BAR();
  }
  STAMP(24);
  STAMP_FLUSH();
  UNDEF_RA();
  asm volatile("" : "=v"(wa_));
  }
  PINS(wv);
  if (wv >= 4) {
    STAMP(0);
    const int hw = wv - 4;
    asm volatile("" :: "v"(cpf), "v"(ra[0]));
    if (hw < 2) {
    const rsrc_t ws = mkrs(p.ws, WS_END);
    const int T = tid - 256, vs = wa_.x;
    const u32 sh = wa_.y;
    u32 al[8][2], ck[4];
#pragma unroll
    for (int i = 0; i < 8; ++i) { const u32 a = ra[i].x, b = ra[i].y, d = ra[i].z; al[i][0] = __builtin_amdgcn_alignbit(b, a, sh); al[i][1] = __builtin_amdgcn_alignbit(d, b, sh); }
    v4u v[4];
#pragma unroll
    for (int e = 0; e < 4; ++e) {
#pragma unroll
      for (int r = 0; r < 4; ++r) v[e][r] = __builtin_amdgcn_perm(al[4 + r][e >> 1], al[r][e >> 1], (e & 1) ? 0x07060302u : 0x05040100u);
      if (!WL) st_dev(ws, vs + e * 128, v[e]);
      ck[e] = sum4(0u, v[e]);
    }
    const u32 c0 = pair_total(ck[0], ck[1], T & 1), c1 = pair_total(ck[2], ck[3], T & 1);
    u32 cq = ((T & 2) ? c1 : c0) + dppu<0x4e>((T & 2) ? c0 : c1);
    cq += dppu<0x104>(cq) + cpf + 1u;
    if (WL) {
      PACE(0, WL);
      flag_wait_lazy(lptr(SCR + L_F + (lane & 3) * 256 + lane * 4), 2);
#pragma unroll
      for (int e = 0; e < 4; ++e) st_dev(ws, vs + e * 128, v[e]);
    }
    st_dev(ws, wa_.z, v4u{cq, dppu<0x55>(cq), dppu<0xaa>(cq), dppu<0xff>(cq)});
    STAMP(1);
  }
    u32 tag;
    v4u y0[8];
    WD_SETUP();
    {
      const rsrc_t rwg = mkrs(p.wg, W1SZ), rwu = mkrs(p.wu, W1SZ);
      const int vx = 4 * (lane >> 3) * (F_ * 2) + ft0 * 32 + (lane & 7) * 16, vx8 = vx + 32 * F_ * 2;
      const int vy = six ? 4 * (lane >> 2) * (F_ * 2) + (ft0 + 4) * 32 + (lane & 3) * 16 : OOB;
      const u32 ax = ((lane >> 1) & 3) * SLOT + (16 * hw + (lane >> 3)) * 512 + (lane & 1) * 128 + (lane & 7) * 16;
#define PUT_X(u, off) do { transpose8(u); const u32 a_ = ax + (off); _Pragma("unroll") for (int i = 0; i < 8; ++i) lds_w128((lp_t)(size_t)(a_ ^ (i * 16)), u[i]); SB(); } while (0)
      v4u rb[8], rc[8], rd[8];
      const lp_t fall = lptr(SCR + L_F + (lane & 3) * 256 + lane * 4);
      const int s0 = 64 * hw * (F_ * 2);
#define THR 4
#define NA 2
#define NB 9
#define N0 0
#define NS 7
#define NL 99
#define YD 1
#define THRU 4
#define NH 2
#define TS 0
#define F1 2
#define F2 5
#define STEP(n) do { constexpr int j = (n) & 7; const int so_ = s0 + ((j & 3) + 256 * (j >> 2)) * (F_ * 2); \
        if ((n) == NA) { flag_wait_lazy(fall, 3); cpf = touch(rwd, lane < 11 ? wg * 43 + hw * 11 + lane : 16384); } if ((n) == NB) flag_wait_lazy(fall, 7); \
        if ((n) == NA + NH) flag_wait_lazy(fall, 4); if ((n) == NB + NH) flag_wait_lazy(fall, 8); \
        if ((n) == N0) flag_wait_lazy(fall, 2); if ((n) == NS) flag_wait_lazy(fall, 6); if ((n) == NL) flag_wait_lazy(fall, 10); \
        if ((n) > 1) asm volatile("s_waitcnt vmcnt(%0)" :: "n"((YD && (n) > 8) ? THRU : THR) : "memory"); \
        if ((n) < 8) { ra[j] = ld_in(rwg, vx, so_); rb[j] = ld_in(rwg, vx8, so_); y0[j] = ld_in(rwg, vy, so_); } \
        else { constexpr int j2 = (2 * (n)) & 7; const int sp_ = s0 + ((j2 & 3) + 256 * (j2 >> 2)) * (F_ * 2); \
          if ((n) < 12) { rc[j2] = ld_in(rwu, vx, sp_); rc[j2 + 1] = ld_in(rwu, vx, sp_ + F_ * 2); } else { rd[j2] = ld_in(rwu, vx8, sp_); rd[j2 + 1] = ld_in(rwu, vx8, sp_ + F_ * 2); } } SB(); } while (0)
      PACE(0, TS);
      STAMP(2);
      STEP(0); STEP(1); STEP(2); STEP(3); STEP(4); STEP(5); STEP(6); STEP(7); STEP(8); STEP(9);
      STAMP(3);
      if (NB > 10) STEP(10);
      if (NB > 11) STEP(11);
      PUT_X(ra, 0);
      PUT_X(rb, 4096);
      if (NB <= 10) STEP(10);
      if (NB <= 11) STEP(11);
      STEP(12); STEP(13); STEP(14); STEP(15);
      PUT_X(rc, 256);
      tag = *(LAS u32*)(fall + L_TG);
      STAMP(4);
      PUT_X(rd, 4096 + 256);
      PIN(tag);
      asm volatile("" :: "v"(cpf));
    }
    STAMP(5);
    BAR();
    const rsrc_t ws = mkrs(p.ws, WS_END);
    const int kh = hw & 1, rl = hw >> 1, g = lane >> 4;
    constexpr int PD = 4;
    const u32 ayo = hw * 8192 + g * 2048 + ((lane >> 2) & 3) * 256 + (lane & 4) * 16 + ((lane & 3) ^ ((lane >> 1) & 2)) * 16;
    const lp_t ay0[2] = {lptr(ayo + 4 * SLOT), lptr((ayo ^ 64) + 4 * SLOT)}, ad = lptr(3 * SLOT + hw * 8192 + lane * 16);
#define PUT_Y(a, y, j0, j1) _Pragma("unroll") for (int j = j0; j < j1; ++j) lds_w128(a[j & 1] + (j >> 2) * 1024 + (j & 2) * 64, y[j])
    PUT_Y(ay0, y0, 0, 8);
    v4u ring[8], hfA[8], hfB[8], hfC[8], hfD[8], hfE[8];
    u32 hkA, hkB, hkC, hkD, hkE, ms = 0, nfix = 0;
    lp_t wd[3][4];
    {
      const u32 dl = (g * 1024 + ((lane >> 2) & 3) * 128 + ((lane >> 3) & 3) * 32 + (lane & 3) * 8) ^ (kh * 64);
#pragma unroll
      for (int i = 0; i < 12; ++i) wd[i >> 2][i & 3] = lptr((dl ^ ((i & 3) * 32)) + (i >> 2) * 2 * SLOT + kh * 16384);
    }
    auto frag = [&](auto NN) -> v4u {
      constexpr int N = NN.value, t = N < 64 ? N >> 4 : N < 76 ? 4 : 5, s = N < 64 ? (N & 15) : N < 76 ? N - 64 : N - 76, slot = (t + 1) % 5;
      const lp_t b = wd[slot >> 1][s & 3] + (slot & 1) * SLOT + (s >> 2) * 4096;
      return lds_tr2(b, b + 512);
    };
    const int hb = WS_H + (4 * rg + 2 * (ch >> 3) + rl) * NFT * HREC;
    int hs[4];
#pragma unroll
    for (int i = 0; i < 4; ++i) { hs[i] = hb + tile0_of(8 * kh + 2 * i) * HREC; PINS(hs[i]); }
    int vl = (g & 1) * 512 + (lane & 15) * 16 + (g >> 1) * 6 * HREC, vl3 = vl - (g >> 1) * 2 * HREC, vl4 = vl + 4 * HREC, vlz = (kh && g >= 2) ? OOB : vl4, vq, vq4;
    {
      const int st = (lane & 2) + ((lane >> 1) & 4) + (g >> 1);
      vq = hb + 1024 + ((2 * (g & 1) + (lane & 1)) * 8 + (lane & 4)) * 4 + tile0_of(8 * kh + st) * HREC;
      vq4 = st < 6 - kh ? vq + 4 * HREC : OOB;
    }
    const int u = 16 * rg + 8 * (ch >> 3) + 4 * rl + ((lane >> 3) & 1), col = 64 * CT2 + 32 * kh;
    int vh = u * LREC + (col & 255) * 32 + g * 128 + (lane & 7) * 16 + (col >> 8) * 8, vs = u * 512 + (lane & 7) * 16 + 4;
    int vout = ((32 * (8 * (u >> 5) + (lane & 7)) + (u & 31)) * D_ + col + 16 * (g & 1) + 8 * (g >> 1)) * 2;
    lp_t rw = lptr(2 * SLOT + hw * 4096 + lane * 16), rr_ = lptr(2 * SLOT + (hw ^ 1) * 4096 + lane * 16);
    int vyd = ((lane >> 2) ^ (g & 1)) * (F_ * 2) + (ft0 + 4) * 32 + ((lane & 3) ^ ((lane >> 3) & 2)) * 16;
    PIN(vyd);
    u32 t8 = 8u * tag, t6 = ((kh && g >= 2) ? 4u : 6u) * tag;
    PIN(vl); PIN(vl3); PIN(vl4); PIN(vlz); PIN(vq); PIN(vq4); PIN(vh); PIN(vs); PIN(vout); PIN(t8); PIN(t6);
#define H_LD(X, t, j) do { \
      if constexpr ((j) == 8) hk##X = ld_dev1(ws, ((t) < 4 ? vq : vq4) + ((t) & 3) * HREC, 0); \
      else if constexpr ((t) < 4 || (j) < 6) hf##X[(j) & 7] = ld_dev(ws, ((t) < 4 ? ((j) < 6 ? vl : vl3) : (j) < 4 ? vl4 : vlz) + ((t) & 3) * HREC + ((j) & 1) * 256, hs[((j) >> 1) & 3]); } while (0)
#define H_LDALL(X, t) static_for<0, 9>([&](auto J) { THROTTLE(J.value); H_LD(X, t, J.value ? J.value - 1 : 8); })
#define H_SUM(X, nf, j) do { if constexpr ((j) >= 0 && (j) < (nf)) { ms = sum4((j) ? ms : hk##X, hf##X[(j) & 7]); u32 t_ = ms; PIN(t_); ms = t_; } \
      else if constexpr ((j) >= (nf) && (j) < (nf) + 3) { u32 t_ = ms + dppu<(j) == (nf) ? 0xb1 : (j) == (nf) + 1 ? 0x4e : 0x128>(ms); PIN(t_); ms = t_; } } while (0)
#define H_SUMALL(X, nf) static_for<0, (nf) + 3>([&](auto J) { H_SUM(X, nf, J.value); })
#define H_FIX(X, t, nf, want) do { if (__builtin_expect(__any(ms != (want)), 0)) { u32 spin_ = 0; _Pragma("nounroll") do { __builtin_amdgcn_s_sleep(RS3); MEMFENCE(); nfix |= 1u << (4 * (t)); \
        H_LDALL(X, t); H_SUMALL(X, nf); } while (__any(ms != (want)) && ++spin_ < SPIN_CAP); } SB(); } while (0)
    v4u d3[8];
    f32x4 o[4][2];
    v2u hr[4], so[2];
#define GRP() asm volatile("s_waitcnt vmcnt(%0)" :: "n"(TH2) : "memory")
#define RAW4(dst, t, i0) do { _Pragma("unroll") for (int i = i0; i < i0 + 4; ++i) { THROTTLE(i); dst[i] = ld_in(rwd, (i & 1) ? vd1 : vd, sd[i >> 1] + ((t) * 16 + (i & 1) * 8) * (D_ * 2)); } SB(); } while (0)
#define DMA4(t, slot, i0) do { _Pragma("unroll") for (int i = i0; i < i0 + 4; ++i) { THROTTLE(i); DMA_WD1(t, slot, i); } } while (0)
#define H4(X, t, h) do { static_for<5 * (h), 5 + 4 * (h)>([&](auto J) { if (J.value != 4) THROTTLE(J.value - 5 * (h)); H_LD(X, t, J.value ? J.value - 1 : 8); }); SB(); } while (0)
#define PUT_D(d, off, i0, i1) do { _Pragma("unroll") for (int i = i0; i < i1; ++i) lds_w128(ad + (off) + i * 1024, d[i]); SB(); } while (0)
#define RESID(i) do { const u32 v0 = hr[i].x, v1 = hr[i].y, mb = so[(i) & 1].x, sb = so[(i) & 1].y; float q[4] = {blo(v0), bhi(v0), blo(v1), bhi(v1)}; \
      _Pragma("unroll") for (int r = 0; r < 4; ++r) { q[r] = __builtin_fmaf(q[r], asf(sb), asf(mb)); PIN(q[r]); o[(i) >> 1][(i) & 1][r] = q[r]; } PIN(o[(i) >> 1][(i) & 1]); SB(); } while (0)
#define PSLEEP 16
    __builtin_amdgcn_s_sleep(PSLEEP);
#define SPRIO 3
#define SPRIO1 0
#define LOWP() __builtin_amdgcn_s_setprio(SPRIO1)
#define HIGHP() __builtin_amdgcn_s_setprio(SPRIO)
#define SPRIO2 3
    __builtin_amdgcn_s_setprio(SPRIO);
    RAW4(d3, 3, 0); RAW4(d3, 3, 4);
    STAMP(6);
    BAR();
    if (YD && six) {
      const rsrc_t rwu = mkrs(p.wu, W1SZ);
#pragma unroll
      for (int i = 0; i < 8; ++i) {
        asm volatile("s_waitcnt vmcnt(4)\n\ts_mov_b32 m0, %0\n\ts_nop 0\n\tbuffer_load_dwordx4 %1, %2, %3 offen lds"
                     :: "s"(hw * 8192 + i * 1024), "v"(vyd), "s"(rwu), "s"((16 * (4 * hw + (i >> 1)) + 256 * (i & 1)) * (F_ * 2)) : "memory");
      }
    }
#pragma unroll
    for (int i = 0; i < 2; ++i) so[i] = __builtin_amdgcn_raw_buffer_load_b64(ws, vs + i * 1024, WS_S, AUX_SC1);
#pragma unroll
    for (int i = 0; i < 4; ++i) hr[i] = __builtin_amdgcn_raw_buffer_load_b64(ws, vh + (i >> 1) * 512, WS_L + (i & 1) * 2 * LREC, AUX_SC1);
    SB();
    DMA4(0, 1, 0); DMA4(0, 1, 4);
    if (YD) VM_WAIT(8);
    STAMP(7);
    BAR();
    PIN(hr[3]);
    DMA4(1, 2, 0); DMA4(1, 2, 4); LOWP(); RESID(0); RESID(1); RESID(2); RESID(3);
    HIGHP();
    H4(A, 0, 0); H4(A, 0, 1);
#define BSL 0
    __builtin_amdgcn_s_sleep(BSL);
    H4(B, 1, 0); H4(B, 1, 1);
    PIN(hfA[7]);
    STAMP(8);
    BAR();
    DMA4(2, 3, 0); DMA4(2, 3, 4);
    LOWP();
    H_SUMALL(A, 8);
    H_FIX(A, 0, 8, t8);
    HIGHP();
    STAMP(9);
    H4(C, 2, 0); H4(C, 2, 1);
    VM_WAIT(9);
    H_SUMALL(B, 8);
    H_FIX(B, 1, 8, t8);
    static_for<0, PD>([&](auto K) { ring[K.value ^ 1] = FRAG(K.value ^ 1); SB(); });
    STAMP(10);
    LOWP();
    STAMP(11);
#define G2_ROUND(X, n0, ns, ...) static_for<0, ns>([&](auto K) { constexpr int s = K.value, n = (n0) + s, ct = s & 3, a = (s >> 2) * 2; \
      mma<n == 2 || n == 3>(o[ct][0], ring[n & 7], hf##X[a]); SB(); \
      if constexpr (n + PD < 88) ring[NXT(n) & 7] = FRAG(n + PD < 88 ? NXT(n) : 0); SB(); \
      mma<n == 2 || n == 3>(o[ct][1], ring[n & 7], hf##X[a + 1]); SB(); \
      __VA_ARGS__; SB(); })
#define PP 0
#define L3 16
#define L4 36
#define L5 50
#define LATE() do { if constexpr (n >= L3 && n < L3 + 9) H_LD(D, 3, n > L3 ? n - L3 - 1 : 8); \
      if constexpr (n >= L4 && n < L4 + 7) H_LD(E, 4, n > L4 ? n - L4 - 1 : 8); if constexpr (n >= L5 && n < L5 + 7) H_LD(A, 5, n > L5 ? n - L5 - 1 : 8); } while (0)
#define PG5 15
#define PS 1
#define MID() do { if constexpr (n == PG5) { BAR_N(15); __builtin_amdgcn_s_setprio(SPRIO2); } \
      if constexpr (n > PG5 && n <= PG5 + 8 * PS && (n - PG5 - 1) % PS == 0) lds_w128(ad + SLOT + ((n - PG5 - 1) / PS) * 1024, d3[((n - PG5 - 1) / PS) & 7]); \
      if constexpr (n == 43) BAR_N(8); LATE(); } while (0)
    static_assert(PG5 >= 15 && PG5 <= 27 && PG5 + 8 * PS <= 39, "");
    G2_ROUND(A, 0, 16, { MID(); });
    FSTAMP(14);
    G2_ROUND(B, 16, 16, { H_SUM(C, 8, s - 5); MID(); });
    H_FIX(C, 2, 8, t8);
    FSTAMP(15);
    G2_ROUND(C, 32, 16, { H_SUM(D, 8, s - 5); MID(); });
    H_FIX(D, 3, 8, t8);
    FSTAMP(16);
    G2_ROUND(D, 48, 16, { LATE(); H_SUM(E, 6, s - 7); if constexpr (s == 11) BAR_N(15); });
    H_FIX(E, 4, 6, t6);
    FSTAMP(17);
    G2_ROUND(E, 64, 12, { H_SUM(A, 6, s - 3); if constexpr (s == 7) BAR_N(15); });
    H_FIX(A, 5, 6, t6);
    FSTAMP(18);
    G2_ROUND(A, 76, 12, { });
    COUNT(23, nfix);
    STAMP(12);
    MMA_DONE(); SB();
#pragma unroll
    for (int i = 0; i < 4; ++i) lds_wf(rw + i * 1024, o[2 + (i & 1)][i >> 1]);
    BAR();
    const rsrc_t ro = mkrs(p.out, S_ * D_ * 2);
#pragma unroll
    for (int i = 0; i < 2; ++i) {
      const f32x4 v0 = o[0][i] + lds_rf(rr_ + i * 2048), v1 = o[1][i] + lds_rf(rr_ + i * 2048 + 1024);
      const auto x = __builtin_amdgcn_permlane16_swap(pk(v0[0], v0[1]), pk(v1[0], v1[1]), false, false);
      const auto y = __builtin_amdgcn_permlane16_swap(pk(v0[2], v0[3]), pk(v1[2], v1[3]), false, false);
      const v4u q = {x[0], y[0], x[1], y[1]};
      __builtin_amdgcn_raw_buffer_store_b128(q, ro, vout + i * 2048, 0, OAUX);
    }
    STAMP(13);
    STAMP_FLUSH();
  }
}

extern "C" void kernel_launch(void* const* d_in, const int* in_sizes, int n_in,
                              void* d_out, int out_size, void* d_ws, size_t ws_size,
                              hipStream_t stream) {
  Params p;
  p.x = (const u16*)d_in[0]; p.wr = (const u16*)d_in[1]; p.wi = (const u16*)d_in[2];
  p.wg = (const u16*)d_in[4]; p.wu = (const u16*)d_in[5]; p.wd = (const u16*)d_in[6];
  p.out = (u16*)d_out; p.ws = d_ws;
  (void)hipFuncSetAttribute((const void*)gfnet_geglu_kernel, hipFuncAttributeMaxDynamicSharedMemorySize, 5 * SLOT);
  gfnet_geglu_kernel<<<256, 512, 5 * SLOT, stream>>>(p);
}
```

```cpp
#define T1 260
#define T2 515
#define T3 725
#define PROBE_R 20
#include <hip/hip_runtime.h>
#include <hip/hip_bf16.h>
#include <type_traits>

typedef unsigned u32;
typedef unsigned short u16;
typedef u32 v4u __attribute__((ext_vector_type(4)));
typedef u32 v2u __attribute__((ext_vector_type(2)));
typedef float f32x2 __attribute__((ext_vector_type(2)));
typedef float f32x4 __attribute__((ext_vector_type(4)));
typedef __bf16 bf16x2 __attribute__((ext_vector_type(2)));
typedef __bf16 bf16x8 __attribute__((ext_vector_type(8)));
typedef short s16x4 __attribute__((ext_vector_type(4)));
#define LAS __attribute__((address_space(3)))
#define DI __device__ __forceinline__
typedef __amdgpu_buffer_rsrc_t rsrc_t;
typedef LAS char* lp_t;

constexpr int S_ = 2048, D_ = 512, F_ = 1376, KF = 257;
constexpr int NFT = 86;
constexpr int WS_E = 0;
#define XPAD 1
#define LPAD 0
#define WPAD 1
constexpr int WREC = 4 * 8192 + 1024 + WPAD * 128, LREC = 8192 + LPAD * 128;
constexpr int XREC = (88 + XPAD) * 128;
constexpr int WS_A = 4096;
constexpr int WS_B = WS_A + 256 * XREC;
constexpr int WS_W = WS_B + 256 * XREC;
constexpr int WS_L = WS_W + 64 * WREC;
constexpr int WS_T = WS_L + 256 * LREC;
constexpr int WS_S = WS_T + (1 << 15);
constexpr int WS_H = WS_S + (1 << 17);
constexpr int HREC = 1152;
constexpr int WS_END = WS_H + 64 * NFT * HREC;
constexpr int WS_ST = 24 << 20;
constexpr u32 NEG2 = 0x80008000u, NEGH = 0x80000000u, NEGL = 0x8000u;
constexpr u32 SPIN_CAP = 1u << 14;
constexpr int AUX_SC1 = 16;
#define OAUX 2
#define NTAUX 2
constexpr int OOB = 0x40000000;
constexpr int CODE_LINES = 260;
constexpr int SLOT = 32768, SCR = 4 * SLOT;
constexpr int L_F = 0, L_TG = 2048;
constexpr int L_IM = 4096, L_SO = 4096, L_X = 16384, L_I2 = 16384, L_I3 = 16384, L_EX = 24576, L_O3 = 24576;

#define DRAIN() asm volatile("s_waitcnt vmcnt(0)" ::: "memory")
#define MEMFENCE() asm volatile("" ::: "memory")
#define PIN(x) asm volatile("" : "+v"(x))
#define PINS(x) asm volatile("" : "+s"(x))
#define SB() __builtin_amdgcn_sched_barrier(0)
#define BAR() asm volatile("s_waitcnt lgkmcnt(0)\n\ts_barrier" ::: "memory")
#define BAR_N(n) asm volatile("s_waitcnt lgkmcnt(" #n ")\n\ts_barrier" ::: "memory")

DI u32 pk(float a, float b) { f32x2 v = {a, b}; return __builtin_bit_cast(u32, __builtin_convertvector(v, bf16x2)); }
DI v4u pk4(f32x4 a, f32x4 b) { v4u r = {pk(a[0], b[0]), pk(a[1], b[1]), pk(a[2], b[2]), pk(a[3], b[3])}; return r; }
DI float asf(u32 v) { return __builtin_bit_cast(float, v); }
DI u32 asu(float v) { return __builtin_bit_cast(u32, v); }
DI float blo(u32 v) { return asf(v << 16); }
DI float bhi(u32 v) { return asf(v & 0xffff0000u); }
DI float dot2(u32 a, u32 b, float c) { return __builtin_amdgcn_fdot2_f32_bf16(__builtin_bit_cast(bf16x2, a), __builtin_bit_cast(bf16x2, b), c, false); }
DI u32 sum4(u32 m, v4u v) { return (m + v.x + v.y) + v.z + v.w; }
template <int CTRL> DI u32 dppu(u32 v) { return (u32)__builtin_amdgcn_update_dpp(0, (int)v, CTRL, 0xf, 0xf, true); }
template <int CTRL> DI float dppf(float v) { return asf(dppu<CTRL>(asu(v))); }
DI u32 pair_total(u32 s0, u32 s1, bool odd) { return (odd ? s1 : s0) + dppu<0xb1>(odd ? s0 : s1); }
DI u32 add_x16(u32 v) { const auto s = __builtin_amdgcn_permlane16_swap(v, v, false, false); return s[0] + s[1]; }
DI u32 add_x32(u32 v) { const auto s = __builtin_amdgcn_permlane32_swap(v, v, false, false); return s[0] + s[1]; }
template <int N> DI void wave_sums(float (&v)[N]) {
#define WS_STEP(C) _Pragma("unroll") for (int i = 0; i < N; ++i) v[i] += dppf<C>(v[i]);
  WS_STEP(0x111) WS_STEP(0x112) WS_STEP(0x114) WS_STEP(0x118) WS_STEP(0x142) WS_STEP(0x143)
#pragma unroll
  for (int i = 0; i < N; ++i) v[i] = asf(__builtin_amdgcn_readlane(asu(v[i]), 63));
}
__host__ __device__ constexpr int rec_q(int k) { return (k & 7) | ((k >> 1) & 0x18); }
__host__ __device__ constexpr int rec_line(int k) { return 11 * ((k >> 3) & 1) + ((rec_q(k) * 11) >> 5); }
__host__ __device__ constexpr int rec_pos(int k) { return rec_q(k) - 3 * ((rec_q(k) * 11) >> 5); }
__host__ __device__ constexpr int im_unit(int k, int j) { return L_IM + (k >> 6) * 3072 + rec_line(k) * 128 + ((j ^ (rec_line(k) + 2 * (k >> 6))) & 7) * 16; }
__host__ __device__ constexpr int im_chk(int k) { return im_unit(k, 6) + 4 * rec_pos(k); }
DI void lds_add(lp_t a, u32 v) { __hip_atomic_fetch_add((LAS u32*)a, v, __ATOMIC_RELAXED, __HIP_MEMORY_SCOPE_WORKGROUP); }
DI f32x4 mm16(v4u a, v4u b, f32x4 c) { return __builtin_amdgcn_mfma_f32_16x16x32_bf16(__builtin_bit_cast(bf16x8, a), __builtin_bit_cast(bf16x8, b), c, 0, 0, 0); }
DI f32x4 mm16p(v4u a, v4u b, f32x4 c) { f32x4 d = mm16(a, b, c); PIN(d); return d; }
template <bool FIRST> DI void mma(f32x4& d, v4u a, v4u b) {
  if constexpr (FIRST) asm volatile("v_mfma_f32_16x16x32_bf16 %0, %1, %2, 0" : "=&v"(d) : "v"(a), "v"(b));
  else asm volatile("v_mfma_f32_16x16x32_bf16 %0, %1, %2, %0" : "+v"(d) : "v"(a), "v"(b));
}
#define MMA_DONE() asm volatile("s_nop 7")
DI v2u lds_tr(lp_t a) { return __builtin_bit_cast(v2u, __builtin_amdgcn_ds_read_tr16_b64_v4i16((LAS s16x4*)a)); }
DI v4u lds_tr2(lp_t a, lp_t b) { v2u x = lds_tr(a), y = lds_tr(b); v4u r = {x.x, x.y, y.x, y.y}; return r; }
DI v4u lds_r128(lp_t a) { return *(LAS v4u*)a; }
DI f32x4 lds_rf(lp_t a) { return *(LAS f32x4*)a; }
DI v2u lds_r64(lp_t a) { return *(LAS v2u*)a; }
DI void lds_w128(lp_t a, v4u v) { *(LAS v4u*)a = v; }
DI void lds_wf(lp_t a, f32x4 v) { *(LAS f32x4*)a = v; }
DI void lds_w64(lp_t a, v2u v) { *(LAS v2u*)a = v; }
DI lp_t lptr(u32 off) { lp_t q = (lp_t)(size_t)off; PIN(q); return q; }
DI rsrc_t mkrs(const void* p, u32 bytes) { return __builtin_amdgcn_make_buffer_rsrc((void*)p, 0, bytes, 0x00020000); }
DI v4u ld_dev(rsrc_t r, int v, int s) { return __builtin_amdgcn_raw_buffer_load_b128(r, v, s, AUX_SC1); }
DI u32 ld_dev1(rsrc_t r, int v, int s) { return __builtin_amdgcn_raw_buffer_load_b32(r, v, s, AUX_SC1); }
DI void st_dev(rsrc_t r, int v, v4u x) { __builtin_amdgcn_raw_buffer_store_b128(x, r, v, 0, AUX_SC1); }
DI void st_dev1(rsrc_t r, int v, u32 x) { __builtin_amdgcn_raw_buffer_store_b32(x, r, v, 0, AUX_SC1); }
DI v4u ld_in(rsrc_t r, int v, int s) { return __builtin_amdgcn_raw_buffer_load_b128(r, v, s, 0); }
DI v4u ld_once(rsrc_t r, int v, int s) { return __builtin_amdgcn_raw_buffer_load_b128(r, v, s, NTAUX); }
DI u32 touch(rsrc_t r, int line) { return __builtin_amdgcn_raw_buffer_load_b32(r, line * 128, 0, 0); }
DI void flag_set(lp_t mine, u32 k) { MEMFENCE(); *(volatile LAS u32*)mine = k; MEMFENCE(); }
#define FLAG_WAIT(theirs, k, ...) do { u32 f_; _Pragma("nounroll") do { f_ = *(volatile LAS u32*)(theirs); MEMFENCE(); __VA_ARGS__; MEMFENCE(); } while (!__all(f_ >= (k))); } while (0)
DI void flag_wait_lazy(lp_t theirs, u32 k) { while (!__all(*(volatile LAS u32*)theirs >= k)) __builtin_amdgcn_s_sleep(1); MEMFENCE(); }
DI float cosr(float rev) { return __builtin_amdgcn_cosf(rev); }
DI float sinr(float rev) { return __builtin_amdgcn_sinf(rev); }
__host__ __device__ constexpr int slot_of(int k1, int k2) { return ((k2 < 8 ? k1 + 32 * k2 : (k1 ? 32 - k1 : 16) + 32 * (15 - k2)) - 1) & 255; }
DI int tile0_of(int c) { const int h = c >> 3, i = c & 7; return 44 * h + 4 * i + 2 * (i < 6 - h ? i : 6 - h); }
template <int I, int N, class F> DI void static_for(F&& f) {
  if constexpr (I < N) { f(std::integral_constant<int, I>{}); static_for<I + 1, N>(f); }
}
DI void transpose8(v4u (&m)[8]) {
  v4u o[8];
#pragma unroll
  for (int i = 0; i < 8; ++i)
#pragma unroll
    for (int j = 0; j < 4; ++j) { const u32 a = m[2 * j][i >> 1], b = m[2 * j + 1][i >> 1]; o[i][j] = __builtin_amdgcn_perm(b, a, (i & 1) ? 0x07060302u : 0x05040100u); }
#pragma unroll
  for (int i = 0; i < 8; ++i) { m[i] = o[i]; PIN(m[i]); }
}
enum { E_SQ, E_POLY, E_ARG, E_EXP, E_INC, E_RCP, E_GU, E_OUT, E_PACK, E_SWA, E_SWB, E_SUM, E_SUM2, E_ST, E_X1, E_X2, E_X8, E_CHK, E_STK };
struct EOp { int kind, v; };
struct EProg { EOp op[520]; int n, first[7]; };
constexpr EProg make_eprog() {
  EProg p = {};
  for (int t = 0; t < 6; ++t) {
    p.first[t] = p.n;
    for (int v = 0; v <= 8; ++v) {
      const int V = 8 * t + v;
      const bool head = v < 8, tail = v > 0, fin = t > 0 && t < 5;
      if (head) p.op[p.n++] = {E_SQ, V};
      if (tail) p.op[p.n++] = {E_INC, V - 1};
      if (head) p.op[p.n++] = {E_POLY, V}; else p.op[p.n++] = {E_GU, V - 1};
      if (tail) p.op[p.n++] = {E_RCP, V - 1}; else if (fin) p.op[p.n++] = {E_X1, V - 1};
      if (head) p.op[p.n++] = {E_ARG, V};
      if (tail && head) p.op[p.n++] = {E_GU, V - 1};
      if (v >= 3 && (v & 1)) p.op[p.n++] = {E_PACK, V - 2};
      if (v == 8) p.op[p.n++] = {E_SWA, V - 1};
      if (fin && v == 0) p.op[p.n++] = {E_X2, V - 1};
      if (fin && v == 1) p.op[p.n++] = {E_CHK, V - 2};
      if (head) p.op[p.n++] = {E_EXP, V};
      if (fin && v == 0) p.op[p.n++] = {E_X8, V - 1};
      if (fin && v == 1) p.op[p.n++] = {E_STK, V - 2};
      if (tail) p.op[p.n++] = {E_OUT, V - 1};
    }
    p.op[p.n++] = {E_PACK, 8 * t + 7}; p.op[p.n++] = {E_SUM, 8 * t + 7}; p.op[p.n++] = {E_SWB, 8 * t + 7}; p.op[p.n++] = {E_ST, 8 * t + 7}; p.op[p.n++] = {E_SUM2, 8 * t + 7};
    if (t >= 4) for (int k = E_X1; k <= E_STK; ++k) p.op[p.n++] = {k, 8 * t + 7};
  }
  p.first[6] = p.n;
  return p;
}
constexpr EProg kEp = make_eprog();
#define ER1 56
constexpr int epos(int n) {
  const int mid = (64 * ER1) / 16, end = kEp.first[5];
  int j = n <= 64 ? 0 : n <= 128 ? ((n - 64) * ER1) / 16 : n <= 192 ? mid + ((end - mid) * (n - 128)) / 64
        : end + ((kEp.n - end) * (n - 192)) / 8;
  for (int t = 2; t < 5; ++t) if (n <= 32 * t + 34 && j > kEp.first[t]) j = kEp.first[t];
  return j;
}
struct Epi { float t[2], q, h[3]; v4u r, k4; u32 sum, fin; };
template <int KIND, int V> DI void epi_op(Epi& e, const f32x4 (&a)[4], u32 tag) {
  constexpr int v = V & 7;
  const float gv = a[v >> 2][v & 3], uv = a[2 + (v >> 2)][v & 3];
  float& t = e.t[V & 1];
  if constexpr (KIND == E_SQ) { t = gv * gv; PIN(t); }
  else if constexpr (KIND == E_POLY) { t = __builtin_fmaf(t, -0.10294324f, -2.3022082f); PIN(t); }
  else if constexpr (KIND == E_ARG) { t = t * gv; PIN(t); }
  else if constexpr (KIND == E_EXP) { t = __builtin_amdgcn_exp2f(t); PIN(t); }
  else if constexpr (KIND == E_INC) { t = t + 1.0f; PIN(t); }
  else if constexpr (KIND == E_RCP) { t = __builtin_amdgcn_rcpf(t); PIN(t); }
  else if constexpr (KIND == E_GU) { e.q = gv * uv; PIN(e.q); }
  else if constexpr (KIND == E_OUT) { e.h[v & 1] = e.q * t; PIN(e.h[v & 1]); }
  else if constexpr (KIND == E_PACK) { u32 r = pk(e.h[0], e.h[1]); PIN(r); e.r[v >> 1] = r; }
  else if constexpr (KIND == E_SWA || KIND == E_SWB) { constexpr int i = KIND == E_SWB; const u32 x = e.r[i], y = e.r[i + 2];
    const auto q = __builtin_amdgcn_permlane16_swap(x, y, false, false); u32 q0 = q[0], q1 = q[1]; PIN(q0); PIN(q1); e.r[i] = q0; e.r[i + 2] = q1; }
  else if constexpr (KIND == E_SUM) { e.sum = e.r.x + e.r.z; PIN(e.sum); }
  else if constexpr (KIND == E_SUM2) { e.fin = e.sum + e.r.y + e.r.w; PIN(e.fin); }
  else if constexpr (KIND == E_X1) { e.fin += dppu<0xb1>(e.fin); PIN(e.fin); }
  else if constexpr (KIND == E_X2) { e.fin += dppu<0x4e>(e.fin); PIN(e.fin); }
  else if constexpr (KIND == E_X8) { e.fin += dppu<0x128>(e.fin); PIN(e.fin); }
  else if constexpr (KIND == E_CHK) { asm volatile("" : "=v"(e.k4)); e.k4.x = tag - e.fin; PIN(e.k4); }
}

constexpr double ct_sin(long n, long d) {
  n %= d; if (n < 0) n += d;
  if (2 * n > d) return -ct_sin(d - n, d);
  if (4 * n > d) return ct_sin(d - 2 * n, 2 * d);
  const double x = 6.283185307179586476925 * (double)n / (double)d;
  double t = x, r = x;
  for (int i = 1; i < 14; ++i) { t *= -x * x / ((2 * i) * (2 * i + 1)); r += t; }
  return r;
}
constexpr double ct_cos(long n, long d) { return ct_sin(4 * n + d, 4 * d); }
constexpr u32 ct_f32(double v) { return __builtin_bit_cast(u32, (float)v); }
constexpr u32 ct_bf(double v) { const u32 u = ct_f32(v); return (u + 0x7fffu + ((u >> 16) & 1u)) >> 16; }
constexpr u32 ct_pk(double a, double b) { return ct_bf(a) | (ct_bf(b) << 16); }
struct Tab { u32 v[6][64][4]; };
constexpr Tab make_tab() {
  Tab t = {};
  for (int l = 0; l < 64; ++l) {
    const int c = l & 15, g = l >> 4;
    for (int j = 0; j < 2; ++j) {
      const int a = 4 * g + 2 * j;
      t.v[0][l][j] = ct_pk(ct_cos(a * c, 32), ct_cos((a + 1) * c, 32));
      t.v[0][l][2 + j] = c ? ct_pk(-ct_sin(a * c, 32), -ct_sin((a + 1) * c, 32)) : ct_pk(1, -1);
    }
    for (int r = 0; r < 4; ++r) {
      const int b = 4 * g + r, sl = slot_of(b, c);
      t.v[1][l][r] = ct_f32(ct_cos(b * c, 512));
      t.v[2][l][r] = ct_f32(ct_sin(b * c, 512));
      t.v[3][l][r] = ct_pk(ct_cos(b * c, 16), ct_sin(b * c, 16));
      t.v[4][l][r] = c == 0 ? ct_pk((r & 1) ? -1 : 1, 0) : c < 8 ? ct_pk(ct_cos(b * c, 16), -ct_sin(b * c, 16)) : ct_pk(ct_cos(b * (31 - 2 * c), 32), -ct_sin(b * (31 - 2 * c), 32));
      t.v[5][l][r] = im_unit(sl, 2 * rec_pos(sl)) | (im_chk(sl) << 16);
    }
  }
  return t;
}
static_assert(true, "");
static __device__ const __attribute__((aligned(128))) Tab g_tab = make_tab();

#define STAMP(i) do { } while (0)
#define STAMP_DECL u32 cn_ = 0
#define STAMP_FLUSH() do { if (cn_) { const u32 t0_ = (u32)__builtin_amdgcn_s_memrealtime(); while ((u32)__builtin_amdgcn_s_memrealtime() - t0_ < 3000u) __builtin_amdgcn_s_sleep(1); } } while (0)
#define COUNT(i, v) do { if ((i) == PROBE_R) cn_ = (v); } while (0)

#define FSTAMP(i) do { } while (0)
#define PACE(d, t) do { __builtin_amdgcn_s_sleep(d); while ((u32)__builtin_amdgcn_s_memrealtime() - t_ref < (t)) __builtin_amdgcn_s_sleep(1); } while (0)
#define RS1 0
#define RS2 10
#define RS3 0
#define D1 1
#define D2 4
#define D3 6
#define TW 240
#define SLIPPED() (*(LAS u32*)(fmine + L_TG + 1024) = (u32)__builtin_amdgcn_s_memrealtime() - t_ref)
#define PACE2(d, t, sl) do { u32 r_ = *(LAS u32*)(fall + L_TG + 1024); r_ = max(r_, dppu<0xb1>(r_)); r_ = max(r_, dppu<0x4e>(r_)); \
      r_ = __builtin_amdgcn_readfirstlane(r_) + (sl); t_ref += r_ > (t) ? r_ - (t) : 0u; PACE(d, t); } while (0)
#define SL2 175
#define SL3 150
#define WAIT1() PACE(D1, T1)
#define WAIT2() PACE2(D2, T2, SL2)
#define WAIT3() PACE2(D3, T3, SL3)
struct Params {
  const u16* x; const u16* wr; const u16* wi; const u16* wg; const u16* wu; const u16* wd; u16* out; void* ws;
};

__global__ void __launch_bounds__(512, 2) gfnet_geglu_kernel(Params p) {
  const int wg = blockIdx.x, tid = threadIdx.x, lane = tid & 63;
  int wv = __builtin_amdgcn_readfirstlane(tid >> 6);
  STAMP_DECL;
  unsigned long long t_entry;
  asm volatile("s_memrealtime %0" : "=s"(t_entry));
  const int rg = 4 * ((wg & 7) >> 1) + (wg >> 6), ch = 8 * (wg & 1) + ((wg >> 3) & 7);
  const int un = 16 * rg + ch;
  const int ft0 = tile0_of(ch);
  const bool six = (ch & 7) < 6 - (ch >> 3);
#define W1SZ (D_ * F_ * 2)
#define CT2 ((ch + 4 - (int)((size_t)p.wd >> 7)) & 7)
#define WD_SETUP() const rsrc_t rwd = mkrs(p.wd, D_ * F_ * 2); \
    int vd = (lane >> 3) * (D_ * 2) + CT2 * 128 + ((lane & 7) ^ ((lane >> 3) & 2)) * 16, vd1 = vd ^ 64, sd[4]; PIN(vd); PIN(vd1); \
    _Pragma("unroll") for (int i = 0; i < 4; ++i) { sd[i] = tile0_of(4 * (wv & 3) + i) * (16 * D_ * 2); PINS(sd[i]); }
#define DMA_WD1(t, slot, i) asm volatile("s_mov_b32 m0, %0\n\ts_nop 0\n\tbuffer_load_dwordx4 %1, %2, %3 offen lds" \
      :: "s"((slot) * SLOT + (wv & 3) * 8192 + (i) * 1024), "v"(((i) & 1) ? vd1 : vd), "s"(rwd), "s"(sd[(i) >> 1] + ((t) * 16 + ((i) & 1) * 8) * (D_ * 2)) : "memory")
#define TH2 6
#define TG 4
#define THROTTLE(i) do { if ((i) % TG == 0) asm volatile("s_waitcnt vmcnt(%0)" :: "n"(TH2) : "memory"); } while (0)
#define DMA_WD(t, slot) do { _Pragma("unroll") for (int i = 0; i < 8; ++i) { THROTTLE(i); DMA_WD1(t, slot, i); } } while (0)
#define DMA_WDS(t, slot) do { _Pragma("unroll") for (int i = 0; i < 4; ++i) DMA_WD1(t, slot, i); if (!(wv & 1)) { _Pragma("unroll") for (int i = 4; i < 8; ++i) DMA_WD1(t, slot, i); } } while (0)
#define VM_WAIT(n) asm volatile("s_waitcnt vmcnt(" #n ")" ::: "memory")
#define PDN 4
  constexpr int PD = PDN;
#define FRAG(N) frag(std::integral_constant<int, (N)>{})
#define NXT(n) (((n) + PD) ^ 1)
  v4u ra[8];
  u32 cpf;
#define UNDEF_RA() do { asm volatile("" : "=v"(cpf)); _Pragma("unroll") for (int i = 0; i < 8; ++i) asm volatile("" : "=v"(ra[i])); } while (0)
  UNDEF_RA();
  if (wv < 4) {
    const int vt = lane * 16;
    int sx = (32 * (8 * (un >> 5) + 2 * wv) + (un & 31)) * (D_ * 2), sy = sx + 32 * D_ * 2;
    const rsrc_t rt = mkrs(&g_tab, sizeof(Tab));
    ra[2] = ld_in(rt, vt, 0); ra[3] = ld_in(rt, vt + 1024, 0);
    PINS(sx); PINS(sy);
    SB();
    const rsrc_t rx = mkrs(p.x, S_ * D_ * 2);
    ra[0] = ld_once(rx, vt, sx); ra[1] = ld_once(rx, vt, sy);
    SB();
#pragma unroll
    for (int i = 0; i < 4; ++i) ra[4 + i] = ld_in(rt, vt + i * 1024, 2048);
    cpf = ld_dev1(mkrs(p.ws, WS_END), 0, WS_E);
    STAMP(0);
  }
  PINS(wv);
  if (wv >= 6) {
    unsigned long long pc0;
    asm volatile("s_getpc_b64 %0" : "=s"(pc0));
    cpf = touch(mkrs((const void*)(pc0 & ~127ull), CODE_LINES * 128), lane < 9 ? (((wg >> 3) ^ (wv & 1)) * 9 + lane) : 4096);
    unsigned long long te;
    asm volatile("s_waitcnt lgkmcnt(0)\n\ts_mov_b64 %0, %1" : "=s"(te) : "s"(t_entry));
    st_dev(mkrs(p.ws, WS_END), (tid >= 504 && wg < 8) ? WS_T + wg * 128 + (lane & 7) * 16 : OOB, v4u{(u32)te, (u32)te, (u32)te, (u32)te});
  }
  PINS(wv);
  if ((wv >> 1) == 2) {
#define WAUX 0
#define WL 1
#define WSLEEP 2
    __builtin_amdgcn_s_sleep(WSLEEP);
    const rsrc_t rwr = mkrs(p.wr, S_ * KF * 2), rwi = mkrs(p.wi, S_ * KF * 2);
    const int vf = (lane & 7) * (KF * 2) + (lane & 1) * 2 + ((tid & 127) >> 3) * 8;
    const int sf = (16 * (un >> 6) + 2 * (un & 4) + 256 * ((un >> 3) & 7)) * (KF * 2) + (un & 3) * 128;
#pragma unroll
    for (int r = 0; r < 4; ++r) { ra[r] = __builtin_amdgcn_raw_buffer_load_b128(rwr, vf, sf + r * (64 * KF * 2), WAUX); ra[4 + r] = __builtin_amdgcn_raw_buffer_load_b128(rwi, vf, sf + r * (64 * KF * 2), WAUX); }
    cpf = ld_dev1(mkrs(p.ws, WS_END), 0, WS_E);
  }
  const lp_t fmine = lptr(SCR + L_F + tid * 4);
  *(volatile LAS u32*)fmine = 0;
  lds_w128(lptr(SCR + L_IM + (tid & 127) * 128 + ((6 ^ (tid + 2 * (((tid & 127) * 43) >> 10))) & 7) * 16), v4u{0u, 0u, 0u, 0u});
  BAR();
  PINS(t_entry);
  u32 t_ref = (u32)t_entry, tb_ = 0;
  v4u wa_;
  asm volatile("" : "=v"(wa_));
  PINS(wv);
  if ((wv >> 1) == 2) {
    const int T = tid - 256, b = WS_W + (un >> 2) * WREC;
    wa_ = v4u{(u32)((T >> 3) * 512 + (T & 7) * 16 + b + (un & 3) * 8192), (lane & 1) ? 0u : 16u, (u32)((T & 7) ? OOB : b + 4 * 8192 + (un & 3) * 256 + T * 2), 0u};
    PIN(wa_);
  }
  PINS(wv);
  if (wv >= 6) {
#define X0SLEEP 8
    __builtin_amdgcn_s_sleep(X0SLEEP);
    const rsrc_t r1 = mkrs((wv & 1) ? p.wu : p.wg, W1SZ);
    ra[0].x = touch(r1, lane < 43 ? wg * 43 + lane : 16384);
  }
  SB();
  PINS(wv);

  if (wv < 4) {
  const int w = wv, c = lane & 15, g = lane >> 4;
  const float fc = (float)c, fg4 = (float)(4 * g);
  const f32x4 z = {0.f, 0.f, 0.f, 0.f};
  f32x4 twc, tws;
  v4u fbre, fbsw;
  u32 tag;
  float mean[2];
  const rsrc_t ws = mkrs(p.ws, WS_END);
  const lp_t fall = lptr(SCR + L_F + (lane & 3) * 256 + lane * 4);
  const lp_t imr = lptr(SCR + L_IM + w * 3072 + (lane >> 3) * 128 + ((lane ^ ((lane >> 3) + 2 * w)) & 7) * 16);
  int vx0 = lane * 16 + WS_A + un * XREC + w * (22 * 128), vx2 = lane < 48 ? vx0 + 2048 : OOB;
  PIN(vx0); PIN(vx2);
  int vq8 = (lane & 7) * 128;
  PIN(vq8);
  u32 tq;
  v2u xk[2][2];
  f32x4 tfc[2], tfs[2], tic[2], tis[2];
  const float fj = (float)(16 * w) + fc;
#pragma unroll
  for (int r = 0; r < 4; ++r) {
#pragma unroll
      for (int m = 0; m < 2; ++m) {
        const float nf = (fg4 + (16 * m + r)) * (fj * (1.f / 2048)), ni = (fg4 + (float)(16 * w) + r) * ((fc + 16 * m) * (1.f / 2048));
        tfc[m][r] = cosr(nf); tfs[m][r] = sinr(nf); tic[m][r] = cosr(ni); tis[m][r] = sinr(ni);
      }
  }
#pragma unroll
  for (int i = 0; i < 2; ++i) { PIN(tfc[i]); PIN(tfs[i]); PIN(tic[i]); PIN(tis[i]); }
  SB();
  {
    const v4u xr[2] = {ra[0], ra[1]}, tb[6] = {ra[2], ra[3], ra[4], ra[5], ra[6], ra[7]};
    const u32 sc = (c & 1) ? NEG2 : 0u, sm = c < 8 ? NEGL : NEGH, p0 = c < 8 ? 0x0c0c0100u : 0x01000c0cu, p1 = c < 8 ? 0x0c0c0302u : 0x03020c0cu;
    v4u fare = {tb[0].x, tb[0].y, tb[0].x ^ sc, tb[0].y ^ sc}, faim = {tb[0].z, tb[0].w, tb[0].z ^ sc, tb[0].w ^ sc}, fbim, f0re, f0im;
    twc = __builtin_bit_cast(f32x4, tb[1]); tws = __builtin_bit_cast(f32x4, tb[2]); fbre = tb[3];
    lp_t t1w[4], t1c[4];
#pragma unroll
    for (int r = 0; r < 4; ++r) {
      const u32 a = tb[3][r], q = tb[4][r];
      fbsw[r] = __builtin_amdgcn_alignbit(a, a, 16);
      fbim[r] = fbsw[r] ^ sm;
      f0re[r] = __builtin_amdgcn_perm(q, q, p0); f0im[r] = __builtin_amdgcn_perm(q, q, p1);
      t1w[r] = lptr((tb[5][r] & 0xffffu) ^ (SCR + w * 8)); t1c[r] = lptr((tb[5][r] >> 16) + SCR);
    }
    float c0m = c == 0 ? 1.f / 16 : 0.f;
    lp_t xw = lptr(SCR + L_X + w * 2048 + lane * 16), xt = lptr(SCR + L_X + w * 2048 + lane * 8);
    lp_t xq = lptr(SCR + L_X + w * 2048 + (4 * c + g) * 8);
    tag = cpf + 1u;
    u32 tag0 = w ? 0u : tag;
    PIN(c0m); PIN(tag); PIN(tag0);
    *(LAS u32*)(fmine + L_TG) = tag;
    *(LAS u32*)(fmine + L_TG + 1024) = 0;
    PIN(fare); PIN(faim); PIN(fbre); PIN(fbim); PIN(f0re); PIN(f0im); PIN(twc); PIN(tws);
    STAMP(1);
    SB();
    v4u a[2];
#pragma unroll
    for (int rr = 0; rr < 2; ++rr) { lds_w128(xw + rr * 1024, xr[rr]); a[rr] = lds_tr2(xt + rr * 1024, xt + rr * 1024 + 512); }
    tq = ld_dev1(ws, vq8, WS_T);
    STAMP(2);
    float st[4] = {0.f, 0.f, 0.f, 0.f};
#pragma unroll
    for (int i = 0; i < 4; ++i)
#pragma unroll
      for (int rr = 0; rr < 2; ++rr) { const u32 v = xr[rr][i]; st[rr] = dot2(v, 0x3f803f80u, st[rr]); st[2 + rr] = dot2(v, v, st[2 + rr]); }
    SB();
    f32x4 ur[2], ui[2];
#pragma unroll
    for (int rr = 0; rr < 2; ++rr) { ur[rr] = mm16p(a[rr], fare, z); ui[rr] = mm16p(a[rr], faim, z); }
    wave_sums<4>(st);
    SB();
    v4u a2[2];
    f32x4 orr[2], oi[2], sr[2], si[2];
#pragma unroll
    for (int rr = 0; rr < 2; ++rr) {
      const f32x4 x = ur[rr] - st[rr] * c0m, y = ui[rr];
      a2[rr] = pk4(x * twc + y * tws, y * twc - x * tws);
      orr[rr] = mm16p(a2[rr], fbre, z); oi[rr] = mm16p(a2[rr], fbim, z); sr[rr] = mm16p(a2[rr], f0re, z); si[rr] = mm16p(a2[rr], f0im, z);
    }
    v4u o[2];
#pragma unroll
    for (int rr = 0; rr < 2; ++rr) {
      mean[rr] = st[rr] * (1.f / 512);
      const float rstd = __builtin_amdgcn_rsqf(fmaxf(st[2 + rr] * (1.f / 512) - mean[rr] * mean[rr], 0.f) + 1e-5f);
      orr[rr][0] = g == 0 ? sr[rr][0] : orr[rr][0]; oi[rr][0] = g == 0 ? si[rr][0] : oi[rr][0];
      o[rr] = pk4(orr[rr] * rstd, oi[rr] * rstd);
    }
#pragma unroll
    for (int r = 0; r < 4; ++r) { v2u v = {o[0][r], o[1][r]}; lds_w64(t1w[r], v); lds_add(t1c[r], v.x + v.y + tag0); }
    STAMP(3);
    flag_set(fmine, 1);
    v4u v0, v1, v2;
    FLAG_WAIT(fall, 1, v0 = lds_r128(imr); v1 = lds_r128(imr + 1024); v2 = lds_r128(imr + 2048));
    st_dev(ws, vx0, v0); st_dev(ws, vx0 + 1024, v1); st_dev(ws, vx2, v2);
    flag_set(fmine, 2);
#pragma unroll
    for (int i = 0; i < 4; ++i) xk[i >> 1][i & 1] = lds_r64(xq + (i >> 1) * 1024 + (i & 1) * 512);
  }
  int vge, vgo, vg = (lane >> 3) * WREC + (lane & 7) * 16;
  {
    const int pay = (lane >> 1) * XREC + rec_pos(un) * 32 + (lane & 1) * 16, chk = (lane & 1) ? OOB : ((lane >> 1) ^ 1) * XREC + 96 + rec_pos(un) * 4;
    vge = (lane & 2) ? chk : pay; vgo = (lane & 2) ? pay : chk;
  }
  const int sg = w * (64 * XREC) + (22 * (un >> 6) + rec_line(un)) * 128;
#define GATHER(BUF) do { xl[0] = ld_dev(ws, vge, BUF + sg); xl[1] = ld_dev(ws, vgo, BUF + sg); xl[2] = ld_dev(ws, vge, BUF + sg + 32 * XREC); xl[3] = ld_dev(ws, vgo, BUF + sg + 32 * XREC); } while (0)
#define VERIFY(extra) ({ u32 k_ = 0; _Pragma("unroll") for (int m = 0; m < 2; ++m) { _Pragma("unroll") for (int i = 0; i < 4; ++i) xg[m][i] = (lane & 2) ? xl[2 * m + 1][i] : xl[2 * m][i]; \
      k_ += (lane & 2) ? xl[2 * m].x : xl[2 * m + 1].x; lds_w128(iw + m * 1024, xg[m]); } \
      u32 t_ = sum4(sum4((extra) - k_, xg[0]), xg[1]); t_ += dppu<0xb1>(t_); t_ += dppu<0x4e>(t_); t_; })
  const lp_t iw = lptr(SCR + L_I2 + w * 2048 + lane * 16);
  const int sw = WS_W + w * 16 * WREC + un * 128;
  v4u wq[2];
  STAMP(4);
  SB();
  v4u f2a[2][2];
  {
    v4u f1a[4], f1b[4], f2b[2][2];
    const u32 sc = (c & 1) ? NEG2 : 0u;
#pragma unroll
    for (int r = 0; r < 4; ++r) {
#pragma unroll
      for (int ks = 0; ks < 2; ++ks) {
        const float n = (fg4 + (16 * ks + r)) * (fj * (1.f / 64));
        const float cs = cosr(n), sn = sinr(n);
        f1a[ks][r] = pk(cs, sn); f1b[ks][r] = pk(-sn, cs);
        f1a[ks + 2][r] = f1a[ks][r] ^ sc; f1b[ks + 2][r] = f1b[ks][r] ^ sc;
      }
      const float n = (fg4 + r) * (fc * (1.f / 32));
      const float cs = cosr(n), sn = sinr(n);
      const u32 a = pk(cs, sn), b = pk(-sn, cs), sr = (r & 1) ? NEG2 : 0u;
      f2a[0][0][r] = a; f2a[1][0][r] = a ^ sr; f2a[0][1][r] = a ^ sc; f2a[1][1][r] = a ^ sc ^ sr;
      f2b[0][0][r] = b; f2b[1][0][r] = b ^ sr; f2b[0][1][r] = b ^ sc; f2b[1][1][r] = b ^ sc ^ sr;
    }
#pragma unroll
    for (int i = 0; i < 4; ++i) { PIN(f1a[i]); PIN(f1b[i]); PIN(f2a[i >> 1][i & 1]); PIN(f2b[i >> 1][i & 1]); }
    lp_t i2r = lptr(SCR + L_I2 + ((g >> 1) * 32 + c) * 32 + (g & 1) * 16);
    lp_t exw = lptr(SCR + L_EX + w * 2048 + lane * 16), exr = lptr(SCR + L_EX + lane * 16);
    lp_t imw[2], imc = lptr(SCR + im_chk(64 * w + lane));
#pragma unroll
    for (int n = 0; n < 2; ++n) { const int k = 64 * w + 32 * (g >> 1) + 16 * n + c; imw[n] = lptr(SCR + im_unit(k, 2 * rec_pos(k) + (g & 1))); }
    vx0 += WS_B - WS_A; vx2 += WS_B - WS_A;
    int vkw = (lane & 6) ? OOB : ((lane >> 3) + 8 * (lane & 1)) * WREC;
    PIN(vkw); PIN(vg); PIN(vge); PIN(vgo); PIN(vx0); PIN(vx2);
    tq -= t_ref; tq = tq < 150u ? tq : 0u;
    tq = max(tq, dppu<0xb1>(tq)); tq = max(tq, dppu<0x4e>(tq)); tq = max(tq, dppu<0x141>(tq));
    t_ref += __builtin_amdgcn_readfirstlane(tq);
    tb_ = t_ref;
    STAMP(5);
    WAIT1();
    STAMP(6);
    v4u xl[4], xg[2];
    f32x4 wr[2], wi[2];
    GATHER(WS_A);
#define WD_ 10
    SB();
    __builtin_amdgcn_s_sleep(WD_);
#define W_LD() do { wq[0] = ld_dev(ws, vg, sw); wq[1] = ld_dev(ws, vg, sw + 8 * WREC); kw = ld_dev1(ws, vkw, sw + 4 * 8192 - un * 124); } while (0)
    u32 kw;
    W_LD();
    flag_set(fmine, 3);
    SB();
    u32 bad = VERIFY(tag), spin = 0;
    if (__any(bad != 0)) {
      _Pragma("nounroll") do {
        MEMFENCE();
        if (bad) GATHER(WS_A);
        bad = VERIFY(tag);
      } while (__any(bad != 0) && ++spin < SPIN_CAP);
      SLIPPED();
      ++spin;
    }
    COUNT(20, spin);
    STAMP(7);
    v4u af[2][4], b2[2], a3[2];
    flag_set(fmine, 4);
    FLAG_WAIT(fall, 4, _Pragma("unroll") for (int i = 0; i < 8; ++i) af[i >> 2][i & 3] = lds_r128(i2r + (i & 3) * 2048 + (i >> 2) * 512));
    STAMP(8);
    SB();
    f32x4 dr[2], di[2];
#pragma unroll
    for (int m = 0; m < 2; ++m)
#pragma unroll
      for (int ks = 0; ks < 4; ++ks) { dr[m] = mm16(af[m][ks], f1a[ks], ks ? dr[m] : z); di[m] = mm16(af[m][ks], f1b[ks], ks ? di[m] : z); }
#pragma unroll
    for (int m = 0; m < 2; ++m) b2[m] = pk4(dr[m] * tfc[m] + di[m] * tfs[m], di[m] * tfc[m] - dr[m] * tfs[m]);
#pragma unroll
    for (int k = 0; k < 2; ++k)
#pragma unroll
      for (int m = 0; m < 2; ++m) { dr[m] = mm16(f2a[m][k], b2[k], k ? dr[m] : z); di[m] = mm16(f2b[m][k], b2[k], k ? di[m] : z); }
    {
#define W_BAD() ({ u32 t_ = sum4(sum4(0u - kw, wq[0]), wq[1]); t_ += dppu<0xb1>(t_); t_ += dppu<0x4e>(t_); t_ += dppu<0x141>(t_); t_ + 2u * tag; })
      u32 bw = W_BAD();
#pragma unroll
      for (int m = 0; m < 2; ++m) { PIN(dr[m]); PIN(di[m]); }
      if (__builtin_expect(__any(bw != 0), 0)) {
        u32 sp = 0;
        _Pragma("nounroll") do { MEMFENCE(); if (bw) W_LD(); bw = W_BAD(); } while (__any(bw != 0) && ++sp < SPIN_CAP);
        COUNT(24, 1);
      }
#pragma unroll
      for (int m = 0; m < 2; ++m)
#pragma unroll
        for (int r = 0; r < 4; ++r) { const u32 v = wq[m][r]; wr[m][r] = blo(v); wi[m][r] = bhi(v); }
    }
#pragma unroll
    for (int m = 0; m < 2; ++m) a3[m] = pk4(dr[m] * wi[m] + di[m] * wr[m], dr[m] * wr[m] - di[m] * wi[m]);
#pragma unroll
    for (int k = 0; k < 2; ++k)
#pragma unroll
      for (int n = 0; n < 2; ++n) { di[n] = mm16(a3[k], f2a[n][k], k ? di[n] : z); dr[n] = mm16(a3[k], f2b[n][k], k ? dr[n] : z); }
#pragma unroll
    for (int n = 0; n < 2; ++n) lds_w128(exw + n * 1024, pk4(di[n] * tic[n] + dr[n] * tis[n], dr[n] * tic[n] - di[n] * tis[n]));
    STAMP(9);
    flag_set(fmine, 5);
    FLAG_WAIT(fall, 5, _Pragma("unroll") for (int i = 0; i < 8; ++i) af[i >> 2][i & 3] = lds_r128(exr + (i & 3) * 2048 + (i >> 2) * 1024));
    SB();
    u32 cs[2];
#pragma unroll
    for (int n = 0; n < 2; ++n)
#pragma unroll
      for (int ks = 0; ks < 4; ++ks) { di[n] = mm16(f1a[ks], af[n][ks], ks ? di[n] : z); dr[n] = mm16(f1b[ks], af[n][ks], ks ? dr[n] : z); }
#pragma unroll
    for (int n = 0; n < 2; ++n) {
      const v4u e = pk4(dr[n], di[n]);
      lds_w128(imw[n], e);
      cs[n] = sum4(0u, e);
    }
    const auto sx = __builtin_amdgcn_permlane16_swap(cs[0], cs[1], false, false);
    *(LAS u32*)imc = tag + sx[0] + sx[1];
    MEMFENCE();
    const v4u v0 = lds_r128(imr), v1 = lds_r128(imr + 1024), v2 = lds_r128(imr + 2048);
    st_dev(ws, vx0, v0); st_dev(ws, vx0 + 1024, v1); st_dev(ws, vx2, v2);
    flag_set(fmine, 6);
  }
  STAMP(10);
  SB();
  {
    v4u gbre, gbim, g0re, g0im, ga[2];
#pragma unroll
    for (int r = 0; r < 4; ++r) {
      gbre[r] = fbre[r] ^ (g < 2 ? NEGH : 0u);
      gbim[r] = fbsw[r] ^ (g < 2 ? 0u : NEGH);
      const float n = fc * ((31.f - 2.f * r - 2.f * fg4) * (1.f / 32));
      u32 q = pk(cosr(n), -sinr(n));
      PIN(q);
      g0re[r] = g < 2 ? gbre[r] : 0u;
      g0im[r] = g < 2 ? 0u : q;
      ga[0][r] = f2a[0][0][r] ^ NEGH; ga[1][r] = f2a[1][0][r] ^ NEGH;
    }
    twc *= 2.f / (2048.f * 512.f); tws *= 2.f / (2048.f * 512.f);
    g0re[0] = g == 0 ? ((c & 1) ? 0xbf00u : 0x3f00u) : g0re[0];
    ga[0][0] = g == 0 ? ((c & 1) ? 0xbf803f80u : 0x3f803f80u) : ga[0][0]; ga[1][0] = ga[0][0];
    PIN(gbre); PIN(gbim); PIN(g0re); PIN(g0im); PIN(ga[0]); PIN(ga[1]); PIN(twc); PIN(tws);
    f32x4 xin[2][2];
#pragma unroll
    for (int rr = 0; rr < 2; ++rr)
#pragma unroll
      for (int t = 0; t < 2; ++t) {
        const v2u v = xk[rr][t];
        const u32 v0 = v.x, v1 = v.y;
        f32x4 q = {blo(v0), bhi(v0), blo(v1), bhi(v1)};
        xin[rr][t] = q - mean[rr]; PIN(xin[rr][t]);
      }
    lp_t i3r[4], o3w[2], o3r[2];
#pragma unroll
    for (int r = 0; r < 4; ++r) i3r[r] = lptr(SCR + L_I3 + slot_of(c, 4 * g + r) * 32 + w * 8);
#pragma unroll
    for (int i = 0; i < 2; ++i) {
      o3w[i] = lptr(SCR + L_O3 + ((4 * c + g) * 8 + ((2 * w + i) ^ (c & 7))) * 16);
      const int q = w * 128 + i * 64 + lane;
      o3r[i] = lptr(SCR + L_O3 + (q ^ ((q >> 5) & 7)) * 16);
    }
    int vs = lane * 16 + WS_L + un * LREC + w * 2048, vt = lane < 8 ? lane * 16 + WS_S + un * 512 + w * 128 : OOB;
    PIN(vs); PIN(vt);
    lp_t sow = lptr(SCR + L_SO + tid * 16), sor = lptr(SCR + L_SO + (lane & 6) * 512 + (lane & 1) * 8);
    WAIT2();
    STAMP(11);
    v4u xl[4], xg[2];
    GATHER(WS_B);
    flag_set(fmine, 7);
    SB();
    u32 bad = VERIFY(tag), spin = 0;
    if (__any(bad != 0)) {
      _Pragma("nounroll") do {
        __builtin_amdgcn_s_sleep(RS1);
        MEMFENCE();
        if (bad) GATHER(WS_B);
        bad = VERIFY(tag);
      } while (__any(bad != 0) && ++spin < SPIN_CAP);
      SLIPPED();
      ++spin;
    }
    COUNT(21, spin);
    STAMP(12);
    v4u a[2], a2[2];
    v2u zr[4];
    flag_set(fmine, 8);
    FLAG_WAIT(fall, 8, _Pragma("unroll") for (int r = 0; r < 4; ++r) zr[r] = lds_r64(i3r[r]));
    STAMP(13);
#pragma unroll
    for (int r = 0; r < 4; ++r) { a[0][r] = zr[r].x; a[1][r] = zr[r].y; }
    f32x4 tr[2], ti[2], sr[2], si[2], h[2][2];
#pragma unroll
    for (int rr = 0; rr < 2; ++rr) { tr[rr] = mm16p(a[rr], gbre, z); ti[rr] = mm16p(a[rr], gbim, z); sr[rr] = mm16p(a[rr], g0re, z); si[rr] = mm16p(a[rr], g0im, z); }
#pragma unroll
    for (int rr = 0; rr < 2; ++rr) {
      tr[rr][0] = g == 0 ? sr[rr][0] : tr[rr][0]; ti[rr][0] = g == 0 ? si[rr][0] : ti[rr][0];
      a2[rr] = pk4(tr[rr] * twc - ti[rr] * tws, ti[rr] * twc + tr[rr] * tws);
      h[rr][0] = mm16p(a2[rr], ga[0], xin[rr][0]); h[rr][1] = mm16p(a2[rr], ga[1], xin[rr][1]);
    }
    float st[2];
#pragma unroll
    for (int rr = 0; rr < 2; ++rr) { const f32x4 q = h[rr][0] * h[rr][0] + h[rr][1] * h[rr][1]; st[rr] = (q[0] + q[2]) + (q[1] + q[3]); }
    wave_sums<2>(st);
    v4u so;
#pragma unroll
    for (int rr = 0; rr < 2; ++rr) {
      const float ve = st[rr] * (1.f / 512) + 1e-5f, rstd = __builtin_amdgcn_rsqf(ve);
      const f32x4 l0 = h[rr][0] * rstd, l1 = h[rr][1] * rstd;
      v4u l4 = {pk(l0[0], l0[1]), pk(l0[2], l0[3]), pk(l1[0], l1[1]), pk(l1[2], l1[3])};
      lds_w128(o3w[rr], l4);
      so[2 * rr] = asu(mean[rr]); so[2 * rr + 1] = asu(ve * rstd);
    }
    lds_w128(sow, so);
    STAMP(14);
    v4u v0, v1;
    v2u ms;
    flag_set(fmine, 9);
    FLAG_WAIT(fall, 9, v0 = lds_r128(o3r[0]); v1 = lds_r128(o3r[1]); ms = lds_r64(sor));
    st_dev(ws, vs, v0); st_dev(ws, vs + 1024, v1);
    u32 s = sum4(sum4(0u, v0), v1);
    s += dppu<0x128>(s); s = add_x32(add_x16(s));
    v4u rec = {tag - s - ms.x - ms.y, ms.x, ms.y, tag};
    st_dev(ws, vt, rec);
    flag_set(fmine, 10);
    if (wg == 0 && w == 0) st_dev1(ws, WS_E, tag);
  }
  SB();
  {
#define LOOKN 6
    constexpr int LOOK = LOOKN;
    v4u ring[8], act[32];
    lp_t wa[4], wb[4];
#pragma unroll
    for (int t = 0; t < 4; ++t) wa[t] = lptr(t * SLOT + g * 512 + ((c ^ (2 * t + (c >> 3))) * 16));
#pragma unroll
    for (int i = 0; i < 4; ++i) wb[i] = lptr(((i & 1) ? 0 : 4 * SLOT) + ((lane >> 2) ^ (g & 1)) * 64 + (((i >> 1) ^ g) & 1) * 32 + (lane & 3) * 8);
    auto frag = [&](auto NN) -> v4u {
      constexpr int N = NN.value;
      if constexpr (N < 64) return lds_r128(wa[(N >> 1) & 1] + (N >> 2) * 2048 + (N & 1) * 256);
      else if constexpr (N < 128) return lds_r128(wa[N >> 5] + ((N & 31) >> 1) * 2048 + (N & 1) * 256);
      else { const lp_t b = wb[((N >> 4) & 2) + (N & 1)] + ((N & 31) >> 1) * 2048; return lds_tr2(b, b + 1024); }
    };
    int vo = WS_H + ((4 * rg + w) * NFT + ft0) * HREC + lane * 16, vk = (lane & 11) ? OOB : vo - lane * 16 + 1024 + ((lane >> 4) * 8 + (lane & 7)) * 4;
    int vo4 = vo + 4 * HREC, vk4 = vk + 4 * HREC;
    PIN(vo); PIN(vk); PIN(vo4); PIN(vk4);
    Epi e;
#define EPI(J0, J1) static_for<(J0), (J1)>([&](auto JJ) { constexpr int kd = kEp.op[JJ.value].kind, V = kEp.op[JJ.value].v, t = V >> 3; \
      if constexpr (kd == E_ST) st_dev(ws, (t < 4 ? vo : vo4) + (t & 3) * HREC, e.r); \
      else if constexpr (kd == E_STK) st_dev(ws, (t < 4 ? vk : vk4) + (t & 3) * HREC, e.k4); \
      else epi_op<kd, V>(e, t % 3 == 0 ? tA : t % 3 == 1 ? tB : tC, tag); \
      SB(); })
    int va0 = ((lane >> 3) & 1) * LREC + g * 128 + (lane & 7) * 16, va1 = va0 + 2 * LREC;
    int vr = ((lane >> 3) & 1) * 512 + g * 128 + (lane & 7) * 16;
    PIN(vr);
    const int sa = WS_L + (4 * rg + w) * 4 * LREC;
#define ACT_LD(i) act[i] = ld_dev(ws, (((i) & 1) ? va1 : va0) + (((i) >> 1) & 7) * 512, sa + ((i) >> 4) * 4096)
    f32x4 tA[4], tB[4], tC[4];
    WD_SETUP();
    v4u s0, s1;
#define A_HEAD() do { MEMFENCE(); PIN(va0); PIN(va1); s0 = ld_dev(ws, vr, WS_S + (4 * rg + w) * 2048); s1 = ld_dev(ws, vr + 1024, WS_S + (4 * rg + w) * 2048); SB(); \
      static_for<0, 2 * LOOK>([&](auto K) { ACT_LD(K.value); SB(); }); } while (0)
    WAIT3();
    STAMP(15);
    u32 spin = 0;
    A_HEAD();
    BAR();
    for (;; ++spin) {
      static_for<0, PD>([&](auto K) { ring[K.value ^ 1] = FRAG(K.value ^ 1); SB(); });
      if (__builtin_expect(__any(s0.w != tag || s1.w != tag), 0)) { _Pragma("nounroll") do { __builtin_amdgcn_s_sleep(RS3); A_HEAD(); } while (__any(s0.w != tag || s1.w != tag) && ++spin < SPIN_CAP); ++spin; }
      PIN(va0); PIN(va1);
      u32 m = (s0.x + s0.y + s0.z) + (s1.x + s1.y + s1.z);
      static_for<0, 64>([&](auto K) {
        constexpr int n = K.value, ks = n >> 2, q = (n & 1) * 2;
        f32x4* acc = (n & 2) ? tB : tA;
        mma<ks == 0>(acc[q], ring[n & 7], act[2 * ks]); SB();
        ring[NXT(n) & 7] = FRAG(NXT(n));
        if constexpr (!(n & 1) && ks + LOOK < 16) ACT_LD(2 * (ks + LOOK) + ((n >> 1) & 1));
        SB();
        mma<ks == 0>(acc[q + 1], ring[n & 7], act[2 * ks + 1]); SB();
        { const v4u v = act[2 * ks + ((n >> 1) & 1)]; u32 t = (n & 1) ? m + v.z + v.w : m + v.x + v.y; PIN(t); m = t; }
        SB();
      });
      if (__builtin_expect(__all(add_x32(add_x16(m)) == 8u * tag) || spin > SPIN_CAP, 1)) break;
      s0.w = ~tag;
    }
    COUNT(22, spin);
    BAR_N(15);
    STAMP(16);
#define G1_TILE(T, acc, ...) static_for<0, 32>([&](auto K) { constexpr int s = K.value, n = 32 * (T) + s, ks = s >> 1, q = (s & 1) * 2, j0 = epos(n), j1 = epos(n + 1), jm = j0 + (j1 - j0 - (T) / 4) / 2; \
      mma<ks == 0>(acc[q], ring[n & 7], act[2 * ks]); SB(); \
      if constexpr (n + PD < 192) ring[NXT(n) & 7] = FRAG(n + PD < 192 ? NXT(n) : 0); SB(); \
      EPI(j0, jm); \
      mma<ks == 0>(acc[q + 1], ring[n & 7], act[2 * ks + 1]); SB(); \
      __VA_ARGS__; EPI(jm, j1); SB(); })
#define PG2 16
#define PG4 12
#define PG3 10
    G1_TILE(2, tC, { });
    FSTAMP(26);
    G1_TILE(3, tA, { if constexpr (s == PG2) BAR_N(15); });
    FSTAMP(27);
    if (__builtin_expect(six, 1)) {
      G1_TILE(4, tB, { });
      FSTAMP(28);
      G1_TILE(5, tC, { if constexpr (s == PG4) BAR_N(15); });
      STAMP(17);
      MMA_DONE(); SB();
      EPI(kEp.first[5], kEp.n);
    } else {
      MMA_DONE(); SB();
      EPI(epos(128), kEp.first[4]);
      static_for<E_X1, E_STK>([&](auto KK) { epi_op<KK.value, 31>(e, tA, tag); });
      st_dev(ws, vk + 3 * HREC, e.k4);
#define SKSL 14
      __builtin_amdgcn_s_sleep(SKSL);
      BAR_N(15);
      FSTAMP(28);
      __builtin_amdgcn_s_sleep(SKSL + 6);
      STAMP(17);
    }
    STAMP(18);
    BAR();
#define CSL 10
    __builtin_amdgcn_s_sleep(CSL);
    DMA_WDS(4, 0);
    DMA_WDS(5, 1);
    BAR();
    if (w & 1) VM_WAIT(4); else VM_WAIT(8);
    BAR();
    VM_WAIT(0);
    BAR();
  }
  STAMP(24);
  STAMP_FLUSH();
  UNDEF_RA();
  asm volatile("" : "=v"(wa_));
  }
  PINS(wv);
  if (wv >= 4) {
    STAMP(0);
    const int hw = wv - 4;
    asm volatile("" :: "v"(cpf), "v"(ra[0]));
    if (hw < 2) {
    const rsrc_t ws = mkrs(p.ws, WS_END);
    const int T = tid - 256, vs = wa_.x;
    const u32 sh = wa_.y;
    u32 al[8][2], ck[4];
#pragma unroll
    for (int i = 0; i < 8; ++i) { const u32 a = ra[i].x, b = ra[i].y, d = ra[i].z; al[i][0] = __builtin_amdgcn_alignbit(b, a, sh); al[i][1] = __builtin_amdgcn_alignbit(d, b, sh); }
    v4u v[4];
#pragma unroll
    for (int e = 0; e < 4; ++e) {
#pragma unroll
      for (int r = 0; r < 4; ++r) v[e][r] = __builtin_amdgcn_perm(al[4 + r][e >> 1], al[r][e >> 1], (e & 1) ? 0x07060302u : 0x05040100u);
      if (!WL) st_dev(ws, vs + e * 128, v[e]);
      ck[e] = sum4(0u, v[e]);
    }
    const u32 c0 = pair_total(ck[0], ck[1], T & 1), c1 = pair_total(ck[2], ck[3], T & 1);
    u32 cq = ((T & 2) ? c1 : c0) + dppu<0x4e>((T & 2) ? c0 : c1);
    cq += dppu<0x104>(cq) + cpf + 1u;
    if (WL) {
      PACE(0, WL);
      flag_wait_lazy(lptr(SCR + L_F + (lane & 3) * 256 + lane * 4), 2);
#pragma unroll
      for (int e = 0; e < 4; ++e) st_dev(ws, vs + e * 128, v[e]);
    }
    st_dev(ws, wa_.z, v4u{cq, dppu<0x55>(cq), dppu<0xaa>(cq), dppu<0xff>(cq)});
    STAMP(1);
  }
    u32 tag;
    v4u y0[8];
    WD_SETUP();
    {
      const rsrc_t rwg = mkrs(p.wg, W1SZ), rwu = mkrs(p.wu, W1SZ);
      const int vx = 4 * (lane >> 3) * (F_ * 2) + ft0 * 32 + (lane & 7) * 16, vx8 = vx + 32 * F_ * 2;
      const int vy = six ? 4 * (lane >> 2) * (F_ * 2) + (ft0 + 4) * 32 + (lane & 3) * 16 : OOB;
      const u32 ax = ((lane >> 1) & 3) * SLOT + (16 * hw + (lane >> 3)) * 512 + (lane & 1) * 128 + (lane & 7) * 16;
#define PUT_X(u, off) do { transpose8(u); const u32 a_ = ax + (off); _Pragma("unroll") for (int i = 0; i < 8; ++i) lds_w128((lp_t)(size_t)(a_ ^ (i * 16)), u[i]); SB(); } while (0)
      v4u rb[8], rc[8], rd[8];
      const lp_t fall = lptr(SCR + L_F + (lane & 3) * 256 + lane * 4);
      const int s0 = 64 * hw * (F_ * 2);
#define THR 4
#define NA 2
#define NB 9
#define N0 0
#define NS 7
#define NL 99
#define YD 1
#define THRU 4
#define NH 2
#define TS 0
#define F1 2
#define F2 5
#define STEP(n) do { constexpr int j = (n) & 7; const int so_ = s0 + ((j & 3) + 256 * (j >> 2)) * (F_ * 2); \
        if ((n) == NA) { flag_wait_lazy(fall, 3); cpf = touch(rwd, lane < 11 ? wg * 43 + hw * 11 + lane : 16384); } if ((n) == NB) flag_wait_lazy(fall, 7); \
        if ((n) == NA + NH) flag_wait_lazy(fall, 4); if ((n) == NB + NH) flag_wait_lazy(fall, 8); \
        if ((n) == N0) flag_wait_lazy(fall, 2); if ((n) == NS) flag_wait_lazy(fall, 6); if ((n) == NL) flag_wait_lazy(fall, 10); \
        if ((n) > 1) asm volatile("s_waitcnt vmcnt(%0)" :: "n"((YD && (n) > 8) ? THRU : THR) : "memory"); \
        if ((n) < 8) { ra[j] = ld_in(rwg, vx, so_); rb[j] = ld_in(rwg, vx8, so_); y0[j] = ld_in(rwg, vy, so_); } \
        else { constexpr int j2 = (2 * (n)) & 7; const int sp_ = s0 + ((j2 & 3) + 256 * (j2 >> 2)) * (F_ * 2); \
          if ((n) < 12) { rc[j2] = ld_in(rwu, vx, sp_); rc[j2 + 1] = ld_in(rwu, vx, sp_ + F_ * 2); } else { rd[j2] = ld_in(rwu, vx8, sp_); rd[j2 + 1] = ld_in(rwu, vx8, sp_ + F_ * 2); } } SB(); } while (0)
      PACE(0, TS);
      STAMP(2);
      STEP(0); STEP(1); STEP(2); STEP(3); STEP(4); STEP(5); STEP(6); STEP(7); STEP(8); STEP(9);
      STAMP(3);
      if (NB > 10) STEP(10);
      if (NB > 11) STEP(11);
      PUT_X(ra, 0);
      PUT_X(rb, 4096);
      if (NB <= 10) STEP(10);
      if (NB <= 11) STEP(11);
      STEP(12); STEP(13); STEP(14); STEP(15);
      PUT_X(rc, 256);
      tag = *(LAS u32*)(fall + L_TG);
      STAMP(4);
      PUT_X(rd, 4096 + 256);
      PIN(tag);
      asm volatile("" :: "v"(cpf));
    }
    STAMP(5);
    BAR();
    const rsrc_t ws = mkrs(p.ws, WS_END);
    const int kh = hw & 1, rl = hw >> 1, g = lane >> 4;
    constexpr int PD = 4;
    const u32 ayo = hw * 8192 + g * 2048 + ((lane >> 2) & 3) * 256 + (lane & 4) * 16 + ((lane & 3) ^ ((lane >> 1) & 2)) * 16;
    const lp_t ay0[2] = {lptr(ayo + 4 * SLOT), lptr((ayo ^ 64) + 4 * SLOT)}, ad = lptr(3 * SLOT + hw * 8192 + lane * 16);
#define PUT_Y(a, y, j0, j1) _Pragma("unroll") for (int j = j0; j < j1; ++j) lds_w128(a[j & 1] + (j >> 2) * 1024 + (j & 2) * 64, y[j])
    PUT_Y(ay0, y0, 0, 8);
    v4u ring[8], hfA[8], hfB[8], hfC[8], hfD[8], hfE[8];
    u32 hkA, hkB, hkC, hkD, hkE, ms = 0, nfix = 0;
    lp_t wd[3][4];
    {
      const u32 dl = (g * 1024 + ((lane >> 2) & 3) * 128 + ((lane >> 3) & 3) * 32 + (lane & 3) * 8) ^ (kh * 64);
#pragma unroll
      for (int i = 0; i < 12; ++i) wd[i >> 2][i & 3] = lptr((dl ^ ((i & 3) * 32)) + (i >> 2) * 2 * SLOT + kh * 16384);
    }
    auto frag = [&](auto NN) -> v4u {
      constexpr int N = NN.value, t = N < 64 ? N >> 4 : N < 76 ? 4 : 5, s = N < 64 ? (N & 15) : N < 76 ? N - 64 : N - 76, slot = (t + 1) % 5;
      const lp_t b = wd[slot >> 1][s & 3] + (slot & 1) * SLOT + (s >> 2) * 4096;
      return lds_tr2(b, b + 512);
    };
    const int hb = WS_H + (4 * rg + 2 * (ch >> 3) + rl) * NFT * HREC;
    int hs[4];
#pragma unroll
    for (int i = 0; i < 4; ++i) { hs[i] = hb + tile0_of(8 * kh + 2 * i) * HREC; PINS(hs[i]); }
    int vl = (g & 1) * 512 + (lane & 15) * 16 + (g >> 1) * 6 * HREC, vl3 = vl - (g >> 1) * 2 * HREC, vl4 = vl + 4 * HREC, vlz = (kh && g >= 2) ? OOB : vl4, vq, vq4;
    {
      const int st = (lane & 2) + ((lane >> 1) & 4) + (g >> 1);
      vq = hb + 1024 + ((2 * (g & 1) + (lane & 1)) * 8 + (lane & 4)) * 4 + tile0_of(8 * kh + st) * HREC;
      vq4 = st < 6 - kh ? vq + 4 * HREC : OOB;
    }
    const int u = 16 * rg + 8 * (ch >> 3) + 4 * rl + ((lane >> 3) & 1), col = 64 * CT2 + 32 * kh;
    int vh = u * LREC + (col & 255) * 32 + g * 128 + (lane & 7) * 16 + (col >> 8) * 8, vs = u * 512 + (lane & 7) * 16 + 4;
    int vout = ((32 * (8 * (u >> 5) + (lane & 7)) + (u & 31)) * D_ + col + 16 * (g & 1) + 8 * (g >> 1)) * 2;
    lp_t rw = lptr(2 * SLOT + hw * 4096 + lane * 16), rr_ = lptr(2 * SLOT + (hw ^ 1) * 4096 + lane * 16);
    int vyd = ((lane >> 2) ^ (g & 1)) * (F_ * 2) + (ft0 + 4) * 32 + ((lane & 3) ^ ((lane >> 3) & 2)) * 16;
    PIN(vyd);
    u32 t8 = 8u * tag, t6 = ((kh && g >= 2) ? 4u : 6u) * tag;
    PIN(vl); PIN(vl3); PIN(vl4); PIN(vlz); PIN(vq); PIN(vq4); PIN(vh); PIN(vs); PIN(vout); PIN(t8); PIN(t6);
#define H_LD(X, t, j) do { \
      if constexpr ((j) == 8) hk##X = ld_dev1(ws, ((t) < 4 ? vq : vq4) + ((t) & 3) * HREC, 0); \
      else if constexpr ((t) < 4 || (j) < 6) hf##X[(j) & 7] = ld_dev(ws, ((t) < 4 ? ((j) < 6 ? vl : vl3) : (j) < 4 ? vl4 : vlz) + ((t) & 3) * HREC + ((j) & 1) * 256, hs[((j) >> 1) & 3]); } while (0)
#define H_LDALL(X, t) static_for<0, 9>([&](auto J) { THROTTLE(J.value); H_LD(X, t, J.value ? J.value - 1 : 8); })
#define H_SUM(X, nf, j) do { if constexpr ((j) >= 0 && (j) < (nf)) { ms = sum4((j) ? ms : hk##X, hf##X[(j) & 7]); u32 t_ = ms; PIN(t_); ms = t_; } \
      else if constexpr ((j) >= (nf) && (j) < (nf) + 3) { u32 t_ = ms + dppu<(j) == (nf) ? 0xb1 : (j) == (nf) + 1 ? 0x4e : 0x128>(ms); PIN(t_); ms = t_; } } while (0)
#define H_SUMALL(X, nf) static_for<0, (nf) + 3>([&](auto J) { H_SUM(X, nf, J.value); })
#define H_FIX(X, t, nf, want) do { if (__builtin_expect(__any(ms != (want)), 0)) { u32 spin_ = 0; _Pragma("nounroll") do { __builtin_amdgcn_s_sleep(RS3); MEMFENCE(); nfix |= 1u << (4 * (t)); \
        H_LDALL(X, t); H_SUMALL(X, nf); } while (__any(ms != (want)) && ++spin_ < SPIN_CAP); } SB(); } while (0)
    v4u d3[8];
    f32x4 o[4][2];
    v2u hr[4], so[2];
#define GRP() asm volatile("s_waitcnt vmcnt(%0)" :: "n"(TH2) : "memory")
#define RAW4(dst, t, i0) do { _Pragma("unroll") for (int i = i0; i < i0 + 4; ++i) { THROTTLE(i); dst[i] = ld_in(rwd, (i & 1) ? vd1 : vd, sd[i >> 1] + ((t) * 16 + (i & 1) * 8) * (D_ * 2)); } SB(); } while (0)
#define DMA4(t, slot, i0) do { _Pragma("unroll") for (int i = i0; i < i0 + 4; ++i) { THROTTLE(i); DMA_WD1(t, slot, i); } } while (0)
#define H4(X, t, h) do { static_for<5 * (h), 5 + 4 * (h)>([&](auto J) { if (J.value != 4) THROTTLE(J.value - 5 * (h)); H_LD(X, t, J.value ? J.value - 1 : 8); }); SB(); } while (0)
#define PUT_D(d, off, i0, i1) do { _Pragma("unroll") for (int i = i0; i < i1; ++i) lds_w128(ad + (off) + i * 1024, d[i]); SB(); } while (0)
#define RESID(i) do { const u32 v0 = hr[i].x, v1 = hr[i].y, mb = so[(i) & 1].x, sb = so[(i) & 1].y; float q[4] = {blo(v0), bhi(v0), blo(v1), bhi(v1)}; \
      _Pragma("unroll") for (int r = 0; r < 4; ++r) { q[r] = __builtin_fmaf(q[r], asf(sb), asf(mb)); PIN(q[r]); o[(i) >> 1][(i) & 1][r] = q[r]; } PIN(o[(i) >> 1][(i) & 1]); SB(); } while (0)
#define PSLEEP 16
    __builtin_amdgcn_s_sleep(PSLEEP);
#define SPRIO 3
#define SPRIO1 0
#define LOWP() __builtin_amdgcn_s_setprio(SPRIO1)
#define HIGHP() __builtin_amdgcn_s_setprio(SPRIO)
#define SPRIO2 3
    __builtin_amdgcn_s_setprio(SPRIO);
    RAW4(d3, 3, 0); RAW4(d3, 3, 4);
    STAMP(6);
    BAR();
    if (YD && six) {
      const rsrc_t rwu = mkrs(p.wu, W1SZ);
#pragma unroll
      for (int i = 0; i < 8; ++i) {
        asm volatile("s_waitcnt vmcnt(4)\n\ts_mov_b32 m0, %0\n\ts_nop 0\n\tbuffer_load_dwordx4 %1, %2, %3 offen lds"
                     :: "s"(hw * 8192 + i * 1024), "v"(vyd), "s"(rwu), "s"((16 * (4 * hw + (i >> 1)) + 256 * (i & 1)) * (F_ * 2)) : "memory");
      }
    }
#pragma unroll
    for (int i = 0; i < 2; ++i) so[i] = __builtin_amdgcn_raw_buffer_load_b64(ws, vs + i * 1024, WS_S, AUX_SC1);
#pragma unroll
    for (int i = 0; i < 4; ++i) hr[i] = __builtin_amdgcn_raw_buffer_load_b64(ws, vh + (i >> 1) * 512, WS_L + (i & 1) * 2 * LREC, AUX_SC1);
    SB();
    DMA4(0, 1, 0); DMA4(0, 1, 4);
    if (YD) VM_WAIT(8);
    STAMP(7);
    BAR();
    PIN(hr[3]);
    DMA4(1, 2, 0); DMA4(1, 2, 4); LOWP(); RESID(0); RESID(1); RESID(2); RESID(3);
    HIGHP();
    H4(A, 0, 0); H4(A, 0, 1);
#define BSL 0
    __builtin_amdgcn_s_sleep(BSL);
    H4(B, 1, 0); H4(B, 1, 1);
    PIN(hfA[7]);
    STAMP(8);
    BAR();
    DMA4(2, 3, 0); DMA4(2, 3, 4);
    LOWP();
    H_SUMALL(A, 8);
    H_FIX(A, 0, 8, t8);
    HIGHP();
    STAMP(9);
    H4(C, 2, 0); H4(C, 2, 1);
    VM_WAIT(9);
    H_SUMALL(B, 8);
    H_FIX(B, 1, 8, t8);
    static_for<0, PD>([&](auto K) { ring[K.value ^ 1] = FRAG(K.value ^ 1); SB(); });
    STAMP(10);
    LOWP();
    STAMP(11);
#define G2_ROUND(X, n0, ns, ...) static_for<0, ns>([&](auto K) { constexpr int s = K.value, n = (n0) + s, ct = s & 3, a = (s >> 2) * 2; \
      mma<n == 2 || n == 3>(o[ct][0], ring[n & 7], hf##X[a]); SB(); \
      if constexpr (n + PD < 88) ring[NXT(n) & 7] = FRAG(n + PD < 88 ? NXT(n) : 0); SB(); \
      mma<n == 2 || n == 3>(o[ct][1], ring[n & 7], hf##X[a + 1]); SB(); \
      __VA_ARGS__; SB(); })
#define PP 0
#define L3 16
#define L4 36
#define L5 50
#define LATE() do { if constexpr (n >= L3 && n < L3 + 9) H_LD(D, 3, n > L3 ? n - L3 - 1 : 8); \
      if constexpr (n >= L4 && n < L4 + 7) H_LD(E, 4, n > L4 ? n - L4 - 1 : 8); if constexpr (n >= L5 && n < L5 + 7) H_LD(A, 5, n > L5 ? n - L5 - 1 : 8); } while (0)
#define PG5 15
#define PS 1
#define MID() do { if constexpr (n == PG5) { BAR_N(15); __builtin_amdgcn_s_setprio(SPRIO2); } \
      if constexpr (n > PG5 && n <= PG5 + 8 * PS && (n - PG5 - 1) % PS == 0) lds_w128(ad + SLOT + ((n - PG5 - 1) / PS) * 1024, d3[((n - PG5 - 1) / PS) & 7]); \
      if constexpr (n == 43) BAR_N(8); LATE(); } while (0)
    static_assert(PG5 >= 15 && PG5 <= 27 && PG5 + 8 * PS <= 39, "");
    G2_ROUND(A, 0, 16, { MID(); });
    FSTAMP(14);
    G2_ROUND(B, 16, 16, { H_SUM(C, 8, s - 5); MID(); });
    H_FIX(C, 2, 8, t8);
    FSTAMP(15);
    G2_ROUND(C, 32, 16, { H_SUM(D, 8, s - 5); MID(); });
    H_FIX(D, 3, 8, t8);
    FSTAMP(16);
    G2_ROUND(D, 48, 16, { LATE(); H_SUM(E, 6, s - 7); if constexpr (s == 11) BAR_N(15); });
    H_FIX(E, 4, 6, t6);
    FSTAMP(17);
    G2_ROUND(E, 64, 12, { H_SUM(A, 6, s - 3); if constexpr (s == 7) BAR_N(15); });
    H_FIX(A, 5, 6, t6);
    FSTAMP(18);
    G2_ROUND(A, 76, 12, { });
    COUNT(23, nfix);
    STAMP(12);
    MMA_DONE(); SB();
#pragma unroll
    for (int i = 0; i < 4; ++i) lds_wf(rw + i * 1024, o[2 + (i & 1)][i >> 1]);
    BAR();
    const rsrc_t ro = mkrs(p.out, S_ * D_ * 2);
#pragma unroll
    for (int i = 0; i < 2; ++i) {
      const f32x4 v0 = o[0][i] + lds_rf(rr_ + i * 2048), v1 = o[1][i] + lds_rf(rr_ + i * 2048 + 1024);
      const auto x = __builtin_amdgcn_permlane16_swap(pk(v0[0], v0[1]), pk(v1[0], v1[1]), false, false);
      const auto y = __builtin_amdgcn_permlane16_swap(pk(v0[2], v0[3]), pk(v1[2], v1[3]), false, false);
      const v4u q = {x[0], y[0], x[1], y[1]};
      __builtin_amdgcn_raw_buffer_store_b128(q, ro, vout + i * 2048, 0, OAUX);
    }
    STAMP(13);
    STAMP_FLUSH();
  }
}

extern "C" void kernel_launch(void* const* d_in, const int* in_sizes, int n_in,
                              void* d_out, int out_size, void* d_ws, size_t ws_size,
                              hipStream_t stream) {
  Params p;
  p.x = (const u16*)d_in[0]; p.wr = (const u16*)d_in[1]; p.wi = (const u16*)d_in[2];
  p.wg = (const u16*)d_in[4]; p.wu = (const u16*)d_in[5]; p.wd = (const u16*)d_in[6];
  p.out = (u16*)d_out; p.ws = d_ws;
  (void)hipFuncSetAttribute((const void*)gfnet_geglu_kernel, hipFuncAttributeMaxDynamicSharedMemorySize, 5 * SLOT);
  gfnet_geglu_kernel<<<256, 512, 5 * SLOT, stream>>>(p);
}
```

```cpp
#define T1 260
#define T2 515
#define T3 725
#include <hip/hip_runtime.h>
#include <hip/hip_bf16.h>
#include <type_traits>

typedef unsigned u32;
typedef unsigned short u16;
typedef u32 v4u __attribute__((ext_vector_type(4)));
typedef u32 v2u __attribute__((ext_vector_type(2)));
typedef float f32x2 __attribute__((ext_vector_type(2)));
typedef float f32x4 __attribute__((ext_vector_type(4)));
typedef __bf16 bf16x2 __attribute__((ext_vector_type(2)));
typedef __bf16 bf16x8 __attribute__((ext_vector_type(8)));
typedef short s16x4 __attribute__((ext_vector_type(4)));
#define LAS __attribute__((address_space(3)))
#define DI __device__ __forceinline__
typedef __amdgpu_buffer_rsrc_t rsrc_t;
typedef LAS char* lp_t;

constexpr int S_ = 2048, D_ = 512, F_ = 1376, KF = 257;
constexpr int NFT = 86;
constexpr int WS_E = 0;
#define XPAD 1
#define LPAD 0
#define WPAD 1
constexpr int WREC = 4 * 8192 + 1024 + WPAD * 128, LREC = 8192 + LPAD * 128;
constexpr int XREC = (88 + XPAD) * 128;
constexpr int WS_A = 4096;
constexpr int WS_B = WS_A + 256 * XREC;
constexpr int WS_W = WS_B + 256 * XREC;
constexpr int WS_L = WS_W + 64 * WREC;
constexpr int WS_T = WS_L + 256 * LREC;
constexpr int WS_S = WS_T + (1 << 15);
constexpr int WS_H = WS_S + (1 << 17);
constexpr int HREC = 1152;
constexpr int WS_END = WS_H + 64 * NFT * HREC;
constexpr int WS_ST = 24 << 20;
constexpr u32 NEG2 = 0x80008000u, NEGH = 0x80000000u, NEGL = 0x8000u;
constexpr u32 SPIN_CAP = 1u << 14;
constexpr int AUX_SC1 = 16;
#define OAUX 2
#define NTAUX 2
constexpr int OOB = 0x40000000;
constexpr int CODE_LINES = 260;
constexpr int SLOT = 32768, SCR = 4 * SLOT;
constexpr int L_F = 0, L_TG = 2048;
constexpr int L_IM = 4096, L_SO = 4096, L_X = 16384, L_I2 = 16384, L_I3 = 16384, L_EX = 24576, L_O3 = 24576;

#define DRAIN() asm volatile("s_waitcnt vmcnt(0)" ::: "memory")
#define MEMFENCE() asm volatile("" ::: "memory")
#define PIN(x) asm volatile("" : "+v"(x))
#define PINS(x) asm volatile("" : "+s"(x))
#define SB() __builtin_amdgcn_sched_barrier(0)
#define BAR() asm volatile("s_waitcnt lgkmcnt(0)\n\ts_barrier" ::: "memory")
#define BAR_N(n) asm volatile("s_waitcnt lgkmcnt(" #n ")\n\ts_barrier" ::: "memory")

DI u32 pk(float a, float b) { f32x2 v = {a, b}; return __builtin_bit_cast(u32, __builtin_convertvector(v, bf16x2)); }
DI v4u pk4(f32x4 a, f32x4 b) { v4u r = {pk(a[0], b[0]), pk(a[1], b[1]), pk(a[2], b[2]), pk(a[3], b[3])}; return r; }
DI float asf(u32 v) { return __builtin_bit_cast(float, v); }
DI u32 asu(float v) { return __builtin_bit_cast(u32, v); }
DI float blo(u32 v) { return asf(v << 16); }
DI float bhi(u32 v) { return asf(v & 0xffff0000u); }
DI float dot2(u32 a, u32 b, float c) { return __builtin_amdgcn_fdot2_f32_bf16(__builtin_bit_cast(bf16x2, a), __builtin_bit_cast(bf16x2, b), c, false); }
DI u32 sum4(u32 m, v4u v) { return (m + v.x + v.y) + v.z + v.w; }
template <int CTRL> DI u32 dppu(u32 v) { return (u32)__builtin_amdgcn_update_dpp(0, (int)v, CTRL, 0xf, 0xf, true); }
template <int CTRL> DI float dppf(float v) { return asf(dppu<CTRL>(asu(v))); }
DI u32 pair_total(u32 s0, u32 s1, bool odd) { return (odd ? s1 : s0) + dppu<0xb1>(odd ? s0 : s1); }
DI u32 add_x16(u32 v) { const auto s = __builtin_amdgcn_permlane16_swap(v, v, false, false); return s[0] + s[1]; }
DI u32 add_x32(u32 v) { const auto s = __builtin_amdgcn_permlane32_swap(v, v, false, false); return s[0] + s[1]; }
template <int N> DI void wave_sums(float (&v)[N]) {
#define WS_STEP(C) _Pragma("unroll") for (int i = 0; i < N; ++i) v[i] += dppf<C>(v[i]);
  WS_STEP(0x111) WS_STEP(0x112) WS_STEP(0x114) WS_STEP(0x118) WS_STEP(0x142) WS_STEP(0x143)
#pragma unroll
  for (int i = 0; i < N; ++i) v[i] = asf(__builtin_amdgcn_readlane(asu(v[i]), 63));
}
__host__ __device__ constexpr int rec_q(int k) { return (k & 7) | ((k >> 1) & 0x18); }
__host__ __device__ constexpr int rec_line(int k) { return 11 * ((k >> 3) & 1) + ((rec_q(k) * 11) >> 5); }
__host__ __device__ constexpr int rec_pos(int k) { return rec_q(k) - 3 * ((rec_q(k) * 11) >> 5); }
__host__ __device__ constexpr int im_unit(int k, int j) { return L_IM + (k >> 6) * 3072 + rec_line(k) * 128 + ((j ^ (rec_line(k) + 2 * (k >> 6))) & 7) * 16; }
__host__ __device__ constexpr int im_chk(int k) { return im_unit(k, 6) + 4 * rec_pos(k); }
DI void lds_add(lp_t a, u32 v) { __hip_atomic_fetch_add((LAS u32*)a, v, __ATOMIC_RELAXED, __HIP_MEMORY_SCOPE_WORKGROUP); }
DI f32x4 mm16(v4u a, v4u b, f32x4 c) { return __builtin_amdgcn_mfma_f32_16x16x32_bf16(__builtin_bit_cast(bf16x8, a), __builtin_bit_cast(bf16x8, b), c, 0, 0, 0); }
DI f32x4 mm16p(v4u a, v4u b, f32x4 c) { f32x4 d = mm16(a, b, c); PIN(d); return d; }
template <bool FIRST> DI void mma(f32x4& d, v4u a, v4u b) {
  if constexpr (FIRST) asm volatile("v_mfma_f32_16x16x32_bf16 %0, %1, %2, 0" : "=&v"(d) : "v"(a), "v"(b));
  else asm volatile("v_mfma_f32_16x16x32_bf16 %0, %1, %2, %0" : "+v"(d) : "v"(a), "v"(b));
}
#define MMA_DONE() asm volatile("s_nop 7")
DI v2u lds_tr(lp_t a) { return __builtin_bit_cast(v2u, __builtin_amdgcn_ds_read_tr16_b64_v4i16((LAS s16x4*)a)); }
DI v4u lds_tr2(lp_t a, lp_t b) { v2u x = lds_tr(a), y = lds_tr(b); v4u r = {x.x, x.y, y.x, y.y}; return r; }
DI v4u lds_r128(lp_t a) { return *(LAS v4u*)a; }
DI f32x4 lds_rf(lp_t a) { return *(LAS f32x4*)a; }
DI v2u lds_r64(lp_t a) { return *(LAS v2u*)a; }
DI void lds_w128(lp_t a, v4u v) { *(LAS v4u*)a = v; }
DI void lds_wf(lp_t a, f32x4 v) { *(LAS f32x4*)a = v; }
DI void lds_w64(lp_t a, v2u v) { *(LAS v2u*)a = v; }
DI lp_t lptr(u32 off) { lp_t q = (lp_t)(size_t)off; PIN(q); return q; }
DI rsrc_t mkrs(const void* p, u32 bytes) { return __builtin_amdgcn_make_buffer_rsrc((void*)p, 0, bytes, 0x00020000); }
DI v4u ld_dev(rsrc_t r, int v, int s) { return __builtin_amdgcn_raw_buffer_load_b128(r, v, s, AUX_SC1); }
DI u32 ld_dev1(rsrc_t r, int v, int s) { return __builtin_amdgcn_raw_buffer_load_b32(r, v, s, AUX_SC1); }
DI void st_dev(rsrc_t r, int v, v4u x) { __builtin_amdgcn_raw_buffer_store_b128(x, r, v, 0, AUX_SC1); }
DI void st_dev1(rsrc_t r, int v, u32 x) { __builtin_amdgcn_raw_buffer_store_b32(x, r, v, 0, AUX_SC1); }
DI v4u ld_in(rsrc_t r, int v, int s) { return __builtin_amdgcn_raw_buffer_load_b128(r, v, s, 0); }
DI v4u ld_once(rsrc_t r, int v, int s) { return __builtin_amdgcn_raw_buffer_load_b128(r, v, s, NTAUX); }
DI u32 touch(rsrc_t r, int line) { return __builtin_amdgcn_raw_buffer_load_b32(r, line * 128, 0, 0); }
DI void flag_set(lp_t mine, u32 k) { MEMFENCE(); *(volatile LAS u32*)mine = k; MEMFENCE(); }
#define FLAG_WAIT(theirs, k, ...) do { u32 f_; _Pragma("nounroll") do { f_ = *(volatile LAS u32*)(theirs); MEMFENCE(); __VA_ARGS__; MEMFENCE(); } while (!__all(f_ >= (k))); } while (0)
DI void flag_wait_lazy(lp_t theirs, u32 k) { while (!__all(*(volatile LAS u32*)theirs >= k)) __builtin_amdgcn_s_sleep(1); MEMFENCE(); }
DI float cosr(float rev) { return __builtin_amdgcn_cosf(rev); }
DI float sinr(float rev) { return __builtin_amdgcn_sinf(rev); }
__host__ __device__ constexpr int slot_of(int k1, int k2) { return ((k2 < 8 ? k1 + 32 * k2 : (k1 ? 32 - k1 : 16) + 32 * (15 - k2)) - 1) & 255; }
DI int tile0_of(int c) { const int h = c >> 3, i = c & 7; return 44 * h + 4 * i + 2 * (i < 6 - h ? i : 6 - h); }
template <int I, int N, class F> DI void static_for(F&& f) {
  if constexpr (I < N) { f(std::integral_constant<int, I>{}); static_for<I + 1, N>(f); }
}
DI void transpose8(v4u (&m)[8]) {
  v4u o[8];
#pragma unroll
  for (int i = 0; i < 8; ++i)
#pragma unroll
    for (int j = 0; j < 4; ++j) { const u32 a = m[2 * j][i >> 1], b = m[2 * j + 1][i >> 1]; o[i][j] = __builtin_amdgcn_perm(b, a, (i & 1) ? 0x07060302u : 0x05040100u); }
#pragma unroll
  for (int i = 0; i < 8; ++i) { m[i] = o[i]; PIN(m[i]); }
}
enum { E_SQ, E_POLY, E_ARG, E_EXP, E_INC, E_RCP, E_GU, E_OUT, E_PACK, E_SWA, E_SWB, E_SUM, E_SUM2, E_ST, E_X1, E_X2, E_X8, E_CHK, E_STK };
struct EOp { int kind, v; };
struct EProg { EOp op[520]; int n, first[7]; };
constexpr EProg make_eprog() {
  EProg p = {};
  for (int t = 0; t < 6; ++t) {
    p.first[t] = p.n;
    for (int v = 0; v <= 8; ++v) {
      const int V = 8 * t + v;
      const bool head = v < 8, tail = v > 0, fin = t > 0 && t < 5;
      if (head) p.op[p.n++] = {E_SQ, V};
      if (tail) p.op[p.n++] = {E_INC, V - 1};
      if (head) p.op[p.n++] = {E_POLY, V}; else p.op[p.n++] = {E_GU, V - 1};
      if (tail) p.op[p.n++] = {E_RCP, V - 1}; else if (fin) p.op[p.n++] = {E_X1, V - 1};
      if (head) p.op[p.n++] = {E_ARG, V};
      if (tail && head) p.op[p.n++] = {E_GU, V - 1};
      if (v >= 3 && (v & 1)) p.op[p.n++] = {E_PACK, V - 2};
      if (v == 8) p.op[p.n++] = {E_SWA, V - 1};
      if (fin && v == 0) p.op[p.n++] = {E_X2, V - 1};
      if (fin && v == 1) p.op[p.n++] = {E_CHK, V - 2};
      if (head) p.op[p.n++] = {E_EXP, V};
      if (fin && v == 0) p.op[p.n++] = {E_X8, V - 1};
      if (fin && v == 1) p.op[p.n++] = {E_STK, V - 2};
      if (tail) p.op[p.n++] = {E_OUT, V - 1};
    }
    p.op[p.n++] = {E_PACK, 8 * t + 7}; p.op[p.n++] = {E_SUM, 8 * t + 7}; p.op[p.n++] = {E_SWB, 8 * t + 7}; p.op[p.n++] = {E_ST, 8 * t + 7}; p.op[p.n++] = {E_SUM2, 8 * t + 7};
    if (t >= 4) for (int k = E_X1; k <= E_STK; ++k) p.op[p.n++] = {k, 8 * t + 7};
  }
  p.first[6] = p.n;
  return p;
}
constexpr EProg kEp = make_eprog();
#define ER1 56
constexpr int epos(int n) {
  const int mid = (64 * ER1) / 16, end = kEp.first[5];
  int j = n <= 64 ? 0 : n <= 128 ? ((n - 64) * ER1) / 16 : n <= 192 ? mid + ((end - mid) * (n - 128)) / 64
        : end + ((kEp.n - end) * (n - 192)) / 8;
  for (int t = 2; t < 5; ++t) if (n <= 32 * t + 34 && j > kEp.first[t]) j = kEp.first[t];
  return j;
}
struct Epi { float t[2], q, h[3]; v4u r, k4; u32 sum, fin; };
template <int KIND, int V> DI void epi_op(Epi& e, const f32x4 (&a)[4], u32 tag) {
  constexpr int v = V & 7;
  const float gv = a[v >> 2][v & 3], uv = a[2 + (v >> 2)][v & 3];
  float& t = e.t[V & 1];
  if constexpr (KIND == E_SQ) { t = gv * gv; PIN(t); }
  else if constexpr (KIND == E_POLY) { t = __builtin_fmaf(t, -0.10294324f, -2.3022082f); PIN(t); }
  else if constexpr (KIND == E_ARG) { t = t * gv; PIN(t); }
  else if constexpr (KIND == E_EXP) { t = __builtin_amdgcn_exp2f(t); PIN(t); }
  else if constexpr (KIND == E_INC) { t = t + 1.0f; PIN(t); }
  else if constexpr (KIND == E_RCP) { t = __builtin_amdgcn_rcpf(t); PIN(t); }
  else if constexpr (KIND == E_GU) { e.q = gv * uv; PIN(e.q); }
  else if constexpr (KIND == E_OUT) { e.h[v & 1] = e.q * t; PIN(e.h[v & 1]); }
  else if constexpr (KIND == E_PACK) { u32 r = pk(e.h[0], e.h[1]); PIN(r); e.r[v >> 1] = r; }
  else if constexpr (KIND == E_SWA || KIND == E_SWB) { constexpr int i = KIND == E_SWB; const u32 x = e.r[i], y = e.r[i + 2];
    const auto q = __builtin_amdgcn_permlane16_swap(x, y, false, false); u32 q0 = q[0], q1 = q[1]; PIN(q0); PIN(q1); e.r[i] = q0; e.r[i + 2] = q1; }
  else if constexpr (KIND == E_SUM) { e.sum = e.r.x + e.r.z; PIN(e.sum); }
  else if constexpr (KIND == E_SUM2) { e.fin = e.sum + e.r.y + e.r.w; PIN(e.fin); }
  else if constexpr (KIND == E_X1) { e.fin += dppu<0xb1>(e.fin); PIN(e.fin); }
  else if constexpr (KIND == E_X2) { e.fin += dppu<0x4e>(e.fin); PIN(e.fin); }
  else if constexpr (KIND == E_X8) { e.fin += dppu<0x128>(e.fin); PIN(e.fin); }
  else if constexpr (KIND == E_CHK) { asm volatile("" : "=v"(e.k4)); e.k4.x = tag - e.fin; PIN(e.k4); }
}

constexpr double ct_sin(long n, long d) {
  n %= d; if (n < 0) n += d;
  if (2 * n > d) return -ct_sin(d - n, d);
  if (4 * n > d) return ct_sin(d - 2 * n, 2 * d);
  const double x = 6.283185307179586476925 * (double)n / (double)d;
  double t = x, r = x;
  for (int i = 1; i < 14; ++i) { t *= -x * x / ((2 * i) * (2 * i + 1)); r += t; }
  return r;
}
constexpr double ct_cos(long n, long d) { return ct_sin(4 * n + d, 4 * d); }
constexpr u32 ct_f32(double v) { return __builtin_bit_cast(u32, (float)v); }
constexpr u32 ct_bf(double v) { const u32 u = ct_f32(v); return (u + 0x7fffu + ((u >> 16) & 1u)) >> 16; }
constexpr u32 ct_pk(double a, double b) { return ct_bf(a) | (ct_bf(b) << 16); }
struct Tab { u32 v[6][64][4]; };
constexpr Tab make_tab() {
  Tab t = {};
  for (int l = 0; l < 64; ++l) {
    const int c = l & 15, g = l >> 4;
    for (int j = 0; j < 2; ++j) {
      const int a = 4 * g + 2 * j;
      t.v[0][l][j] = ct_pk(ct_cos(a * c, 32), ct_cos((a + 1) * c, 32));
      t.v[0][l][2 + j] = c ? ct_pk(-ct_sin(a * c, 32), -ct_sin((a + 1) * c, 32)) : ct_pk(1, -1);
    }
    for (int r = 0; r < 4; ++r) {
      const int b = 4 * g + r, sl = slot_of(b, c);
      t.v[1][l][r] = ct_f32(ct_cos(b * c, 512));
      t.v[2][l][r] = ct_f32(ct_sin(b * c, 512));
      t.v[3][l][r] = ct_pk(ct_cos(b * c, 16), ct_sin(b * c, 16));
      t.v[4][l][r] = c == 0 ? ct_pk((r & 1) ? -1 : 1, 0) : c < 8 ? ct_pk(ct_cos(b * c, 16), -ct_sin(b * c, 16)) : ct_pk(ct_cos(b * (31 - 2 * c), 32), -ct_sin(b * (31 - 2 * c), 32));
      t.v[5][l][r] = im_unit(sl, 2 * rec_pos(sl)) | (im_chk(sl) << 16);
    }
  }
  return t;
}
static_assert(true, "");
static __device__ const __attribute__((aligned(128))) Tab g_tab = make_tab();

#define STAMP(i) do { } while (0)
#define STAMP_DECL do { } while (0)
#define STAMP_FLUSH() do { } while (0)
#define COUNT(i, v) do { } while (0)

#define FSTAMP(i) do { } while (0)
#define PACE(d, t) do { __builtin_amdgcn_s_sleep(d); while ((u32)__builtin_amdgcn_s_memrealtime() - t_ref < (t)) __builtin_amdgcn_s_sleep(1); } while (0)
#define RS1 0
#define RS2 10
#define RS3 0
#define D1 1
#define D2 4
#define D3 6
#define TW 240
#define SLIPPED() (*(LAS u32*)(fmine + L_TG + 1024) = (u32)__builtin_amdgcn_s_memrealtime() - t_ref)
#define PACE2(d, t, sl) do { u32 r_ = *(LAS u32*)(fall + L_TG + 1024); r_ = max(r_, dppu<0xb1>(r_)); r_ = max(r_, dppu<0x4e>(r_)); \
      r_ = __builtin_amdgcn_readfirstlane(r_) + (sl); t_ref += r_ > (t) ? r_ - (t) : 0u; PACE(d, t); } while (0)
#define SL2 175
#define SL3 150
#define WAIT1() PACE(D1, T1)
#define WAIT2() PACE2(D2, T2, SL2)
#define WAIT3() PACE2(D3, T3, SL3)
struct Params {
  const u16* x; const u16* wr; const u16* wi; const u16* wg; const u16* wu; const u16* wd; u16* out; void* ws;
};

__global__ void __launch_bounds__(512, 2) gfnet_geglu_kernel(Params p) {
  const int wg = blockIdx.x, tid = threadIdx.x, lane = tid & 63;
  int wv = __builtin_amdgcn_readfirstlane(tid >> 6);
  STAMP_DECL;
  unsigned long long t_entry;
  asm volatile("s_memrealtime %0" : "=s"(t_entry));
  const int rg = 4 * ((wg & 7) >> 1) + (wg >> 6), ch = 8 * (wg & 1) + ((wg >> 3) & 7);
  const int un = 16 * rg + ch;
  const int ft0 = tile0_of(ch);
  const bool six = (ch & 7) < 6 - (ch >> 3);
#define W1SZ (D_ * F_ * 2)
#define CT2 ((ch + 4 - (int)((size_t)p.wd >> 7)) & 7)
#define WD_SETUP() const rsrc_t rwd = mkrs(p.wd, D_ * F_ * 2); \
    int vd = (lane >> 3) * (D_ * 2) + CT2 * 128 + ((lane & 7) ^ ((lane >> 3) & 2)) * 16, vd1 = vd ^ 64, sd[4]; PIN(vd); PIN(vd1); \
    _Pragma("unroll") for (int i = 0; i < 4; ++i) { sd[i] = tile0_of(4 * (wv & 3) + i) * (16 * D_ * 2); PINS(sd[i]); }
#define DMA_WD1(t, slot, i) asm volatile("s_mov_b32 m0, %0\n\ts_nop 0\n\tbuffer_load_dwordx4 %1, %2, %3 offen lds" \
      :: "s"((slot) * SLOT + (wv & 3) * 8192 + (i) * 1024), "v"(((i) & 1) ? vd1 : vd), "s"(rwd), "s"(sd[(i) >> 1] + ((t) * 16 + ((i) & 1) * 8) * (D_ * 2)) : "memory")
#define TH2 6
#define TG 4
#define THROTTLE(i) do { if ((i) % TG == 0) asm volatile("s_waitcnt vmcnt(%0)" :: "n"(TH2) : "memory"); } while (0)
#define DMA_WD(t, slot) do { _Pragma("unroll") for (int i = 0; i < 8; ++i) { THROTTLE(i); DMA_WD1(t, slot, i); } } while (0)
#define DMA_WDS(t, slot) do { _Pragma("unroll") for (int i = 0; i < 4; ++i) DMA_WD1(t, slot, i); if (!(wv & 1)) { _Pragma("unroll") for (int i = 4; i < 8; ++i) DMA_WD1(t, slot, i); } } while (0)
#define VM_WAIT(n) asm volatile("s_waitcnt vmcnt(" #n ")" ::: "memory")
#define PDN 4
  constexpr int PD = PDN;
#define FRAG(N) frag(std::integral_constant<int, (N)>{})
#define NXT(n) (((n) + PD) ^ 1)
  v4u ra[8];
  u32 cpf;
#define UNDEF_RA() do { asm volatile("" : "=v"(cpf)); _Pragma("unroll") for (int i = 0; i < 8; ++i) asm volatile("" : "=v"(ra[i])); } while (0)
  UNDEF_RA();
  if (wv < 4) {
    const int vt = lane * 16;
    int sx = (32 * (8 * (un >> 5) + 2 * wv) + (un & 31)) * (D_ * 2), sy = sx + 32 * D_ * 2;
    const rsrc_t rt = mkrs(&g_tab, sizeof(Tab));
    ra[2] = ld_in(rt, vt, 0); ra[3] = ld_in(rt, vt + 1024, 0);
    PINS(sx); PINS(sy);
    SB();
    const rsrc_t rx = mkrs(p.x, S_ * D_ * 2);
    ra[0] = ld_once(rx, vt, sx); ra[1] = ld_once(rx, vt, sy);
    SB();
#pragma unroll
    for (int i = 0; i < 4; ++i) ra[4 + i] = ld_in(rt, vt + i * 1024, 2048);
    cpf = ld_dev1(mkrs(p.ws, WS_END), 0, WS_E);
    STAMP(0);
  }
  PINS(wv);
  if (wv >= 6) {
    unsigned long long pc0;
    asm volatile("s_getpc_b64 %0" : "=s"(pc0));
    cpf = touch(mkrs((const void*)(pc0 & ~127ull), CODE_LINES * 128), lane < 9 ? (((wg >> 3) ^ (wv & 1)) * 9 + lane) : 4096);
    unsigned long long te;
    asm volatile("s_waitcnt lgkmcnt(0)\n\ts_mov_b64 %0, %1" : "=s"(te) : "s"(t_entry));
    st_dev(mkrs(p.ws, WS_END), (tid >= 504 && wg < 8) ? WS_T + wg * 128 + (lane & 7) * 16 : OOB, v4u{(u32)te, (u32)te, (u32)te, (u32)te});
  }
  PINS(wv);
  if ((wv >> 1) == 2) {
#define WAUX 0
#define WL 1
#define WSLEEP 2
    __builtin_amdgcn_s_sleep(WSLEEP);
    const rsrc_t rwr = mkrs(p.wr, S_ * KF * 2), rwi = mkrs(p.wi, S_ * KF * 2);
    const int vf = (lane & 7) * (KF * 2) + (lane & 1) * 2 + ((tid & 127) >> 3) * 8;
    const int sf = (16 * (un >> 6) + 2 * (un & 4) + 256 * ((un >> 3) & 7)) * (KF * 2) + (un & 3) * 128;
#pragma unroll
    for (int r = 0; r < 4; ++r) { ra[r] = __builtin_amdgcn_raw_buffer_load_b128(rwr, vf, sf + r * (64 * KF * 2), WAUX); ra[4 + r] = __builtin_amdgcn_raw_buffer_load_b128(rwi, vf, sf + r * (64 * KF * 2), WAUX); }
    cpf = ld_dev1(mkrs(p.ws, WS_END), 0, WS_E);
  }
  const lp_t fmine = lptr(SCR + L_F + tid * 4);
  *(volatile LAS u32*)fmine = 0;
  lds_w128(lptr(SCR + L_IM + (tid & 127) * 128 + ((6 ^ (tid + 2 * (((tid & 127) * 43) >> 10))) & 7) * 16), v4u{0u, 0u, 0u, 0u});
  BAR();
  PINS(t_entry);
  u32 t_ref = (u32)t_entry, tb_ = 0;
  v4u wa_;
  asm volatile("" : "=v"(wa_));
  PINS(wv);
  if ((wv >> 1) == 2) {
    const int T = tid - 256, b = WS_W + (un >> 2) * WREC;
    wa_ = v4u{(u32)((T >> 3) * 512 + (T & 7) * 16 + b + (un & 3) * 8192), (lane & 1) ? 0u : 16u, (u32)((T & 7) ? OOB : b + 4 * 8192 + (un & 3) * 256 + T * 2), 0u};
    PIN(wa_);
  }
  PINS(wv);
  if (wv >= 6) {
#define X0SLEEP 8
    __builtin_amdgcn_s_sleep(X0SLEEP);
    const rsrc_t r1 = mkrs((wv & 1) ? p.wu : p.wg, W1SZ);
    ra[0].x = touch(r1, lane < 43 ? wg * 43 + lane : 16384);
  }
  SB();
  PINS(wv);

  if (wv < 4) {
  const int w = wv, c = lane & 15, g = lane >> 4;
  const float fc = (float)c, fg4 = (float)(4 * g);
  const f32x4 z = {0.f, 0.f, 0.f, 0.f};
  f32x4 twc, tws;
  v4u fbre, fbsw;
  u32 tag;
  float mean[2];
  const rsrc_t ws = mkrs(p.ws, WS_END);
  const lp_t fall = lptr(SCR + L_F + (lane & 3) * 256 + lane * 4);
  const lp_t imr = lptr(SCR + L_IM + w * 3072 + (lane >> 3) * 128 + ((lane ^ ((lane >> 3) + 2 * w)) & 7) * 16);
  int vx0 = lane * 16 + WS_A + un * XREC + w * (22 * 128), vx2 = lane < 48 ? vx0 + 2048 : OOB;
  PIN(vx0); PIN(vx2);
  int vq8 = (lane & 7) * 128;
  PIN(vq8);
  u32 tq;
  v2u xk[2][2];
  f32x4 tfc[2], tfs[2], tic[2], tis[2];
  const float fj = (float)(16 * w) + fc;
#pragma unroll
  for (int r = 0; r < 4; ++r) {
#pragma unroll
      for (int m = 0; m < 2; ++m) {
        const float nf = (fg4 + (16 * m + r)) * (fj * (1.f / 2048)), ni = (fg4 + (float)(16 * w) + r) * ((fc + 16 * m) * (1.f / 2048));
        tfc[m][r] = cosr(nf); tfs[m][r] = sinr(nf); tic[m][r] = cosr(ni); tis[m][r] = sinr(ni);
      }
  }
#pragma unroll
  for (int i = 0; i < 2; ++i) { PIN(tfc[i]); PIN(tfs[i]); PIN(tic[i]); PIN(tis[i]); }
  SB();
  {
    const v4u xr[2] = {ra[0], ra[1]}, tb[6] = {ra[2], ra[3], ra[4], ra[5], ra[6], ra[7]};
    const u32 sc = (c & 1) ? NEG2 : 0u, sm = c < 8 ? NEGL : NEGH, p0 = c < 8 ? 0x0c0c0100u : 0x01000c0cu, p1 = c < 8 ? 0x0c0c0302u : 0x03020c0cu;
    v4u fare = {tb[0].x, tb[0].y, tb[0].x ^ sc, tb[0].y ^ sc}, faim = {tb[0].z, tb[0].w, tb[0].z ^ sc, tb[0].w ^ sc}, fbim, f0re, f0im;
    twc = __builtin_bit_cast(f32x4, tb[1]); tws = __builtin_bit_cast(f32x4, tb[2]); fbre = tb[3];
    lp_t t1w[4], t1c[4];
#pragma unroll
    for (int r = 0; r < 4; ++r) {
      const u32 a = tb[3][r], q = tb[4][r];
      fbsw[r] = __builtin_amdgcn_alignbit(a, a, 16);
      fbim[r] = fbsw[r] ^ sm;
      f0re[r] = __builtin_amdgcn_perm(q, q, p0); f0im[r] = __builtin_amdgcn_perm(q, q, p1);
      t1w[r] = lptr((tb[5][r] & 0xffffu) ^ (SCR + w * 8)); t1c[r] = lptr((tb[5][r] >> 16) + SCR);
    }
    float c0m = c == 0 ? 1.f / 16 : 0.f;
    lp_t xw = lptr(SCR + L_X + w * 2048 + lane * 16), xt = lptr(SCR + L_X + w * 2048 + lane * 8);
    lp_t xq = lptr(SCR + L_X + w * 2048 + (4 * c + g) * 8);
    tag = cpf + 1u;
    u32 tag0 = w ? 0u : tag;
    PIN(c0m); PIN(tag); PIN(tag0);
    *(LAS u32*)(fmine + L_TG) = tag;
    *(LAS u32*)(fmine + L_TG + 1024) = 0;
    PIN(fare); PIN(faim); PIN(fbre); PIN(fbim); PIN(f0re); PIN(f0im); PIN(twc); PIN(tws);
    STAMP(1);
    SB();
    v4u a[2];
#pragma unroll
    for (int rr = 0; rr < 2; ++rr) { lds_w128(xw + rr * 1024, xr[rr]); a[rr] = lds_tr2(xt + rr * 1024, xt + rr * 1024 + 512); }
    tq = ld_dev1(ws, vq8, WS_T);
    STAMP(2);
    float st[4] = {0.f, 0.f, 0.f, 0.f};
#pragma unroll
    for (int i = 0; i < 4; ++i)
#pragma unroll
      for (int rr = 0; rr < 2; ++rr) { const u32 v = xr[rr][i]; st[rr] = dot2(v, 0x3f803f80u, st[rr]); st[2 + rr] = dot2(v, v, st[2 + rr]); }
    SB();
    f32x4 ur[2], ui[2];
#pragma unroll
    for (int rr = 0; rr < 2; ++rr) { ur[rr] = mm16p(a[rr], fare, z); ui[rr] = mm16p(a[rr], faim, z); }
    wave_sums<4>(st);
    SB();
    v4u a2[2];
    f32x4 orr[2], oi[2], sr[2], si[2];
#pragma unroll
    for (int rr = 0; rr < 2; ++rr) {
      const f32x4 x = ur[rr] - st[rr] * c0m, y = ui[rr];
      a2[rr] = pk4(x * twc + y * tws, y * twc - x * tws);
      orr[rr] = mm16p(a2[rr], fbre, z); oi[rr] = mm16p(a2[rr], fbim, z); sr[rr] = mm16p(a2[rr], f0re, z); si[rr] = mm16p(a2[rr], f0im, z);
    }
    v4u o[2];
#pragma unroll
    for (int rr = 0; rr < 2; ++rr) {
      mean[rr] = st[rr] * (1.f / 512);
      const float rstd = __builtin_amdgcn_rsqf(fmaxf(st[2 + rr] * (1.f / 512) - mean[rr] * mean[rr], 0.f) + 1e-5f);
      orr[rr][0] = g == 0 ? sr[rr][0] : orr[rr][0]; oi[rr][0] = g == 0 ? si[rr][0] : oi[rr][0];
      o[rr] = pk4(orr[rr] * rstd, oi[rr] * rstd);
    }
#pragma unroll
    for (int r = 0; r < 4; ++r) { v2u v = {o[0][r], o[1][r]}; lds_w64(t1w[r], v); lds_add(t1c[r], v.x + v.y + tag0); }
    STAMP(3);
    flag_set(fmine, 1);
    v4u v0, v1, v2;
    FLAG_WAIT(fall, 1, v0 = lds_r128(imr); v1 = lds_r128(imr + 1024); v2 = lds_r128(imr + 2048));
    st_dev(ws, vx0, v0); st_dev(ws, vx0 + 1024, v1); st_dev(ws, vx2, v2);
    flag_set(fmine, 2);
#pragma unroll
    for (int i = 0; i < 4; ++i) xk[i >> 1][i & 1] = lds_r64(xq + (i >> 1) * 1024 + (i & 1) * 512);
  }
  int vge, vgo, vg = (lane >> 3) * WREC + (lane & 7) * 16;
  {
    const int pay = (lane >> 1) * XREC + rec_pos(un) * 32 + (lane & 1) * 16, chk = (lane & 1) ? OOB : ((lane >> 1) ^ 1) * XREC + 96 + rec_pos(un) * 4;
    vge = (lane & 2) ? chk : pay; vgo = (lane & 2) ? pay : chk;
  }
  const int sg = w * (64 * XREC) + (22 * (un >> 6) + rec_line(un)) * 128;
#define GATHER(BUF) do { xl[0] = ld_dev(ws, vge, BUF + sg); xl[1] = ld_dev(ws, vgo, BUF + sg); xl[2] = ld_dev(ws, vge, BUF + sg + 32 * XREC); xl[3] = ld_dev(ws, vgo, BUF + sg + 32 * XREC); } while (0)
#define VERIFY(extra) ({ u32 k_ = 0; _Pragma("unroll") for (int m = 0; m < 2; ++m) { _Pragma("unroll") for (int i = 0; i < 4; ++i) xg[m][i] = (lane & 2) ? xl[2 * m + 1][i] : xl[2 * m][i]; \
      k_ += (lane & 2) ? xl[2 * m].x : xl[2 * m + 1].x; lds_w128(iw + m * 1024, xg[m]); } \
      u32 t_ = sum4(sum4((extra) - k_, xg[0]), xg[1]); t_ += dppu<0xb1>(t_); t_ += dppu<0x4e>(t_); t_; })
  const lp_t iw = lptr(SCR + L_I2 + w * 2048 + lane * 16);
  const int sw = WS_W + w * 16 * WREC + un * 128;
  v4u wq[2];
  STAMP(4);
  SB();
  v4u f2a[2][2];
  {
    v4u f1a[4], f1b[4], f2b[2][2];
    const u32 sc = (c & 1) ? NEG2 : 0u;
#pragma unroll
    for (int r = 0; r < 4; ++r) {
#pragma unroll
      for (int ks = 0; ks < 2; ++ks) {
        const float n = (fg4 + (16 * ks + r)) * (fj * (1.f / 64));
        const float cs = cosr(n), sn = sinr(n);
        f1a[ks][r] = pk(cs, sn); f1b[ks][r] = pk(-sn, cs);
        f1a[ks + 2][r] = f1a[ks][r] ^ sc; f1b[ks + 2][r] = f1b[ks][r] ^ sc;
      }
      const float n = (fg4 + r) * (fc * (1.f / 32));
      const float cs = cosr(n), sn = sinr(n);
      const u32 a = pk(cs, sn), b = pk(-sn, cs), sr = (r & 1) ? NEG2 : 0u;
      f2a[0][0][r] = a; f2a[1][0][r] = a ^ sr; f2a[0][1][r] = a ^ sc; f2a[1][1][r] = a ^ sc ^ sr;
      f2b[0][0][r] = b; f2b[1][0][r] = b ^ sr; f2b[0][1][r] = b ^ sc; f2b[1][1][r] = b ^ sc ^ sr;
    }
#pragma unroll
    for (int i = 0; i < 4; ++i) { PIN(f1a[i]); PIN(f1b[i]); PIN(f2a[i >> 1][i & 1]); PIN(f2b[i >> 1][i & 1]); }
    lp_t i2r = lptr(SCR + L_I2 + ((g >> 1) * 32 + c) * 32 + (g & 1) * 16);
    lp_t exw = lptr(SCR + L_EX + w * 2048 + lane * 16), exr = lptr(SCR + L_EX + lane * 16);
    lp_t imw[2], imc = lptr(SCR + im_chk(64 * w + lane));
#pragma unroll
    for (int n = 0; n < 2; ++n) { const int k = 64 * w + 32 * (g >> 1) + 16 * n + c; imw[n] = lptr(SCR + im_unit(k, 2 * rec_pos(k) + (g & 1))); }
    vx0 += WS_B - WS_A; vx2 += WS_B - WS_A;
    int vkw = (lane & 6) ? OOB : ((lane >> 3) + 8 * (lane & 1)) * WREC;
    PIN(vkw); PIN(vg); PIN(vge); PIN(vgo); PIN(vx0); PIN(vx2);
    tq -= t_ref; tq = tq < 150u ? tq : 0u;
    tq = max(tq, dppu<0xb1>(tq)); tq = max(tq, dppu<0x4e>(tq)); tq = max(tq, dppu<0x141>(tq));
    t_ref += __builtin_amdgcn_readfirstlane(tq);
    tb_ = t_ref;
    STAMP(5);
    WAIT1();
    STAMP(6);
    v4u xl[4], xg[2];
    f32x4 wr[2], wi[2];
    GATHER(WS_A);
#define WD_ 10
    SB();
    __builtin_amdgcn_s_sleep(WD_);
#define W_LD() do { wq[0] = ld_dev(ws, vg, sw); wq[1] = ld_dev(ws, vg, sw + 8 * WREC); kw = ld_dev1(ws, vkw, sw + 4 * 8192 - un * 124); } while (0)
    u32 kw;
    W_LD();
    flag_set(fmine, 3);
    SB();
    u32 bad = VERIFY(tag), spin = 0;
    if (__any(bad != 0)) {
      _Pragma("nounroll") do {
        MEMFENCE();
        if (bad) GATHER(WS_A);
        bad = VERIFY(tag);
      } while (__any(bad != 0) && ++spin < SPIN_CAP);
      SLIPPED();
      ++spin;
    }
    COUNT(20, spin);
    STAMP(7);
    v4u af[2][4], b2[2], a3[2];
    flag_set(fmine, 4);
    FLAG_WAIT(fall, 4, _Pragma("unroll") for (int i = 0; i < 8; ++i) af[i >> 2][i & 3] = lds_r128(i2r + (i & 3) * 2048 + (i >> 2) * 512));
    STAMP(8);
    SB();
    f32x4 dr[2], di[2];
#pragma unroll
    for (int m = 0; m < 2; ++m)
#pragma unroll
      for (int ks = 0; ks < 4; ++ks) { dr[m] = mm16(af[m][ks], f1a[ks], ks ? dr[m] : z); di[m] = mm16(af[m][ks], f1b[ks], ks ? di[m] : z); }
#pragma unroll
    for (int m = 0; m < 2; ++m) b2[m] = pk4(dr[m] * tfc[m] + di[m] * tfs[m], di[m] * tfc[m] - dr[m] * tfs[m]);
#pragma unroll
    for (int k = 0; k < 2; ++k)
#pragma unroll
      for (int m = 0; m < 2; ++m) { dr[m] = mm16(f2a[m][k], b2[k], k ? dr[m] : z); di[m] = mm16(f2b[m][k], b2[k], k ? di[m] : z); }
    {
#define W_BAD() ({ u32 t_ = sum4(sum4(0u - kw, wq[0]), wq[1]); t_ += dppu<0xb1>(t_); t_ += dppu<0x4e>(t_); t_ += dppu<0x141>(t_); t_ + 2u * tag; })
      u32 bw = W_BAD();
#pragma unroll
      for (int m = 0; m < 2; ++m) { PIN(dr[m]); PIN(di[m]); }
      if (__builtin_expect(__any(bw != 0), 0)) {
        u32 sp = 0;
        _Pragma("nounroll") do { MEMFENCE(); if (bw) W_LD(); bw = W_BAD(); } while (__any(bw != 0) && ++sp < SPIN_CAP);
        COUNT(24, 1);
      }
#pragma unroll
      for (int m = 0; m < 2; ++m)
#pragma unroll
        for (int r = 0; r < 4; ++r) { const u32 v = wq[m][r]; wr[m][r] = blo(v); wi[m][r] = bhi(v); }
    }
#pragma unroll
    for (int m = 0; m < 2; ++m) a3[m] = pk4(dr[m] * wi[m] + di[m] * wr[m], dr[m] * wr[m] - di[m] * wi[m]);
#pragma unroll
    for (int k = 0; k < 2; ++k)
#pragma unroll
      for (int n = 0; n < 2; ++n) { di[n] = mm16(a3[k], f2a[n][k], k ? di[n] : z); dr[n] = mm16(a3[k], f2b[n][k], k ? dr[n] : z); }
#pragma unroll
    for (int n = 0; n < 2; ++n) lds_w128(exw + n * 1024, pk4(di[n] * tic[n] + dr[n] * tis[n], dr[n] * tic[n] - di[n] * tis[n]));
    STAMP(9);
    flag_set(fmine, 5);
    FLAG_WAIT(fall, 5, _Pragma("unroll") for (int i = 0; i < 8; ++i) af[i >> 2][i & 3] = lds_r128(exr + (i & 3) * 2048 + (i >> 2) * 1024));
    SB();
    u32 cs[2];
#pragma unroll
    for (int n = 0; n < 2; ++n)
#pragma unroll
      for (int ks = 0; ks < 4; ++ks) { di[n] = mm16(f1a[ks], af[n][ks], ks ? di[n] : z); dr[n] = mm16(f1b[ks], af[n][ks], ks ? dr[n] : z); }
#pragma unroll
    for (int n = 0; n < 2; ++n) {
      const v4u e = pk4(dr[n], di[n]);
      lds_w128(imw[n], e);
      cs[n] = sum4(0u, e);
    }
    const auto sx = __builtin_amdgcn_permlane16_swap(cs[0], cs[1], false, false);
    *(LAS u32*)imc = tag + sx[0] + sx[1];
    MEMFENCE();
    const v4u v0 = lds_r128(imr), v1 = lds_r128(imr + 1024), v2 = lds_r128(imr + 2048);
    st_dev(ws, vx0, v0); st_dev(ws, vx0 + 1024, v1); st_dev(ws, vx2, v2);
    flag_set(fmine, 6);
  }
  STAMP(10);
  SB();
  {
    v4u gbre, gbim, g0re, g0im, ga[2];
#pragma unroll
    for (int r = 0; r < 4; ++r) {
      gbre[r] = fbre[r] ^ (g < 2 ? NEGH : 0u);
      gbim[r] = fbsw[r] ^ (g < 2 ? 0u : NEGH);
      const float n = fc * ((31.f - 2.f * r - 2.f * fg4) * (1.f / 32));
      u32 q = pk(cosr(n), -sinr(n));
      PIN(q);
      g0re[r] = g < 2 ? gbre[r] : 0u;
      g0im[r] = g < 2 ? 0u : q;
      ga[0][r] = f2a[0][0][r] ^ NEGH; ga[1][r] = f2a[1][0][r] ^ NEGH;
    }
    twc *= 2.f / (2048.f * 512.f); tws *= 2.f / (2048.f * 512.f);
    g0re[0] = g == 0 ? ((c & 1) ? 0xbf00u : 0x3f00u) : g0re[0];
    ga[0][0] = g == 0 ? ((c & 1) ? 0xbf803f80u : 0x3f803f80u) : ga[0][0]; ga[1][0] = ga[0][0];
    PIN(gbre); PIN(gbim); PIN(g0re); PIN(g0im); PIN(ga[0]); PIN(ga[1]); PIN(twc); PIN(tws);
    f32x4 xin[2][2];
#pragma unroll
    for (int rr = 0; rr < 2; ++rr)
#pragma unroll
      for (int t = 0; t < 2; ++t) {
        const v2u v = xk[rr][t];
        const u32 v0 = v.x, v1 = v.y;
        f32x4 q = {blo(v0), bhi(v0), blo(v1), bhi(v1)};
        xin[rr][t] = q - mean[rr]; PIN(xin[rr][t]);
      }
    lp_t i3r[4], o3w[2], o3r[2];
#pragma unroll
    for (int r = 0; r < 4; ++r) i3r[r] = lptr(SCR + L_I3 + slot_of(c, 4 * g + r) * 32 + w * 8);
#pragma unroll
    for (int i = 0; i < 2; ++i) {
      o3w[i] = lptr(SCR + L_O3 + ((4 * c + g) * 8 + ((2 * w + i) ^ (c & 7))) * 16);
      const int q = w * 128 + i * 64 + lane;
      o3r[i] = lptr(SCR + L_O3 + (q ^ ((q >> 5) & 7)) * 16);
    }
    int vs = lane * 16 + WS_L + un * LREC + w * 2048, vt = lane < 8 ? lane * 16 + WS_S + un * 512 + w * 128 : OOB;
    PIN(vs); PIN(vt);
    lp_t sow = lptr(SCR + L_SO + tid * 16), sor = lptr(SCR + L_SO + (lane & 6) * 512 + (lane & 1) * 8);
    WAIT2();
    STAMP(11);
    v4u xl[4], xg[2];
    GATHER(WS_B);
    flag_set(fmine, 7);
    SB();
    u32 bad = VERIFY(tag), spin = 0;
    if (__any(bad != 0)) {
      _Pragma("nounroll") do {
        __builtin_amdgcn_s_sleep(RS1);
        MEMFENCE();
        if (bad) GATHER(WS_B);
        bad = VERIFY(tag);
      } while (__any(bad != 0) && ++spin < SPIN_CAP);
      SLIPPED();
      ++spin;
    }
    COUNT(21, spin);
    STAMP(12);
    v4u a[2], a2[2];
    v2u zr[4];
    flag_set(fmine, 8);
    FLAG_WAIT(fall, 8, _Pragma("unroll") for (int r = 0; r < 4; ++r) zr[r] = lds_r64(i3r[r]));
    STAMP(13);
#pragma unroll
    for (int r = 0; r < 4; ++r) { a[0][r] = zr[r].x; a[1][r] = zr[r].y; }
    f32x4 tr[2], ti[2], sr[2], si[2], h[2][2];
#pragma unroll
    for (int rr = 0; rr < 2; ++rr) { tr[rr] = mm16p(a[rr], gbre, z); ti[rr] = mm16p(a[rr], gbim, z); sr[rr] = mm16p(a[rr], g0re, z); si[rr] = mm16p(a[rr], g0im, z); }
#pragma unroll
    for (int rr = 0; rr < 2; ++rr) {
      tr[rr][0] = g == 0 ? sr[rr][0] : tr[rr][0]; ti[rr][0] = g == 0 ? si[rr][0] : ti[rr][0];
      a2[rr] = pk4(tr[rr] * twc - ti[rr] * tws, ti[rr] * twc + tr[rr] * tws);
      h[rr][0] = mm16p(a2[rr], ga[0], xin[rr][0]); h[rr][1] = mm16p(a2[rr], ga[1], xin[rr][1]);
    }
    float st[2];
#pragma unroll
    for (int rr = 0; rr < 2; ++rr) { const f32x4 q = h[rr][0] * h[rr][0] + h[rr][1] * h[rr][1]; st[rr] = (q[0] + q[2]) + (q[1] + q[3]); }
    wave_sums<2>(st);
    v4u so;
#pragma unroll
    for (int rr = 0; rr < 2; ++rr) {
      const float ve = st[rr] * (1.f / 512) + 1e-5f, rstd = __builtin_amdgcn_rsqf(ve);
      const f32x4 l0 = h[rr][0] * rstd, l1 = h[rr][1] * rstd;
      v4u l4 = {pk(l0[0], l0[1]), pk(l0[2], l0[3]), pk(l1[0], l1[1]), pk(l1[2], l1[3])};
      lds_w128(o3w[rr], l4);
      so[2 * rr] = asu(mean[rr]); so[2 * rr + 1] = asu(ve * rstd);
    }
    lds_w128(sow, so);
    STAMP(14);
    v4u v0, v1;
    v2u ms;
    flag_set(fmine, 9);
    FLAG_WAIT(fall, 9, v0 = lds_r128(o3r[0]); v1 = lds_r128(o3r[1]); ms = lds_r64(sor));
    st_dev(ws, vs, v0); st_dev(ws, vs + 1024, v1);
    u32 s = sum4(sum4(0u, v0), v1);
    s += dppu<0x128>(s); s = add_x32(add_x16(s));
    v4u rec = {tag - s - ms.x - ms.y, ms.x, ms.y, tag};
    st_dev(ws, vt, rec);
    flag_set(fmine, 10);
    if (wg == 0 && w == 0) st_dev1(ws, WS_E, tag);
  }
  SB();
  {
#define LOOKN 6
    constexpr int LOOK = LOOKN;
    v4u ring[8], act[32];
    lp_t wa[4], wb[4];
#pragma unroll
    for (int t = 0; t < 4; ++t) wa[t] = lptr(t * SLOT + g * 512 + ((c ^ (2 * t + (c >> 3))) * 16));
#pragma unroll
    for (int i = 0; i < 4; ++i) wb[i] = lptr(((i & 1) ? 0 : 4 * SLOT) + ((lane >> 2) ^ (g & 1)) * 64 + (((i >> 1) ^ g) & 1) * 32 + (lane & 3) * 8);
    auto frag = [&](auto NN) -> v4u {
      constexpr int N = NN.value;
      if constexpr (N < 64) return lds_r128(wa[(N >> 1) & 1] + (N >> 2) * 2048 + (N & 1) * 256);
      else if constexpr (N < 128) return lds_r128(wa[N >> 5] + ((N & 31) >> 1) * 2048 + (N & 1) * 256);
      else { const lp_t b = wb[((N >> 4) & 2) + (N & 1)] + ((N & 31) >> 1) * 2048; return lds_tr2(b, b + 1024); }
    };
    int vo = WS_H + ((4 * rg + w) * NFT + ft0) * HREC + lane * 16, vk = (lane & 11) ? OOB : vo - lane * 16 + 1024 + ((lane >> 4) * 8 + (lane & 7)) * 4;
    int vo4 = vo + 4 * HREC, vk4 = vk + 4 * HREC;
    PIN(vo); PIN(vk); PIN(vo4); PIN(vk4);
    Epi e;
#define EPI(J0, J1) static_for<(J0), (J1)>([&](auto JJ) { constexpr int kd = kEp.op[JJ.value].kind, V = kEp.op[JJ.value].v, t = V >> 3; \
      if constexpr (kd == E_ST) st_dev(ws, (t < 4 ? vo : vo4) + (t & 3) * HREC, e.r); \
      else if constexpr (kd == E_STK) st_dev(ws, (t < 4 ? vk : vk4) + (t & 3) * HREC, e.k4); \
      else epi_op<kd, V>(e, t % 3 == 0 ? tA : t % 3 == 1 ? tB : tC, tag); \
      SB(); })
    int va0 = ((lane >> 3) & 1) * LREC + g * 128 + (lane & 7) * 16, va1 = va0 + 2 * LREC;
    int vr = ((lane >> 3) & 1) * 512 + g * 128 + (lane & 7) * 16;
    PIN(vr);
    const int sa = WS_L + (4 * rg + w) * 4 * LREC;
#define ACT_LD(i) act[i] = ld_dev(ws, (((i) & 1) ? va1 : va0) + (((i) >> 1) & 7) * 512, sa + ((i) >> 4) * 4096)
    f32x4 tA[4], tB[4], tC[4];
    WD_SETUP();
    v4u s0, s1;
#define A_HEAD() do { MEMFENCE(); PIN(va0); PIN(va1); s0 = ld_dev(ws, vr, WS_S + (4 * rg + w) * 2048); s1 = ld_dev(ws, vr + 1024, WS_S + (4 * rg + w) * 2048); SB(); \
      static_for<0, 2 * LOOK>([&](auto K) { ACT_LD(K.value); SB(); }); } while (0)
    WAIT3();
    STAMP(15);
    u32 spin = 0;
    A_HEAD();
    BAR();
    for (;; ++spin) {
      static_for<0, PD>([&](auto K) { ring[K.value ^ 1] = FRAG(K.value ^ 1); SB(); });
      if (__builtin_expect(__any(s0.w != tag || s1.w != tag), 0)) { _Pragma("nounroll") do { __builtin_amdgcn_s_sleep(RS3); A_HEAD(); } while (__any(s0.w != tag || s1.w != tag) && ++spin < SPIN_CAP); ++spin; }
      PIN(va0); PIN(va1);
      u32 m = (s0.x + s0.y + s0.z) + (s1.x + s1.y + s1.z);
      static_for<0, 64>([&](auto K) {
        constexpr int n = K.value, ks = n >> 2, q = (n & 1) * 2;
        f32x4* acc = (n & 2) ? tB : tA;
        mma<ks == 0>(acc[q], ring[n & 7], act[2 * ks]); SB();
        ring[NXT(n) & 7] = FRAG(NXT(n));
        if constexpr (!(n & 1) && ks + LOOK < 16) ACT_LD(2 * (ks + LOOK) + ((n >> 1) & 1));
        SB();
        mma<ks == 0>(acc[q + 1], ring[n & 7], act[2 * ks + 1]); SB();
        { const v4u v = act[2 * ks + ((n >> 1) & 1)]; u32 t = (n & 1) ? m + v.z + v.w : m + v.x + v.y; PIN(t); m = t; }
        SB();
      });
      if (__builtin_expect(__all(add_x32(add_x16(m)) == 8u * tag) || spin > SPIN_CAP, 1)) break;
      s0.w = ~tag;
    }
    COUNT(22, spin);
    BAR_N(15);
    STAMP(16);
#define G1_TILE(T, acc, ...) static_for<0, 32>([&](auto K) { constexpr int s = K.value, n = 32 * (T) + s, ks = s >> 1, q = (s & 1) * 2, j0 = epos(n), j1 = epos(n + 1), jm = j0 + (j1 - j0 - (T) / 4) / 2; \
      mma<ks == 0>(acc[q], ring[n & 7], act[2 * ks]); SB(); \
      if constexpr (n + PD < 192) ring[NXT(n) & 7] = FRAG(n + PD < 192 ? NXT(n) : 0); SB(); \
      EPI(j0, jm); \
      mma<ks == 0>(acc[q + 1], ring[n & 7], act[2 * ks + 1]); SB(); \
      __VA_ARGS__; EPI(jm, j1); SB(); })
#define PG2 16
#define PG4 12
#define PG3 10
    G1_TILE(2, tC, { });
    FSTAMP(26);
    G1_TILE(3, tA, { if constexpr (s == PG2) BAR_N(15); });
    FSTAMP(27);
    if (__builtin_expect(six, 1)) {
      G1_TILE(4, tB, { });
      FSTAMP(28);
      G1_TILE(5, tC, { if constexpr (s == PG4) BAR_N(15); });
      STAMP(17);
      MMA_DONE(); SB();
      EPI(kEp.first[5], kEp.n);
    } else {
      MMA_DONE(); SB();
      EPI(epos(128), kEp.first[4]);
      static_for<E_X1, E_STK>([&](auto KK) { epi_op<KK.value, 31>(e, tA, tag); });
      st_dev(ws, vk + 3 * HREC, e.k4);
#define SKSL 14
      __builtin_amdgcn_s_sleep(SKSL);
      BAR_N(15);
      FSTAMP(28);
      __builtin_amdgcn_s_sleep(SKSL + 6);
      STAMP(17);
    }
    STAMP(18);
    BAR();
#define CSL 10
    __builtin_amdgcn_s_sleep(CSL);
    DMA_WDS(4, 0);
    DMA_WDS(5, 1);
    BAR();
    if (w & 1) VM_WAIT(4); else VM_WAIT(8);
    BAR();
    VM_WAIT(0);
    BAR();
  }
  STAMP(24);
  STAMP_FLUSH();
  UNDEF_RA();
  asm volatile("" : "=v"(wa_));
  }
  PINS(wv);
  if (wv >= 4) {
    STAMP(0);
    const int hw = wv - 4;
    asm volatile("" :: "v"(cpf), "v"(ra[0]));
    if (hw < 2) {
    const rsrc_t ws = mkrs(p.ws, WS_END);
    const int T = tid - 256, vs = wa_.x;
    const u32 sh = wa_.y;
    u32 al[8][2], ck[4];
#pragma unroll
    for (int i = 0; i < 8; ++i) { const u32 a = ra[i].x, b = ra[i].y, d = ra[i].z; al[i][0] = __builtin_amdgcn_alignbit(b, a, sh); al[i][1] = __builtin_amdgcn_alignbit(d, b, sh); }
    v4u v[4];
#pragma unroll
    for (int e = 0; e < 4; ++e) {
#pragma unroll
      for (int r = 0; r < 4; ++r) v[e][r] = __builtin_amdgcn_perm(al[4 + r][e >> 1], al[r][e >> 1], (e & 1) ? 0x07060302u : 0x05040100u);
      if (!WL) st_dev(ws, vs + e * 128, v[e]);
      ck[e] = sum4(0u, v[e]);
    }
    const u32 c0 = pair_total(ck[0], ck[1], T & 1), c1 = pair_total(ck[2], ck[3], T & 1);
    u32 cq = ((T & 2) ? c1 : c0) + dppu<0x4e>((T & 2) ? c0 : c1);
    cq += dppu<0x104>(cq) + cpf + 1u;
    if (WL) {
      PACE(0, WL);
      flag_wait_lazy(lptr(SCR + L_F + (lane & 3) * 256 + lane * 4), 2);
#pragma unroll
      for (int e = 0; e < 4; ++e) st_dev(ws, vs + e * 128, v[e]);
    }
    st_dev(ws, wa_.z, v4u{cq, dppu<0x55>(cq), dppu<0xaa>(cq), dppu<0xff>(cq)});
    STAMP(1);
  }
    u32 tag;
    v4u y0[8];
    WD_SETUP();
    {
      const rsrc_t rwg = mkrs(p.wg, W1SZ), rwu = mkrs(p.wu, W1SZ);
      const int vx = 4 * (lane >> 3) * (F_ * 2) + ft0 * 32 + (lane & 7) * 16, vx8 = vx + 32 * F_ * 2;
      const int vy = six ? 4 * (lane >> 2) * (F_ * 2) + (ft0 + 4) * 32 + (lane & 3) * 16 : OOB;
      const u32 ax = ((lane >> 1) & 3) * SLOT + (16 * hw + (lane >> 3)) * 512 + (lane & 1) * 128 + (lane & 7) * 16;
#define PUT_X(u, off) do { transpose8(u); const u32 a_ = ax + (off); _Pragma("unroll") for (int i = 0; i < 8; ++i) lds_w128((lp_t)(size_t)(a_ ^ (i * 16)), u[i]); SB(); } while (0)
      v4u rb[8], rc[8], rd[8];
      const lp_t fall = lptr(SCR + L_F + (lane & 3) * 256 + lane * 4);
      const int s0 = 64 * hw * (F_ * 2);
#define THR 4
#define NA 2
#define NB 9
#define N0 0
#define NS 7
#define NL 99
#define YD 1
#define THRU 4
#define NH 2
#define TS 0
#define F1 2
#define F2 5
#define STEP(n) do { constexpr int j = (n) & 7; const int so_ = s0 + ((j & 3) + 256 * (j >> 2)) * (F_ * 2); \
        if ((n) == NA) { flag_wait_lazy(fall, 3); cpf = touch(rwd, lane < 11 ? wg * 43 + hw * 11 + lane : 16384); } if ((n) == NB) flag_wait_lazy(fall, 7); \
        if ((n) == NA + NH) flag_wait_lazy(fall, 4); if ((n) == NB + NH) flag_wait_lazy(fall, 8); \
        if ((n) == N0) flag_wait_lazy(fall, 2); if ((n) == NS) flag_wait_lazy(fall, 6); if ((n) == NL) flag_wait_lazy(fall, 10); \
        if ((n) > 1) asm volatile("s_waitcnt vmcnt(%0)" :: "n"((YD && (n) > 8) ? THRU : THR) : "memory"); \
        if ((n) < 8) { ra[j] = ld_in(rwg, vx, so_); rb[j] = ld_in(rwg, vx8, so_); y0[j] = ld_in(rwg, vy, so_); } \
        else { constexpr int j2 = (2 * (n)) & 7; const int sp_ = s0 + ((j2 & 3) + 256 * (j2 >> 2)) * (F_ * 2); \
          if ((n) < 12) { rc[j2] = ld_in(rwu, vx, sp_); rc[j2 + 1] = ld_in(rwu, vx, sp_ + F_ * 2); } else { rd[j2] = ld_in(rwu, vx8, sp_); rd[j2 + 1] = ld_in(rwu, vx8, sp_ + F_ * 2); } } SB(); } while (0)
      PACE(0, TS);
      STAMP(2);
      STEP(0); STEP(1); STEP(2); STEP(3); STEP(4); STEP(5); STEP(6); STEP(7); STEP(8); STEP(9);
      STAMP(3);
      if (NB > 10) STEP(10);
      if (NB > 11) STEP(11);
      PUT_X(ra, 0);
      PUT_X(rb, 4096);
      if (NB <= 10) STEP(10);
      if (NB <= 11) STEP(11);
      STEP(12); STEP(13); STEP(14); STEP(15);
      PUT_X(rc, 256);
      tag = *(LAS u32*)(fall + L_TG);
      STAMP(4);
      PUT_X(rd, 4096 + 256);
      PIN(tag);
      asm volatile("" :: "v"(cpf));
    }
    STAMP(5);
    BAR();
    const rsrc_t ws = mkrs(p.ws, WS_END);
    const int kh = hw & 1, rl = hw >> 1, g = lane >> 4;
    constexpr int PD = 4;
    const u32 ayo = hw * 8192 + g * 2048 + ((lane >> 2) & 3) * 256 + (lane & 4) * 16 + ((lane & 3) ^ ((lane >> 1) & 2)) * 16;
    const lp_t ay0[2] = {lptr(ayo + 4 * SLOT), lptr((ayo ^ 64) + 4 * SLOT)}, ad = lptr(3 * SLOT + hw * 8192 + lane * 16);
#define PUT_Y(a, y, j0, j1) _Pragma("unroll") for (int j = j0; j < j1; ++j) lds_w128(a[j & 1] + (j >> 2) * 1024 + (j & 2) * 64, y[j])
    PUT_Y(ay0, y0, 0, 8);
    v4u ring[8], hfA[8], hfB[8], hfC[8], hfD[8], hfE[8];
    u32 hkA, hkB, hkC, hkD, hkE, ms = 0, nfix = 0;
    lp_t wd[3][4];
    {
      const u32 dl = (g * 1024 + ((lane >> 2) & 3) * 128 + ((lane >> 3) & 3) * 32 + (lane & 3) * 8) ^ (kh * 64);
#pragma unroll
      for (int i = 0; i < 12; ++i) wd[i >> 2][i & 3] = lptr((dl ^ ((i & 3) * 32)) + (i >> 2) * 2 * SLOT + kh * 16384);
    }
    auto frag = [&](auto NN) -> v4u {
      constexpr int N = NN.value, t = N < 64 ? N >> 4 : N < 76 ? 4 : 5, s = N < 64 ? (N & 15) : N < 76 ? N - 64 : N - 76, slot = (t + 1) % 5;
      const lp_t b = wd[slot >> 1][s & 3] + (slot & 1) * SLOT + (s >> 2) * 4096;
      return lds_tr2(b, b + 512);
    };
    const int hb = WS_H + (4 * rg + 2 * (ch >> 3) + rl) * NFT * HREC;
    int hs[4];
#pragma unroll
    for (int i = 0; i < 4; ++i) { hs[i] = hb + tile0_of(8 * kh + 2 * i) * HREC; PINS(hs[i]); }
    int vl = (g & 1) * 512 + (lane & 15) * 16 + (g >> 1) * 6 * HREC, vl3 = vl - (g >> 1) * 2 * HREC, vl4 = vl + 4 * HREC, vlz = (kh && g >= 2) ? OOB : vl4, vq, vq4;
    {
      const int st = (lane & 2) + ((lane >> 1) & 4) + (g >> 1);
      vq = hb + 1024 + ((2 * (g & 1) + (lane & 1)) * 8 + (lane & 4)) * 4 + tile0_of(8 * kh + st) * HREC;
      vq4 = st < 6 - kh ? vq + 4 * HREC : OOB;
    }
    const int u = 16 * rg + 8 * (ch >> 3) + 4 * rl + ((lane >> 3) & 1), col = 64 * CT2 + 32 * kh;
    int vh = u * LREC + (col & 255) * 32 + g * 128 + (lane & 7) * 16 + (col >> 8) * 8, vs = u * 512 + (lane & 7) * 16 + 4;
    int vout = ((32 * (8 * (u >> 5) + (lane & 7)) + (u & 31)) * D_ + col + 16 * (g & 1) + 8 * (g >> 1)) * 2;
    lp_t rw = lptr(2 * SLOT + hw * 4096 + lane * 16), rr_ = lptr(2 * SLOT + (hw ^ 1) * 4096 + lane * 16);
    int vyd = ((lane >> 2) ^ (g & 1)) * (F_ * 2) + (ft0 + 4) * 32 + ((lane & 3) ^ ((lane >> 3) & 2)) * 16;
    PIN(vyd);
    u32 t8 = 8u * tag, t6 = ((kh && g >= 2) ? 4u : 6u) * tag;
    PIN(vl); PIN(vl3); PIN(vl4); PIN(vlz); PIN(vq); PIN(vq4); PIN(vh); PIN(vs); PIN(vout); PIN(t8); PIN(t6);
#define H_LD(X, t, j) do { \
      if constexpr ((j) == 8) hk##X = ld_dev1(ws, ((t) < 4 ? vq : vq4) + ((t) & 3) * HREC, 0); \
      else if constexpr ((t) < 4 || (j) < 6) hf##X[(j) & 7] = ld_dev(ws, ((t) < 4 ? ((j) < 6 ? vl : vl3) : (j) < 4 ? vl4 : vlz) + ((t) & 3) * HREC + ((j) & 1) * 256, hs[((j) >> 1) & 3]); } while (0)
#define H_LDALL(X, t) static_for<0, 9>([&](auto J) { THROTTLE(J.value); H_LD(X, t, J.value ? J.value - 1 : 8); })
#define H_SUM(X, nf, j) do { if constexpr ((j) >= 0 && (j) < (nf)) { ms = sum4((j) ? ms : hk##X, hf##X[(j) & 7]); u32 t_ = ms; PIN(t_); ms = t_; } \
      else if constexpr ((j) >= (nf) && (j) < (nf) + 3) { u32 t_ = ms + dppu<(j) == (nf) ? 0xb1 : (j) == (nf) + 1 ? 0x4e : 0x128>(ms); PIN(t_); ms = t_; } } while (0)
#define H_SUMALL(X, nf) static_for<0, (nf) + 3>([&](auto J) { H_SUM(X, nf, J.value); })
#define H_FIX(X, t, nf, want) do { if (__builtin_expect(__any(ms != (want)), 0)) { u32 spin_ = 0; _Pragma("nounroll") do { __builtin_amdgcn_s_sleep(RS3); MEMFENCE(); nfix |= 1u << (4 * (t)); \
        H_LDALL(X, t); H_SUMALL(X, nf); } while (__any(ms != (want)) && ++spin_ < SPIN_CAP); } SB(); } while (0)
    v4u d3[8];
    f32x4 o[4][2];
    v2u hr[4], so[2];
#define GRP() asm volatile("s_waitcnt vmcnt(%0)" :: "n"(TH2) : "memory")
#define RAW4(dst, t, i0) do { _Pragma("unroll") for (int i = i0; i < i0 + 4; ++i) { THROTTLE(i); dst[i] = ld_in(rwd, (i & 1) ? vd1 : vd, sd[i >> 1] + ((t) * 16 + (i & 1) * 8) * (D_ * 2)); } SB(); } while (0)
#define DMA4(t, slot, i0) do { _Pragma("unroll") for (int i = i0; i < i0 + 4; ++i) { THROTTLE(i); DMA_WD1(t, slot, i); } } while (0)
#define H4(X, t, h) do { static_for<5 * (h), 5 + 4 * (h)>([&](auto J) { if (J.value != 4) THROTTLE(J.value - 5 * (h)); H_LD(X, t, J.value ? J.value - 1 : 8); }); SB(); } while (0)
#define PUT_D(d, off, i0, i1) do { _Pragma("unroll") for (int i = i0; i < i1; ++i) lds_w128(ad + (off) + i * 1024, d[i]); SB(); } while (0)
#define RESID(i) do { const u32 v0 = hr[i].x, v1 = hr[i].y, mb = so[(i) & 1].x, sb = so[(i) & 1].y; float q[4] = {blo(v0), bhi(v0), blo(v1), bhi(v1)}; \
      _Pragma("unroll") for (int r = 0; r < 4; ++r) { q[r] = __builtin_fmaf(q[r], asf(sb), asf(mb)); PIN(q[r]); o[(i) >> 1][(i) & 1][r] = q[r]; } PIN(o[(i) >> 1][(i) & 1]); SB(); } while (0)
#define PSLEEP 16
    __builtin_amdgcn_s_sleep(PSLEEP);
#define SPRIO 3
#define SPRIO1 0
#define LOWP() __builtin_amdgcn_s_setprio(SPRIO1)
#define HIGHP() __builtin_amdgcn_s_setprio(SPRIO)
#define SPRIO2 3
    __builtin_amdgcn_s_setprio(SPRIO);
    RAW4(d3, 3, 0); RAW4(d3, 3, 4);
    STAMP(6);
    BAR();
    if (YD && six) {
      const rsrc_t rwu = mkrs(p.wu, W1SZ);
#pragma unroll
      for (int i = 0; i < 8; ++i) {
        asm volatile("s_waitcnt vmcnt(4)\n\ts_mov_b32 m0, %0\n\ts_nop 0\n\tbuffer_load_dwordx4 %1, %2, %3 offen lds"
                     :: "s"(hw * 8192 + i * 1024), "v"(vyd), "s"(rwu), "s"((16 * (4 * hw + (i >> 1)) + 256 * (i & 1)) * (F_ * 2)) : "memory");
      }
    }
#pragma unroll
    for (int i = 0; i < 2; ++i) so[i] = __builtin_amdgcn_raw_buffer_load_b64(ws, vs + i * 1024, WS_S, AUX_SC1);
#pragma unroll
    for (int i = 0; i < 4; ++i) hr[i] = __builtin_amdgcn_raw_buffer_load_b64(ws, vh + (i >> 1) * 512, WS_L + (i & 1) * 2 * LREC, AUX_SC1);
    SB();
    DMA4(0, 1, 0); DMA4(0, 1, 4);
    if (YD) VM_WAIT(8);
    STAMP(7);
    BAR();
    PIN(hr[3]);
    DMA4(1, 2, 0); DMA4(1, 2, 4); LOWP(); RESID(0); RESID(1); RESID(2); RESID(3);
    HIGHP();
    H4(A, 0, 0); H4(A, 0, 1);
#define BSL 0
    __builtin_amdgcn_s_sleep(BSL);
    H4(B, 1, 0); H4(B, 1, 1);
    PIN(hfA[7]);
    STAMP(8);
    BAR();
    DMA4(2, 3, 0); DMA4(2, 3, 4);
    LOWP();
    H_SUMALL(A, 8);
    H_FIX(A, 0, 8, t8);
    HIGHP();
    STAMP(9);
    H4(C, 2, 0); H4(C, 2, 1);
    VM_WAIT(9);
    H_SUMALL(B, 8);
    H_FIX(B, 1, 8, t8);
    static_for<0, PD>([&](auto K) { ring[K.value ^ 1] = FRAG(K.value ^ 1); SB(); });
    STAMP(10);
    LOWP();
    STAMP(11);
#define G2_ROUND(X, n0, ns, ...) static_for<0, ns>([&](auto K) { constexpr int s = K.value, n = (n0) + s, ct = s & 3, a = (s >> 2) * 2; \
      mma<n == 2 || n == 3>(o[ct][0], ring[n & 7], hf##X[a]); SB(); \
      if constexpr (n + PD < 88) ring[NXT(n) & 7] = FRAG(n + PD < 88 ? NXT(n) : 0); SB(); \
      mma<n == 2 || n == 3>(o[ct][1], ring[n & 7], hf##X[a + 1]); SB(); \
      __VA_ARGS__; SB(); })
#define PP 0
#define L3 16
#define L4 36
#define L5 50
#define LATE() do { if constexpr (n >= L3 && n < L3 + 9) H_LD(D, 3, n > L3 ? n - L3 - 1 : 8); \
      if constexpr (n >= L4 && n < L4 + 7) H_LD(E, 4, n > L4 ? n - L4 - 1 : 8); if constexpr (n >= L5 && n < L5 + 7) H_LD(A, 5, n > L5 ? n - L5 - 1 : 8); } while (0)
#define PG5 15
#define PS 1
#define MID() do { if constexpr (n == PG5) { BAR_N(15); __builtin_amdgcn_s_setprio(SPRIO2); } \
      if constexpr (n > PG5 && n <= PG5 + 8 * PS && (n - PG5 - 1) % PS == 0) lds_w128(ad + SLOT + ((n - PG5 - 1) / PS) * 1024, d3[((n - PG5 - 1) / PS) & 7]); \
      if constexpr (n == 43) BAR_N(8); LATE(); } while (0)
    static_assert(PG5 >= 15 && PG5 <= 27 && PG5 + 8 * PS <= 39, "");
    G2_ROUND(A, 0, 16, { MID(); });
    FSTAMP(14);
    G2_ROUND(B, 16, 16, { H_SUM(C, 8, s - 5); MID(); });
    H_FIX(C, 2, 8, t8);
    FSTAMP(15);
    G2_ROUND(C, 32, 16, { H_SUM(D, 8, s - 5); MID(); });
    H_FIX(D, 3, 8, t8);
    FSTAMP(16);
    G2_ROUND(D, 48, 16, { LATE(); H_SUM(E, 6, s - 7); if constexpr (s == 11) BAR_N(15); });
    H_FIX(E, 4, 6, t6);
    FSTAMP(17);
    G2_ROUND(E, 64, 12, { H_SUM(A, 6, s - 3); if constexpr (s == 7) BAR_N(15); });
    H_FIX(A, 5, 6, t6);
    FSTAMP(18);
    G2_ROUND(A, 76, 12, { });
    COUNT(23, nfix);
    STAMP(12);
    MMA_DONE(); SB();
#pragma unroll
    for (int i = 0; i < 4; ++i) lds_wf(rw + i * 1024, o[2 + (i & 1)][i >> 1]);
    BAR();
    const rsrc_t ro = mkrs(p.out, S_ * D_ * 2);
#pragma unroll
    for (int i = 0; i < 2; ++i) {
      const f32x4 v0 = o[0][i] + lds_rf(rr_ + i * 2048), v1 = o[1][i] + lds_rf(rr_ + i * 2048 + 1024);
      const auto x = __builtin_amdgcn_permlane16_swap(pk(v0[0], v0[1]), pk(v1[0], v1[1]), false, false);
      const auto y = __builtin_amdgcn_permlane16_swap(pk(v0[2], v0[3]), pk(v1[2], v1[3]), false, false);
      const v4u q = {x[0], y[0], x[1], y[1]};
      __builtin_amdgcn_raw_buffer_store_b128(q, ro, vout + i * 2048, 0, OAUX);
    }
    STAMP(13);
    STAMP_FLUSH();
  }
}

extern "C" void kernel_launch(void* const* d_in, const int* in_sizes, int n_in,
                              void* d_out, int out_size, void* d_ws, size_t ws_size,
                              hipStream_t stream) {
  Params p;
  p.x = (const u16*)d_in[0]; p.wr = (const u16*)d_in[1]; p.wi = (const u16*)d_in[2];
  p.wg = (const u16*)d_in[4]; p.wu = (const u16*)d_in[5]; p.wd = (const u16*)d_in[6];
  p.out = (u16*)d_out; p.ws = d_ws;
  (void)hipFuncSetAttribute((const void*)gfnet_geglu_kernel, hipFuncAttributeMaxDynamicSharedMemorySize, 5 * SLOT);
  gfnet_geglu_kernel<<<256, 512, 5 * SLOT, stream>>>(p);
}
```

```cpp
#define T1 280
#define T2 540
#define T3 755
#include <hip/hip_runtime.h>
#include <hip/hip_bf16.h>
#include <type_traits>

typedef unsigned u32;
typedef unsigned short u16;
typedef u32 v4u __attribute__((ext_vector_type(4)));
typedef u32 v2u __attribute__((ext_vector_type(2)));
typedef float f32x2 __attribute__((ext_vector_type(2)));
typedef float f32x4 __attribute__((ext_vector_type(4)));
typedef __bf16 bf16x2 __attribute__((ext_vector_type(2)));
typedef __bf16 bf16x8 __attribute__((ext_vector_type(8)));
typedef short s16x4 __attribute__((ext_vector_type(4)));
#define LAS __attribute__((address_space(3)))
#define DI __device__ __forceinline__
typedef __amdgpu_buffer_rsrc_t rsrc_t;
typedef LAS char* lp_t;

constexpr int S_ = 2048, D_ = 512, F_ = 1376, KF = 257;
constexpr int NFT = 86;
constexpr int WS_E = 0;
#define XPAD 1
#define LPAD 0
#define WPAD 1
constexpr int WREC = 4 * 8192 + WPAD * 128, LREC = 8192 + LPAD * 128;
constexpr int XREC = (88 + XPAD) * 128;
constexpr int WS_A = 4096;
constexpr int WS_B = WS_A + 256 * XREC;
constexpr int WS_W = WS_B + 256 * XREC;
constexpr int WS_L = WS_W + 64 * WREC;
constexpr int WS_T = WS_L + 256 * LREC;
constexpr int WS_S = WS_T + (1 << 15);
constexpr int WS_H = WS_S + (1 << 17);
constexpr int HREC = 1152;
constexpr int WS_END = WS_H + 64 * NFT * HREC;
constexpr int WS_ST = 24 << 20;
constexpr u32 NEG2 = 0x80008000u, NEGH = 0x80000000u, NEGL = 0x8000u;
constexpr u32 SPIN_CAP = 1u << 14;
constexpr int AUX_SC1 = 16;
#define OAUX 2
#define NTAUX 2
constexpr int OOB = 0x40000000;
constexpr int CODE_LINES = 260;
constexpr int SLOT = 32768, SCR = 4 * SLOT;
constexpr int L_F = 0, L_TG = 2048;
constexpr int L_IM = 4096, L_SO = 4096, L_X = 16384, L_I2 = 16384, L_I3 = 16384, L_EX = 24576, L_O3 = 24576;

#define DRAIN() asm volatile("s_waitcnt vmcnt(0)" ::: "memory")
#define MEMFENCE() asm volatile("" ::: "memory")
#define PIN(x) asm volatile("" : "+v"(x))
#define PINS(x) asm volatile("" : "+s"(x))
#define SB() __builtin_amdgcn_sched_barrier(0)
#define BAR() asm volatile("s_waitcnt lgkmcnt(0)\n\ts_barrier" ::: "memory")
#define BAR_N(n) asm volatile("s_waitcnt lgkmcnt(" #n ")\n\ts_barrier" ::: "memory")

DI u32 pk(float a, float b) { f32x2 v = {a, b}; return __builtin_bit_cast(u32, __builtin_convertvector(v, bf16x2)); }
DI v4u pk4(f32x4 a, f32x4 b) { v4u r = {pk(a[0], b[0]), pk(a[1], b[1]), pk(a[2], b[2]), pk(a[3], b[3])}; return r; }
DI float asf(u32 v) { return __builtin_bit_cast(float, v); }
DI u32 asu(float v) { return __builtin_bit_cast(u32, v); }
DI float blo(u32 v) { return asf(v << 16); }
DI float bhi(u32 v) { return asf(v & 0xffff0000u); }
DI float dot2(u32 a, u32 b, float c) { return __builtin_amdgcn_fdot2_f32_bf16(__builtin_bit_cast(bf16x2, a), __builtin_bit_cast(bf16x2, b), c, false); }
DI u32 sum4(u32 m, v4u v) { return (m + v.x + v.y) + v.z + v.w; }
template <int CTRL> DI u32 dppu(u32 v) { return (u32)__builtin_amdgcn_update_dpp(0, (int)v, CTRL, 0xf, 0xf, true); }
template <int CTRL> DI float dppf(float v) { return asf(dppu<CTRL>(asu(v))); }
DI u32 pair_total(u32 s0, u32 s1, bool odd) { return (odd ? s1 : s0) + dppu<0xb1>(odd ? s0 : s1); }
DI u32 add_x16(u32 v) { const auto s = __builtin_amdgcn_permlane16_swap(v, v, false, false); return s[0] + s[1]; }
DI u32 add_x32(u32 v) { const auto s = __builtin_amdgcn_permlane32_swap(v, v, false, false); return s[0] + s[1]; }
template <int N> DI void wave_sums(float (&v)[N]) {
#define WS_STEP(C) _Pragma("unroll") for (int i = 0; i < N; ++i) v[i] += dppf<C>(v[i]);
  WS_STEP(0x111) WS_STEP(0x112) WS_STEP(0x114) WS_STEP(0x118) WS_STEP(0x142) WS_STEP(0x143)
#pragma unroll
  for (int i = 0; i < N; ++i) v[i] = asf(__builtin_amdgcn_readlane(asu(v[i]), 63));
}
__host__ __device__ constexpr int rec_q(int k) { return (k & 7) | ((k >> 1) & 0x18); }
__host__ __device__ constexpr int rec_line(int k) { return 11 * ((k >> 3) & 1) + ((rec_q(k) * 11) >> 5); }
__host__ __device__ constexpr int rec_pos(int k) { return rec_q(k) - 3 * ((rec_q(k) * 11) >> 5); }
__host__ __device__ constexpr int im_unit(int k, int j) { return L_IM + (k >> 6) * 3072 + rec_line(k) * 128 + ((j ^ (rec_line(k) + 2 * (k >> 6))) & 7) * 16; }
__host__ __device__ constexpr int im_chk(int k) { return im_unit(k, 6) + 4 * rec_pos(k); }
__host__ __device__ constexpr int im_chkw(int k) { return im_unit(k, 6 + ((rec_pos(k) + 3) >> 2)) + 4 * ((rec_pos(k) + 3) & 3); }
DI void lds_add(lp_t a, u32 v) { __hip_atomic_fetch_add((LAS u32*)a, v, __ATOMIC_RELAXED, __HIP_MEMORY_SCOPE_WORKGROUP); }
DI f32x4 mm16(v4u a, v4u b, f32x4 c) { return __builtin_amdgcn_mfma_f32_16x16x32_bf16(__builtin_bit_cast(bf16x8, a), __builtin_bit_cast(bf16x8, b), c, 0, 0, 0); }
DI f32x4 mm16p(v4u a, v4u b, f32x4 c) { f32x4 d = mm16(a, b, c); PIN(d); return d; }
template <bool FIRST> DI void mma(f32x4& d, v4u a, v4u b) {
  if constexpr (FIRST) asm volatile("v_mfma_f32_16x16x32_bf16 %0, %1, %2, 0" : "=&v"(d) : "v"(a), "v"(b));
  else asm volatile("v_mfma_f32_16x16x32_bf16 %0, %1, %2, %0" : "+v"(d) : "v"(a), "v"(b));
}
#define MMA_DONE() asm volatile("s_nop 7")
DI v2u lds_tr(lp_t a) { return __builtin_bit_cast(v2u, __builtin_amdgcn_ds_read_tr16_b64_v4i16((LAS s16x4*)a)); }
DI v4u lds_tr2(lp_t a, lp_t b) { v2u x = lds_tr(a), y = lds_tr(b); v4u r = {x.x, x.y, y.x, y.y}; return r; }
DI v4u lds_r128(lp_t a) { return *(LAS v4u*)a; }
DI f32x4 lds_rf(lp_t a) { return *(LAS f32x4*)a; }
DI v2u lds_r64(lp_t a) { return *(LAS v2u*)a; }
DI void lds_w128(lp_t a, v4u v) { *(LAS v4u*)a = v; }
DI void lds_wf(lp_t a, f32x4 v) { *(LAS f32x4*)a = v; }
DI void lds_w64(lp_t a, v2u v) { *(LAS v2u*)a = v; }
DI lp_t lptr(u32 off) { lp_t q = (lp_t)(size_t)off; PIN(q); return q; }
DI rsrc_t mkrs(const void* p, u32 bytes) { return __builtin_amdgcn_make_buffer_rsrc((void*)p, 0, bytes, 0x00020000); }
DI v4u ld_dev(rsrc_t r, int v, int s) { return __builtin_amdgcn_raw_buffer_load_b128(r, v, s, AUX_SC1); }
DI u32 ld_dev1(rsrc_t r, int v, int s) { return __builtin_amdgcn_raw_buffer_load_b32(r, v, s, AUX_SC1); }
DI void st_dev(rsrc_t r, int v, v4u x) { __builtin_amdgcn_raw_buffer_store_b128(x, r, v, 0, AUX_SC1); }
DI void st_dev1(rsrc_t r, int v, u32 x) { __builtin_amdgcn_raw_buffer_store_b32(x, r, v, 0, AUX_SC1); }
DI v4u ld_in(rsrc_t r, int v, int s) { return __builtin_amdgcn_raw_buffer_load_b128(r, v, s, 0); }
DI v4u ld_once(rsrc_t r, int v, int s) { return __builtin_amdgcn_raw_buffer_load_b128(r, v, s, NTAUX); }
DI u32 touch(rsrc_t r, int line) { return __builtin_amdgcn_raw_buffer_load_b32(r, line * 128, 0, 0); }
DI void flag_set(lp_t mine, u32 k) { MEMFENCE(); *(volatile LAS u32*)mine = k; MEMFENCE(); }
#define FLAG_WAIT(theirs, k, ...) do { u32 f_; _Pragma("nounroll") do { f_ = *(volatile LAS u32*)(theirs); MEMFENCE(); __VA_ARGS__; MEMFENCE(); } while (!__all(f_ >= (k))); } while (0)
DI void flag_wait_lazy(lp_t theirs, u32 k) { while (!__all(*(volatile LAS u32*)theirs >= k)) __builtin_amdgcn_s_sleep(1); MEMFENCE(); }
DI float cosr(float rev) { return __builtin_amdgcn_cosf(rev); }
DI float sinr(float rev) { return __builtin_amdgcn_sinf(rev); }
__host__ __device__ constexpr int slot_of(int k1, int k2) { return ((k2 < 8 ? k1 + 32 * k2 : (k1 ? 32 - k1 : 16) + 32 * (15 - k2)) - 1) & 255; }
DI int tile0_of(int c) { const int h = c >> 3, i = c & 7; return 44 * h + 4 * i + 2 * (i < 6 - h ? i : 6 - h); }
template <int I, int N, class F> DI void static_for(F&& f) {
  if constexpr (I < N) { f(std::integral_constant<int, I>{}); static_for<I + 1, N>(f); }
}
DI void transpose8(v4u (&m)[8]) {
  v4u o[8];
#pragma unroll
  for (int i = 0; i < 8; ++i)
#pragma unroll
    for (int j = 0; j < 4; ++j) { const u32 a = m[2 * j][i >> 1], b = m[2 * j + 1][i >> 1]; o[i][j] = __builtin_amdgcn_perm(b, a, (i & 1) ? 0x07060302u : 0x05040100u); }
#pragma unroll
  for (int i = 0; i < 8; ++i) { m[i] = o[i]; PIN(m[i]); }
}
enum { E_SQ, E_POLY, E_ARG, E_EXP, E_INC, E_RCP, E_GU, E_OUT, E_PACK, E_SWA, E_SWB, E_SUM, E_SUM2, E_ST, E_X1, E_X2, E_X8, E_CHK, E_STK };
struct EOp { int kind, v; };
struct EProg { EOp op[520]; int n, first[7]; };
constexpr EProg make_eprog() {
  EProg p = {};
  for (int t = 0; t < 6; ++t) {
    p.first[t] = p.n;
    for (int v = 0; v <= 8; ++v) {
      const int V = 8 * t + v;
      const bool head = v < 8, tail = v > 0, fin = t > 0 && t < 5;
      if (head) p.op[p.n++] = {E_SQ, V};
      if (tail) p.op[p.n++] = {E_INC, V - 1};
      if (head) p.op[p.n++] = {E_POLY, V}; else p.op[p.n++] = {E_GU, V - 1};
      if (tail) p.op[p.n++] = {E_RCP, V - 1}; else if (fin) p.op[p.n++] = {E_X1, V - 1};
      if (head) p.op[p.n++] = {E_ARG, V};
      if (tail && head) p.op[p.n++] = {E_GU, V - 1};
      if (v >= 3 && (v & 1)) p.op[p.n++] = {E_PACK, V - 2};
      if (v == 8) p.op[p.n++] = {E_SWA, V - 1};
      if (fin && v == 0) p.op[p.n++] = {E_X2, V - 1};
      if (fin && v == 1) p.op[p.n++] = {E_CHK, V - 2};
      if (head) p.op[p.n++] = {E_EXP, V};
      if (fin && v == 0) p.op[p.n++] = {E_X8, V - 1};
      if (fin && v == 1) p.op[p.n++] = {E_STK, V - 2};
      if (tail) p.op[p.n++] = {E_OUT, V - 1};
    }
    p.op[p.n++] = {E_PACK, 8 * t + 7}; p.op[p.n++] = {E_SUM, 8 * t + 7}; p.op[p.n++] = {E_SWB, 8 * t + 7}; p.op[p.n++] = {E_ST, 8 * t + 7}; p.op[p.n++] = {E_SUM2, 8 * t + 7};
    if (t >= 4) for (int k = E_X1; k <= E_STK; ++k) p.op[p.n++] = {k, 8 * t + 7};
  }
  p.first[6] = p.n;
  return p;
}
constexpr EProg kEp = make_eprog();
#define ER1 56
constexpr int epos(int n) {
  const int mid = (64 * ER1) / 16, end = kEp.first[5];
  int j = n <= 64 ? 0 : n <= 128 ? ((n - 64) * ER1) / 16 : n <= 192 ? mid + ((end - mid) * (n - 128)) / 64
        : end + ((kEp.n - end) * (n - 192)) / 8;
  for (int t = 2; t < 5; ++t) if (n <= 32 * t + 34 && j > kEp.first[t]) j = kEp.first[t];
  return j;
}
struct Epi { float t[2], q, h[3]; v4u r, k4; u32 sum, fin; };
template <int KIND, int V> DI void epi_op(Epi& e, const f32x4 (&a)[4], u32 tag) {
  constexpr int v = V & 7;
  const float gv = a[v >> 2][v & 3], uv = a[2 + (v >> 2)][v & 3];
  float& t = e.t[V & 1];
  if constexpr (KIND == E_SQ) { t = gv * gv; PIN(t); }
  else if constexpr (KIND == E_POLY) { t = __builtin_fmaf(t, -0.10294324f, -2.3022082f); PIN(t); }
  else if constexpr (KIND == E_ARG) { t = t * gv; PIN(t); }
  else if constexpr (KIND == E_EXP) { t = __builtin_amdgcn_exp2f(t); PIN(t); }
  else if constexpr (KIND == E_INC) { t = t + 1.0f; PIN(t); }
  else if constexpr (KIND == E_RCP) { t = __builtin_amdgcn_rcpf(t); PIN(t); }
  else if constexpr (KIND == E_GU) { e.q = gv * uv; PIN(e.q); }
  else if constexpr (KIND == E_OUT) { e.h[v & 1] = e.q * t; PIN(e.h[v & 1]); }
  else if constexpr (KIND == E_PACK) { u32 r = pk(e.h[0], e.h[1]); PIN(r); e.r[v >> 1] = r; }
  else if constexpr (KIND == E_SWA || KIND == E_SWB) { constexpr int i = KIND == E_SWB; const u32 x = e.r[i], y = e.r[i + 2];
    const auto q = __builtin_amdgcn_permlane16_swap(x, y, false, false); u32 q0 = q[0], q1 = q[1]; PIN(q0); PIN(q1); e.r[i] = q0; e.r[i + 2] = q1; }
  else if constexpr (KIND == E_SUM) { e.sum = e.r.x + e.r.z; PIN(e.sum); }
  else if constexpr (KIND == E_SUM2) { e.fin = e.sum + e.r.y + e.r.w; PIN(e.fin); }
  else if constexpr (KIND == E_X1) { e.fin += dppu<0xb1>(e.fin); PIN(e.fin); }
  else if constexpr (KIND == E_X2) { e.fin += dppu<0x4e>(e.fin); PIN(e.fin); }
  else if constexpr (KIND == E_X8) { e.fin += dppu<0x128>(e.fin); PIN(e.fin); }
  else if constexpr (KIND == E_CHK) { asm volatile("" : "=v"(e.k4)); e.k4.x = tag - e.fin; PIN(e.k4); }
}

constexpr double ct_sin(long n, long d) {
  n %= d; if (n < 0) n += d;
  if (2 * n > d) return -ct_sin(d - n, d);
  if (4 * n > d) return ct_sin(d - 2 * n, 2 * d);
  const double x = 6.283185307179586476925 * (double)n / (double)d;
  double t = x, r = x;
  for (int i = 1; i < 14; ++i) { t *= -x * x / ((2 * i) * (2 * i + 1)); r += t; }
  return r;
}
constexpr double ct_cos(long n, long d) { return ct_sin(4 * n + d, 4 * d); }
constexpr u32 ct_f32(double v) { return __builtin_bit_cast(u32, (float)v); }
constexpr u32 ct_bf(double v) { const u32 u = ct_f32(v); return (u + 0x7fffu + ((u >> 16) & 1u)) >> 16; }
constexpr u32 ct_pk(double a, double b) { return ct_bf(a) | (ct_bf(b) << 16); }
struct Tab { u32 v[6][64][4]; };
constexpr Tab make_tab() {
  Tab t = {};
  for (int l = 0; l < 64; ++l) {
    const int c = l & 15, g = l >> 4;
    for (int j = 0; j < 2; ++j) {
      const int a = 4 * g + 2 * j;
      t.v[0][l][j] = ct_pk(ct_cos(a * c, 32), ct_cos((a + 1) * c, 32));
      t.v[0][l][2 + j] = c ? ct_pk(-ct_sin(a * c, 32), -ct_sin((a + 1) * c, 32)) : ct_pk(1, -1);
    }
    for (int r = 0; r < 4; ++r) {
      const int b = 4 * g + r, sl = slot_of(b, c);
      t.v[1][l][r] = ct_f32(ct_cos(b * c, 512));
      t.v[2][l][r] = ct_f32(ct_sin(b * c, 512));
      t.v[3][l][r] = ct_pk(ct_cos(b * c, 16), ct_sin(b * c, 16));
      t.v[4][l][r] = c == 0 ? ct_pk((r & 1) ? -1 : 1, 0) : c < 8 ? ct_pk(ct_cos(b * c, 16), -ct_sin(b * c, 16)) : ct_pk(ct_cos(b * (31 - 2 * c), 32), -ct_sin(b * (31 - 2 * c), 32));
      t.v[5][l][r] = im_unit(sl, 2 * rec_pos(sl)) | (im_chk(sl) << 16);
    }
  }
  return t;
}
static_assert(true, "");
static __device__ const __attribute__((aligned(128))) Tab g_tab = make_tab();

#define STAMP(i) do { } while (0)
#define STAMP_DECL do { } while (0)
#define STAMP_FLUSH() do { } while (0)
#define COUNT(i, v) do { } while (0)

#define FSTAMP(i) do { } while (0)
#define PACE(d, t) do { __builtin_amdgcn_s_sleep(d); while ((u32)__builtin_amdgcn_s_memrealtime() - t_ref < (t)) __builtin_amdgcn_s_sleep(1); } while (0)
#define RS1 0
#define RS2 10
#define RS3 0
#define D1 1
#define D2 4
#define D3 6
#define TW 240
#define SLIPPED() (*(LAS u32*)(fmine + L_TG + 1024) = (u32)__builtin_amdgcn_s_memrealtime() - t_ref)
#define PACE2(d, t, sl) do { u32 r_ = *(LAS u32*)(fall + L_TG + 1024); r_ = max(r_, dppu<0xb1>(r_)); r_ = max(r_, dppu<0x4e>(r_)); \
      r_ = __builtin_amdgcn_readfirstlane(r_) + (sl); t_ref += r_ > (t) ? r_ - (t) : 0u; PACE(d, t); } while (0)
#define SL2 175
#define SL3 150
#define WAIT1() PACE(D1, T1)
#define WAIT2() PACE2(D2, T2, SL2)
#define WAIT3() PACE2(D3, T3, SL3)
struct Params {
  const u16* x; const u16* wr; const u16* wi; const u16* wg; const u16* wu; const u16* wd; u16* out; void* ws;
};

__global__ void __launch_bounds__(512, 2) gfnet_geglu_kernel(Params p) {
  const int wg = blockIdx.x, tid = threadIdx.x, lane = tid & 63;
  int wv = __builtin_amdgcn_readfirstlane(tid >> 6);
  STAMP_DECL;
  unsigned long long t_entry;
  asm volatile("s_memrealtime %0" : "=s"(t_entry));
  const int rg = 4 * ((wg & 7) >> 1) + (wg >> 6), ch = 8 * (wg & 1) + ((wg >> 3) & 7);
  const int un = 16 * rg + ch;
  const int ft0 = tile0_of(ch);
  const bool six = (ch & 7) < 6 - (ch >> 3);
#define W1SZ (D_ * F_ * 2)
#define CT2 ((ch + 4 - (int)((size_t)p.wd >> 7)) & 7)
#define WD_SETUP() const rsrc_t rwd = mkrs(p.wd, D_ * F_ * 2); \
    int vd = (lane >> 3) * (D_ * 2) + CT2 * 128 + ((lane & 7) ^ ((lane >> 3) & 2)) * 16, vd1 = vd ^ 64, sd[4]; PIN(vd); PIN(vd1); \
    _Pragma("unroll") for (int i = 0; i < 4; ++i) { sd[i] = tile0_of(4 * (wv & 3) + i) * (16 * D_ * 2); PINS(sd[i]); }
#define DMA_WD1(t, slot, i) asm volatile("s_mov_b32 m0, %0\n\ts_nop 0\n\tbuffer_load_dwordx4 %1, %2, %3 offen lds" \
      :: "s"((slot) * SLOT + (wv & 3) * 8192 + (i) * 1024), "v"(((i) & 1) ? vd1 : vd), "s"(rwd), "s"(sd[(i) >> 1] + ((t) * 16 + ((i) & 1) * 8) * (D_ * 2)) : "memory")
#define TH2 6
#define TG 4
#define THROTTLE(i) do { if ((i) % TG == 0) asm volatile("s_waitcnt vmcnt(%0)" :: "n"(TH2) : "memory"); } while (0)
#define DMA_WD(t, slot) do { _Pragma("unroll") for (int i = 0; i < 8; ++i) { THROTTLE(i); DMA_WD1(t, slot, i); } } while (0)
#define DMA_WDS(t, slot) do { _Pragma("unroll") for (int i = 0; i < 4; ++i) DMA_WD1(t, slot, i); if (!(wv & 1)) { _Pragma("unroll") for (int i = 4; i < 8; ++i) DMA_WD1(t, slot, i); } } while (0)
#define VM_WAIT(n) asm volatile("s_waitcnt vmcnt(" #n ")" ::: "memory")
#define PDN 4
  constexpr int PD = PDN;
#define FRAG(N) frag(std::integral_constant<int, (N)>{})
#define NXT(n) (((n) + PD) ^ 1)
  v4u ra[8];
  u32 cpf;
#define UNDEF_RA() do { asm volatile("" : "=v"(cpf)); _Pragma("unroll") for (int i = 0; i < 8; ++i) asm volatile("" : "=v"(ra[i])); } while (0)
  UNDEF_RA();
  if (wv < 4) {
    const int vt = lane * 16;
    int sx = (32 * (8 * (un >> 5) + 2 * wv) + (un & 31)) * (D_ * 2), sy = sx + 32 * D_ * 2;
    const rsrc_t rt = mkrs(&g_tab, sizeof(Tab));
    ra[2] = ld_in(rt, vt, 0); ra[3] = ld_in(rt, vt + 1024, 0);
    PINS(sx); PINS(sy);
    SB();
    const rsrc_t rx = mkrs(p.x, S_ * D_ * 2);
    ra[0] = ld_once(rx, vt, sx); ra[1] = ld_once(rx, vt, sy);
    SB();
#pragma unroll
    for (int i = 0; i < 4; ++i) ra[4 + i] = ld_in(rt, vt + i * 1024, 2048);
    cpf = ld_dev1(mkrs(p.ws, WS_END), 0, WS_E);
    STAMP(0);
  }
  PINS(wv);
  if (wv >= 6) {
    unsigned long long pc0;
    asm volatile("s_getpc_b64 %0" : "=s"(pc0));
    cpf = touch(mkrs((const void*)(pc0 & ~127ull), CODE_LINES * 128), lane < 9 ? (((wg >> 3) ^ (wv & 1)) * 9 + lane) : 4096);
    unsigned long long te;
    asm volatile("s_waitcnt lgkmcnt(0)\n\ts_mov_b64 %0, %1" : "=s"(te) : "s"(t_entry));
    st_dev(mkrs(p.ws, WS_END), (tid >= 504 && wg < 8) ? WS_T + wg * 128 + (lane & 7) * 16 : OOB, v4u{(u32)te, (u32)te, (u32)te, (u32)te});
  }
  PINS(wv);
  if ((wv >> 1) == 2) {
#define WAUX 0
#define WSLEEP 2
    __builtin_amdgcn_s_sleep(WSLEEP);
    const rsrc_t rwr = mkrs(p.wr, S_ * KF * 2), rwi = mkrs(p.wi, S_ * KF * 2);
    const int vf = (lane & 7) * (KF * 2) + (lane & 1) * 2 + ((tid & 127) >> 3) * 8;
    const int sf = (16 * (un >> 6) + 2 * (un & 4) + 256 * ((un >> 3) & 7)) * (KF * 2) + (un & 3) * 128;
#pragma unroll
    for (int r = 0; r < 4; ++r) { ra[r] = __builtin_amdgcn_raw_buffer_load_b128(rwr, vf, sf + r * (64 * KF * 2), WAUX); ra[4 + r] = __builtin_amdgcn_raw_buffer_load_b128(rwi, vf, sf + r * (64 * KF * 2), WAUX); }
  }
  const lp_t fmine = lptr(SCR + L_F + tid * 4);
  *(volatile LAS u32*)fmine = 0;
  lds_w128(lptr(SCR + L_IM + (tid & 127) * 128 + (((6 + ((tid >> 7) & 1)) ^ (tid + 2 * (((tid & 127) * 43) >> 10))) & 7) * 16), v4u{0u, 0u, 0u, 0u});
  BAR();
  PINS(t_entry);
  u32 t_ref = (u32)t_entry, tb_ = 0;
  v4u wa_;
  asm volatile("" : "=v"(wa_));
  PINS(wv);
  if ((wv >> 1) == 2) {
    const int T = tid - 256, k = 64 * (un & 3) + 4 * (T >> 3) + (T & 3);
    wa_ = v4u{(u32)((T >> 3) * 512 + (T & 7) * 16 + WS_W + (un >> 2) * WREC + (un & 3) * 8192), (lane & 1) ? 0u : 16u, (u32)(SCR + im_chkw(k)), (u32)(SCR + im_chk(k))};
    PIN(wa_);
  }
  PINS(wv);
  if (wv >= 6) {
#define X0SLEEP 8
    __builtin_amdgcn_s_sleep(X0SLEEP);
    const rsrc_t r1 = mkrs((wv & 1) ? p.wu : p.wg, W1SZ);
    ra[0].x = touch(r1, lane < 43 ? wg * 43 + lane : 16384);
  }
  SB();
  PINS(wv);

  if (wv < 4) {
  const int w = wv, c = lane & 15, g = lane >> 4;
  const float fc = (float)c, fg4 = (float)(4 * g);
  const f32x4 z = {0.f, 0.f, 0.f, 0.f};
  f32x4 twc, tws;
  v4u fbre, fbsw;
  u32 tag;
  float mean[2];
  const rsrc_t ws = mkrs(p.ws, WS_END);
  const lp_t fall = lptr(SCR + L_F + (lane & 3) * 256 + lane * 4);
  const lp_t imr = lptr(SCR + L_IM + w * 3072 + (lane >> 3) * 128 + ((lane ^ ((lane >> 3) + 2 * w)) & 7) * 16);
  int vx0 = lane * 16 + WS_A + un * XREC + w * (22 * 128), vx2 = lane < 48 ? vx0 + 2048 : OOB;
  PIN(vx0); PIN(vx2);
  int vq8 = (lane & 7) * 128;
  PIN(vq8);
  u32 tq;
  v2u xk[2][2];
  f32x4 tfc[2], tfs[2], tic[2], tis[2];
  const float fj = (float)(16 * w) + fc;
#pragma unroll
  for (int r = 0; r < 4; ++r) {
#pragma unroll
      for (int m = 0; m < 2; ++m) {
        const float nf = (fg4 + (16 * m + r)) * (fj * (1.f / 2048)), ni = (fg4 + (float)(16 * w) + r) * ((fc + 16 * m) * (1.f / 2048));
        tfc[m][r] = cosr(nf); tfs[m][r] = sinr(nf); tic[m][r] = cosr(ni); tis[m][r] = sinr(ni);
      }
  }
#pragma unroll
  for (int i = 0; i < 2; ++i) { PIN(tfc[i]); PIN(tfs[i]); PIN(tic[i]); PIN(tis[i]); }
  SB();
  {
    const v4u xr[2] = {ra[0], ra[1]}, tb[6] = {ra[2], ra[3], ra[4], ra[5], ra[6], ra[7]};
    const u32 sc = (c & 1) ? NEG2 : 0u, sm = c < 8 ? NEGL : NEGH, p0 = c < 8 ? 0x0c0c0100u : 0x01000c0cu, p1 = c < 8 ? 0x0c0c0302u : 0x03020c0cu;
    v4u fare = {tb[0].x, tb[0].y, tb[0].x ^ sc, tb[0].y ^ sc}, faim = {tb[0].z, tb[0].w, tb[0].z ^ sc, tb[0].w ^ sc}, fbim, f0re, f0im;
    twc = __builtin_bit_cast(f32x4, tb[1]); tws = __builtin_bit_cast(f32x4, tb[2]); fbre = tb[3];
    lp_t t1w[4], t1c[4];
#pragma unroll
    for (int r = 0; r < 4; ++r) {
      const u32 a = tb[3][r], q = tb[4][r];
      fbsw[r] = __builtin_amdgcn_alignbit(a, a, 16);
      fbim[r] = fbsw[r] ^ sm;
      f0re[r] = __builtin_amdgcn_perm(q, q, p0); f0im[r] = __builtin_amdgcn_perm(q, q, p1);
      t1w[r] = lptr((tb[5][r] & 0xffffu) ^ (SCR + w * 8)); t1c[r] = lptr((tb[5][r] >> 16) + SCR);
    }
    float c0m = c == 0 ? 1.f / 16 : 0.f;
    lp_t xw = lptr(SCR + L_X + w * 2048 + lane * 16), xt = lptr(SCR + L_X + w * 2048 + lane * 8);
    lp_t xq = lptr(SCR + L_X + w * 2048 + (4 * c + g) * 8), f6 = lptr(SCR + L_F + (lane % 6) * 256 + lane * 4);
    tag = cpf + 1u;
    u32 tag0 = w ? 0u : tag;
    PIN(c0m); PIN(tag); PIN(tag0);
    *(LAS u32*)(fmine + L_TG) = tag;
    *(LAS u32*)(fmine + L_TG + 1024) = 0;
    PIN(fare); PIN(faim); PIN(fbre); PIN(fbim); PIN(f0re); PIN(f0im); PIN(twc); PIN(tws);
    STAMP(1);
    SB();
    v4u a[2];
#pragma unroll
    for (int rr = 0; rr < 2; ++rr) { lds_w128(xw + rr * 1024, xr[rr]); a[rr] = lds_tr2(xt + rr * 1024, xt + rr * 1024 + 512); }
    tq = ld_dev1(ws, vq8, WS_T);
    STAMP(2);
    float st[4] = {0.f, 0.f, 0.f, 0.f};
#pragma unroll
    for (int i = 0; i < 4; ++i)
#pragma unroll
      for (int rr = 0; rr < 2; ++rr) { const u32 v = xr[rr][i]; st[rr] = dot2(v, 0x3f803f80u, st[rr]); st[2 + rr] = dot2(v, v, st[2 + rr]); }
    SB();
    f32x4 ur[2], ui[2];
#pragma unroll
    for (int rr = 0; rr < 2; ++rr) { ur[rr] = mm16p(a[rr], fare, z); ui[rr] = mm16p(a[rr], faim, z); }
    wave_sums<4>(st);
    SB();
    v4u a2[2];
    f32x4 orr[2], oi[2], sr[2], si[2];
#pragma unroll
    for (int rr = 0; rr < 2; ++rr) {
      const f32x4 x = ur[rr] - st[rr] * c0m, y = ui[rr];
      a2[rr] = pk4(x * twc + y * tws, y * twc - x * tws);
      orr[rr] = mm16p(a2[rr], fbre, z); oi[rr] = mm16p(a2[rr], fbim, z); sr[rr] = mm16p(a2[rr], f0re, z); si[rr] = mm16p(a2[rr], f0im, z);
    }
    v4u o[2];
#pragma unroll
    for (int rr = 0; rr < 2; ++rr) {
      mean[rr] = st[rr] * (1.f / 512);
      const float rstd = __builtin_amdgcn_rsqf(fmaxf(st[2 + rr] * (1.f / 512) - mean[rr] * mean[rr], 0.f) + 1e-5f);
      orr[rr][0] = g == 0 ? sr[rr][0] : orr[rr][0]; oi[rr][0] = g == 0 ? si[rr][0] : oi[rr][0];
      o[rr] = pk4(orr[rr] * rstd, oi[rr] * rstd);
    }
#pragma unroll
    for (int r = 0; r < 4; ++r) { v2u v = {o[0][r], o[1][r]}; lds_w64(t1w[r], v); lds_add(t1c[r], v.x + v.y + tag0); }
    STAMP(3);
    flag_set(fmine, 1);
    v4u v0, v1, v2;
    FLAG_WAIT(f6, 1, v0 = lds_r128(imr); v1 = lds_r128(imr + 1024); v2 = lds_r128(imr + 2048));
    st_dev(ws, vx0, v0); st_dev(ws, vx0 + 1024, v1); st_dev(ws, vx2, v2);
    flag_set(fmine, 2);
#pragma unroll
    for (int i = 0; i < 4; ++i) xk[i >> 1][i & 1] = lds_r64(xq + (i >> 1) * 1024 + (i & 1) * 512);
  }
  int vge, vgo, vg = (lane >> 3) * WREC + (lane & 7) * 16;
  {
    const int pay = (lane >> 1) * XREC + rec_pos(un) * 32 + (lane & 1) * 16, chk = (lane & 1) ? OOB : ((lane >> 1) ^ 1) * XREC + 96 + rec_pos(un) * 4;
    vge = (lane & 2) ? chk : pay; vgo = (lane & 2) ? pay : chk;
  }
  const int sg = w * (64 * XREC) + (22 * (un >> 6) + rec_line(un)) * 128;
#define GATHER(BUF) do { xl[0] = ld_dev(ws, vge, BUF + sg); xl[1] = ld_dev(ws, vgo, BUF + sg); xl[2] = ld_dev(ws, vge, BUF + sg + 32 * XREC); xl[3] = ld_dev(ws, vgo, BUF + sg + 32 * XREC); } while (0)
#define VERIFY(extra) ({ u32 k_ = 0; _Pragma("unroll") for (int m = 0; m < 2; ++m) { _Pragma("unroll") for (int i = 0; i < 4; ++i) xg[m][i] = (lane & 2) ? xl[2 * m + 1][i] : xl[2 * m][i]; \
      k_ += (lane & 2) ? xl[2 * m].x : xl[2 * m + 1].x; lds_w128(iw + m * 1024, xg[m]); } \
      u32 t_ = sum4(sum4((extra) - k_, xg[0]), xg[1]); t_ += dppu<0xb1>(t_); t_ += dppu<0x4e>(t_); t_; })
  const lp_t iw = lptr(SCR + L_I2 + w * 2048 + lane * 16);
  const int sw = WS_W + w * 16 * WREC + un * 128;
  v4u wq[2];
  STAMP(4);
  SB();
  v4u f2a[2][2];
  {
    v4u f1a[4], f1b[4], f2b[2][2];
    const u32 sc = (c & 1) ? NEG2 : 0u;
#pragma unroll
    for (int r = 0; r < 4; ++r) {
#pragma unroll
      for (int ks = 0; ks < 2; ++ks) {
        const float n = (fg4 + (16 * ks + r)) * (fj * (1.f / 64));
        const float cs = cosr(n), sn = sinr(n);
        f1a[ks][r] = pk(cs, sn); f1b[ks][r] = pk(-sn, cs);
        f1a[ks + 2][r] = f1a[ks][r] ^ sc; f1b[ks + 2][r] = f1b[ks][r] ^ sc;
      }
      const float n = (fg4 + r) * (fc * (1.f / 32));
      const float cs = cosr(n), sn = sinr(n);
      const u32 a = pk(cs, sn), b = pk(-sn, cs), sr = (r & 1) ? NEG2 : 0u;
      f2a[0][0][r] = a; f2a[1][0][r] = a ^ sr; f2a[0][1][r] = a ^ sc; f2a[1][1][r] = a ^ sc ^ sr;
      f2b[0][0][r] = b; f2b[1][0][r] = b ^ sr; f2b[0][1][r] = b ^ sc; f2b[1][1][r] = b ^ sc ^ sr;
    }
#pragma unroll
    for (int i = 0; i < 4; ++i) { PIN(f1a[i]); PIN(f1b[i]); PIN(f2a[i >> 1][i & 1]); PIN(f2b[i >> 1][i & 1]); }
    lp_t i2r = lptr(SCR + L_I2 + ((g >> 1) * 32 + c) * 32 + (g & 1) * 16);
    lp_t exw = lptr(SCR + L_EX + w * 2048 + lane * 16), exr = lptr(SCR + L_EX + lane * 16);
    lp_t imw[2], imc = lptr(SCR + im_chk(64 * w + lane));
#pragma unroll
    for (int n = 0; n < 2; ++n) { const int k = 64 * w + 32 * (g >> 1) + 16 * n + c; imw[n] = lptr(SCR + im_unit(k, 2 * rec_pos(k) + (g & 1))); }
    vx0 += WS_B - WS_A; vx2 += WS_B - WS_A;
    PIN(vg); PIN(vge); PIN(vgo); PIN(vx0); PIN(vx2);
    tq -= t_ref; tq = tq < 150u ? tq : 0u;
    tq = max(tq, dppu<0xb1>(tq)); tq = max(tq, dppu<0x4e>(tq)); tq = max(tq, dppu<0x141>(tq));
    t_ref += __builtin_amdgcn_readfirstlane(tq);
    tb_ = t_ref;
    STAMP(5);
    WAIT1();
    STAMP(6);
    v4u xl[4], xg[2];
    f32x4 wr[2], wi[2];
    GATHER(WS_A);
#define WD_ 8
    SB();
    __builtin_amdgcn_s_sleep(WD_);
    wq[0] = ld_dev(ws, vg, sw); wq[1] = ld_dev(ws, vg, sw + 8 * WREC);
    flag_set(fmine, 3);
    SB();
#define KW() (((lane & 2) ? xl[0].w : xl[1].w) + ((lane & 2) ? xl[2].w : xl[3].w))
    u32 kw = KW(), spin = 0;
    u32 bad = VERIFY(tag + kw);
    if (__any(bad != 0)) {
      _Pragma("nounroll") do {
        MEMFENCE();
        if (bad) GATHER(WS_A);
        kw = KW();
        bad = VERIFY(tag + kw);
      } while (__any(bad != 0) && ++spin < SPIN_CAP);
      SLIPPED();
      ++spin;
    }
    COUNT(20, spin);
    STAMP(7);
    v4u af[2][4], b2[2], a3[2];
    flag_set(fmine, 4);
    FLAG_WAIT(fall, 4, _Pragma("unroll") for (int i = 0; i < 8; ++i) af[i >> 2][i & 3] = lds_r128(i2r + (i & 3) * 2048 + (i >> 2) * 512));
    STAMP(8);
    SB();
    f32x4 dr[2], di[2];
#pragma unroll
    for (int m = 0; m < 2; ++m)
#pragma unroll
      for (int ks = 0; ks < 4; ++ks) { dr[m] = mm16(af[m][ks], f1a[ks], ks ? dr[m] : z); di[m] = mm16(af[m][ks], f1b[ks], ks ? di[m] : z); }
#pragma unroll
    for (int m = 0; m < 2; ++m) b2[m] = pk4(dr[m] * tfc[m] + di[m] * tfs[m], di[m] * tfc[m] - dr[m] * tfs[m]);
#pragma unroll
    for (int k = 0; k < 2; ++k)
#pragma unroll
      for (int m = 0; m < 2; ++m) { dr[m] = mm16(f2a[m][k], b2[k], k ? dr[m] : z); di[m] = mm16(f2b[m][k], b2[k], k ? di[m] : z); }
    {
#define W_BAD() ({ u32 t_ = sum4(sum4(0u - kw, wq[0]), wq[1]); t_ += dppu<0xb1>(t_); t_ += dppu<0x4e>(t_); t_ += dppu<0x141>(t_); t_; })
      u32 bw = W_BAD();
#pragma unroll
      for (int m = 0; m < 2; ++m) { PIN(dr[m]); PIN(di[m]); }
      if (__builtin_expect(__any(bw != 0), 0)) {
        u32 sp = 0;
        _Pragma("nounroll") do { MEMFENCE(); if (bw) { wq[0] = ld_dev(ws, vg, sw); wq[1] = ld_dev(ws, vg, sw + 8 * WREC); } bw = W_BAD(); } while (__any(bw != 0) && ++sp < SPIN_CAP);
      }
#pragma unroll
      for (int m = 0; m < 2; ++m)
#pragma unroll
        for (int r = 0; r < 4; ++r) { const u32 v = wq[m][r]; wr[m][r] = blo(v); wi[m][r] = bhi(v); }
    }
#pragma unroll
    for (int m = 0; m < 2; ++m) a3[m] = pk4(dr[m] * wi[m] + di[m] * wr[m], dr[m] * wr[m] - di[m] * wi[m]);
#pragma unroll
    for (int k = 0; k < 2; ++k)
#pragma unroll
      for (int n = 0; n < 2; ++n) { di[n] = mm16(a3[k], f2a[n][k], k ? di[n] : z); dr[n] = mm16(a3[k], f2b[n][k], k ? dr[n] : z); }
#pragma unroll
    for (int n = 0; n < 2; ++n) lds_w128(exw + n * 1024, pk4(di[n] * tic[n] + dr[n] * tis[n], dr[n] * tic[n] - di[n] * tis[n]));
    STAMP(9);
    flag_set(fmine, 5);
    FLAG_WAIT(fall, 5, _Pragma("unroll") for (int i = 0; i < 8; ++i) af[i >> 2][i & 3] = lds_r128(exr + (i & 3) * 2048 + (i >> 2) * 1024));
    SB();
    u32 cs[2];
#pragma unroll
    for (int n = 0; n < 2; ++n)
#pragma unroll
      for (int ks = 0; ks < 4; ++ks) { di[n] = mm16(f1a[ks], af[n][ks], ks ? di[n] : z); dr[n] = mm16(f1b[ks], af[n][ks], ks ? dr[n] : z); }
#pragma unroll
    for (int n = 0; n < 2; ++n) {
      const v4u e = pk4(dr[n], di[n]);
      lds_w128(imw[n], e);
      cs[n] = sum4(0u, e);
    }
    const auto sx = __builtin_amdgcn_permlane16_swap(cs[0], cs[1], false, false);
    *(LAS u32*)imc = tag + sx[0] + sx[1];
    MEMFENCE();
    const v4u v0 = lds_r128(imr), v1 = lds_r128(imr + 1024), v2 = lds_r128(imr + 2048);
    st_dev(ws, vx0, v0); st_dev(ws, vx0 + 1024, v1); st_dev(ws, vx2, v2);
    flag_set(fmine, 6);
    if (wg == 0 && w == 0) st_dev1(ws, WS_E, tag);
  }
  STAMP(10);
  SB();
  {
    v4u gbre, gbim, g0re, g0im, ga[2];
#pragma unroll
    for (int r = 0; r < 4; ++r) {
      gbre[r] = fbre[r] ^ (g < 2 ? NEGH : 0u);
      gbim[r] = fbsw[r] ^ (g < 2 ? 0u : NEGH);
      const float n = fc * ((31.f - 2.f * r - 2.f * fg4) * (1.f / 32));
      u32 q = pk(cosr(n), -sinr(n));
      PIN(q);
      g0re[r] = g < 2 ? gbre[r] : 0u;
      g0im[r] = g < 2 ? 0u : q;
      ga[0][r] = f2a[0][0][r] ^ NEGH; ga[1][r] = f2a[1][0][r] ^ NEGH;
    }
    twc *= 2.f / (2048.f * 512.f); tws *= 2.f / (2048.f * 512.f);
    g0re[0] = g == 0 ? ((c & 1) ? 0xbf00u : 0x3f00u) : g0re[0];
    ga[0][0] = g == 0 ? ((c & 1) ? 0xbf803f80u : 0x3f803f80u) : ga[0][0]; ga[1][0] = ga[0][0];
    PIN(gbre); PIN(gbim); PIN(g0re); PIN(g0im); PIN(ga[0]); PIN(ga[1]); PIN(twc); PIN(tws);
    f32x4 xin[2][2];
#pragma unroll
    for (int rr = 0; rr < 2; ++rr)
#pragma unroll
      for (int t = 0; t < 2; ++t) {
        const v2u v = xk[rr][t];
        const u32 v0 = v.x, v1 = v.y;
        f32x4 q = {blo(v0), bhi(v0), blo(v1), bhi(v1)};
        xin[rr][t] = q - mean[rr]; PIN(xin[rr][t]);
      }
    lp_t i3r[4], o3w[2], o3r[2];
#pragma unroll
    for (int r = 0; r < 4; ++r) i3r[r] = lptr(SCR + L_I3 + slot_of(c, 4 * g + r) * 32 + w * 8);
#pragma unroll
    for (int i = 0; i < 2; ++i) {
      o3w[i] = lptr(SCR + L_O3 + ((4 * c + g) * 8 + ((2 * w + i) ^ (c & 7))) * 16);
      const int q = w * 128 + i * 64 + lane;
      o3r[i] = lptr(SCR + L_O3 + (q ^ ((q >> 5) & 7)) * 16);
    }
    int vs = lane * 16 + WS_L + un * LREC + w * 2048, vt = lane < 8 ? lane * 16 + WS_S + un * 512 + w * 128 : OOB;
    PIN(vs); PIN(vt);
    lp_t sow = lptr(SCR + L_SO + tid * 16), sor = lptr(SCR + L_SO + (lane & 6) * 512 + (lane & 1) * 8);
    WAIT2();
    STAMP(11);
    v4u xl[4], xg[2];
    GATHER(WS_B);
    flag_set(fmine, 7);
    SB();
    u32 bad = VERIFY(tag), spin = 0;
    if (__any(bad != 0)) {
      _Pragma("nounroll") do {
        __builtin_amdgcn_s_sleep(RS1);
        MEMFENCE();
        if (bad) GATHER(WS_B);
        bad = VERIFY(tag);
      } while (__any(bad != 0) && ++spin < SPIN_CAP);
      SLIPPED();
      ++spin;
    }
    COUNT(21, spin);
    STAMP(12);
    v4u a[2], a2[2];
    v2u zr[4];
    flag_set(fmine, 8);
    FLAG_WAIT(fall, 8, _Pragma("unroll") for (int r = 0; r < 4; ++r) zr[r] = lds_r64(i3r[r]));
    STAMP(13);
#pragma unroll
    for (int r = 0; r < 4; ++r) { a[0][r] = zr[r].x; a[1][r] = zr[r].y; }
    f32x4 tr[2], ti[2], sr[2], si[2], h[2][2];
#pragma unroll
    for (int rr = 0; rr < 2; ++rr) { tr[rr] = mm16p(a[rr], gbre, z); ti[rr] = mm16p(a[rr], gbim, z); sr[rr] = mm16p(a[rr], g0re, z); si[rr] = mm16p(a[rr], g0im, z); }
#pragma unroll
    for (int rr = 0; rr < 2; ++rr) {
      tr[rr][0] = g == 0 ? sr[rr][0] : tr[rr][0]; ti[rr][0] = g == 0 ? si[rr][0] : ti[rr][0];
      a2[rr] = pk4(tr[rr] * twc - ti[rr] * tws, ti[rr] * twc + tr[rr] * tws);
      h[rr][0] = mm16p(a2[rr], ga[0], xin[rr][0]); h[rr][1] = mm16p(a2[rr], ga[1], xin[rr][1]);
    }
    float st[2];
#pragma unroll
    for (int rr = 0; rr < 2; ++rr) { const f32x4 q = h[rr][0] * h[rr][0] + h[rr][1] * h[rr][1]; st[rr] = (q[0] + q[2]) + (q[1] + q[3]); }
    wave_sums<2>(st);
    v4u so;
#pragma unroll
    for (int rr = 0; rr < 2; ++rr) {
      const float ve = st[rr] * (1.f / 512) + 1e-5f, rstd = __builtin_amdgcn_rsqf(ve);
      const f32x4 l0 = h[rr][0] * rstd, l1 = h[rr][1] * rstd;
      v4u l4 = {pk(l0[0], l0[1]), pk(l0[2], l0[3]), pk(l1[0], l1[1]), pk(l1[2], l1[3])};
      lds_w128(o3w[rr], l4);
      so[2 * rr] = asu(mean[rr]); so[2 * rr + 1] = asu(ve * rstd);
    }
    lds_w128(sow, so);
    STAMP(14);
    v4u v0, v1;
    v2u ms;
    flag_set(fmine, 9);
    FLAG_WAIT(fall, 9, v0 = lds_r128(o3r[0]); v1 = lds_r128(o3r[1]); ms = lds_r64(sor));
    st_dev(ws, vs, v0); st_dev(ws, vs + 1024, v1);
    u32 s = sum4(sum4(0u, v0), v1);
    s += dppu<0x128>(s); s = add_x32(add_x16(s));
    v4u rec = {tag - s - ms.x - ms.y, ms.x, ms.y, tag};
    st_dev(ws, vt, rec);
    flag_set(fmine, 10);
  }
  SB();
  {
#define LOOKN 6
    constexpr int LOOK = LOOKN;
    v4u ring[8], act[32];
    lp_t wa[4], wb[4];
#pragma unroll
    for (int t = 0; t < 4; ++t) wa[t] = lptr(t * SLOT + g * 512 + ((c ^ (2 * t + (c >> 3))) * 16));
#pragma unroll
    for (int i = 0; i < 4; ++i) wb[i] = lptr(((i & 1) ? 0 : 4 * SLOT) + ((lane >> 2) ^ (g & 1)) * 64 + (((i >> 1) ^ g) & 1) * 32 + (lane & 3) * 8);
    auto frag = [&](auto NN) -> v4u {
      constexpr int N = NN.value;
      if constexpr (N < 64) return lds_r128(wa[(N >> 1) & 1] + (N >> 2) * 2048 + (N & 1) * 256);
      else if constexpr (N < 128) return lds_r128(wa[N >> 5] + ((N & 31) >> 1) * 2048 + (N & 1) * 256);
      else { const lp_t b = wb[((N >> 4) & 2) + (N & 1)] + ((N & 31) >> 1) * 2048; return lds_tr2(b, b + 1024); }
    };
    int vo = WS_H + ((4 * rg + w) * NFT + ft0) * HREC + lane * 16, vk = (lane & 11) ? OOB : vo - lane * 16 + 1024 + ((lane >> 4) * 8 + (lane & 7)) * 4;
    int vo4 = vo + 4 * HREC, vk4 = vk + 4 * HREC;
    PIN(vo); PIN(vk); PIN(vo4); PIN(vk4);
    Epi e;
#define EPI(J0, J1) static_for<(J0), (J1)>([&](auto JJ) { constexpr int kd = kEp.op[JJ.value].kind, V = kEp.op[JJ.value].v, t = V >> 3; \
      if constexpr (kd == E_ST) st_dev(ws, (t < 4 ? vo : vo4) + (t & 3) * HREC, e.r); \
      else if constexpr (kd == E_STK) st_dev(ws, (t < 4 ? vk : vk4) + (t & 3) * HREC, e.k4); \
      else epi_op<kd, V>(e, t % 3 == 0 ? tA : t % 3 == 1 ? tB : tC, tag); \
      SB(); })
    int va0 = ((lane >> 3) & 1) * LREC + g * 128 + (lane & 7) * 16, va1 = va0 + 2 * LREC;
    int vr = ((lane >> 3) & 1) * 512 + g * 128 + (lane & 7) * 16;
    PIN(vr);
    const int sa = WS_L + (4 * rg + w) * 4 * LREC;
#define ACT_LD(i) act[i] = ld_dev(ws, (((i) & 1) ? va1 : va0) + (((i) >> 1) & 7) * 512, sa + ((i) >> 4) * 4096)
    f32x4 tA[4], tB[4], tC[4];
    WD_SETUP();
    v4u s0, s1;
#define A_HEAD() do { MEMFENCE(); PIN(va0); PIN(va1); s0 = ld_dev(ws, vr, WS_S + (4 * rg + w) * 2048); s1 = ld_dev(ws, vr + 1024, WS_S + (4 * rg + w) * 2048); SB(); \
      static_for<0, 2 * LOOK>([&](auto K) { ACT_LD(K.value); SB(); }); } while (0)
    WAIT3();
    STAMP(15);
    u32 spin = 0;
    A_HEAD();
    BAR();
    for (;; ++spin) {
      static_for<0, PD>([&](auto K) { ring[K.value ^ 1] = FRAG(K.value ^ 1); SB(); });
      if (__builtin_expect(__any(s0.w != tag || s1.w != tag), 0)) { _Pragma("nounroll") do { __builtin_amdgcn_s_sleep(RS3); A_HEAD(); } while (__any(s0.w != tag || s1.w != tag) && ++spin < SPIN_CAP); ++spin; }
      PIN(va0); PIN(va1);
      u32 m = (s0.x + s0.y + s0.z) + (s1.x + s1.y + s1.z);
      static_for<0, 64>([&](auto K) {
        constexpr int n = K.value, ks = n >> 2, q = (n & 1) * 2;
        f32x4* acc = (n & 2) ? tB : tA;
        mma<ks == 0>(acc[q], ring[n & 7], act[2 * ks]); SB();
        ring[NXT(n) & 7] = FRAG(NXT(n));
        if constexpr (!(n & 1) && ks + LOOK < 16) ACT_LD(2 * (ks + LOOK) + ((n >> 1) & 1));
        SB();
        mma<ks == 0>(acc[q + 1], ring[n & 7], act[2 * ks + 1]); SB();
        { const v4u v = act[2 * ks + ((n >> 1) & 1)]; u32 t = (n & 1) ? m + v.z + v.w : m + v.x + v.y; PIN(t); m = t; }
        SB();
      });
      if (__builtin_expect(__all(add_x32(add_x16(m)) == 8u * tag) || spin > SPIN_CAP, 1)) break;
      s0.w = ~tag;
    }
    COUNT(22, spin);
    BAR_N(15);
    STAMP(16);
#define G1_TILE(T, acc, ...) static_for<0, 32>([&](auto K) { constexpr int s = K.value, n = 32 * (T) + s, ks = s >> 1, q = (s & 1) * 2, j0 = epos(n), j1 = epos(n + 1), jm = j0 + (j1 - j0 - (T) / 4) / 2; \
      mma<ks == 0>(acc[q], ring[n & 7], act[2 * ks]); SB(); \
      if constexpr (n + PD < 192) ring[NXT(n) & 7] = FRAG(n + PD < 192 ? NXT(n) : 0); SB(); \
      EPI(j0, jm); \
      mma<ks == 0>(acc[q + 1], ring[n & 7], act[2 * ks + 1]); SB(); \
      __VA_ARGS__; EPI(jm, j1); SB(); })
#define PG2 16
#define PG4 12
#define PG3 10
    G1_TILE(2, tC, { });
    FSTAMP(26);
    G1_TILE(3, tA, { if constexpr (s == PG2) BAR_N(15); });
    FSTAMP(27);
    if (__builtin_expect(six, 1)) {
      G1_TILE(4, tB, { });
      FSTAMP(28);
      G1_TILE(5, tC, { if constexpr (s == PG4) BAR_N(15); });
      STAMP(17);
      MMA_DONE(); SB();
      EPI(kEp.first[5], kEp.n);
    } else {
      MMA_DONE(); SB();
      EPI(epos(128), kEp.first[4]);
      static_for<E_X1, E_STK>([&](auto KK) { epi_op<KK.value, 31>(e, tA, tag); });
      st_dev(ws, vk + 3 * HREC, e.k4);
#define SKSL 14
      __builtin_amdgcn_s_sleep(SKSL);
      BAR_N(15);
      FSTAMP(28);
      __builtin_amdgcn_s_sleep(SKSL + 6);
      STAMP(17);
    }
    STAMP(18);
    BAR();
#define CSL 10
    __builtin_amdgcn_s_sleep(CSL);
    DMA_WDS(4, 0);
    DMA_WDS(5, 1);
    BAR();
    if (w & 1) VM_WAIT(4); else VM_WAIT(8);
    BAR();
    VM_WAIT(0);
    BAR();
  }
  STAMP(24);
  STAMP_FLUSH();
  UNDEF_RA();
  asm volatile("" : "=v"(wa_));
  }
  PINS(wv);
  if (wv >= 4) {
    STAMP(0);
    const int hw = wv - 4;
    asm volatile("" :: "v"(cpf), "v"(ra[0]));
    if (hw < 2) {
    const rsrc_t ws = mkrs(p.ws, WS_END);
    const int T = tid - 256, vs = wa_.x;
    const u32 sh = wa_.y;
    const lp_t ca0 = (lp_t)(size_t)wa_.z, ca1 = (lp_t)(size_t)wa_.w;
    u32 al[8][2], ck[4];
#pragma unroll
    for (int i = 0; i < 8; ++i) { const u32 a = ra[i].x, b = ra[i].y, d = ra[i].z; al[i][0] = __builtin_amdgcn_alignbit(b, a, sh); al[i][1] = __builtin_amdgcn_alignbit(d, b, sh); }
    v4u v[4];
#pragma unroll
    for (int e = 0; e < 4; ++e) {
#pragma unroll
      for (int r = 0; r < 4; ++r) v[e][r] = __builtin_amdgcn_perm(al[4 + r][e >> 1], al[r][e >> 1], (e & 1) ? 0x07060302u : 0x05040100u);
      st_dev(ws, vs + e * 128, v[e]);
      ck[e] = sum4(0u, v[e]);
    }
    const u32 c0 = pair_total(ck[0], ck[1], T & 1), c1 = pair_total(ck[2], ck[3], T & 1);
    const u32 cq = ((T & 2) ? c1 : c0) + dppu<0x4e>((T & 2) ? c0 : c1);
    lds_add(ca0, cq); lds_add(ca1, cq);
    flag_set(fmine, 1);
    STAMP(1);
  }
    u32 tag;
    v4u y0[8];
    WD_SETUP();
    {
      const rsrc_t rwg = mkrs(p.wg, W1SZ), rwu = mkrs(p.wu, W1SZ);
      const int vx = 4 * (lane >> 3) * (F_ * 2) + ft0 * 32 + (lane & 7) * 16, vx8 = vx + 32 * F_ * 2;
      const int vy = six ? 4 * (lane >> 2) * (F_ * 2) + (ft0 + 4) * 32 + (lane & 3) * 16 : OOB;
      const u32 ax = ((lane >> 1) & 3) * SLOT + (16 * hw + (lane >> 3)) * 512 + (lane & 1) * 128 + (lane & 7) * 16;
#define PUT_X(u, off) do { transpose8(u); const u32 a_ = ax + (off); _Pragma("unroll") for (int i = 0; i < 8; ++i) lds_w128((lp_t)(size_t)(a_ ^ (i * 16)), u[i]); SB(); } while (0)
      v4u rb[8], rc[8], rd[8];
      const lp_t fall = lptr(SCR + L_F + (lane & 3) * 256 + lane * 4);
      const int s0 = 64 * hw * (F_ * 2);
#define THR 4
#define NA 2
#define NB 10
#define N0 0
#define NS 7
#define NL 99
#define YD 1
#define THRU 4
#define NH 2
#define TS 0
#define F1 2
#define F2 5
#define STEP(n) do { constexpr int j = (n) & 7; const int so_ = s0 + ((j & 3) + 256 * (j >> 2)) * (F_ * 2); \
        if ((n) == NA) { flag_wait_lazy(fall, 3); cpf = touch(rwd, lane < 11 ? wg * 43 + hw * 11 + lane : 16384); } if ((n) == NB) flag_wait_lazy(fall, 7); \
        if ((n) == NA + NH) flag_wait_lazy(fall, 4); if ((n) == NB + NH) flag_wait_lazy(fall, 8); \
        if ((n) == N0) flag_wait_lazy(fall, 2); if ((n) == NS) flag_wait_lazy(fall, 6); if ((n) == NL) flag_wait_lazy(fall, 10); \
        if ((n) > 1) asm volatile("s_waitcnt vmcnt(%0)" :: "n"((YD && (n) > 8) ? THRU : THR) : "memory"); \
        if ((n) < 8) { ra[j] = ld_in(rwg, vx, so_); rb[j] = ld_in(rwg, vx8, so_); y0[j] = ld_in(rwg, vy, so_); } \
        else { constexpr int j2 = (2 * (n)) & 7; const int sp_ = s0 + ((j2 & 3) + 256 * (j2 >> 2)) * (F_ * 2); \
          if ((n) < 12) { rc[j2] = ld_in(rwu, vx, sp_); rc[j2 + 1] = ld_in(rwu, vx, sp_ + F_ * 2); } else { rd[j2] = ld_in(rwu, vx8, sp_); rd[j2 + 1] = ld_in(rwu, vx8, sp_ + F_ * 2); } } SB(); } while (0)
      PACE(0, TS);
      STAMP(2);
      STEP(0); STEP(1); STEP(2); STEP(3); STEP(4); STEP(5); STEP(6); STEP(7); STEP(8); STEP(9);
      STAMP(3);
      if (NB > 10) STEP(10);
      if (NB > 11) STEP(11);
      PUT_X(ra, 0);
      PUT_X(rb, 4096);
      if (NB <= 10) STEP(10);
      if (NB <= 11) STEP(11);
      STEP(12); STEP(13); STEP(14); STEP(15);
      PUT_X(rc, 256);
      tag = *(LAS u32*)(fall + L_TG);
      STAMP(4);
      PUT_X(rd, 4096 + 256);
      PIN(tag);
      asm volatile("" :: "v"(cpf));
    }
    STAMP(5);
    BAR();
    const rsrc_t ws = mkrs(p.ws, WS_END);
    const int kh = hw & 1, rl = hw >> 1, g = lane >> 4;
    constexpr int PD = 4;
    const u32 ayo = hw * 8192 + g * 2048 + ((lane >> 2) & 3) * 256 + (lane & 4) * 16 + ((lane & 3) ^ ((lane >> 1) & 2)) * 16;
    const lp_t ay0[2] = {lptr(ayo + 4 * SLOT), lptr((ayo ^ 64) + 4 * SLOT)}, ad = lptr(3 * SLOT + hw * 8192 + lane * 16);
#define PUT_Y(a, y, j0, j1) _Pragma("unroll") for (int j = j0; j < j1; ++j) lds_w128(a[j & 1] + (j >> 2) * 1024 + (j & 2) * 64, y[j])
    PUT_Y(ay0, y0, 0, 8);
    v4u ring[8], hfA[8], hfB[8], hfC[8], hfD[8], hfE[8];
    u32 hkA, hkB, hkC, hkD, hkE, ms = 0, nfix = 0;
    lp_t wd[3][4];
    {
      const u32 dl = (g * 1024 + ((lane >> 2) & 3) * 128 + ((lane >> 3) & 3) * 32 + (lane & 3) * 8) ^ (kh * 64);
#pragma unroll
      for (int i = 0; i < 12; ++i) wd[i >> 2][i & 3] = lptr((dl ^ ((i & 3) * 32)) + (i >> 2) * 2 * SLOT + kh * 16384);
    }
    auto frag = [&](auto NN) -> v4u {
      constexpr int N = NN.value, t = N < 64 ? N >> 4 : N < 76 ? 4 : 5, s = N < 64 ? (N & 15) : N < 76 ? N - 64 : N - 76, slot = (t + 1) % 5;
      const lp_t b = wd[slot >> 1][s & 3] + (slot & 1) * SLOT + (s >> 2) * 4096;
      return lds_tr2(b, b + 512);
    };
    const int hb = WS_H + (4 * rg + 2 * (ch >> 3) + rl) * NFT * HREC;
    int hs[4];
#pragma unroll
    for (int i = 0; i < 4; ++i) { hs[i] = hb + tile0_of(8 * kh + 2 * i) * HREC; PINS(hs[i]); }
    int vl = (g & 1) * 512 + (lane & 15) * 16 + (g >> 1) * 6 * HREC, vl3 = vl - (g >> 1) * 2 * HREC, vl4 = vl + 4 * HREC, vlz = (kh && g >= 2) ? OOB : vl4, vq, vq4;
    {
      const int st = (lane & 2) + ((lane >> 1) & 4) + (g >> 1);
      vq = hb + 1024 + ((2 * (g & 1) + (lane & 1)) * 8 + (lane & 4)) * 4 + tile0_of(8 * kh + st) * HREC;
      vq4 = st < 6 - kh ? vq + 4 * HREC : OOB;
    }
    const int u = 16 * rg + 8 * (ch >> 3) + 4 * rl + ((lane >> 3) & 1), col = 64 * CT2 + 32 * kh;
    int vh = u * LREC + (col & 255) * 32 + g * 128 + (lane & 7) * 16 + (col >> 8) * 8, vs = u * 512 + (lane & 7) * 16 + 4;
    int vout = ((32 * (8 * (u >> 5) + (lane & 7)) + (u & 31)) * D_ + col + 16 * (g & 1) + 8 * (g >> 1)) * 2;
    lp_t rw = lptr(2 * SLOT + hw * 4096 + lane * 16), rr_ = lptr(2 * SLOT + (hw ^ 1) * 4096 + lane * 16);
    int vyd = ((lane >> 2) ^ (g & 1)) * (F_ * 2) + (ft0 + 4) * 32 + ((lane & 3) ^ ((lane >> 3) & 2)) * 16;
    PIN(vyd);
    u32 t8 = 8u * tag, t6 = ((kh && g >= 2) ? 4u : 6u) * tag;
    PIN(vl); PIN(vl3); PIN(vl4); PIN(vlz); PIN(vq); PIN(vq4); PIN(vh); PIN(vs); PIN(vout); PIN(t8); PIN(t6);
#define H_LD(X, t, j) do { \
      if constexpr ((j) == 8) hk##X = ld_dev1(ws, ((t) < 4 ? vq : vq4) + ((t) & 3) * HREC, 0); \
      else if constexpr ((t) < 4 || (j) < 6) hf##X[(j) & 7] = ld_dev(ws, ((t) < 4 ? ((j) < 6 ? vl : vl3) : (j) < 4 ? vl4 : vlz) + ((t) & 3) * HREC + ((j) & 1) * 256, hs[((j) >> 1) & 3]); } while (0)
#define H_LDALL(X, t) static_for<0, 9>([&](auto J) { THROTTLE(J.value); H_LD(X, t, J.value ? J.value - 1 : 8); })
#define H_SUM(X, nf, j) do { if constexpr ((j) >= 0 && (j) < (nf)) { ms = sum4((j) ? ms : hk##X, hf##X[(j) & 7]); u32 t_ = ms; PIN(t_); ms = t_; } \
      else if constexpr ((j) >= (nf) && (j) < (nf) + 3) { u32 t_ = ms + dppu<(j) == (nf) ? 0xb1 : (j) == (nf) + 1 ? 0x4e : 0x128>(ms); PIN(t_); ms = t_; } } while (0)
#define H_SUMALL(X, nf) static_for<0, (nf) + 3>([&](auto J) { H_SUM(X, nf, J.value); })
#define H_FIX(X, t, nf, want) do { if (__builtin_expect(__any(ms != (want)), 0)) { u32 spin_ = 0; _Pragma("nounroll") do { __builtin_amdgcn_s_sleep(RS3); MEMFENCE(); nfix |= 1u << (4 * (t)); \
        H_LDALL(X, t); H_SUMALL(X, nf); } while (__any(ms != (want)) && ++spin_ < SPIN_CAP); } SB(); } while (0)
    v4u d3[8];
    f32x4 o[4][2];
    v2u hr[4], so[2];
#define GRP() asm volatile("s_waitcnt vmcnt(%0)" :: "n"(TH2) : "memory")
#define RAW4(dst, t, i0) do { _Pragma("unroll") for (int i = i0; i < i0 + 4; ++i) { THROTTLE(i); dst[i] = ld_in(rwd, (i & 1) ? vd1 : vd, sd[i >> 1] + ((t) * 16 + (i & 1) * 8) * (D_ * 2)); } SB(); } while (0)
#define DMA4(t, slot, i0) do { _Pragma("unroll") for (int i = i0; i < i0 + 4; ++i) { THROTTLE(i); DMA_WD1(t, slot, i); } } while (0)
#define H4(X, t, h) do { static_for<5 * (h), 5 + 4 * (h)>([&](auto J) { if (J.value != 4) THROTTLE(J.value - 5 * (h)); H_LD(X, t, J.value ? J.value - 1 : 8); }); SB(); } while (0)
#define PUT_D(d, off, i0, i1) do { _Pragma("unroll") for (int i = i0; i < i1; ++i) lds_w128(ad + (off) + i * 1024, d[i]); SB(); } while (0)
#define RESID(i) do { const u32 v0 = hr[i].x, v1 = hr[i].y, mb = so[(i) & 1].x, sb = so[(i) & 1].y; float q[4] = {blo(v0), bhi(v0), blo(v1), bhi(v1)}; \
      _Pragma("unroll") for (int r = 0; r < 4; ++r) { q[r] = __builtin_fmaf(q[r], asf(sb), asf(mb)); PIN(q[r]); o[(i) >> 1][(i) & 1][r] = q[r]; } PIN(o[(i) >> 1][(i) & 1]); SB(); } while (0)
#define PSLEEP 16
    __builtin_amdgcn_s_sleep(PSLEEP);
#define SPRIO 3
#define SPRIO1 0
#define LOWP() __builtin_amdgcn_s_setprio(SPRIO1)
#define HIGHP() __builtin_amdgcn_s_setprio(SPRIO)
#define SPRIO2 3
    __builtin_amdgcn_s_setprio(SPRIO);
    RAW4(d3, 3, 0); RAW4(d3, 3, 4);
    STAMP(6);
    BAR();
    if (YD && six) {
      const rsrc_t rwu = mkrs(p.wu, W1SZ);
#pragma unroll
      for (int i = 0; i < 8; ++i) {
        asm volatile("s_waitcnt vmcnt(4)\n\ts_mov_b32 m0, %0\n\ts_nop 0\n\tbuffer_load_dwordx4 %1, %2, %3 offen lds"
                     :: "s"(hw * 8192 + i * 1024), "v"(vyd), "s"(rwu), "s"((16 * (4 * hw + (i >> 1)) + 256 * (i & 1)) * (F_ * 2)) : "memory");
      }
    }
#pragma unroll
    for (int i = 0; i < 2; ++i) so[i] = __builtin_amdgcn_raw_buffer_load_b64(ws, vs + i * 1024, WS_S, AUX_SC1);
#pragma unroll
    for (int i = 0; i < 4; ++i) hr[i] = __builtin_amdgcn_raw_buffer_load_b64(ws, vh + (i >> 1) * 512, WS_L + (i & 1) * 2 * LREC, AUX_SC1);
    SB();
    DMA4(0, 1, 0); DMA4(0, 1, 4);
    if (YD) VM_WAIT(8);
    STAMP(7);
    BAR();
    PIN(hr[3]);
    DMA4(1, 2, 0); DMA4(1, 2, 4); LOWP(); RESID(0); RESID(1); RESID(2); RESID(3);
    HIGHP();
    H4(A, 0, 0); H4(A, 0, 1);
#define BSL 0
    __builtin_amdgcn_s_sleep(BSL);
    H4(B, 1, 0); H4(B, 1, 1);
    PIN(hfA[7]);
    STAMP(8);
    BAR();
    DMA4(2, 3, 0); DMA4(2, 3, 4);
    LOWP();
    H_SUMALL(A, 8);
    H_FIX(A, 0, 8, t8);
    HIGHP();
    STAMP(9);
    H4(C, 2, 0); H4(C, 2, 1);
    VM_WAIT(9);
    H_SUMALL(B, 8);
    H_FIX(B, 1, 8, t8);
    static_for<0, PD>([&](auto K) { ring[K.value ^ 1] = FRAG(K.value ^ 1); SB(); });
    STAMP(10);
    LOWP();
    STAMP(11);
#define G2_ROUND(X, n0, ns, ...) static_for<0, ns>([&](auto K) { constexpr int s = K.value, n = (n0) + s, ct = s & 3, a = (s >> 2) * 2; \
      mma<n == 2 || n == 3>(o[ct][0], ring[n & 7], hf##X[a]); SB(); \
      if constexpr (n + PD < 88) ring[NXT(n) & 7] = FRAG(n + PD < 88 ? NXT(n) : 0); SB(); \
      mma<n == 2 || n == 3>(o[ct][1], ring[n & 7], hf##X[a + 1]); SB(); \
      __VA_ARGS__; SB(); })
#define PP 0
#define L3 16
#define L4 36
#define L5 50
#define LATE() do { if constexpr (n >= L3 && n < L3 + 9) H_LD(D, 3, n > L3 ? n - L3 - 1 : 8); \
      if constexpr (n >= L4 && n < L4 + 7) H_LD(E, 4, n > L4 ? n - L4 - 1 : 8); if constexpr (n >= L5 && n < L5 + 7) H_LD(A, 5, n > L5 ? n - L5 - 1 : 8); } while (0)
#define PG5 15
#define PS 1
#define MID() do { if constexpr (n == PG5) { BAR_N(15); __builtin_amdgcn_s_setprio(SPRIO2); } \
      if constexpr (n > PG5 && n <= PG5 + 8 * PS && (n - PG5 - 1) % PS == 0) lds_w128(ad + SLOT + ((n - PG5 - 1) / PS) * 1024, d3[((n - PG5 - 1) / PS) & 7]); \
      if constexpr (n == 43) BAR_N(8); LATE(); } while (0)
    static_assert(PG5 >= 15 && PG5 <= 27 && PG5 + 8 * PS <= 39, "");
    G2_ROUND(A, 0, 16, { MID(); });
    FSTAMP(14);
    G2_ROUND(B, 16, 16, { H_SUM(C, 8, s - 5); MID(); });
    H_FIX(C, 2, 8, t8);
    FSTAMP(15);
    G2_ROUND(C, 32, 16, { H_SUM(D, 8, s - 5); MID(); });
    H_FIX(D, 3, 8, t8);
    FSTAMP(16);
    G2_ROUND(D, 48, 16, { LATE(); H_SUM(E, 6, s - 7); if constexpr (s == 11) BAR_N(15); });
    H_FIX(E, 4, 6, t6);
    FSTAMP(17);
    G2_ROUND(E, 64, 12, { H_SUM(A, 6, s - 3); if constexpr (s == 7) BAR_N(15); });
    H_FIX(A, 5, 6, t6);
    FSTAMP(18);
    G2_ROUND(A, 76, 12, { });
    COUNT(23, nfix);
    STAMP(12);
    MMA_DONE(); SB();
#pragma unroll
    for (int i = 0; i < 4; ++i) lds_wf(rw + i * 1024, o[2 + (i & 1)][i >> 1]);
    BAR();
    const rsrc_t ro = mkrs(p.out, S_ * D_ * 2);
#pragma unroll
    for (int i = 0; i < 2; ++i) {
      const f32x4 v0 = o[0][i] + lds_rf(rr_ + i * 2048), v1 = o[1][i] + lds_rf(rr_ + i * 2048 + 1024);
      const auto x = __builtin_amdgcn_permlane16_swap(pk(v0[0], v0[1]), pk(v1[0], v1[1]), false, false);
      const auto y = __builtin_amdgcn_permlane16_swap(pk(v0[2], v0[3]), pk(v1[2], v1[3]), false, false);
      const v4u q = {x[0], y[0], x[1], y[1]};
      __builtin_amdgcn_raw_buffer_store_b128(q, ro, vout + i * 2048, 0, OAUX);
    }
    STAMP(13);
    STAMP_FLUSH();
  }
}

extern "C" void kernel_launch(void* const* d_in, const int* in_sizes, int n_in,
                              void* d_out, int out_size, void* d_ws, size_t ws_size,
                              hipStream_t stream) {
  Params p;
  p.x = (const u16*)d_in[0]; p.wr = (const u16*)d_in[1]; p.wi = (const u16*)d_in[2];
  p.wg = (const u16*)d_in[4]; p.wu = (const u16*)d_in[5]; p.wd = (const u16*)d_in[6];
  p.out = (u16*)d_out; p.ws = d_ws;
  (void)hipFuncSetAttribute((const void*)gfnet_geglu_kernel, hipFuncAttributeMaxDynamicSharedMemorySize, 5 * SLOT);
  gfnet_geglu_kernel<<<256, 512, 5 * SLOT, stream>>>(p);
}
```
